```python
import torch

def _softmax(x, axis=-1):
    return torch.softmax(x, dim=axis)

def _gelu(x):
    return torch.nn.functional.gelu(x, approximate='tanh')

def _silu(x):
    return torch.nn.functional.silu(x)

def _sigmoid(x):
    return torch.sigmoid(x)

def _layernorm(x, axis=-1, eps=1e-05):
    m = x.mean(dim=axis, keepdim=True)
    v = x.var(dim=axis, keepdim=True, unbiased=False)
    return (x - m) / torch.sqrt(v + eps)

def _scatter_add(tgt, idx, src, axis=0):
    return tgt.clone().index_add_(axis, idx, src)

def _segment_sum(src, seg, n):
    out = torch.zeros((n,) + src.shape[1:], dtype=src.dtype)
    return out.index_add_(0, seg, src)

def _pad(x, pw):
    flat = tuple((v for pair in reversed(pw) for v in pair))
    return torch.nn.functional.pad(x, flat)
kernel_name = 'adaLN_cond__compressed_kv_cla__mixer_gmlp__prompt'

def setup_inputs():

    _w = lambda a: (lambda t: t.to(torch.bfloat16) if t.is_floating_point() else t)(torch.as_tensor(a))

    def _A_setup():
        B, H, Sq, Sk, D, M = (1, 32, 2048, 2048, 128, 512)
        Dc = 4 * D
        return {'x': _w((lambda *s: torch.randn(*s, dtype=torch.bfloat16) * 0.05)(B, Sq, M)), 'c_kv': _w((lambda *s: torch.randn(*s, dtype=torch.bfloat16) * 1.6)(B, Sk, Dc)), 'w_q': _w((lambda *s: torch.randn(*s, dtype=torch.bfloat16) * 0.8)(M, H, D) * M ** (-0.5)), 'w_uk': _w((lambda *s: torch.randn(*s, dtype=torch.bfloat16) * 0.8)(Dc, H, D) * Dc ** (-0.5)), 'w_uv': _w((lambda *s: torch.randn(*s, dtype=torch.bfloat16) * 1.6)(Dc, H, D) * Dc ** (-0.5)), 'w_o': _w((lambda *s: torch.randn(*s, dtype=torch.bfloat16) * 1.6)(H, D, M) * (H * D) ** (-0.5))}
    _A_in = _A_setup()

    def _B_setup():
        B, S, D, F = (1, 2048, 512, 1376)
        Fh = F // 2
        return {'x': _w((lambda *s: torch.randn(*s, dtype=torch.bfloat16) * 0.05)(B, S, D)), 'w_in': _w((lambda *s: torch.randn(*s, dtype=torch.bfloat16) * 0.4)(D, 2 * Fh)), 'w_sp': _w(0.01 * (lambda *s: torch.randn(*s, dtype=torch.bfloat16) * 0.4)(S, S)), 'b_sp': _w(1.0 + 0.01 * (lambda *s: torch.randn(*s, dtype=torch.bfloat16) * 0.05)(S)), 'w_out': _w((lambda *s: torch.randn(*s, dtype=torch.bfloat16) * 0.05)(Fh, D))}
    _B_in = _B_setup()
    return {'A_x': _A_in['x'], 'A_c_kv': _A_in['c_kv'], 'A_w_q': _A_in['w_q'], 'A_w_uk': _A_in['w_uk'], 'A_w_uv': _A_in['w_uv'], 'A_w_o': _A_in['w_o'], 'B_x': _B_in['x'], 'B_w_in': _B_in['w_in'], 'B_w_sp': _B_in['w_sp'], 'B_b_sp': _B_in['b_sp'], 'B_w_out': _B_in['w_out'], 'c': _w((lambda *s: torch.randn(*s, dtype=torch.bfloat16) * 0.1)(1, 512) * 8.0), 'w_mod': _w((lambda *s: torch.randn(*s, dtype=torch.bfloat16) * 0.1)(512, 6 * 512) * 512 ** (-0.5) * 8.0)}

def reference(A_x, A_c_kv, A_w_q, A_w_uk, A_w_uv, A_w_o, B_x, B_w_in, B_w_sp, B_b_sp, B_w_out, c, w_mod):

    def _A_ref(x, c_kv, w_q, w_uk, w_uv, w_o):
        B, Sq, M = (x.shape[0], x.shape[1], x.shape[2])
        Sk = c_kv.shape[1]
        H, D = (w_q.shape[1], w_q.shape[2])
        q = torch.einsum('bqm,mhd->bhqd', x, w_q)
        k = torch.einsum('bkc,chd->bhkd', c_kv, w_uk)
        v = torch.einsum('bkc,chd->bhkd', c_kv, w_uv)
        scores = torch.einsum('bhqd,bhkd->bhqk', q, k) * D ** (-0.5)
        off = Sk - Sq
        qi = torch.arange(Sq)[:, None]
        kj = torch.arange(Sk)[None, :]
        cmask = kj <= qi + off
        scores = torch.where(cmask[None, None], scores, -1000000000.0)
        p = _softmax(scores, axis=-1)
        ctx = torch.einsum('bhqk,bhkd->bhqd', p, v)
        return torch.einsum('bhqd,hdm->bqm', ctx, w_o)

    def _B_ref(x, w_in, w_sp, b_sp, w_out):
        B, S, D = x.shape
        F = w_in.shape[-1]
        Fh = F // 2
        z = _gelu(torch.einsum('bsd,df->bsf', x, w_in))
        z1 = z[..., :Fh].clone()
        z2 = _layernorm(z[..., Fh:], axis=-1)
        g = torch.einsum('ts,bsf->btf', w_sp, z2) + b_sp[None, :, None]
        return torch.einsum('bsf,fd->bsd', z1 * g, w_out)
    x = A_x
    mod = (_silu(c) @ w_mod).reshape(c.shape[0], 6, 1, -1)
    sa, ca, ga, sb, cb, gb = (mod[:, 0].clone(), mod[:, 1].clone(), mod[:, 2].clone(), mod[:, 3].clone(), mod[:, 4].clone(), mod[:, 5].clone())
    h = _layernorm(x, axis=-1) * (1 + ca) + sa
    x = x + ga * _A_ref(h, A_c_kv, A_w_q, A_w_uk, A_w_uv, A_w_o)
    h = _layernorm(x, axis=-1) * (1 + cb) + sb
    x = x + gb * _B_ref(h, B_w_in, B_w_sp, B_b_sp, B_w_out)
    return x
```

```cpp
#include <hip/hip_runtime.h>
#include <hip/hip_bf16.h>
#include <stdint.h>

typedef unsigned short u16;
typedef __attribute__((ext_vector_type(8))) short bf16x8;
typedef __attribute__((ext_vector_type(4))) short s16x4;
typedef __attribute__((ext_vector_type(16))) float f32x16;
typedef __attribute__((ext_vector_type(4))) float f32x4;
typedef __attribute__((ext_vector_type(4))) unsigned u32x4;
typedef __attribute__((ext_vector_type(2))) unsigned u32x2;
#define LDSP __attribute__((address_space(3)))

constexpr int S_ = 2048, DM = 512, NH = 32, HD = 4096, FF = 1376, FH = 688, FHP = 704;

__device__ __forceinline__ float bf2f(unsigned v16) { return __uint_as_float(v16 << 16); }
__device__ __forceinline__ float bflo(unsigned w) { return __uint_as_float(w << 16); }
__device__ __forceinline__ float bfhi(unsigned w) { return __uint_as_float(w & 0xffff0000u); }
typedef __attribute__((ext_vector_type(2))) __bf16 bf16x2_t;
typedef __attribute__((ext_vector_type(2))) float f32x2_t;
__device__ __forceinline__ unsigned pack2(float lo, float hi) {
  f32x2_t f = {lo, hi};
  bf16x2_t r = __builtin_convertvector(f, bf16x2_t);
  return __builtin_bit_cast(unsigned, r);
}
__device__ __forceinline__ float wave_sum(float v) {
#pragma unroll
  for (int o = 32; o > 0; o >>= 1) v += __shfl_xor(v, o);
  return v;
}
__device__ __forceinline__ void glds16(const void* g, char* lds) {
  __builtin_amdgcn_global_load_lds((const unsigned*)g, (LDSP unsigned*)lds, 16, 0, 0);
}

__global__ __launch_bounds__(256) void k_mod(const u16* __restrict__ c, const u16* __restrict__ wmod, float* __restrict__ mod) {
  __shared__ float red[64][33];
  const int tid = threadIdx.x, cc = tid & 3, rg = tid >> 2;
  const int n0 = blockIdx.x * 32 + cc * 8;
  float acc[8] = {0, 0, 0, 0, 0, 0, 0, 0};
  u32x4 w[8]; float sv[8];
#pragma unroll
  for (int i = 0; i < 8; ++i) { w[i] = *(const u32x4*)(wmod + (size_t)(rg * 8 + i) * 3072 + n0); sv[i] = bf2f(c[rg * 8 + i]); }
#pragma unroll
  for (int i = 0; i < 8; ++i) {
    float cv = sv[i]; float s = cv / (1.f + __expf(-cv));
#pragma unroll
    for (int j = 0; j < 4; ++j) { acc[2 * j] += s * bflo(w[i][j]); acc[2 * j + 1] += s * bfhi(w[i][j]); }
  }
#pragma unroll
  for (int j = 0; j < 8; ++j) red[rg][cc * 8 + j] = acc[j];
  __syncthreads();
  if (tid < 32) {
    float s = 0;
#pragma unroll 8
    for (int i = 0; i < 64; ++i) s += red[i][tid];
    mod[blockIdx.x * 32 + tid] = s;
  }
}

__global__ __launch_bounds__(256) void k_ln1(const u16* __restrict__ x, const float* __restrict__ mod, u16* __restrict__ h) {
  const int lane = threadIdx.x & 63, row = blockIdx.x * 4 + (threadIdx.x >> 6);
  u32x4 xv = *(const u32x4*)(x + (size_t)row * DM + lane * 8);
  float v[8];
#pragma unroll
  for (int j = 0; j < 4; ++j) { v[2 * j] = bflo(xv[j]); v[2 * j + 1] = bfhi(xv[j]); }
  float s = 0;
#pragma unroll
  for (int j = 0; j < 8; ++j) s += v[j];
  const float mean = wave_sum(s) * (1.f / DM);
  float q = 0;
#pragma unroll
  for (int j = 0; j < 8; ++j) { v[j] -= mean; q += v[j] * v[j]; }
  const float rstd = rsqrtf(wave_sum(q) * (1.f / DM) + 1e-5f);
  const f32x4 sa0 = *(const f32x4*)(mod + lane * 8), sa1 = *(const f32x4*)(mod + lane * 8 + 4);
  const f32x4 ca0 = *(const f32x4*)(mod + DM + lane * 8), ca1 = *(const f32x4*)(mod + DM + lane * 8 + 4);
  float o[8];
#pragma unroll
  for (int j = 0; j < 4; ++j) { o[j] = v[j] * rstd * (1.f + ca0[j]) + sa0[j]; o[4 + j] = v[4 + j] * rstd * (1.f + ca1[j]) + sa1[j]; }
  u32x4 ov = {pack2(o[0], o[1]), pack2(o[2], o[3]), pack2(o[4], o[5]), pack2(o[6], o[7])};
  *(u32x4*)(h + (size_t)row * DM + lane * 8) = ov;
}

template <int NPART>
__global__ __launch_bounds__(256) void k_ln2(const u16* __restrict__ x, const float* __restrict__ part, const float* __restrict__ mod,
                                             float* __restrict__ x1, u16* __restrict__ h2) {
  const int lane = threadIdx.x & 63, row = blockIdx.x * 4 + (threadIdx.x >> 6);
  u32x4 xv = *(const u32x4*)(x + (size_t)row * DM + lane * 8);
  f32x4 a0 = {0, 0, 0, 0}, a1 = {0, 0, 0, 0};
#pragma unroll
  for (int p = 0; p < NPART; ++p) {
    const float* pp = part + (size_t)p * S_ * DM + (size_t)row * DM + lane * 8;
    a0 += *(const f32x4*)pp; a1 += *(const f32x4*)(pp + 4);
  }
  const f32x4 ga0 = *(const f32x4*)(mod + 2 * DM + lane * 8), ga1 = *(const f32x4*)(mod + 2 * DM + lane * 8 + 4);
  float v[8];
#pragma unroll
  for (int j = 0; j < 2; ++j) {
    v[2 * j] = bflo(xv[j]) + ga0[2 * j] * a0[2 * j]; v[2 * j + 1] = bfhi(xv[j]) + ga0[2 * j + 1] * a0[2 * j + 1];
    v[4 + 2 * j] = bflo(xv[2 + j]) + ga1[2 * j] * a1[2 * j]; v[4 + 2 * j + 1] = bfhi(xv[2 + j]) + ga1[2 * j + 1] * a1[2 * j + 1];
  }
  *(f32x4*)(x1 + (size_t)row * DM + lane * 8) = f32x4{v[0], v[1], v[2], v[3]};
  *(f32x4*)(x1 + (size_t)row * DM + lane * 8 + 4) = f32x4{v[4], v[5], v[6], v[7]};
  float s = 0;
#pragma unroll
  for (int j = 0; j < 8; ++j) s += v[j];
  const float mean = wave_sum(s) * (1.f / DM);
  float q = 0;
#pragma unroll
  for (int j = 0; j < 8; ++j) { v[j] -= mean; q += v[j] * v[j]; }
  const float rstd = rsqrtf(wave_sum(q) * (1.f / DM) + 1e-5f);
  const f32x4 sb0 = *(const f32x4*)(mod + 3 * DM + lane * 8), sb1 = *(const f32x4*)(mod + 3 * DM + lane * 8 + 4);
  const f32x4 cb0 = *(const f32x4*)(mod + 4 * DM + lane * 8), cb1 = *(const f32x4*)(mod + 4 * DM + lane * 8 + 4);
  float o[8];
#pragma unroll
  for (int j = 0; j < 4; ++j) { o[j] = v[j] * rstd * (1.f + cb0[j]) + sb0[j]; o[4 + j] = v[4 + j] * rstd * (1.f + cb1[j]) + sb1[j]; }
  u32x4 ov = {pack2(o[0], o[1]), pack2(o[2], o[3]), pack2(o[4], o[5]), pack2(o[6], o[7])};
  *(u32x4*)(h2 + (size_t)row * DM + lane * 8) = ov;
}

__global__ __launch_bounds__(256) void k_lnz(const u16* __restrict__ z, u16* __restrict__ z2n) {
  const int lane = threadIdx.x & 63, row = blockIdx.x * 4 + (threadIdx.x >> 6);
  const u16* zp = z + (size_t)row * FF + FH;
  const bool has2 = lane < 22;
  u32x4 a = *(const u32x4*)(zp + lane * 8);
  u32x4 b = {0, 0, 0, 0};
  if (has2) b = *(const u32x4*)(zp + 512 + lane * 8);
  float v[16];
#pragma unroll
  for (int j = 0; j < 4; ++j) { v[2 * j] = bflo(a[j]); v[2 * j + 1] = bfhi(a[j]); v[8 + 2 * j] = bflo(b[j]); v[8 + 2 * j + 1] = bfhi(b[j]); }
  float s = 0;
#pragma unroll
  for (int j = 0; j < 16; ++j) s += v[j];
  const float mean = wave_sum(s) * (1.f / FH);
  float q = 0;
#pragma unroll
  for (int j = 0; j < 8; ++j) { v[j] -= mean; q += v[j] * v[j]; }
  if (has2) {
#pragma unroll
    for (int j = 8; j < 16; ++j) { v[j] -= mean; q += v[j] * v[j]; }
  }
  const float rstd = rsqrtf(wave_sum(q) * (1.f / FH) + 1e-5f);
  u16* op = z2n + (size_t)row * FH;
  *(u32x4*)(op + lane * 8) = u32x4{pack2(v[0] * rstd, v[1] * rstd), pack2(v[2] * rstd, v[3] * rstd), pack2(v[4] * rstd, v[5] * rstd), pack2(v[6] * rstd, v[7] * rstd)};
  if (has2) *(u32x4*)(op + 512 + lane * 8) = u32x4{pack2(v[8] * rstd, v[9] * rstd), pack2(v[10] * rstd, v[11] * rstd), pack2(v[12] * rstd, v[13] * rstd), pack2(v[14] * rstd, v[15] * rstd)};
}

template <int BM, int BN, int WM, int WN, bool SWAP>
__device__ __forceinline__ void gemm_loop(const u16* __restrict__ A, int lda, const u16* __restrict__ B, int ldb,
                                          int m0, int n0, int k0, int nk, int nlim, int klim, char* smem,
                                          f32x16 (&acc)[BM / WM / 32][BN / WN / 32]) {
  constexpr int NW = WM * WN, TM = BM / WM, TN = BN / WN, MI = TM / 32, NI = TN / 32;
  constexpr int ABYTES = BM * 128, BBYTES = BN * 128, BUF = ABYTES + BBYTES;
  constexpr int AINS = BM / 8, BINS = BN / 8;
  static_assert(AINS % NW == 0 && BINS % NW == 0, "staging split");
  constexpr int AP = AINS / NW, BP = BINS / NW;
  const int tid = threadIdx.x, lane = tid & 63, wave = __builtin_amdgcn_readfirstlane(tid >> 6);
  const int wm = wave / WN, wn = wave % WN, r32 = lane & 31, hi = lane >> 5;
  const u16* asrc[AP]; const u16* bsrc[BP]; int brow[BP];
#pragma unroll
  for (int i = 0; i < AP; ++i) {
    const int ins = i * NW + wave, row = ins * 8 + (lane >> 3), ch = (lane & 7) ^ ((row >> 1) & 7);
    asrc[i] = A + (size_t)(m0 + row) * lda + k0 + ch * 8;
  }
#pragma unroll
  for (int i = 0; i < BP; ++i) {
    const int ins = i * NW + wave, st = ins * 4 + (lane >> 4), k4 = st / (BN / 32), n32 = st % (BN / 32);
    int col = n0 + n32 * 32 + (lane & 3) * 8; if (col > nlim - 8) col = nlim - 8;
    brow[i] = k0 + k4 * 4 + ((lane >> 2) & 3);
    bsrc[i] = B + col;
  }
  auto stage = [&](int t, int b) {
    char* base = smem + b * BUF;
#pragma unroll
    for (int i = 0; i < AP; ++i) glds16(asrc[i] + t * 64, base + (i * NW + wave) * 1024);
#pragma unroll
    for (int i = 0; i < BP; ++i) {
      int r = brow[i] + t * 64; if (r > klim - 1) r = klim - 1;
      glds16(bsrc[i] + (size_t)r * ldb, base + ABYTES + (i * NW + wave) * 1024);
    }
  };
  int aoff[MI];
#pragma unroll
  for (int i = 0; i < MI; ++i) { const int row = wm * TM + i * 32 + r32; aoff[i] = row * 128; }
  const int asw = ((wm * TM + r32) >> 1) & 7;
  const int boff = ABYTES + (2 * hi * (BN / 32) + wn * NI) * 256 + ((lane >> 2) & 3) * 64 + ((lane >> 4) & 1) * 32 + (lane & 3) * 8;

  stage(0, 0);
  asm volatile("s_waitcnt vmcnt(0)" ::: "memory");
  __syncthreads();
  for (int t = 0; t < nk; ++t) {
    const int b = t & 1;
    if (t + 1 < nk) stage(t + 1, b ^ 1);
    char* base = smem + b * BUF;
#pragma unroll
    for (int s = 0; s < 4; ++s) {
      bf16x8 af[MI], bfr[NI];
#pragma unroll
      for (int i = 0; i < MI; ++i) af[i] = *(const bf16x8*)(base + aoff[i] + (((2 * s + hi) ^ asw) * 16));
#pragma unroll
      for (int j = 0; j < NI; ++j) {
        const char* p = base + boff + (4 * s * (BN / 32) + j) * 256;
        s16x4 lo = __builtin_amdgcn_ds_read_tr16_b64_v4i16((LDSP s16x4*)p);
        s16x4 hh = __builtin_amdgcn_ds_read_tr16_b64_v4i16((LDSP s16x4*)(p + (BN / 32) * 256));
        bfr[j] = bf16x8{lo[0], lo[1], lo[2], lo[3], hh[0], hh[1], hh[2], hh[3]};
      }
#pragma unroll
      for (int i = 0; i < MI; ++i)
#pragma unroll
        for (int j = 0; j < NI; ++j) {
          if (SWAP) acc[i][j] = __builtin_amdgcn_mfma_f32_32x32x16_bf16(bfr[j], af[i], acc[i][j], 0, 0, 0);
          else      acc[i][j] = __builtin_amdgcn_mfma_f32_32x32x16_bf16(af[i], bfr[j], acc[i][j], 0, 0, 0);
        }
    }
    asm volatile("s_waitcnt vmcnt(0)" ::: "memory");
    __syncthreads();
  }
}

__global__ __launch_bounds__(256, 2) void k_qkv(const u16* __restrict__ h, const u16* __restrict__ ckv,
                                                const u16* __restrict__ wq, const u16* __restrict__ wuk, const u16* __restrict__ wuv,
                                                u16* __restrict__ q, u16* __restrict__ kimg, u16* __restrict__ vimg) {
  extern __shared__ __attribute__((aligned(16))) char smem[];
  constexpr int BM = 128, BN = 128, WM = 2, WN = 2;
  const int bid = blockIdx.x;
  const int mt = bid & 15, nt = bid >> 4;
  const int which = nt >> 5, head = nt & 31;
  const int m0 = mt * BM, n0 = head * 128;
  const int tid = threadIdx.x, lane = tid & 63, wave = tid >> 6, wm = wave / WN, wn = wave % WN, r32 = lane & 31, hi = lane >> 5;
  f32x16 acc[2][2] = {};
  if (which == 2) {
    gemm_loop<BM, BN, WM, WN, false>(ckv, DM, wuv, HD, m0, n0, 0, 8, HD, DM, smem, acc);
#pragma unroll
    for (int i = 0; i < 2; ++i)
#pragma unroll
      for (int j = 0; j < 2; ++j) {
        const int d = wn * 64 + j * 32 + r32;
        const int key0 = m0 + wm * 64 + i * 32;
        char* tile = (char*)vimg + ((size_t)head * 32 + (key0 >> 6)) * 16384 + d * 128;
#pragma unroll
        for (int sp = 0; sp < 2; ++sp) {
          const int c = 2 * (((key0 >> 5) & 1) * 2 + sp) + hi;
          const f32x16& a = acc[i][j];
          u32x4 w = {pack2(a[8 * sp], a[8 * sp + 1]), pack2(a[8 * sp + 2], a[8 * sp + 3]), pack2(a[8 * sp + 4], a[8 * sp + 5]), pack2(a[8 * sp + 6], a[8 * sp + 7])};
          *(u32x4*)(tile + ((c ^ ((d >> 1) & 7)) * 16)) = w;
        }
      }
  } else {
    gemm_loop<BM, BN, WM, WN, true>(which == 0 ? h : ckv, DM, which == 0 ? wq : wuk, HD, m0, n0, 0, 8, HD, DM, smem, acc);
#pragma unroll
    for (int i = 0; i < 2; ++i)
#pragma unroll
      for (int j = 0; j < 2; ++j) {
        const int m = m0 + wm * 64 + i * 32 + r32;
        const f32x16& a = acc[i][j];
#pragma unroll
        for (int g = 0; g < 4; ++g) {
          const int d = wn * 64 + j * 32 + 8 * g + 4 * hi;
          u32x2 w = {pack2(a[4 * g], a[4 * g + 1]), pack2(a[4 * g + 2], a[4 * g + 3])};
          if (which == 0) *(u32x2*)(q + (size_t)m * HD + n0 + d) = w;
          else {
            const int r = m & 63;
            char* tile = (char*)kimg + ((size_t)head * 32 + (m >> 6)) * 16384 + r * 256;
            *(u32x2*)(tile + (((d >> 3) ^ (r & 15)) * 16) + (d & 7) * 2) = w;
          }
        }
      }
  }
}

__global__ __launch_bounds__(512, 2) void k_attn(const u16* __restrict__ q, const u16* __restrict__ kimg, const u16* __restrict__ vimg,
                                                 u16* __restrict__ ctx) {
  extern __shared__ __attribute__((aligned(16))) char smem[];
  constexpr float C = 0.088388347648318440f * 1.4426950408889634f;
  const int bid = blockIdx.x;
  const int head = (bid & 7) + 8 * (bid >> 6), pr = (bid >> 3) & 7;
  const int tid = threadIdx.x, lane = tid & 63, wave = __builtin_amdgcn_readfirstlane(tid >> 6);
  const int rg = wave & 3, kh = wave >> 2, r32 = lane & 31, hi = lane >> 5;
  const char* kg = (const char*)kimg + (size_t)head * 32 * 16384 + wave * 4096 + lane * 16;
  const char* vg = (const char*)vimg + (size_t)head * 32 * 16384 + wave * 4096 + lane * 16;
  auto stage = [&](int t, int b) {
    char* base = smem + b * 65536 + wave * 4096;
#pragma unroll
    for (int i = 0; i < 4; ++i) glds16(kg + (size_t)t * 32768 + i * 1024, base + i * 1024);
#pragma unroll
    for (int i = 0; i < 4; ++i) glds16(vg + (size_t)t * 32768 + i * 1024, base + 32768 + i * 1024);
  };
  const int koff = kh * 16384 + r32 * 256, ksw = r32 & 15;
  const int voff = 32768 + kh * 16384 + r32 * 128, vsw = (r32 >> 1) & 7;

  for (int pass = 0; pass < 2; ++pass) {
    const int qb = pass == 0 ? 15 - pr : pr, nst = qb + 1;
    stage(0, 0);
    const u16* qp = q + (size_t)(qb * 128 + rg * 32 + r32) * HD + head * 128 + hi * 8;
    bf16x8 qf[8];
#pragma unroll
    for (int s = 0; s < 8; ++s) qf[s] = *(const bf16x8*)(qp + s * 16);
    float m = -1e30f, l = 0.f;
    f32x16 o[4] = {};
    asm volatile("s_waitcnt vmcnt(0)" ::: "memory");
    __syncthreads();
    for (int t = 0; t < nst; ++t) {
      const int b = t & 1;
      if (t + 1 < nst) stage(t + 1, b ^ 1);
      const bool diag = (t == nst - 1);
      if (!(diag && kh == 1 && rg < 2)) {
        const char* kb = smem + b * 65536 + koff;
        f32x16 p0 = {}, p1 = {};
#pragma unroll
        for (int s = 0; s < 8; ++s) {
          const int co = ((2 * s + hi) ^ ksw) * 16;
          bf16x8 k0 = *(const bf16x8*)(kb + co), k1 = *(const bf16x8*)(kb + 32 * 256 + co);
          p0 = __builtin_amdgcn_mfma_f32_32x32x16_bf16(k0, qf[s], p0, 0, 0, 0);
          p1 = __builtin_amdgcn_mfma_f32_32x32x16_bf16(k1, qf[s], p1, 0, 0, 0);
        }
        if (diag) {
          const int qrel = rg * 32 + r32 - kh * 64 - 4 * hi;
#pragma unroll
          for (int r = 0; r < 16; ++r) {
            const int kk = (r & 3) + 8 * (r >> 2);
            if (kk > qrel) p0[r] = -1e30f;
            if (kk + 32 > qrel) p1[r] = -1e30f;
          }
        }
        float pm = p0[0];
#pragma unroll
        for (int r = 1; r < 16; ++r) pm = fmaxf(pm, p0[r]);
#pragma unroll
        for (int r = 0; r < 16; ++r) pm = fmaxf(pm, p1[r]);
        { auto rr = __builtin_amdgcn_permlane32_swap(__float_as_uint(pm), __float_as_uint(pm), false, false);
          pm = fmaxf(__uint_as_float(rr[0]), __uint_as_float(rr[1])); }
        const float mn = fmaxf(m, pm);
        const float alpha = __builtin_amdgcn_exp2f((m - mn) * C);
        m = mn;
        const float mc = -mn * C;
        float ps = 0.f;
#pragma unroll
        for (int r = 0; r < 16; ++r) { p0[r] = __builtin_amdgcn_exp2f(fmaf(p0[r], C, mc)); ps += p0[r]; }
#pragma unroll
        for (int r = 0; r < 16; ++r) { p1[r] = __builtin_amdgcn_exp2f(fmaf(p1[r], C, mc)); ps += p1[r]; }
        l = l * alpha + ps;
#pragma unroll
        for (int d = 0; d < 4; ++d)
#pragma unroll
          for (int r = 0; r < 16; ++r) o[d][r] *= alpha;
        bf16x8 pf[4];
#pragma unroll
        for (int s = 0; s < 2; ++s) {
          u32x4 w0 = {pack2(p0[8 * s], p0[8 * s + 1]), pack2(p0[8 * s + 2], p0[8 * s + 3]), pack2(p0[8 * s + 4], p0[8 * s + 5]), pack2(p0[8 * s + 6], p0[8 * s + 7])};
          u32x4 w1 = {pack2(p1[8 * s], p1[8 * s + 1]), pack2(p1[8 * s + 2], p1[8 * s + 3]), pack2(p1[8 * s + 4], p1[8 * s + 5]), pack2(p1[8 * s + 6], p1[8 * s + 7])};
          pf[s] = *(bf16x8*)&w0; pf[2 + s] = *(bf16x8*)&w1;
        }
        const char* vb = smem + b * 65536 + voff;
#pragma unroll
        for (int d = 0; d < 4; ++d)
#pragma unroll
          for (int s = 0; s < 4; ++s) {
            bf16x8 vf = *(const bf16x8*)(vb + d * 32 * 128 + (((2 * s + hi) ^ vsw) * 16));
            o[d] = __builtin_amdgcn_mfma_f32_32x32x16_bf16(vf, pf[s], o[d], 0, 0, 0);
          }
      }
      asm volatile("s_waitcnt vmcnt(0)" ::: "memory");
      __syncthreads();
    }
    { auto rr = __builtin_amdgcn_permlane32_swap(__float_as_uint(l), __float_as_uint(l), false, false);
      l = __uint_as_float(rr[0]) + __uint_as_float(rr[1]); }
    float* mo = (float*)(smem + rg * 16384);
    float* ml = (float*)(smem + 65536 + rg * 512);
    if (kh == 1) {
#pragma unroll
      for (int d = 0; d < 4; ++d)
#pragma unroll
        for (int g = 0; g < 4; ++g)
          *(f32x4*)(mo + ((d * 4 + g) * 64 + lane) * 4) = f32x4{o[d][4 * g], o[d][4 * g + 1], o[d][4 * g + 2], o[d][4 * g + 3]};
      ml[lane] = m; ml[64 + lane] = l;
    }
    __syncthreads();
    if (kh == 0) {
      const float m1 = ml[lane], l1 = ml[64 + lane];
      const float mt = fmaxf(m, m1);
      const float a0 = __builtin_amdgcn_exp2f((m - mt) * C), a1 = __builtin_amdgcn_exp2f((m1 - mt) * C);
      const float inv = 1.f / (a0 * l + a1 * l1);
      const float f0 = a0 * inv, f1 = a1 * inv;
      u16* cp = ctx + (size_t)(qb * 128 + rg * 32 + r32) * HD + head * 128 + 4 * hi;
#pragma unroll
      for (int d = 0; d < 4; ++d)
#pragma unroll
        for (int g = 0; g < 4; ++g) {
          const f32x4 x = *(const f32x4*)(mo + ((d * 4 + g) * 64 + lane) * 4);
          u32x2 w = {pack2(o[d][4 * g] * f0 + x[0] * f1, o[d][4 * g + 1] * f0 + x[1] * f1), pack2(o[d][4 * g + 2] * f0 + x[2] * f1, o[d][4 * g + 3] * f0 + x[3] * f1)};
          *(u32x2*)(cp + d * 32 + g * 8) = w;
        }
    }
    __syncthreads();
  }
}

__global__ __launch_bounds__(256, 2) void k_oproj(const u16* __restrict__ ctx, const u16* __restrict__ wo, float* __restrict__ part) {
  extern __shared__ __attribute__((aligned(16))) char smem[];
  constexpr int BM = 128, BN = 128, WM = 2, WN = 2;
  const int bid = blockIdx.x;
  const int mt = bid & 15, nt = (bid >> 4) & 3, sp = bid >> 6;
  const int m0 = mt * BM, n0 = nt * BN;
  const int tid = threadIdx.x, lane = tid & 63, wave = tid >> 6, wm = wave / WN, wn = wave % WN, r32 = lane & 31, hi = lane >> 5;
  f32x16 acc[2][2] = {};
  gemm_loop<BM, BN, WM, WN, true>(ctx, HD, wo, DM, m0, n0, sp * 1024, 16, DM, HD, smem, acc);
  float* pp = part + (size_t)sp * S_ * DM;
#pragma unroll
  for (int i = 0; i < 2; ++i)
#pragma unroll
    for (int j = 0; j < 2; ++j) {
      const int m = m0 + wm * 64 + i * 32 + r32;
      const f32x16& a = acc[i][j];
#pragma unroll
      for (int g = 0; g < 4; ++g) {
        const int n = n0 + wn * 64 + j * 32 + 8 * g + 4 * hi;
        *(f32x4*)(pp + (size_t)m * DM + n) = f32x4{a[4 * g], a[4 * g + 1], a[4 * g + 2], a[4 * g + 3]};
      }
    }
}

__device__ __forceinline__ float gelu_t(float x) {
  const float y = 0.7978845608028654f * (x + 0.044715f * x * x * x);
  return x / (1.f + __expf(-2.f * y));
}

__global__ __launch_bounds__(256, 2) void k_in(const u16* __restrict__ h2, const u16* __restrict__ win, u16* __restrict__ z) {
  extern __shared__ __attribute__((aligned(16))) char smem[];
  constexpr int BM = 64, BN = 128, WM = 2, WN = 2;
  const int bid = blockIdx.x;
  const int mt = bid & 31, nt = bid >> 5;
  const int m0 = mt * BM, n0 = nt * BN;
  const int tid = threadIdx.x, lane = tid & 63, wave = tid >> 6, wm = wave / WN, wn = wave % WN, r32 = lane & 31, hi = lane >> 5;
  f32x16 acc[1][2] = {};
  gemm_loop<BM, BN, WM, WN, true>(h2, DM, win, FF, m0, n0, 0, 8, FF, DM, smem, acc);
#pragma unroll
  for (int j = 0; j < 2; ++j) {
    const int m = m0 + wm * 32 + r32;
    const f32x16& a = acc[0][j];
#pragma unroll
    for (int g = 0; g < 4; ++g) {
      const int n = n0 + wn * 64 + j * 32 + 8 * g + 4 * hi;
      if (n < FF) {
        u32x2 w = {pack2(gelu_t(a[4 * g]), gelu_t(a[4 * g + 1])), pack2(gelu_t(a[4 * g + 2]), gelu_t(a[4 * g + 3]))};
        *(u32x2*)(z + (size_t)m * FF + n) = w;
      }
    }
  }
}

__global__ __launch_bounds__(256, 2) void k_sp(const u16* __restrict__ wsp, const u16* __restrict__ z2n, const u16* __restrict__ bsp,
                                               const u16* __restrict__ z, u16* __restrict__ u) {
  extern __shared__ __attribute__((aligned(16))) char smem[];
  constexpr int BM = 64, BN = 64, WM = 2, WN = 2;
  const int bid = blockIdx.x;
  const int mt = bid & 31, nt = bid >> 5;
  const int m0 = mt * BM, n0 = nt * BN;
  const int tid = threadIdx.x, lane = tid & 63, wave = tid >> 6, wm = wave / WN, wn = wave % WN, r32 = lane & 31, hi = lane >> 5;
  f32x16 acc[1][1] = {};
  gemm_loop<BM, BN, WM, WN, true>(wsp, S_, z2n, FH, m0, n0, 0, 32, FH, S_, smem, acc);
  const int m = m0 + wm * 32 + r32;
  const float bb = bf2f(bsp[m]);
  const f32x16& a = acc[0][0];
#pragma unroll
  for (int g = 0; g < 4; ++g) {
    const int n = n0 + wn * 32 + 8 * g + 4 * hi;
    u32x2 w = {0u, 0u};
    if (n < FH) {
      const u32x2 zz = *(const u32x2*)(z + (size_t)m * FF + n);
      w = u32x2{pack2((a[4 * g] + bb) * bflo(zz[0]), (a[4 * g + 1] + bb) * bfhi(zz[0])), pack2((a[4 * g + 2] + bb) * bflo(zz[1]), (a[4 * g + 3] + bb) * bfhi(zz[1]))};
    }
    *(u32x2*)(u + (size_t)m * FHP + n) = w;
  }
}

__global__ __launch_bounds__(256, 2) void k_out(const u16* __restrict__ u, const u16* __restrict__ wout, const float* __restrict__ x1,
                                                const float* __restrict__ mod, u16* __restrict__ out) {
  extern __shared__ __attribute__((aligned(16))) char smem[];
  constexpr int BM = 64, BN = 64, WM = 2, WN = 2;
  const int bid = blockIdx.x;
  const int mt = bid & 31, nt = bid >> 5;
  const int m0 = mt * BM, n0 = nt * BN;
  const int tid = threadIdx.x, lane = tid & 63, wave = tid >> 6, wm = wave / WN, wn = wave % WN, r32 = lane & 31, hi = lane >> 5;
  f32x16 acc[1][1] = {};
  gemm_loop<BM, BN, WM, WN, true>(u, FHP, wout, DM, m0, n0, 0, 11, DM, FH, smem, acc);
  const int m = m0 + wm * 32 + r32;
  const f32x16& a = acc[0][0];
#pragma unroll
  for (int g = 0; g < 4; ++g) {
    const int n = n0 + wn * 32 + 8 * g + 4 * hi;
    const f32x4 xx = *(const f32x4*)(x1 + (size_t)m * DM + n);
    const f32x4 gb = *(const f32x4*)(mod + 5 * DM + n);
    u32x2 w = {pack2(xx[0] + gb[0] * a[4 * g], xx[1] + gb[1] * a[4 * g + 1]), pack2(xx[2] + gb[2] * a[4 * g + 2], xx[3] + gb[3] * a[4 * g + 3])};
    *(u32x2*)(out + (size_t)m * DM + n) = w;
  }
}

extern "C" void kernel_launch(void* const* d_in, const int* in_sizes, int n_in,
                              void* d_out, int out_size, void* d_ws, size_t ws_size,
                              hipStream_t stream) {
  const u16* x = (const u16*)d_in[0];
  const u16* ckv = (const u16*)d_in[1];
  const u16* wq = (const u16*)d_in[2];
  const u16* wuk = (const u16*)d_in[3];
  const u16* wuv = (const u16*)d_in[4];
  const u16* wo = (const u16*)d_in[5];
  const u16* win = (const u16*)d_in[7];
  const u16* wsp = (const u16*)d_in[8];
  const u16* bsp = (const u16*)d_in[9];
  const u16* wout = (const u16*)d_in[10];
  const u16* c = (const u16*)d_in[11];
  const u16* wmod = (const u16*)d_in[12];
  u16* out = (u16*)d_out;

  char* ws = (char*)d_ws;
  const size_t MB = 1024 * 1024;
  float* mod = (float*)(ws);
  u16* h = (u16*)(ws + 1 * MB);
  u16* q = (u16*)(ws + 4 * MB);
  u16* kimg = (u16*)(ws + 20 * MB);
  u16* vimg = (u16*)(ws + 36 * MB);
  u16* ctx = (u16*)(ws + 52 * MB);
  float* part = (float*)(ws + 68 * MB);
  float* x1 = (float*)(ws + 84 * MB);
  u16* h2 = (u16*)(ws + 88 * MB);
  u16* z = (u16*)(ws + 90 * MB);
  u16* z2n = (u16*)(ws + 96 * MB);
  u16* u = (u16*)(ws + 100 * MB);

  (void)hipFuncSetAttribute((const void*)k_attn, hipFuncAttributeMaxDynamicSharedMemorySize, 131072);
  (void)hipFuncSetAttribute((const void*)k_qkv, hipFuncAttributeMaxDynamicSharedMemorySize, 65536);
  (void)hipFuncSetAttribute((const void*)k_oproj, hipFuncAttributeMaxDynamicSharedMemorySize, 65536);

  k_mod<<<96, 256, 0, stream>>>(c, wmod, mod);
  k_ln1<<<512, 256, 0, stream>>>(x, mod, h);
  k_qkv<<<1536, 256, 65536, stream>>>(h, ckv, wq, wuk, wuv, q, kimg, vimg);
  k_attn<<<256, 512, 131072, stream>>>(q, kimg, vimg, ctx);
  k_oproj<<<256, 256, 65536, stream>>>(ctx, wo, part);
  k_ln2<4><<<512, 256, 0, stream>>>(x, part, mod, x1, h2);
  k_in<<<32 * 11, 256, 2 * (64 + 128) * 128, stream>>>(h2, win, z);
  k_lnz<<<512, 256, 0, stream>>>(z, z2n);
  k_sp<<<32 * 11, 256, 2 * (64 + 64) * 128, stream>>>(wsp, z2n, bsp, z, u);
  k_out<<<32 * 8, 256, 2 * (64 + 64) * 128, stream>>>(u, wout, x1, mod, out);
}
```

```cpp
#define FUSE 1
#define GRP 2
#define WIN_EARLY 1
#define FL_P 0
#define FL_H 0
#define FL_U 0
#include <hip/hip_runtime.h>
#include <hip/hip_bf16.h>
#include <stdint.h>

typedef unsigned short u16;
typedef __attribute__((ext_vector_type(8))) short bf16x8;
typedef __attribute__((ext_vector_type(4))) short s16x4;
typedef __attribute__((ext_vector_type(16))) float f32x16;
typedef __attribute__((ext_vector_type(4))) float f32x4;
typedef __attribute__((ext_vector_type(4))) unsigned u32x4;
typedef __attribute__((ext_vector_type(2))) unsigned u32x2;
#define LDSP __attribute__((address_space(3)))

constexpr size_t WS_FL = 65536, WS_CTX = 52u << 20, WS_PART = 68u << 20, WS_X1 = 84u << 20, WS_H2 = 86u << 20, WS_Z2 = 88u << 20, WS_U = 363u << 18, WS_ST2 = 187u << 19;
constexpr int S_ = 2048, DM = 512, NH = 32, HD = 4096, FF = 1376, FH = 688, FHP = 704;

__device__ __forceinline__ float bf2f(unsigned v16) { return __uint_as_float(v16 << 16); }
__device__ __forceinline__ float bflo(unsigned w) { return __uint_as_float(w << 16); }
__device__ __forceinline__ float bfhi(unsigned w) { return __uint_as_float(w & 0xffff0000u); }
typedef __attribute__((ext_vector_type(2))) __bf16 bf16x2_t;
typedef __attribute__((ext_vector_type(2))) float f32x2_t;
__device__ __forceinline__ unsigned pack2(float lo, float hi) {
  f32x2_t f = {lo, hi};
  bf16x2_t r = __builtin_convertvector(f, bf16x2_t);
  return __builtin_bit_cast(unsigned, r);
}
__device__ __forceinline__ void glds16(const void* g, char* lds) {
  __builtin_amdgcn_global_load_lds((const unsigned*)g, (LDSP unsigned*)lds, 16, 0, 0);
}
__device__ __forceinline__ unsigned touch(const void* p) { unsigned r; asm volatile("global_load_dword %0, %1, off" : "=v"(r) : "v"(p) : "memory"); return r; }
#define KEEP(x) asm volatile("" :: "v"(x))
#define WAITV(n) asm volatile("s_waitcnt vmcnt(%0)" :: "n"(n) : "memory")
#define RAW_BAR() do { asm volatile("s_waitcnt lgkmcnt(0)" ::: "memory"); __builtin_amdgcn_s_barrier(); } while (0)
template <int OFF> __device__ __forceinline__ bf16x8 ldsr128(unsigned a) {
  bf16x8 r; asm volatile("ds_read_b128 %0, %1 offset:%2" : "=v"(r) : "v"(a), "i"(OFF)); return r;
}
template <int OFF> __device__ __forceinline__ s16x4 ldsrtr(unsigned a) {
  s16x4 r; asm volatile("ds_read_b64_tr_b16 %0, %1 offset:%2" : "=v"(r) : "v"(a), "i"(OFF)); return r;
}
__device__ __forceinline__ unsigned lds_addr(const void* p) { return (unsigned)(size_t)(LDSP const char*)p; }
#define PIN(x) asm volatile("" : "+v"(x))
#define WAITL(n) asm volatile("s_waitcnt lgkmcnt(%0)" :: "n"(n))
__device__ __forceinline__ u32x4 pack8(const f32x16& a, int sp) {
  return u32x4{pack2(a[8 * sp], a[8 * sp + 1]), pack2(a[8 * sp + 2], a[8 * sp + 3]), pack2(a[8 * sp + 4], a[8 * sp + 5]), pack2(a[8 * sp + 6], a[8 * sp + 7])};
}
template <int V> struct IC { static constexpr int v = V; };
template <int I, int N, class F> __device__ __forceinline__ void sfor(F&& f) {
  if constexpr (I < N) { f(IC<I>{}); sfor<I + 1, N>(f); }
}
template <int OFF> __device__ __forceinline__ bf16x8 gld128(const void* sb, unsigned vo) {
  bf16x8 r; asm volatile("global_load_dwordx4 %0, %1, %2 offset:%3" : "=v"(r) : "v"(vo), "s"(sb), "i"(OFF) : "memory"); return r;
}
template <int OFF> __device__ __forceinline__ s16x4 ldsr64(unsigned a) {
  s16x4 r; asm volatile("ds_read_b64 %0, %1 offset:%2" : "=v"(r) : "v"(a), "i"(OFF)); return r;
}
__device__ __forceinline__ bf16x8 cat8(s16x4 lo, s16x4 hi) { return bf16x8{lo[0], lo[1], lo[2], lo[3], hi[0], hi[1], hi[2], hi[3]}; }
__device__ __forceinline__ f32x16 mfma32(bf16x8 a, bf16x8 b, f32x16 c) { return __builtin_amdgcn_mfma_f32_32x32x16_bf16(a, b, c, 0, 0, 0); }
__device__ __forceinline__ void red_put(char* base, int slot, int lane, const f32x16& a, int sp) {
  *(f32x4*)(base + slot * 2048 + lane * 16) = f32x4{a[8 * sp], a[8 * sp + 1], a[8 * sp + 2], a[8 * sp + 3]};
  *(f32x4*)(base + slot * 2048 + 1024 + lane * 16) = f32x4{a[8 * sp + 4], a[8 * sp + 5], a[8 * sp + 6], a[8 * sp + 7]};
}

__device__ __forceinline__ void st128(void* p, u32x4 v) { asm volatile("global_store_dwordx4 %0, %1, off sc1\n\ts_nop 1" :: "v"(p), "v"(v) : "memory"); }
__device__ __forceinline__ void st128f(void* p, f32x4 v) { asm volatile("global_store_dwordx4 %0, %1, off sc1\n\ts_nop 1" :: "v"(p), "v"(v) : "memory"); }
__device__ __forceinline__ unsigned ipf(unsigned off) {
  unsigned r; unsigned long long pc;
  asm volatile("s_getpc_b64 %1\n\tLPC_%=:\n\tv_min_u32 %0, LTEXT_END-LPC_%=-4, %2\n\tglobal_load_dword %0, %0, %1" : "=&v"(r), "=&s"(pc) : "v"(off) : "memory");
  return r;
}
constexpr int op_wait(int t, int NK, int NB, int PA) {
  int n = 0;
  if (t < PA) { for (int i = t + 1; i < NB; ++i) n += 2; for (int i = t + 1; i < PA; ++i) n += 2; for (int j = 0; j < t; ++j) n += (j + NB < NK ? 2 : 0) + (j + PA < NK ? 2 : 0); }
  else { for (int j = t - PA + 1; j < t; ++j) n += (j + NB < NK ? 2 : 0) + (j + PA < NK ? 2 : 0); }
  return n;
}
template <int WKI>
__device__ __forceinline__ void op_tail(f32x16 (&acc)[4], char* smem, int wm, int lane, float* __restrict__ dst) {
#pragma unroll
  for (int u = 0; u < 4; ++u) red_put(smem, (wm * 2 + WKI) * 4 + u, lane, acc[(1 - WKI) * 2 + (u >> 1)], u & 1);
  RAW_BAR();
#pragma unroll
  for (int u = 0; u < 4; ++u) {
    const f32x16& a = acc[WKI * 2 + (u >> 1)]; const int sp = u & 1;
    const char* p = smem + ((wm * 2 + 1 - WKI) * 4 + u) * 2048 + lane * 16;
    const f32x4 s0 = f32x4{a[8 * sp], a[8 * sp + 1], a[8 * sp + 2], a[8 * sp + 3]} + *(const f32x4*)p;
    const f32x4 s1 = f32x4{a[8 * sp + 4], a[8 * sp + 5], a[8 * sp + 6], a[8 * sp + 7]} + *(const f32x4*)(p + 1024);
    *(f32x4*)(dst + (WKI * 4 + u) * 512 + lane * 4) = s0;
    *(f32x4*)(dst + (WKI * 4 + u) * 512 + 256 + lane * 4) = s1;
  }
}
__global__ __launch_bounds__(512, 2) void k_oproj(const u16* __restrict__ ctx, const u16* __restrict__ wo, float* __restrict__ part) {
  extern __shared__ __attribute__((aligned(16))) char smem[];
  constexpr int NST = 7, NB = NST - 1, PA = 4, NK = 16;
  const int bid = blockIdx.x, xcd = bid & 7, xi = bid >> 3;
  const int sp = xcd & 3, mt = (xcd >> 2) * 8 + (xi & 7), nt = xi >> 3;
  const int tid = threadIdx.x, lane = tid & 63, wave = __builtin_amdgcn_readfirstlane(tid >> 6), hi = lane >> 5;
  const int wm = wave & 3, wk = wave >> 2;
  const char* abase = (const char*)ctx + ((size_t)(mt * 4 + wm) * 256 + sp * 64 + wk * 2) * 1024;
  const unsigned av = lane * 16;
  const u16* bptr = wo + (size_t)(sp * 1024 + wave * 8 + ((lane >> 2) & 3)) * DM + nt * 128 + (lane >> 4) * 32 + (lane & 3) * 8;
  const unsigned bb = lds_addr(smem) + wk * 8192 + ((lane >> 2) & 3) * 64 + ((lane >> 4) & 1) * 32 + (lane & 3) * 8 + hi * 1024;
  bf16x8 a[PA][2];
  f32x16 acc[4] = {};
#define OP_B(t_) do { glds16(bptr + (size_t)((t_) * 64) * DM, smem + ((t_) % NST) * 16384 + wave * 2048);                        \
                      glds16(bptr + (size_t)((t_) * 64 + 4) * DM, smem + ((t_) % NST) * 16384 + wave * 2048 + 1024); } while (0)
#define OP_A(t_) do { a[(t_) % PA][0] = gld128<0>(abase + (t_) * 4096, av); a[(t_) % PA][1] = gld128<1024>(abase + (t_) * 4096, av); } while (0)
  sfor<0, NB>([&](auto I) { constexpr int i = decltype(I)::v; OP_B(i); if constexpr (i < PA) OP_A(i); });
  sfor<0, NK>([&](auto T) {
    constexpr int t = decltype(T)::v, so = ((t % NST) * 16384) & 65535, pa = t % PA;
    const unsigned bq = (t % NST) >= 4 ? bb + 65536 : bb;
    WAITV(op_wait(t, NK, NB, PA));
    RAW_BAR();
    if constexpr (t + NB < NK) OP_B(t + NB);
    s16x4 lo[8], hh[8];
    sfor<0, 8>([&](auto J) { constexpr int j = decltype(J)::v; lo[j] = ldsrtr<so + (j >> 2) * 4096 + (j & 3) * 256>(bq); hh[j] = ldsrtr<so + (j >> 2) * 4096 + (j & 3) * 256 + 2048>(bq); });
    PIN(a[pa][0]); PIN(a[pa][1]);
    sfor<0, 8>([&](auto J) { constexpr int j = decltype(J)::v;
      WAITL(14 - 2 * j); PIN(lo[j]); PIN(hh[j]);
      acc[j & 3] = mfma32(cat8(lo[j], hh[j]), a[pa][j >> 2], acc[j & 3]); });
    if constexpr (t + PA < NK) OP_A(t + PA);
  });
#undef OP_A
#undef OP_B
  RAW_BAR();
  float* dst = part + (((size_t)sp * 64 + mt * 4 + wm) * 32 + nt * 8) * 512;
  if (wk == 0) op_tail<0>(acc, smem, wm, lane, dst); else op_tail<1>(acc, smem, wm, lane, dst);
}

__global__ __launch_bounds__(256) void k_ln2(const u16* __restrict__ x, const float* __restrict__ part, const float* __restrict__ mod,
                                             u16* __restrict__ x1, u16* __restrict__ h2) {
  __shared__ float red[2][4][8];
  const int tid = threadIdx.x, r = tid & 7, hi = (tid >> 3) & 1, uq = tid >> 4, wave = tid >> 6;
  const int mblk = blockIdx.x >> 2, l2 = (blockIdx.x & 3) * 8 + r + 32 * hi, m = mblk * 32 + (blockIdx.x & 3) * 8 + r;
  float v[2][8];
#pragma unroll
  for (int t = 0; t < 2; ++t) {
    const int un = uq + 16 * t, n = un * 16 + 4 * hi;
    f32x4 a0 = {0, 0, 0, 0}, a1 = {0, 0, 0, 0};
#pragma unroll
    for (int p = 0; p < 4; ++p) {
      const float* pp = part + (((size_t)p * 64 + mblk) * 32 + un) * 512 + l2 * 4;
      a0 += *(const f32x4*)pp; a1 += *(const f32x4*)(pp + 256);
    }
    const u32x2 x0 = *(const u32x2*)(x + (size_t)m * DM + n), x1v = *(const u32x2*)(x + (size_t)m * DM + n + 8);
    const f32x4 g0 = *(const f32x4*)(mod + 2 * DM + n), g1 = *(const f32x4*)(mod + 2 * DM + n + 8);
    v[t][0] = bflo(x0[0]) + g0[0] * a0[0]; v[t][1] = bfhi(x0[0]) + g0[1] * a0[1]; v[t][2] = bflo(x0[1]) + g0[2] * a0[2]; v[t][3] = bfhi(x0[1]) + g0[3] * a0[3];
    v[t][4] = bflo(x1v[0]) + g1[0] * a1[0]; v[t][5] = bfhi(x1v[0]) + g1[1] * a1[1]; v[t][6] = bflo(x1v[1]) + g1[2] * a1[2]; v[t][7] = bfhi(x1v[1]) + g1[3] * a1[3];
    *(u32x4*)(x1 + ((size_t)mblk * 32 + un) * 512 + l2 * 8) = u32x4{pack2(v[t][0], v[t][1]), pack2(v[t][2], v[t][3]), pack2(v[t][4], v[t][5]), pack2(v[t][6], v[t][7])};
  }
  float s = 0.f;
#pragma unroll
  for (int j = 0; j < 8; ++j) s += v[0][j] + v[1][j];
  s += __shfl_xor(s, 8); s += __shfl_xor(s, 16); s += __shfl_xor(s, 32);
  if ((tid & 63) < 8) red[0][wave][r] = s;
  __syncthreads();
  const float mean = ((red[0][0][r] + red[0][1][r]) + (red[0][2][r] + red[0][3][r])) * (1.f / DM);
  float q = 0.f;
#pragma unroll
  for (int j = 0; j < 8; ++j) { v[0][j] -= mean; v[1][j] -= mean; q += v[0][j] * v[0][j] + v[1][j] * v[1][j]; }
  q += __shfl_xor(q, 8); q += __shfl_xor(q, 16); q += __shfl_xor(q, 32);
  if ((tid & 63) < 8) red[1][wave][r] = q;
  __syncthreads();
  const float rstd = rsqrtf(((red[1][0][r] + red[1][1][r]) + (red[1][2][r] + red[1][3][r])) * (1.f / DM) + 1e-5f);
#pragma unroll
  for (int t = 0; t < 2; ++t) {
    const int un = uq + 16 * t, n = un * 16 + 4 * hi;
    const f32x4 s0 = *(const f32x4*)(mod + 3 * DM + n), s1 = *(const f32x4*)(mod + 3 * DM + n + 8);
    const f32x4 c0 = *(const f32x4*)(mod + 4 * DM + n), c1 = *(const f32x4*)(mod + 4 * DM + n + 8);
    float o[8];
#pragma unroll
    for (int j = 0; j < 4; ++j) { o[j] = v[t][j] * rstd * (1.f + c0[j]) + s0[j]; o[4 + j] = v[t][4 + j] * rstd * (1.f + c1[j]) + s1[j]; }
    *(u32x4*)(h2 + ((size_t)mblk * 32 + un) * 512 + l2 * 8) = u32x4{pack2(o[0], o[1]), pack2(o[2], o[3]), pack2(o[4], o[5]), pack2(o[6], o[7])};
  }
}

__device__ __forceinline__ float gelu_t(float x) {
  const float e = __builtin_amdgcn_exp2f(x * fmaf(x * x, -0.10294324f, -2.3022082f));
  return x * __builtin_amdgcn_rcpf(1.f + e);
}

template <bool Z2, int WKI>
__device__ __forceinline__ void in_tail(f32x16 (&acc)[2][3], char* smem, int wm, int lane, int mt, int tt, u16* __restrict__ dst) {
  sfor<0, 12>([&](auto U) {
    constexpr int u = decltype(U)::v, o = u / 3;
    if constexpr (o != WKI) red_put(smem, ((wm * 4 + o) * 3 + (WKI < o ? WKI : WKI - 1)) * 3 + u % 3, lane, acc[u / 6][(u % 6) >> 1], u & 1);
  });
  RAW_BAR();
  sfor<0, 3>([&](auto UU) {
    constexpr int uu = decltype(UU)::v, u = WKI * 3 + uu, i = u / 6, j = (u % 6) >> 1, sp = u & 1;
    const f32x16& a = acc[i][j];
    f32x4 s0 = {a[8 * sp], a[8 * sp + 1], a[8 * sp + 2], a[8 * sp + 3]}, s1 = {a[8 * sp + 4], a[8 * sp + 5], a[8 * sp + 6], a[8 * sp + 7]};
#pragma unroll
    for (int s = 0; s < 3; ++s) {
      const char* p = smem + (((wm * 4 + WKI) * 3 + s) * 3 + uu) * 2048 + lane * 16;
      s0 += *(const f32x4*)p; s1 += *(const f32x4*)(p + 1024);
    }
    const u32x4 o = {pack2(gelu_t(s0[0]), gelu_t(s0[1])), pack2(gelu_t(s0[2]), gelu_t(s0[3])), pack2(gelu_t(s1[0]), gelu_t(s1[1])), pack2(gelu_t(s1[2]), gelu_t(s1[3]))};
    const int rb = mt * 4 + wm * 2 + i, cbk = tt * 3 + j;
    if (Z2) { if (cbk < 22) *(u32x4*)(dst + ((size_t)cbk * 128 + rb * 2 + sp) * 512 + lane * 8) = o; }
    else    { if (cbk * 2 + sp < 43) *(u32x4*)(dst + ((size_t)rb * 44 + cbk * 2 + sp) * 512 + lane * 8) = o; }
  });
}
template <bool Z2>
__device__ __forceinline__ void in_mma(const bf16x8 (&a)[2][8], unsigned bb, f32x16 (&acc)[2][3]) {
  s16x4 lo[2][3], hh[2][3];
#define IN_RD(s_, p_) do { lo[p_][0] = ldsrtr<(s_) * 3072>(bb); hh[p_][0] = ldsrtr<(s_) * 3072 + 1536>(bb);                 \
                           lo[p_][1] = ldsrtr<(s_) * 3072 + 256>(bb); hh[p_][1] = ldsrtr<(s_) * 3072 + 1792>(bb);           \
                           lo[p_][2] = ldsrtr<(s_) * 3072 + 512>(bb); hh[p_][2] = ldsrtr<(s_) * 3072 + 2048>(bb); } while (0)
  IN_RD(0, 0);
  sfor<0, 8>([&](auto S) {
    constexpr int s = decltype(S)::v, p = s & 1;
    if constexpr (s + 1 < 8) { IN_RD(s + 1, p ^ 1); WAITL(6); } else WAITL(0);
#pragma unroll
    for (int j = 0; j < 3; ++j) { PIN(lo[p][j]); PIN(hh[p][j]); }
#pragma unroll
    for (int j = 0; j < 3; ++j) {
      const bf16x8 b = cat8(lo[p][j], hh[p][j]);
#pragma unroll
      for (int i = 0; i < 2; ++i) acc[i][j] = Z2 ? mfma32(a[i][s], b, acc[i][j]) : mfma32(b, a[i][s], acc[i][j]);
    }
  });
#undef IN_RD
}
__global__ __launch_bounds__(512, 2) void k_in(const u16* __restrict__ x1, const u16* __restrict__ win,
                                               u16* __restrict__ z1, u16* __restrict__ z2) {
  extern __shared__ __attribute__((aligned(16))) char smem[];
  const int bid = blockIdx.x, xi = bid >> 3;
  const int mt = (bid & 7) * 2 + (xi & 1), half = (xi >> 1) & 1, tt = xi >> 2;
  const int tid = threadIdx.x, lane = tid & 63, wave = __builtin_amdgcn_readfirstlane(tid >> 6), r32 = lane & 31, hi = lane >> 5;
  const int wm = wave & 1, wk = wave >> 1;
  bf16x8 a[2][8];
  {
    const char* ab = (const char*)x1 + ((size_t)(mt * 4 + wm * 2) * 32 + wk * 8) * 1024;
    const unsigned v0 = lane * 16 + 4096, v1 = lane * 16 + 4096 + 32768;
    sfor<0, 8>([&](auto S) { constexpr int s = decltype(S)::v; a[0][s] = gld128<s * 1024 - 4096>(ab, v0); a[1][s] = gld128<s * 1024 - 4096>(ab, v1); });
  }
  {
    const int q = lane >> 4, ncl = FH - tt * 96;
    const u16* wb = win + half * FH + tt * 96 + (size_t)(wave * 64 + ((lane >> 2) & 3)) * FF;
#pragma unroll
    for (int b = 0; b < 3; ++b) {
      const int st = 4 * b + q, k4 = st / 3, n32 = st % 3;
      int col = n32 * 32 + (lane & 3) * 8; if (col > ncl - 8) col = ncl - 8;
#pragma unroll
      for (int aa = 0; aa < 4; ++aa)
        glds16(wb + (size_t)(aa * 16 + k4 * 4) * FF + col, smem + (wave * 12 + aa * 3 + b) * 1024);
    }
  }
  WAITV(0);
#pragma unroll
  for (int i = 0; i < 2; ++i)
#pragma unroll
    for (int s = 0; s < 8; ++s) PIN(a[i][s]);
  RAW_BAR();
  const unsigned bb = lds_addr(smem) + wk * 24576 + ((lane >> 2) & 3) * 64 + ((lane >> 4) & 1) * 32 + (lane & 3) * 8 + hi * 768;
  f32x16 acc[2][3] = {};
  if (half) in_mma<true>(a, bb, acc); else in_mma<false>(a, bb, acc);
  RAW_BAR();
  if (half) {
    if (wk == 0) in_tail<true, 0>(acc, smem, wm, lane, mt, tt, z2); else if (wk == 1) in_tail<true, 1>(acc, smem, wm, lane, mt, tt, z2);
    else if (wk == 2) in_tail<true, 2>(acc, smem, wm, lane, mt, tt, z2); else in_tail<true, 3>(acc, smem, wm, lane, mt, tt, z2);
  } else {
    if (wk == 0) in_tail<false, 0>(acc, smem, wm, lane, mt, tt, z1); else if (wk == 1) in_tail<false, 1>(acc, smem, wm, lane, mt, tt, z1);
    else if (wk == 2) in_tail<false, 2>(acc, smem, wm, lane, mt, tt, z1); else in_tail<false, 3>(acc, smem, wm, lane, mt, tt, z1);
  }
}

__device__ __forceinline__ float half_sum(float v) {
  v += __builtin_bit_cast(float, __builtin_amdgcn_update_dpp(0, __builtin_bit_cast(int, v), 0xB1, 0xf, 0xf, false));
  v += __builtin_bit_cast(float, __builtin_amdgcn_update_dpp(0, __builtin_bit_cast(int, v), 0x4E, 0xf, 0xf, false));
  v += __builtin_bit_cast(float, __builtin_amdgcn_update_dpp(0, __builtin_bit_cast(int, v), 0x141, 0xf, 0xf, false));
  v += __builtin_bit_cast(float, __builtin_amdgcn_update_dpp(0, __builtin_bit_cast(int, v), 0x140, 0xf, 0xf, false));
  auto rr = __builtin_amdgcn_permlane16_swap(__float_as_uint(v), __float_as_uint(v), false, false);
  return __uint_as_float(rr[0]) + __uint_as_float(rr[1]);
}
__global__ __launch_bounds__(512) void k_lnz(const u16* __restrict__ z2, u16* __restrict__ z2n) {
  __shared__ __attribute__((aligned(16))) float red[2][8][2][8];
  const int s16 = blockIdx.x, tid = threadIdx.x, lane = tid & 63, wave = tid >> 6, r32 = lane & 31, hi = lane >> 5;
  float v[3][8]; bool ok[3];
#pragma unroll
  for (int t = 0; t < 3; ++t) {
    const int nb = wave + 8 * t;
    ok[t] = nb < 21 || (nb == 21 && r32 < 16);
    u32x4 w = {0u, 0u, 0u, 0u};
    if (ok[t]) w = *(const u32x4*)(z2 + ((size_t)nb * 128 + s16) * 512 + lane * 8);
#pragma unroll
    for (int j = 0; j < 4; ++j) { v[t][2 * j] = bflo(w[j]); v[t][2 * j + 1] = bfhi(w[j]); }
  }
  float mean[8], rstd[8];
#pragma unroll
  for (int j = 0; j < 8; ++j) { const float s = half_sum(v[0][j] + v[1][j] + v[2][j]); if (r32 == 0) red[0][wave][hi][j] = s; }
  __syncthreads();
#pragma unroll
  for (int j = 0; j < 8; ++j) mean[j] = 0.f;
#pragma unroll
  for (int w = 0; w < 8; ++w) {
    const f32x4 a = *(const f32x4*)&red[0][w][hi][0], b = *(const f32x4*)&red[0][w][hi][4];
#pragma unroll
    for (int j = 0; j < 4; ++j) { mean[j] += a[j]; mean[4 + j] += b[j]; }
  }
#pragma unroll
  for (int j = 0; j < 8; ++j) {
    mean[j] *= (1.f / FH);
    float q = 0.f;
#pragma unroll
    for (int t = 0; t < 3; ++t) { v[t][j] = ok[t] ? v[t][j] - mean[j] : 0.f; q += v[t][j] * v[t][j]; }
    q = half_sum(q); if (r32 == 0) red[1][wave][hi][j] = q;
  }
  __syncthreads();
#pragma unroll
  for (int j = 0; j < 8; ++j) rstd[j] = 0.f;
#pragma unroll
  for (int w = 0; w < 8; ++w) {
    const f32x4 a = *(const f32x4*)&red[1][w][hi][0], b = *(const f32x4*)&red[1][w][hi][4];
#pragma unroll
    for (int j = 0; j < 4; ++j) { rstd[j] += a[j]; rstd[4 + j] += b[j]; }
  }
#pragma unroll
  for (int j = 0; j < 8; ++j) rstd[j] = rsqrtf(rstd[j] * (1.f / FH) + 1e-5f);
#pragma unroll
  for (int t = 0; t < 3; ++t) {
    const int nb = wave + 8 * t;
    if (nb < 22)
      *(u32x4*)(z2n + ((size_t)nb * 128 + s16) * 512 + lane * 8) =
          u32x4{pack2(v[t][0] * rstd[0], v[t][1] * rstd[1]), pack2(v[t][2] * rstd[2], v[t][3] * rstd[3]),
                pack2(v[t][4] * rstd[4], v[t][5] * rstd[5]), pack2(v[t][6] * rstd[6], v[t][7] * rstd[7])};
  }
}

__global__ __launch_bounds__(512, 2) void k_sp(const u16* __restrict__ wsp, const u16* __restrict__ z2n, const u16* __restrict__ bsp,
                                               const u16* __restrict__ z1, u16* __restrict__ u) {
  extern __shared__ __attribute__((aligned(16))) char smem[];
  const int bid = blockIdx.x, xi = bid >> 3;
  const int mt = (bid & 7) * 4 + (xi & 3), nt = xi >> 2;
  const int tid = threadIdx.x, lane = tid & 63, wave = __builtin_amdgcn_readfirstlane(tid >> 6), r32 = lane & 31, hi = lane >> 5;
  char* lw = smem + wave * 16384;
  const u16* aptr = wsp + (size_t)(mt * 64 + (lane >> 3)) * S_ + wave * 256;
  const int che = ((lane & 7) ^ (lane >> 4)) * 8, cho = ((lane & 7) ^ ((lane >> 4) | 4)) * 8;
  const char* bbase = (const char*)z2n + ((size_t)(nt * 3) * 128 + wave * 16) * 1024;
  const unsigned bv0 = lane * 16, bv1 = lane * 16 + (nt < 7 ? 131072u : 0u), bv2 = lane * 16 + (nt < 7 ? 262144u : 0u);
  unsigned aa[8];
  { const unsigned b0 = lds_addr(lw) + r32 * 128 + hi * 8; const int asw = (r32 >> 1) & 7;
#pragma unroll
    for (int c8 = 0; c8 < 8; ++c8) aa[c8] = b0 + ((c8 ^ asw) * 16); }
  bf16x8 b[2][4][3];
  f32x16 acc[2][3] = {};
#define SP_A(c_) do { _Pragma("unroll") for (int ins = 0; ins < 8; ++ins)                                                      \
    glds16(aptr + (size_t)ins * 8 * S_ + (c_) * 64 + ((ins & 1) ? cho : che), lw + ((c_) & 1) * 8192 + ins * 1024); } while (0)
#define SP_B(c_, s_) do { b[(c_) & 1][s_][0] = gld128<(s_) * 1024>(bbase + (c_) * 4096, bv0); b[(c_) & 1][s_][1] = gld128<(s_) * 1024>(bbase + (c_) * 4096, bv1); \
                          b[(c_) & 1][s_][2] = gld128<(s_) * 1024>(bbase + (c_) * 4096, bv2); } while (0)
  SP_A(0); SP_B(0, 0); SP_B(0, 1); SP_B(0, 2); SP_B(0, 3);
  SP_A(1); SP_B(1, 0); SP_B(1, 1); SP_B(1, 2); SP_B(1, 3);
  sfor<0, 16>([&](auto T) {
    constexpr int t = decltype(T)::v, c = t >> 2, s = t & 3, p = c & 1;
    constexpr int wv = c < 2 ? 29 : c == 2 ? (s < 3 ? 23 : 20) : (s < 3 ? 3 : 0);
    WAITV(wv);
    s16x4 l0 = ldsr64<p * 8192>(aa[2 * s]), h0 = ldsr64<p * 8192>(aa[2 * s + 1]);
    s16x4 l1 = ldsr64<p * 8192 + 4096>(aa[2 * s]), h1 = ldsr64<p * 8192 + 4096>(aa[2 * s + 1]);
    PIN(b[p][s][0]); PIN(b[p][s][1]); PIN(b[p][s][2]);
    WAITL(2); PIN(l0); PIN(h0);
    { const bf16x8 a0 = cat8(l0, h0);
      acc[0][0] = mfma32(b[p][s][0], a0, acc[0][0]); acc[0][1] = mfma32(b[p][s][1], a0, acc[0][1]); acc[0][2] = mfma32(b[p][s][2], a0, acc[0][2]); }
    asm volatile("s_waitcnt lgkmcnt(0)" ::: "memory"); PIN(l1); PIN(h1);
    if constexpr (c < 2 && s == 3) SP_A(c + 2);
    { const bf16x8 a1 = cat8(l1, h1);
      acc[1][0] = mfma32(b[p][s][0], a1, acc[1][0]); acc[1][1] = mfma32(b[p][s][1], a1, acc[1][1]); acc[1][2] = mfma32(b[p][s][2], a1, acc[1][2]); }
    if constexpr (c < 2) SP_B(c + 2, s);
  });
#undef SP_A
#undef SP_B
#pragma unroll
  for (int i = 0; i < 2; ++i) {
    const int un = wave - 2 * i;
    const bool fin = un >= 0 && un < 6;
    const int n16 = nt * 6 + un, rb = mt * 2 + i;
    u32x4 zz = {0u, 0u, 0u, 0u}; float bb = 0.f;
    if (fin && n16 < 43) { zz = *(const u32x4*)(z1 + ((size_t)rb * 44 + n16) * 512 + lane * 8); bb = bf2f(bsp[rb * 32 + r32]); }
    RAW_BAR();
#pragma unroll
    for (int uu = 0; uu < 6; ++uu) red_put(smem, wave * 6 + uu, lane, acc[i][uu >> 1], uu & 1);
    RAW_BAR();
    if (fin && n16 < 44) {
      f32x4 s0 = {bb, bb, bb, bb}, s1 = {bb, bb, bb, bb};
#pragma unroll
      for (int w = 0; w < 8; ++w) {
        s0 += *(const f32x4*)(smem + (w * 6 + un) * 2048 + lane * 16);
        s1 += *(const f32x4*)(smem + (w * 6 + un) * 2048 + 1024 + lane * 16);
      }
      u32x4 o = {pack2(s0[0] * bflo(zz[0]), s0[1] * bfhi(zz[0])), pack2(s0[2] * bflo(zz[1]), s0[3] * bfhi(zz[1])),
                 pack2(s1[0] * bflo(zz[2]), s1[1] * bfhi(zz[2])), pack2(s1[2] * bflo(zz[3]), s1[3] * bfhi(zz[3]))};
      if (n16 == 43) o = u32x4{0u, 0u, 0u, 0u};
      *(u32x4*)(u + ((size_t)rb * 44 + n16) * 512 + lane * 8) = o;
    }
  }
}

__global__ __launch_bounds__(512, 2) void k_out(const u16* __restrict__ u, const u16* __restrict__ wout, const u16* __restrict__ x1,
                                                const float* __restrict__ mod, u16* __restrict__ out) {
  extern __shared__ __attribute__((aligned(16))) char smem[];
  const int bid = blockIdx.x, xi = bid >> 3;
  const int mt = (bid & 7) * 4 + (xi & 3), nt = xi >> 2;
  const int tid = threadIdx.x, lane = tid & 63, wave = __builtin_amdgcn_readfirstlane(tid >> 6), r32 = lane & 31, hi = lane >> 5;
  const char* abase = (const char*)u + ((size_t)(mt * 2) * 44 + wave * 2) * 1024;
  const unsigned av0 = lane * 16, av1 = lane * 16 + 44 * 1024;
  const int krow = wave * 32 + hi * 4 + ((lane >> 2) & 3);
  const u16* bptr = wout + nt * 64 + ((lane >> 4) & 1) * 32 + (lane & 3) * 8;
  char* lw = smem + wave * 12288;
  const unsigned bb = lds_addr(lw) + ((lane >> 2) & 3) * 64 + ((lane >> 4) & 1) * 32 + (lane & 3) * 8 + hi * 512;
  const bool three = wave < 6;
  bf16x8 a[3][4];
  f32x16 acc[2][2] = {};
#define OU_LD(c_) do { _Pragma("unroll") for (int ins = 0; ins < 4; ++ins) { int r = krow + (c_) * 256 + ins * 8; if (r > FH - 1) r = FH - 1;   \
      glds16(bptr + (size_t)r * DM, lw + (c_) * 4096 + ins * 1024); }                                                                           \
    a[c_][0] = gld128<0>(abase + (c_) * 16384, av0); a[c_][1] = gld128<0>(abase + (c_) * 16384, av1);                                           \
    a[c_][2] = gld128<1024>(abase + (c_) * 16384, av0); a[c_][3] = gld128<1024>(abase + (c_) * 16384, av1); } while (0)
#define OU_MM(c_) do { s16x4 lo[4], hh[4];                                                                                                      \
    lo[0] = ldsrtr<(c_) * 4096>(bb); hh[0] = ldsrtr<(c_) * 4096 + 1024>(bb); lo[1] = ldsrtr<(c_) * 4096 + 256>(bb); hh[1] = ldsrtr<(c_) * 4096 + 1280>(bb);       \
    lo[2] = ldsrtr<(c_) * 4096 + 2048>(bb); hh[2] = ldsrtr<(c_) * 4096 + 3072>(bb); lo[3] = ldsrtr<(c_) * 4096 + 2304>(bb); hh[3] = ldsrtr<(c_) * 4096 + 3328>(bb); \
    PIN(a[c_][0]); PIN(a[c_][1]); PIN(a[c_][2]); PIN(a[c_][3]);                                                                                  \
    WAITL(4); PIN(lo[0]); PIN(hh[0]); PIN(lo[1]); PIN(hh[1]);                                                                                    \
    { const bf16x8 b0 = cat8(lo[0], hh[0]), b1 = cat8(lo[1], hh[1]);                                                                             \
      acc[0][0] = mfma32(b0, a[c_][0], acc[0][0]); acc[1][0] = mfma32(b0, a[c_][1], acc[1][0]);                                                  \
      acc[0][1] = mfma32(b1, a[c_][0], acc[0][1]); acc[1][1] = mfma32(b1, a[c_][1], acc[1][1]); }                                                \
    WAITL(0); PIN(lo[2]); PIN(hh[2]); PIN(lo[3]); PIN(hh[3]);                                                                                    \
    { const bf16x8 b0 = cat8(lo[2], hh[2]), b1 = cat8(lo[3], hh[3]);                                                                             \
      acc[0][0] = mfma32(b0, a[c_][2], acc[0][0]); acc[1][0] = mfma32(b0, a[c_][3], acc[1][0]);                                                  \
      acc[0][1] = mfma32(b1, a[c_][2], acc[0][1]); acc[1][1] = mfma32(b1, a[c_][3], acc[1][1]); } } while (0)
  const int em = mt * 64 + (wave >> 2) * 32 + r32, en = nt * 64 + (wave & 3) * 16 + 4 * hi;
  const u32x4 xx = *(const u32x4*)(x1 + ((size_t)(mt * 2 + (wave >> 2)) * 32 + nt * 4 + (wave & 3)) * 512 + lane * 8);
  const f32x4 g0 = *(const f32x4*)(mod + 5 * DM + en), g1 = *(const f32x4*)(mod + 5 * DM + en + 8);
  OU_LD(0); OU_LD(1);
  if (three) {
    OU_LD(2);
    WAITV(16); OU_MM(0); WAITV(8); OU_MM(1); WAITV(0); OU_MM(2);
  } else {
    WAITV(8); OU_MM(0); WAITV(0); OU_MM(1);
  }
#undef OU_LD
#undef OU_MM
  RAW_BAR();
#pragma unroll
  for (int uu = 0; uu < 8; ++uu) red_put(smem, wave * 8 + uu, lane, acc[uu >> 2][(uu >> 1) & 1], uu & 1);
  RAW_BAR();
  f32x4 s0 = {0, 0, 0, 0}, s1 = {0, 0, 0, 0};
#pragma unroll
  for (int w = 0; w < 8; ++w) {
    s0 += *(const f32x4*)(smem + (w * 8 + wave) * 2048 + lane * 16);
    s1 += *(const f32x4*)(smem + (w * 8 + wave) * 2048 + 1024 + lane * 16);
  }
  *(u32x2*)(out + (size_t)em * DM + en) = u32x2{pack2(bflo(xx[0]) + g0[0] * s0[0], bfhi(xx[0]) + g0[1] * s0[1]), pack2(bflo(xx[1]) + g0[2] * s0[2], bfhi(xx[1]) + g0[3] * s0[3])};
  *(u32x2*)(out + (size_t)em * DM + en + 8) = u32x2{pack2(bflo(xx[2]) + g1[0] * s1[0], bfhi(xx[2]) + g1[1] * s1[1]), pack2(bflo(xx[3]) + g1[2] * s1[2], bfhi(xx[3]) + g1[3] * s1[3])};
}

#define TS_DECL
#define CNT(i_)
#define TS_PAR
#define TS_ARG
#define TS(i_)
#define TS_OUT(b)
template <int OFF> __device__ __forceinline__ bf16x8 gld128s(const void* sb, unsigned vo) {
  bf16x8 r; asm volatile("global_load_dwordx4 %0, %1, %2 offset:%3 sc1" : "=v"(r) : "v"(vo), "s"(sb), "i"(OFF) : "memory"); return r;
}
template <int OFF> __device__ __forceinline__ f32x4 gld128fs(const void* sb, unsigned vo) {
  f32x4 r; asm volatile("global_load_dwordx4 %0, %1, %2 offset:%3 sc1" : "=v"(r) : "v"(vo), "s"(sb), "i"(OFF) : "memory"); return r;
}
template <int OFF> __device__ __forceinline__ unsigned gld32s(const void* sb, unsigned vo) {
  unsigned r; asm volatile("global_load_dword %0, %1, %2 offset:%3 sc1" : "=v"(r) : "v"(vo), "s"(sb), "i"(OFF) : "memory"); return r;
}
template <int OFF> __device__ __forceinline__ unsigned gld16(const void* sb, unsigned vo) {
  unsigned r; asm volatile("global_load_ushort %0, %1, %2 offset:%3" : "=v"(r) : "v"(vo), "s"(sb), "i"(OFF) : "memory"); return r;
}
template <int OFF> __device__ __forceinline__ f32x4 gld128f(const void* sb, unsigned vo) {
  f32x4 r; asm volatile("global_load_dwordx4 %0, %1, %2 offset:%3" : "=v"(r) : "v"(vo), "s"(sb), "i"(OFF) : "memory"); return r;
}
template <int OFF> __device__ __forceinline__ u32x2 gld64(const void* sb, unsigned vo) {
  u32x2 r; asm volatile("global_load_dwordx2 %0, %1, %2 offset:%3" : "=v"(r) : "v"(vo), "s"(sb), "i"(OFF) : "memory"); return r;
}
template <int OFF, class V> __device__ __forceinline__ void gst128s(const void* sb, unsigned vo, V v) {
  asm volatile("global_store_dwordx4 %0, %1, %2 offset:%3 sc1\n\ts_nop 1" :: "v"(vo), "v"(v), "s"(sb), "i"(OFF) : "memory");
}
__device__ __forceinline__ void dma16(const void* sb, unsigned vo, unsigned lds) {
  asm volatile("s_mov_b32 m0, %2\n\ts_nop 0\n\tglobal_load_lds_dwordx4 %0, %1" :: "v"(vo), "s"(sb), "s"(lds) : "memory");
}
template <int OFF> __device__ __forceinline__ f32x4 ldsr128f(unsigned a) {
  f32x4 r; asm volatile("ds_read_b128 %0, %1 offset:%2" : "=v"(r) : "v"(a), "i"(OFF)); return r;
}
template <int OFF> __device__ __forceinline__ u32x2 ldsr64u(unsigned a) {
  u32x2 r; asm volatile("ds_read_b64 %0, %1 offset:%2" : "=v"(r) : "v"(a), "i"(OFF)); return r;
}
#define HO_CAP (1u << 14)
#define HO_FORCE 0
template <class V> __device__ __forceinline__ unsigned mx4(unsigned m, V v) {
  const u32x4 w = __builtin_bit_cast(u32x4, v);
  return max(max(max(m, w[0]), w[1]), max(w[2], w[3]));
}
__device__ __forceinline__ bool ho_ok(unsigned m) { return __builtin_amdgcn_ballot_w64(m == 0xffffffffu) == 0ull; }
#define HO_LOOP _Pragma("unroll 1") for (unsigned ho_sp = 0; ho_sp < HO_CAP; ++ho_sp)
#define HO_BAD(m_) __builtin_expect(!ho_ok(m_) || (HO_FORCE), 0)

#define FL_Z 1
#define FL_SLEEP 1
constexpr unsigned FZ_OFF = 256u << 10, FH_OFF = 272u << 10, FU_OFF = 304u << 10, FP_OFF = 352u << 10;
__device__ __forceinline__ void fl_set(const void* base, unsigned off, int lane) {
  if (lane == 0) asm volatile("global_store_dword %0, %1, %2 sc1" :: "v"(off), "v"(0u), "s"(base) : "memory");
}
__device__ __forceinline__ void fl_wait(const void* base, unsigned off) {
  HO_LOOP {
    unsigned q = gld32s<0>(base, off);
    WAITV(0); PIN(q);
    if (ho_ok(q)) break;
    __builtin_amdgcn_s_sleep(FL_SLEEP);
  }
}
#define WO_LATE 1
#define S5L 0
#define ZA 2
#define ZB 14
#define SV_AT 34
#define OU_PROBE 1
#define MAP 0
#define OU_G 6
#define OU_SL 1
#define X_HOT 0
#define Z2_N0 4
#define ZR0 24
#define P2_SL2 2
template <int OFF> __device__ __forceinline__ unsigned gld32(const void* sb, unsigned vo) {
  unsigned r; asm volatile("global_load_dword %0, %1, %2 offset:%3" : "=v"(r) : "v"(vo), "s"(sb), "i"(OFF) : "memory"); return r;
}
template <int OFF> __device__ __forceinline__ u32x2 gld64s(const void* sb, unsigned vo) {
  u32x2 r; asm volatile("global_load_dwordx2 %0, %1, %2 offset:%3 sc1" : "=v"(r) : "v"(vo), "s"(sb), "i"(OFF) : "memory"); return r;
}
template <int OFF> __device__ __forceinline__ void gst64s(const void* sb, unsigned vo, u32x2 v) {
  asm volatile("global_store_dwordx2 %0, %1, %2 offset:%3 sc1\n\ts_nop 1" :: "v"(vo), "v"(v), "s"(sb), "i"(OFF) : "memory");
}
template <int OFF> __device__ __forceinline__ s16x4 ldsrtra(unsigned a) {
  s16x4 r; asm volatile("ds_read_b64_tr_b16 %0, %1 offset:%2" : "=a"(r) : "v"(a), "i"(OFF)); return r;
}
#define PINA(x) asm volatile("" : "+a"(x))
template <int C> __device__ __forceinline__ unsigned vxor(unsigned a) {
  unsigned r; asm volatile("v_xor_b32 %0, %1, %2" : "=v"(r) : "i"(C), "v"(a)); return r;
}
__device__ __forceinline__ f32x4 gelu4(f32x4 x) {
  f32x4 t = x * (x * x * -0.10294324f + -2.3022082f);
#pragma unroll
  for (int e = 0; e < 4; ++e) t[e] = __builtin_amdgcn_exp2f(t[e]);
  t += 1.f;
#pragma unroll
  for (int e = 0; e < 4; ++e) t[e] = __builtin_amdgcn_rcpf(t[e]);
  return x * t;
}
template <int Q> __device__ __forceinline__ f32x4 qt(const f32x16& a) { return f32x4{a[4 * Q], a[4 * Q + 1], a[4 * Q + 2], a[4 * Q + 3]}; }
template <int Q2> __device__ __forceinline__ f32x4 qsel(const f32x16& a, int h) { return h ? qt<2 + Q2>(a) : qt<Q2>(a); }
template <int L, int C> __device__ __forceinline__ void dma_c(const void* sb, unsigned vo, unsigned lds) {
  unsigned t; asm volatile("s_add_u32 m0, %3, %4\n\tv_add_u32 %0, %5, %1\n\tglobal_load_lds_dwordx4 %0, %2" : "=&v"(t) : "v"(vo), "s"(sb), "s"(lds), "i"(L), "i"(C) : "memory", "scc");
}
template <int C> __device__ __forceinline__ void gst_c(const void* sb, unsigned vo, u32x4 v) {
  unsigned t; asm volatile("v_add_u32 %0, %4, %1\n\tglobal_store_dwordx4 %0, %2, %3 sc1" : "=&v"(t) : "v"(vo), "v"(v), "s"(sb), "i"(C) : "memory");
}
__device__ __forceinline__ unsigned apack(float a, float b) {
  unsigned r, t; asm volatile("v_accvgpr_read_b32 %0, %2\n\tv_accvgpr_read_b32 %1, %3\n\tv_cvt_pk_bf16_f32 %0, %0, %1" : "=&v"(r), "=&v"(t) : "a"(a), "a"(b)); return r;
}
template <int I0, int I1, int C, int L>
__device__ __forceinline__ void sp_a(const u16* const (&ab)[2], unsigned ve, unsigned vo, unsigned lds) {
  sfor<I0, I1>([&](auto I) { constexpr int i = decltype(I)::v; dma_c<L + i * 1024, (i & 3) * 16 * S_ + C * 128>(ab[i >> 2], (i & 1) ? vo : ve, lds); });
}
template <int X, int Z> __device__ __forceinline__ void in_piece(const u16* wt, const unsigned (&iv)[3], unsigned lw) { dma_c<65536 + X * 1024, (X / 3) * 32 * FF + Z * FH * 2>(wt, iv[X % 3], lw); }
#define SP_NB 12
constexpr int sp_v(int T) { return (T >> 2) & 1; }
constexpr int sp_c(int T) { return T >> 3; }
constexpr int sp_k(int T) { return sp_v(T) * 16 + sp_c(T) * 4 + (T & 3); }
constexpr int sp_wait(int t) {
  int pos = 0, need = -1;
  for (int i = 0; i < SP_NB; ++i) { pos += 3; if (i == t + 1) need = pos; }
  for (int x = 0; x < t; ++x) {
    if (x + SP_NB < 32) { pos += 3; if (x + SP_NB == t + 1) need = pos; }
    if (x == 2 || x == 6) { pos += 4; if (t + 1 == 14 + x) need = pos; }
    if (x == 18) pos += WO_LATE ? 1 : 9;
    if (x == 22) pos += WO_LATE ? 0 : 8;
  }
  return pos - need;
}
#define OP_AL0 48
#define OP_PS0 193
constexpr int op_r(int s) { return s < 4 ? s : s == 4 ? 9 : s - 1; }
constexpr int op_s(int t) { return t < 8 ? t : (t + 4) & 7; }
constexpr int op_f(int i) { return i < 192 ? i >> 2 : 48 + (i & 15); }
constexpr int op_c(int i) { return i < 192 ? i & 3 : (i >> 4) & 3; }
constexpr int op_ck(int g) { return g < 4 ? 123 + 16 * g : 161 + 5 * g; }
constexpr int OP_IN[24] = {182, 185, 188, 191, 209, 211, 216, 218, 222, 224, 227, 233, 25, 28, 31, 34, 37, 40, 43, 46, 164, 170, 176, 179};
constexpr int OP_ST[14] = {133, 139, 145, 151, 157, 163, 169, 175, 214, 220, 230, 236, 246, 252};
constexpr int op_act(int i) {
  if (i < 24 && (i & 1) == 0) return 64 + (i >> 1);
  if (i == 101) return 95;
  if (i >= OP_AL0 && i < OP_AL0 + 64 && ((i - OP_AL0) & 1) == 0) return (i - OP_AL0) >> 1;
  for (int n = 0; n < 24; ++n) if (OP_IN[n] == i) return 32 + n;
  for (int n = 0; n < 14; ++n) if (OP_ST[n] == i) return 80 + n;
  if (i >= 112 && i < 132 && (i & 1) == 0) return 96 + ((i - 112) >> 1);
  if (i >= OP_PS0 && i < OP_PS0 + 16 && ((i - OP_PS0) & 1) == 0) return 112 + ((i - OP_PS0) >> 1);
  return -1;
}
constexpr int op_pk(int i) { for (int n = 0; n < 14; ++n) if (i >= OP_ST[n] - 4 && i < OP_ST[n]) return n * 4 + i - OP_ST[n] + 4; return -1; }
constexpr bool op_chk() { int n = 0; for (int i = 0; i < 256; ++i) n += op_act(i) >= 0; return n == 101; }
constexpr int op_yg(int i, int id) {
  int n = id >= 200 ? (207 - id) * 4 : -1000;
  for (int g = 0; g < i; ++g) { const int a = op_act(g); if (a >= 0) n = a == id ? 0 : n + 1; }
  return n < 0 ? -1 : n < 63 ? n : 63;
}
constexpr int imin(int a, int b) { return a < b ? a : b; }
constexpr int op_wait(int t) { return imin(imin(t < 8 ? op_yg(16 * t, 200 + t) : 63, t >= 4 && t <= 6 ? op_yg(16 * t, 51 + 4 * t) : 63), t == 8 ? op_yg(16 * t, 95) : 63); }
static_assert(OP_AL0 + 64 <= 112 && op_chk(), "schedule");
#define AHI "a128", "a129", "a130", "a131", "a132", "a133", "a134", "a135", "a136", "a137", "a138", "a139", "a140", "a141", "a142", "a143", "a144", "a145", "a146", "a147", "a148", "a149", "a150", "a151", "a152", "a153", "a154", "a155", "a156", "a157", "a158", "a159", "a160", "a161", "a162", "a163", "a164", "a165", "a166", "a167", "a168", "a169", "a170", "a171", "a172", "a173", "a174", "a175", "a176", "a177", "a178", "a179", "a180", "a181", "a182", "a183", "a184", "a185", "a186", "a187", "a188", "a189", "a190", "a191", "a192", "a193", "a194", "a195", "a196", "a197", "a198", "a199", "a200", "a201", "a202", "a203", "a204", "a205", "a206", "a207", "a208", "a209", "a210", "a211", "a212", "a213", "a214", "a215", "a216", "a217", "a218", "a219", "a220", "a221", "a222", "a223", "a224", "a225", "a226", "a227", "a228", "a229", "a230", "a231", "a232", "a233", "a234", "a235", "a236", "a237", "a238", "a239", "a240", "a241", "a242", "a243", "a244", "a245", "a246", "a247", "a248", "a249", "a250", "a251", "a252", "a253", "a254", "a255"
template <int F> __device__ __forceinline__ void mfma_s(f32x16& acc, bf16x8 w) {
  if constexpr (F) asm volatile("v_mfma_f32_32x32x16_bf16 %0, %1, a[%2:%3], %0" : "+a"(acc) : "v"(w), "i"(128 + 4 * F), "i"(131 + 4 * F) : AHI);
  else asm volatile("v_mfma_f32_32x32x16_bf16 %0, %1, a[128:131], 0" : "=a"(acc) : "v"(w) : AHI);
}
template <int F> __device__ __forceinline__ void mfma_l(f32x16& acc, bf16x8 w, bf16x8 b) {
  if constexpr (F) asm volatile("v_mfma_f32_32x32x16_bf16 %0, %1, %2, %0" : "+a"(acc) : "v"(w), "v"(b));
  else asm volatile("v_mfma_f32_32x32x16_bf16 %0, %1, %2, 0" : "=a"(acc) : "v"(w), "v"(b));
}
template <bool FIRST, class V> __device__ __forceinline__ void vmx(unsigned& m, V v) {
  const u32x4 w = __builtin_bit_cast(u32x4, v);
  if constexpr (FIRST) asm volatile("v_max_u32 %0, %1, %2\n\tv_max3_u32 %0, %0, %3, %4" : "=&v"(m) : "v"(w[0]), "v"(w[1]), "v"(w[2]), "v"(w[3]));
  else asm volatile("v_max3_u32 %0, %0, %1, %2\n\tv_max3_u32 %0, %0, %3, %4" : "+v"(m) : "v"(w[0]), "v"(w[1]), "v"(w[2]), "v"(w[3]));
}
template <int S> __device__ __forceinline__ bf16x8 ou_a(const char* ab, unsigned v0) { return gld128s<(S & 7) * 1024 - 4096>(ab + (S >> 3) * 8192, v0); }
template <bool SC, int OFF> __device__ __forceinline__ bf16x8 gldA(const void* sb, unsigned vo) { if constexpr (SC) return gld128s<OFF>(sb, vo); else return gld128<OFF>(sb, vo); }
#define SB() __builtin_amdgcn_sched_barrier(0)
template <int OFF> __device__ __forceinline__ void rld128s(bf16x8& r, unsigned m, const void* sb, unsigned vo) {
  asm volatile("v_cmp_eq_u32 vcc, -1, %1\n\ts_mov_b64 exec, vcc\n\tglobal_load_dwordx4 %0, %2, %3 offset:%4 sc1\n\ts_mov_b64 exec, -1" : "+v"(r) : "v"(m), "v"(vo), "s"(sb), "i"(OFF) : "memory", "vcc");
}
#define MR2 1
#define MR3 1
#define P3_SL 2

template <bool FUSED>
__device__ __forceinline__ void tail4(char* smem, const int bid, const int wave, const int lane,
                                      const u16* ctx, const u16* __restrict__ wo, const u16* __restrict__ x, const float* __restrict__ mod,
                                      const u16* __restrict__ win, const u16* __restrict__ wsp, const u16* __restrict__ bsp, const u16* __restrict__ wout,
                                      u16* part, u16* x1, u16* h2, u16* z2, float* st2, u16* u, u16* __restrict__ out, unsigned* fl, unsigned wl) {
  TS_DECL; TS(0);
  const int xcd = bid & 7, xi = bid >> 3;
  const int mt = MAP ? xcd + 8 * (xi >> 4) : (xi & 7) * 2 + (xcd >> 2), jj = MAP ? xi & 15 : (xcd & 3) * 4 + (xi >> 3);
  const int r32 = lane & 31, hi = lane >> 5;
  const int half = jj >> 3, tt = jj & 7, rt = mt * 2 + half;
  const unsigned lb = lds_addr(smem);
  const unsigned trl = ((lane >> 2) & 3) * 64 + ((lane >> 4) & 1) * 32 + (lane & 3) * 8;
  const unsigned pl = (r32 >> 2) * 128 + hi * 64 + (lane & 3) * 16;
  const int mblk = (rt & 15) * 2 + (mt >> 3), p2u = wave * 8 + (lane >> 3);
  const unsigned p2o = tt * 128 + (lane & 7) * 16, op = p2u * 8192 + p2o + 4096;
  const u16* pb = part + (size_t)mblk * 131072;
  const u16* wt = win + (X_HOT & 1 ? 0 : tt * 96);
  const unsigned lw = lb + wave * 1024;
  unsigned iv[3], iw[3];
#pragma unroll
  for (int r = 0; r < 3; ++r) {
    const int un = (r + wave) * 4 + (lane >> 4), k4 = (un * 43) >> 7, c = (un - 3 * k4) * 32 + (lane & 3) * 8;
    iv[r] = ((k4 * 4 + ((lane >> 2) & 3)) * FF + min(c, FH - 8 - tt * 96)) * 2;
    iw[r] = ((k4 * 4 + ((lane >> 2) & 3)) * FF + min(c < 80 ? c + 16 : c - 80, FH - 8 - tt * 96)) * 2;
  }
  u32x2 xa[2], xb[2]; f32x4 g0, g1, s0, s1, c0, c1; u32x4 xo[2];
  bf16x8 pq[2][8];
  {
    const int pr = xi & 7, nt = bid >> 6;
    const char* al = (const char*)ctx + ((size_t)((15 - pr) * 4 + wave) * 256 + xcd * 8) * 1024;
    const unsigned av = lane * 16;
    const u16* bptr = wo + (size_t)(xcd * 128 + wave * 16) * DM + nt * 128;
    const unsigned bv = (((lane >> 2) & 3) * DM + (lane >> 4) * 32 + (lane & 3) * 8) * 2;
    const unsigned bb = lb + trl + hi * 1024, bbh = bb + 65536, bbg = bb + 131072;
    const u16* ds = part + (((size_t)(pr * 4 + wave) * 32 + nt * 8) * 8 + xcd) * 512;
    const u16* dl = part + (((size_t)((15 - pr) * 4 + wave) * 32 + nt * 8) * 8 + xcd) * 512;
    bf16x8 b[32];
    f32x16 acc[2][4];
    s16x4 lo[8], hh[8];
    f32x4 fa;
    u32x4 pk;
    unsigned mk;
#define OP_H(s_) ((((s_) >> 1) + nt + 2) & 3)
#define OP_RD(i_) do { constexpr int rg_ = op_r(op_s(op_f(i_) >> 2)), o_ = (rg_ & 3) * 16384 + (op_f(i_) & 3) * 4096 + op_c(i_) * 256; const unsigned q_ = rg_ >= 8 ? bbg : rg_ >= 4 ? bbh : bb;      \
      lo[(i_) & 7] = ldsrtr<o_>(q_); hh[(i_) & 7] = ldsrtr<o_ + 2048>(q_); } while (0)
#define OP_AL(n_) (al + OP_H(op_s(8 + ((n_) >> 2))) * 65536 + (((n_) >> 2) & 1) * 4096)
    KEEP(wl);
    __builtin_amdgcn_s_barrier();
    sfor<0, 8>([&](auto J) { constexpr int j = decltype(J)::v; OP_RD(j); });
    sfor<0, 16>([&](auto T) {
      constexpr int t = decltype(T)::v, h = t >> 3;
      if constexpr (op_wait(t) < 63) WAITV(op_wait(t));
      if constexpr ((t >= 4 && t <= 6) || t == 10 || t == 12) __builtin_amdgcn_s_barrier();
      if constexpr (t == 3) TS(14);
#define TS2(i_) TS(i_)
#define TS3(i_)
      if constexpr (t == 8) {
        PIN(fa);
        if (HO_BAD(mx4(0u, fa))) {
          WAITL(0);
          HO_LOOP {
            fa = gld128fs<16>(fl, lane * 128);
            WAITV(0); PIN(fa);
            if (ho_ok(mx4(0u, fa))) break;
            __builtin_amdgcn_s_sleep(1);
          }
        }
      }
      sfor<0, 16>([&](auto J) { constexpr int j = decltype(J)::v, i = t * 16 + j, c = op_c(i), f = op_f(i) & 31;
        if constexpr ((i & 1) == 0) WAITL(i < 248 ? 12 : 2 * (254 - i));
        if constexpr (h) mfma_l<f>(acc[1][c], cat8(lo[i & 7], hh[i & 7]), b[f]);
        else mfma_s<f>(acc[0][c], cat8(lo[i & 7], hh[i & 7]));
        constexpr int n = op_act(i), k = op_pk(i);
        constexpr int sl = 5 + ((n - 64) >> 2);
        if constexpr (n >= 80 && n < 94) { gst_c<(n & 7) * 8192>(n < 88 ? ds : dl, pl, pk); if constexpr (i + 8 >= 256) asm volatile("s_nop 1"); }
        if constexpr (i + 8 < 256) OP_RD(i + 8);
        if constexpr (n >= 0 && n < 32) b[n] = gld128s<(n & 3) * 1024>(OP_AL(n), av);
        if constexpr (n == 95) fa = gld128fs<16>(fl, lane * 128);
        if constexpr (n >= 32 && n < 56) in_piece<(n - 32) * 4, 0>(wt, iv, lw);
        if constexpr (n >= 64 && n < 76) dma_c<op_r(sl) * 16384 + (n & 3) * 1024, ((sl & 1) * 64 + (n & 3) * 4) * DM * 2>(bptr + (size_t)OP_H(sl) * 1024 * DM, bv, lb + wave * 4096);
        if constexpr (n >= 96 && n < 112) {
          const int o = p2u * 16 + (lane & 4), m = mblk * 32 + tt * 4 + (lane & 3);
          if constexpr (n == 96) xa[0] = gld64<0>(x, (m * DM + o) * 2);
          if constexpr (n == 97) xb[0] = gld64<16>(x, (m * DM + o) * 2);
          if constexpr (n == 98) g0 = gld128f<0>(mod + 2 * DM, o * 4);
          if constexpr (n == 99) g1 = gld128f<32>(mod + 2 * DM, o * 4);
          if constexpr (n == 100) s0 = gld128f<2048>(mod + 2 * DM, o * 4);
          if constexpr (n == 101) s1 = gld128f<2080>(mod + 2 * DM, o * 4);
          if constexpr (n == 102) c0 = gld128f<0>(mod + 4 * DM, o * 4);
          if constexpr (n == 103) c1 = gld128f<32>(mod + 4 * DM, o * 4);
          if constexpr (n == 104) xa[1] = gld64<0>(x + 1024 * DM, (m * DM + o) * 2);
          if constexpr (n == 105) xb[1] = gld64<16>(x + 1024 * DM, (m * DM + o) * 2);
        }
        if constexpr (n >= 112) pq[0][n & 7] = gld128s<(n & 7) * 1024 - 4096>(pb, op);
        if constexpr (k >= 0) pk[k & 3] = apack(acc[k >> 5][(k >> 3) & 3][(k & 7) * 2], acc[k >> 5][(k >> 3) & 3][(k & 7) * 2 + 1]);
        sfor<0, 8>([&](auto G) { constexpr int g = decltype(G)::v, q = i - op_ck(g) + 4;
          if constexpr (q == 0) WAITV(op_yg(i + 1, 4 * g + 3));
          if constexpr (q >= 0 && q < 4) vmx<q == 0>(mk, b[4 * g + q]);
          if constexpr (q == 4) {
            if (HO_BAD(mk)) {
              WAITL(0); WAITV(0);
              HO_LOOP {
                sfor<4 * g, 32>([&](auto N) { constexpr int n = decltype(N)::v; PIN(b[n]); rld128s<(n & 3) * 1024>(b[n], mx4(0u, b[n]), OP_AL(n), av); });
                WAITV(0);
                _Pragma("unroll") for (int n = 4 * g; n < 32; ++n) PIN(b[n]);
                if (ho_ok(mx4(mx4(mx4(mx4(0u, b[4 * g]), b[4 * g + 1]), b[4 * g + 2]), b[4 * g + 3]))) break;
              }
            }
          } }); });
    });
    TS(1);
    asm volatile("s_nop 7\n\ts_nop 2");
    PINA(acc[1][3]);
    gst128s<0>(dl, pl + 6 * 8192, pack8(acc[1][3], 0)); gst128s<0>(dl, pl + 7 * 8192, pack8(acc[1][3], 1));
#undef OP_RD
#undef OP_AL
#undef OP_H
  }
  TS(2);
  const int om = wave & 1, oh = wave >> 1;
  const u16* const sab[2] = {wsp + (size_t)(rt * 64 + om * 32) * S_ + wave * 512, wsp + (size_t)(rt * 64 + 32 - om * 32) * S_ + wave * 512};
  const unsigned sve = ((lane >> 3) * S_ + ((lane & 7) ^ (lane >> 4)) * 8) * 2, svo = ((lane >> 3) * S_ + ((lane & 7) ^ ((lane >> 4) | 4)) * 8) * 2;
  const unsigned slds = lb + wave * 16384, elds = lb + 131072 + wave * 8192;
  const int lx = lane ^ (oh * 16), rx = lx & 31;
  const unsigned a0x = slds + rx * 128 + (hi ^ ((rx >> 1) & 7)) * 16;
  const unsigned bb = lb + 65536 + oh * 49152 + (trl ^ (oh * 32)) + hi * 768;
  s16x4 zl[16][3], zh[16][3];
  {
    float* rw = (float*)(smem + ((lane >> 2) & 3) * 16384 + 15360) + (lane & 3) * 8 + ((lane >> 4) & 1) * 4 + wave;
    const float* rr = (const float*)(smem + wave * 16384 + 15360) + (lane & 3) * 8;
#define Z2(k0_, k1_) do { SB(); sfor<imin(k0_, 24), imin(k1_, 24)>([&](auto Q) { in_piece<decltype(Q)::v * 4, 1>(wt, iw, lw); }); SB(); } while (0)
    float v[8];
    auto p2a = [&](auto E) { constexpr int e = decltype(E)::v;
#pragma unroll
      for (int p = 0; p < 8; ++p) PIN(pq[e][p]);
      unsigned mk;
      sfor<0, 8>([&](auto P) { vmx<decltype(P)::v == 0>(mk, pq[e][decltype(P)::v]); });
      if (HO_BAD(mk)) {
#define P2_RL() sfor<0, 8>([&](auto P) { constexpr int p = decltype(P)::v; rld128s<p * 1024 - 4096>(pq[e][p], mx4(0u, pq[e][p]), pb + e * (32 * 131072), op); })
        WAITL(0);
        P2_RL();
        WAITV(0);
        HO_LOOP {
          unsigned m = 0u;
#pragma unroll
          for (int p = 0; p < 8; ++p) { PIN(pq[e][p]); m = mx4(m, pq[e][p]); }
          if (ho_ok(m)) break;
          __builtin_amdgcn_s_sleep(P2_SL2);
          P2_RL();
          WAITV(0);
        }
#pragma unroll
        for (int p = 0; p < 8; ++p) PIN(pq[e][p]);
#undef P2_RL
      }
      if constexpr (e) { TS2(3); TS3(15); }
      f32x4 a0, a1;
      u32x4 w[8];
#pragma unroll
      for (int p = 0; p < 8; ++p) w[p] = __builtin_bit_cast(u32x4, pq[e][p]);
#define P2_S(f_, j_) (((f_(w[0][j_]) + f_(w[1][j_])) + (f_(w[2][j_]) + f_(w[3][j_]))) + ((f_(w[4][j_]) + f_(w[5][j_])) + (f_(w[6][j_]) + f_(w[7][j_]))))
      sfor<0, 2>([&](auto J) { constexpr int j = decltype(J)::v;
        a0[2 * j] = P2_S(bflo, j); a0[2 * j + 1] = P2_S(bfhi, j);
        if constexpr (e == 0) Z2(ZA * (2 * j + 1), ZA * (2 * j + 2));
        a1[2 * j] = P2_S(bflo, 2 + j); a1[2 * j + 1] = P2_S(bfhi, 2 + j);
        if constexpr (e == 0) Z2(ZA * (2 * j + 2), ZA * (2 * j + 3)); });
#undef P2_S
      xo[e] = u32x4{pack2(bflo(xa[e][0]) + g0[0] * a0[0], bfhi(xa[e][0]) + g0[1] * a0[1]), pack2(bflo(xa[e][1]) + g0[2] * a0[2], bfhi(xa[e][1]) + g0[3] * a0[3]),
                    pack2(bflo(xb[e][0]) + g1[0] * a1[0], bfhi(xb[e][0]) + g1[1] * a1[1]), pack2(bflo(xb[e][1]) + g1[2] * a1[2], bfhi(xb[e][1]) + g1[3] * a1[3])};
#pragma unroll
      for (int j = 0; j < 4; ++j) { v[2 * j] = bflo(xo[e][j]); v[2 * j + 1] = bfhi(xo[e][j]); }
      if constexpr (e == 0) Z2(ZA * 5, ZA * 6);
      float s = ((v[0] + v[1]) + (v[2] + v[3])) + ((v[4] + v[5]) + (v[6] + v[7]));
      float q = ((v[0] * v[0] + v[1] * v[1]) + (v[2] * v[2] + v[3] * v[3])) + ((v[4] * v[4] + v[5] * v[5]) + (v[6] * v[6] + v[7] * v[7])), t;
      asm volatile(
        "s_nop 1\n\t"
        "v_permlane16_swap_b32 %0, %1\n\ts_nop 1\n\tv_add_f32 %0, %0, %1\n\ts_nop 1\n\t"
        "v_add_f32_dpp %0, %0, %0 row_ror:8 row_mask:0xf bank_mask:0xf\n\ts_nop 1\n\t"
        "v_add_f32_dpp %0, %0, %0 row_ror:4 row_mask:0xf bank_mask:0xf\n\t"
        "v_mov_b32 %2, %0\n\ts_nop 1\n\tv_permlane32_swap_b32 %0, %2\n\ts_nop 1\n\tv_add_f32 %0, %0, %2"
        : "+v"(s), "+v"(q), "=&v"(t));
      rw[e * 32] = s;
    };
    auto p2b = [&](auto E) { constexpr int e = decltype(E)::v;
      const f32x4 q0 = *(const f32x4*)(rr + e * 32), q1 = *(const f32x4*)(rr + e * 32 + 4);
      const float mu = ((q0[0] + q0[1]) + (q0[2] + q0[3])) * (1.f / DM);
      const float ra = __builtin_amdgcn_rsqf(fmaxf(((q1[0] + q1[1]) + (q1[2] + q1[3])) * (1.f / DM) - mu * mu, 0.f) + 1e-5f), rb = -mu * ra;
      float o[8];
#pragma unroll
      for (int j = 0; j < 4; ++j) { o[j] = (v[j] * ra + rb) * c0[j] + s0[j]; o[4 + j] = (v[4 + j] * ra + rb) * c1[j] + s1[j]; }
      gst128s<0>(h2 + (size_t)(mblk + 32 * e) * 16384, p2u * 1024 + p2o, u32x4{pack2(o[0], o[1]), pack2(o[2], o[3]), pack2(o[4], o[5]), pack2(o[6], o[7])});
    };
    sfor<0, 8>([&](auto P) { constexpr int p = decltype(P)::v; pq[1][p] = gld128s<p * 1024 - 4096>(pb + 32 * 131072, op); });
    TS3(3);
    WAITV(op_yg(256, 43) + 10);
    __builtin_amdgcn_s_barrier();
    TS3(4);
    PIN(xa[0]); PIN(xb[0]); PIN(xa[1]); PIN(xb[1]); PIN(g0); PIN(g1); PIN(s0); PIN(s1); PIN(c0); PIN(c1);
    c0 += 1.f; c1 += 1.f;
    sfor<0, 48>([&](auto I) { constexpr int i = decltype(I)::v; zl[i / 3][i % 3] = ldsrtra<(i / 3) * 3072 + (i % 3) * 256>(bb); zh[i / 3][i % 3] = ldsrtra<(i / 3) * 3072 + (i % 3) * 256 + 1536>(bb); });
    RAW_BAR();
    TS3(5);
#pragma unroll
    for (int i = 0; i < 48; ++i) { PINA(zl[i / 3][i % 3]); PINA(zh[i / 3][i % 3]); }
    Z2(0, ZA);
    p2a(IC<0>{});
    Z2(ZA * 6, ZA * 7);
    RAW_BAR();
    TS3(6);
    p2b(IC<0>{});
    Z2(ZA * 7, ZB);
    WAITV(imin(ZB, 24) + 1);
    TS3(13);
    p2a(IC<1>{});
    RAW_BAR();
    p2b(IC<1>{});
    TS3(7);
    Z2(ZB, 24);
#undef Z2
    sp_a<0, 8, 1, 0>(sab, sve, svo, slds); sp_a<0, 8 - S5L, 5, 8192>(sab, sve, svo, slds);
  }
  TS2(4);
  bf16x8 rl[8][2];
  u32x2 zq[2][3];
  {
    bf16x8 a[16];
    const char* ab = (const char*)h2 + ((size_t)(rt * 2 + om) * 32 + oh * 16) * 1024;
    const unsigned v0 = (pl ^ (oh * 512)) + 4096;
    const unsigned b0 = bb + (rx < 16 ? 544 : -32), b1 = bb + (rx < 16 ? 32 : 224);
    f32x16 acc[3];
    s16x4 lo[2][3], hh[2][3];
#define IN_RD(s_, p_) do { lo[p_][0] = ldsrtr<(s_) * 3072>(b0); hh[p_][0] = ldsrtr<(s_) * 3072 + 1536>(b0);                 \
                           lo[p_][1] = ldsrtr<(s_) * 3072>(b1); hh[p_][1] = ldsrtr<(s_) * 3072 + 1536>(b1);                 \
                           lo[p_][2] = ldsrtr<(s_) * 3072 + 256>(b1); hh[p_][2] = ldsrtr<(s_) * 3072 + 1792>(b1); } while (0)
    sfor<0, 16>([&](auto S) { constexpr int s = decltype(S)::v; a[s] = ou_a<s>(ab, v0); });
    sp_a<8 - S5L, 8, 5, 8192>(sab, sve, svo, slds);
    WAITV(32);
    __builtin_amdgcn_s_barrier();
    TS3(8);
    IN_RD(0, 0);
    WAITL(0);
#pragma unroll
    for (int j = 0; j < 3; ++j) { PIN(lo[0][j]); PIN(hh[0][j]); }
    WAITV(S5L);
    HO_LOOP {
      unsigned m = 0u, mp[16];
#pragma unroll
      for (int s = 0; s < 16; ++s) { PIN(a[s]); mp[s] = mx4(0u, a[s]); m = max(m, mp[s]); }
      if (ho_ok(m)) break;
      __builtin_amdgcn_s_sleep(P3_SL);
      sfor<0, 16>([&](auto S) { constexpr int s = decltype(S)::v; rld128s<(s & 7) * 1024 - 4096>(a[s], mp[s], ab + (s >> 3) * 8192, v0); });
      WAITV(0);
    }
#pragma unroll
    for (int s = 0; s < 16; ++s) PIN(a[s]);
    TS2(5);
    sfor<0, 16>([&](auto S) {
      constexpr int s = decltype(S)::v, p = s & 1;
      rl[s >> 1][s & 1] = ldsr128<(s >> 3) * 8192 + (s & 1) * 4096>(vxor<((s >> 1) & 3) * 32>(a0x));
      if constexpr (s + 1 < 16) { IN_RD(s + 1, p ^ 1); WAITL(7); } else WAITL(0);
#pragma unroll
      for (int j = 0; j < 3; ++j) { PIN(lo[p][j]); PIN(hh[p][j]); }
#pragma unroll
      for (int j = 0; j < 3; ++j) acc[j] = mfma32(a[s], cat8(lo[p][j], hh[p][j]), s ? acc[j] : f32x16{});
    });
#undef IN_RD
    TS2(6);
#pragma unroll
    for (int i = 0; i < 16; ++i) PIN(rl[i >> 1][i & 1]);
    { char* d = smem + wave * 16384 + 10240 + lx * 16;
#pragma unroll
      for (int j = 0; j < 3; ++j) { *(f32x4*)(d + j * 2048) = qt<2>(acc[j]); *(f32x4*)(d + j * 2048 + 1024) = qt<3>(acc[j]); } }
    f32x4 s[2][3];
#pragma unroll
    for (int j = 0; j < 3; ++j) { s[0][j] = qt<0>(acc[j]); s[1][j] = qt<1>(acc[j]); }
    RAW_BAR();
    { const char* own = smem + (wave ^ 2) * 16384 + 10240 + lx * 16;
#pragma unroll
      for (int j = 0; j < 6; ++j) s[j & 1][j >> 1] += *(const f32x4*)(own + j * 1024); }
#pragma unroll
    for (int j = 0; j < 6; ++j) s[j & 1][j >> 1] = gelu4(s[j & 1][j >> 1]);
    if (tt == 7) {
#pragma unroll
      for (int q2 = 0; q2 < 2; ++q2) { s[q2][0] *= rx < 16 ? 1.f : 0.f; s[q2][1] = s[q2][2] = f32x4{0.f, 0.f, 0.f, 0.f}; }
    }
    const int zr = rt * 64 + om * 32 + oh * 16;
#pragma unroll
    for (int q2 = 0; q2 < 2; ++q2) {
      const f32x4 sa = s[q2][0] + s[q2][1] + s[q2][2], qa = s[q2][0] * s[q2][0] + s[q2][1] * s[q2][1] + s[q2][2] * s[q2][2];
      float x0 = sa[0], x1_ = sa[1], x2 = sa[2], x3 = sa[3], y0 = qa[0], y1 = qa[1], y2 = qa[2], y3 = qa[3];
      asm volatile(
        "s_nop 1\n\t"
        "v_permlane16_swap_b32 %0, %4\n\tv_permlane16_swap_b32 %1, %5\n\tv_permlane16_swap_b32 %2, %6\n\tv_permlane16_swap_b32 %3, %7\n\t"
        "s_nop 1\n\t"
        "v_add_f32 %0, %0, %4\n\tv_add_f32 %1, %1, %5\n\tv_add_f32 %2, %2, %6\n\tv_add_f32 %3, %3, %7\n\t"
        "s_nop 1\n\t"
        "v_add_f32_dpp %0, %0, %0 row_ror:8 row_mask:0xf bank_mask:0x3\n\tv_add_f32_dpp %1, %1, %1 row_ror:8 row_mask:0xf bank_mask:0x3\n\t"
        "v_add_f32_dpp %0, %2, %2 row_ror:8 row_mask:0xf bank_mask:0xc\n\tv_add_f32_dpp %1, %3, %3 row_ror:8 row_mask:0xf bank_mask:0xc\n\t"
        "s_nop 1\n\t"
        "v_add_f32_dpp %0, %0, %0 row_half_mirror row_mask:0xf bank_mask:0x5\n\tv_add_f32_dpp %0, %1, %1 row_half_mirror row_mask:0xf bank_mask:0xa\n\t"
        "s_nop 1\n\t"
        "v_add_f32_dpp %0, %0, %0 quad_perm:[1,0,3,2] row_mask:0xf bank_mask:0xf\n\ts_nop 1\n\t"
        "v_add_f32_dpp %0, %0, %0 quad_perm:[2,3,0,1] row_mask:0xf bank_mask:0xf\n\t"
        "s_nop 1"
        : "+v"(x0), "+v"(x1_), "+v"(x2), "+v"(x3), "+v"(y0), "+v"(y1), "+v"(y2), "+v"(y3));
      if ((lane & 3) == 0)
        asm volatile("global_store_dword %0, %1, %2 sc1\n\ts_nop 1" :: "v"((4 * hi + ((lane >> 2) & 3)) * 8 + ((lane >> 4) & 1) * 4), "v"(x0), "s"(st2 + ((size_t)tt * S_ + zr + 8 * q2) * 2) : "memory");
    }
    TS2(13);
    u32x2 sv[2];
    const unsigned svo_ = (zr + 4 * hi + (lane & 3)) * 8 + ((lane >> 2) & 7) * (S_ * 8);
    SB();
    sfor<0, 48>([&](auto I) { constexpr int i = decltype(I)::v, k = i / 3, j = i % 3;
      acc[j] = mfma32(cat8(zl[k][j], zh[k][j]), a[k], k ? acc[j] : f32x16{});
      SB();
      if constexpr (i % 2 == 0 && i < 20) sp_a<(i / 2) & 7, ((i / 2) & 7) + 1, (i / 16) * 4, (i / 16) * 8192>(sab, sve, svo, slds);
      if constexpr (i % 2 == 0 && i >= 20 && i < 32) sp_a<i / 2 - 10, i / 2 - 9, 3, 65536>(sab, sve, svo, slds);
      if constexpr (i == SV_AT) { sv[0] = gld64s<0>(st2, svo_); sv[1] = gld64s<64>(st2, svo_); }
      if constexpr (i == SV_AT + 2 || i == SV_AT + 4) gst128s<0>(x1 + (size_t)(mblk + (i - SV_AT - 2) * 16) * 16384, p2u * 1024 + p2o, xo[(i - SV_AT - 2) / 2]);
      SB(); });
    WAITV(2);
    HO_LOOP {
      PIN(sv[0]); PIN(sv[1]);
      if (ho_ok(max(max(sv[0][0], sv[0][1]), max(sv[1][0], sv[1][1])))) break;
      sv[0] = gld64s<0>(st2, svo_); sv[1] = gld64s<64>(st2, svo_);
      WAITV(0);
    }
    PIN(sv[0]); PIN(sv[1]);
    TS2(15);
    const u16* zd = z2 + ((size_t)(tt * 3) * 128 + rt * 4 + om * 2 + oh) * 512;
#pragma unroll
    for (int q2 = 0; q2 < 2; ++q2) {
      float v0_ = __uint_as_float(sv[q2][0]), v1_ = __uint_as_float(sv[q2][1]), t0, t1;
      asm volatile(
        "s_nop 1\n\t"
        "v_add_f32_dpp %0, %0, %0 row_ror:8 row_mask:0xf bank_mask:0xf\n\tv_add_f32_dpp %1, %1, %1 row_ror:8 row_mask:0xf bank_mask:0xf\n\ts_nop 1\n\t"
        "v_add_f32_dpp %0, %0, %0 row_ror:4 row_mask:0xf bank_mask:0xf\n\tv_add_f32_dpp %1, %1, %1 row_ror:4 row_mask:0xf bank_mask:0xf\n\t"
        "v_mov_b32 %2, %0\n\tv_mov_b32 %3, %1\n\ts_nop 1\n\t"
        "v_permlane16_swap_b32 %0, %2\n\tv_permlane16_swap_b32 %1, %3\n\ts_nop 1\n\t"
        "v_add_f32 %0, %0, %2\n\tv_add_f32 %1, %1, %3"
        : "+v"(v0_), "+v"(v1_), "=&v"(t0), "=&v"(t1));
      v0_ *= 1.f / FH;
      float ra = __builtin_amdgcn_rsqf(fmaxf(v1_ * (1.f / FH) - v0_ * v0_, 0.f) + 1e-5f), rb = -v0_ * ra;
      float a0, a1, a2, a3, b0, b1, b2, b3;
      asm volatile("s_nop 1\n\t"
        "v_mov_b32_dpp %0, %8 quad_perm:[0,0,0,0] row_mask:0xf bank_mask:0xf\n\tv_mov_b32_dpp %1, %8 quad_perm:[1,1,1,1] row_mask:0xf bank_mask:0xf\n\t"
        "v_mov_b32_dpp %2, %8 quad_perm:[2,2,2,2] row_mask:0xf bank_mask:0xf\n\tv_mov_b32_dpp %3, %8 quad_perm:[3,3,3,3] row_mask:0xf bank_mask:0xf\n\t"
        "v_mov_b32_dpp %4, %9 quad_perm:[0,0,0,0] row_mask:0xf bank_mask:0xf\n\tv_mov_b32_dpp %5, %9 quad_perm:[1,1,1,1] row_mask:0xf bank_mask:0xf\n\t"
        "v_mov_b32_dpp %6, %9 quad_perm:[2,2,2,2] row_mask:0xf bank_mask:0xf\n\tv_mov_b32_dpp %7, %9 quad_perm:[3,3,3,3] row_mask:0xf bank_mask:0xf"
        : "=&v"(a0), "=&v"(a1), "=&v"(a2), "=&v"(a3), "=&v"(b0), "=&v"(b1), "=&v"(b2), "=&v"(b3) : "v"(ra), "v"(rb));
      const f32x4 av = {a0, a1, a2, a3}, bv = {b0, b1, b2, b3};
#pragma unroll
      for (int j = 0; j < 3; ++j) {
        const f32x4 n = s[q2][j] * av + bv;
        if (tt < 7 || j == 0) gst64s<0>(zd + (size_t)j * 128 * 512 + q2 * 256, rx * 16 + hi * 8, u32x2{pack2(n[0], n[1]), pack2(n[2], n[3])});
      }
    }
    fl_set(st2, FZ_OFF + (tt * 4 + (rt >> 3)) * 128 + ((rt & 7) * 4 + wave) * 4, lane);
    sp_a<6, 8, 3, 65536>(sab, sve, svo, slds); sp_a<0, 6, 7, 65536 + 8192>(sab, sve, svo, slds);
#pragma unroll
    for (int j = 0; j < 3; ++j) *(u32x4*)(smem + 131072 + (wave ^ 2) * 8192 + j * 1024 + lx * 16) = pack8(acc[j], 1);
#pragma unroll
    for (int j = 0; j < 3; ++j) { s[0][j] = qt<0>(acc[j]); s[1][j] = qt<1>(acc[j]); }
    RAW_BAR();
#pragma unroll
    for (int j = 0; j < 3; ++j) {
      const u32x4 q = *(const u32x4*)(smem + 131072 + wave * 8192 + j * 1024 + lx * 16);
      s[0][j] += f32x4{bflo(q[0]), bfhi(q[0]), bflo(q[1]), bfhi(q[1])}; s[1][j] += f32x4{bflo(q[2]), bfhi(q[2]), bflo(q[3]), bfhi(q[3])};
    }
#pragma unroll
    for (int j = 0; j < 6; ++j) PIN(s[j & 1][j >> 1]);
    asm volatile("s_waitcnt lgkmcnt(0)" ::: "memory");
    sfor<0, 6>([&](auto J) { constexpr int j = decltype(J)::v;
      SB();
      if constexpr (j < 2) sp_a<2 + 3 * j, 5 + 3 * j, 4, 8192>(sab, sve, svo, slds);
      else if constexpr (j < 4) sp_a<0, 4, 4 * j - 6, 4096 * (j - 2)>(sab, sve, svo, elds);
      else if constexpr (j == 4) sp_a<6, 8, 7, 65536 + 8192>(sab, sve, svo, slds);
      SB();
      const f32x4 g = gelu4(s[j & 1][j >> 1]); zq[j & 1][j >> 1] = u32x2{pack2(g[0], g[1]), pack2(g[2], g[3])}; PIN(zq[j & 1][j >> 1]); });
  }
  TS2(7);
  {
    const int nt = tt;
    const char* bbase = (const char*)z2 + ((size_t)(nt * 3) * 128 + wave * 32) * 1024;
    const unsigned bv0 = lx * 16, bv1 = lx * 16 + (nt < 7 ? 131072u : 0u), bv2 = lx * 16 + (nt < 7 ? 262144u : 0u);
    bf16x8 b[SP_NB][3];
#define SP_SPEC 0
#define SP_SC 1
#define SP_B2(t_, sc_) do { constexpr int k_ = sp_k(t_); b[(t_) % SP_NB][0] = gldA<sc_, (k_ & 3) * 1024>(bbase + (k_ >> 2) * 4096, bv0); b[(t_) % SP_NB][1] = gldA<sc_, (k_ & 3) * 1024>(bbase + (k_ >> 2) * 4096, bv1); \
                      b[(t_) % SP_NB][2] = gldA<sc_, (k_ & 3) * 1024>(bbase + (k_ >> 2) * 4096, bv2); } while (0)
#define SP_B(t_) SP_B2(t_, SP_SC)
    if constexpr (SP_SPEC) {
      unsigned fq = gld32s<0>(st2, FZ_OFF + (nt * 4 + wave) * 128 + r32 * 4);
      sfor<0, SP_NB>([&](auto I) { constexpr int i = decltype(I)::v; SP_B(i); });
      WAITV(3 * SP_NB); PIN(fq);
      if (HO_BAD(fq)) {
        fl_wait(st2, FZ_OFF + (nt * 4 + wave) * 128 + r32 * 4);
        sfor<0, SP_NB>([&](auto I) { constexpr int i = decltype(I)::v, k = sp_k(i);
          PIN(b[i][0]); PIN(b[i][1]); PIN(b[i][2]);
          rld128s<(k & 3) * 1024>(b[i][0], mx4(0u, b[i][0]), bbase + (k >> 2) * 4096, bv0); rld128s<(k & 3) * 1024>(b[i][1], mx4(0u, b[i][1]), bbase + (k >> 2) * 4096, bv1);
          rld128s<(k & 3) * 1024>(b[i][2], mx4(0u, b[i][2]), bbase + (k >> 2) * 4096, bv2); });
        WAITV(0);
      }
      TS2(8);
    } else {
      fl_wait(st2, FZ_OFF + (nt * 4 + wave) * 128 + r32 * 4);
      TS2(8);
      sfor<0, SP_NB>([&](auto I) { constexpr int i = decltype(I)::v; SP_B(i); });
    }
    f32x16 acc[2][3];
    bf16x8 f0[2], f1[2];
#define SP_RD(t_) do { if constexpr (sp_c(t_) != 1) {                                                                                \
      const unsigned x0 = vxor<((t_) & 3) * 32>(a0x) + (sp_c(t_) == 3 ? 65536 : 0);                                                   \
      if constexpr (sp_c(t_) == 2) f0[(t_) & 1] = ldsr128<sp_v(t_) * 4096>(x0 + (elds - slds)); else f0[(t_) & 1] = ldsr128<sp_v(t_) * 8192>(x0);   \
      f1[(t_) & 1] = ldsr128<sp_v(t_) * 8192 + 4096>(x0); } } while (0)
#define SP_CK(t_) do { constexpr int p_ = (t_) % SP_NB; PIN(b[p_][0]); PIN(b[p_][1]); PIN(b[p_][2]);                               \
      if (HO_BAD(mx4(mx4(mx4(0u, b[p_][0]), b[p_][1]), b[p_][2]))) {                                                                 \
        HO_LOOP { SP_B2(t_, 1); CNT(1); WAITV(0); PIN(b[p_][0]); PIN(b[p_][1]); PIN(b[p_][2]); if (ho_ok(mx4(mx4(mx4(0u, b[p_][0]), b[p_][1]), b[p_][2]))) break; } } } while (0)
    SP_RD(0);
    WAITV(3 * SP_NB - 3); SP_CK(0);
    unsigned bq;
    sfor<0, 32>([&](auto T) {
      constexpr int t = decltype(T)::v, p = t % SP_NB;
      constexpr int q = t & 1;
      if constexpr (sp_c(t) != 1) { asm volatile("s_waitcnt lgkmcnt(0)" ::: "memory"); PIN(f0[q]); PIN(f1[q]); }
      const bf16x8 fa0 = sp_c(t) == 1 ? rl[t & 7][0] : f0[q], fa1 = sp_c(t) == 1 ? rl[t & 7][1] : f1[q];
      if constexpr (t + 1 < 32) { WAITV(sp_wait(t)); SP_RD(t + 1); }
      SB();
#pragma unroll
      for (int j = 0; j < 3; ++j) acc[0][j] = mfma32(b[p][j], fa0, t ? acc[0][j] : f32x16{});
      SB();
      if constexpr (t + 1 < 32) SP_CK(t + 1);
      SB();
#pragma unroll
      for (int j = 0; j < 3; ++j) acc[1][j] = mfma32(b[p][j], fa1, t ? acc[1][j] : f32x16{});
      SB();
      if constexpr (t + SP_NB < 32) SP_B(t + SP_NB);
      if constexpr (t == 2 || t == 6) { asm volatile("s_waitcnt lgkmcnt(0)" ::: "memory"); sp_a<4, 8, 2 + (t >> 2) * 4, (t >> 2) * 8192>(sab, sve, svo, slds); }
      if constexpr (t == 18) bq = gld16<0>(bsp + rt * 64 + om * 32, rx * 2);
      if constexpr (!WO_LATE && (t == 18 || t == 22)) {
        asm volatile("s_waitcnt lgkmcnt(0)" ::: "memory");
        const unsigned wv = ((hi * 4 + ((lane >> 2) & 3)) * DM + ((lane >> 4) & 1) * 32 + (lane & 3) * 8) * 2;
        const u16* wb = wout + (size_t)(wave * 176 + (t == 22 ? 64 : 0)) * DM + nt * 64;
#pragma unroll
        for (int e = 0; e < 8; ++e) dma16(wb + (size_t)(e * 8) * DM, wv, slds + (t == 22 ? 8192 : 0) + e * 1024);
      }
    });
#undef SP_B
#undef SP_RD
#undef SP_CK
    TS(9);
#pragma unroll
    for (int q = 1; q < 4; ++q)
#pragma unroll
      for (int j = 0; j < 3; ++j) *(u32x4*)(smem + 65536 + wave * 16384 + (q * 3 + j - 3) * 1024 + lx * 16) = pack8(acc[q & 1][j], q >> 1);
    WAITV(0); PIN(bq);
    RAW_BAR();
    u32x4 w[3][3];
#pragma unroll
    for (int j = 0; j < 3; ++j)
#pragma unroll
      for (int q = 1; q < 4; ++q) w[j][q - 1] = *(const u32x4*)(smem + 65536 + (wave ^ q) * 16384 + (q * 3 + j - 3) * 1024 + lx * 16);
    const u16* ud = u + ((size_t)(rt * 2 + om) * 44 + nt * 6 + oh) * 512;
    const float bb_ = bf2f(bq);
    u32x2 o[3][2];
#pragma unroll
    for (int j = 0; j < 3; ++j) {
      float s[8];
#pragma unroll
      for (int e = 0; e < 4; ++e) {
        s[2 * e] = ((acc[0][j][2 * e] + bb_) + bflo(w[j][0][e])) + (bflo(w[j][1][e]) + bflo(w[j][2][e]));
        s[2 * e + 1] = ((acc[0][j][2 * e + 1] + bb_) + bfhi(w[j][0][e])) + (bfhi(w[j][1][e]) + bfhi(w[j][2][e]));
      }
#pragma unroll
      for (int q2 = 0; q2 < 2; ++q2) {
        o[j][q2] = u32x2{pack2(s[4 * q2] * bflo(zq[q2][j][0]), s[4 * q2 + 1] * bfhi(zq[q2][j][0])), pack2(s[4 * q2 + 2] * bflo(zq[q2][j][1]), s[4 * q2 + 3] * bfhi(zq[q2][j][1]))};
        if (j == 0 && nt == 7 && oh) o[j][q2] = u32x2{0u, 0u};
        if (nt < 7 || j == 0) gst64s<0>(ud + j * 1024 + q2 * 256, rx * 16 + hi * 8, o[j][q2]);
      }
    }
  }
  TS(10);
  if constexpr (WO_LATE) {
    const int r0 = hi * 4 + ((lane >> 2) & 3), col = ((lane >> 4) & 1) * 32 + (lane & 3) * 8;
    const u16* wb = wout + (size_t)(wave * 176) * DM + tt * 64;
    const unsigned wv = (r0 * DM + col) * 2;
    sfor<0, 16>([&](auto E) { constexpr int e = decltype(E)::v; dma_c<e * 1024, e * 16 * DM>(wb, wv, slds); });
    sfor<0, 4>([&](auto E) { constexpr int e = decltype(E)::v; dma_c<e * 1024, (16 + e) * 16 * DM>(wb, wv, elds); });
    const unsigned wl_ = wave == 3 ? wv - 16 * 2 * DM : wv;
    dma_c<4096, 20 * 16 * DM>(wb, wl_, elds); dma_c<5120, 21 * 16 * DM>(wb, wl_, elds);
  }
  {
    const int nt = tt;
    const char* ab = (const char*)u + ((size_t)(rt * 2 + om) * 44 + oh * 22) * 1024;
    const unsigned v0 = lane * 16 + 4096;
    const unsigned be = lb + oh * (32768 + 256) + trl + hi * 1024, bl = lb + 131072 + oh * (16384 + 256) + trl + hi * 1024, bf = be ^ 256, bm = bl ^ 256;
    const int em = rt * 64 + om * 32 + r32, en = nt * 64 + oh * 32 + 4 * hi;
    bf16x8 a[22];
    f32x16 acc[2];
    bf16x8 xq[2];
    f32x4 gg[4];
    sfor<0, 4>([&](auto I) { constexpr int i = decltype(I)::v; gg[i] = gld128f<i * 32>(mod + 5 * DM, en * 4); });
    const u16* xb_ = x1 + ((size_t)(rt * 2 + om) * 32 + nt * 4 + oh * 2) * 512;
    sfor<0, 22>([&](auto S) { constexpr int s = decltype(S)::v; a[s] = ou_a<s>(ab, v0); });
    xq[0] = gld128s<0>(xb_, pl); xq[1] = gld128s<1024>(xb_, pl);
    WAITV(WO_LATE ? 28 : 30);
    __builtin_amdgcn_s_barrier();
    s16x4 lo[4][2], hh[4][2];
#define OU_RD(s_, p_) do { constexpr int q_ = (s_) / 11, k_ = (s_) % 11;                                                                                                                         \
      if constexpr (k_ < 8) { constexpr int o_ = q_ * 16384 + k_ * 2048; lo[p_][0] = ldsrtr<o_>(be); hh[p_][0] = ldsrtr<o_ + 512>(be); lo[p_][1] = ldsrtr<o_>(bf); hh[p_][1] = ldsrtr<o_ + 512>(bf); }   \
      else { constexpr int o_ = q_ * 8192 + (k_ - 8) * 2048; lo[p_][0] = ldsrtr<o_>(bl); hh[p_][0] = ldsrtr<o_ + 512>(bl); lo[p_][1] = ldsrtr<o_>(bm); hh[p_][1] = ldsrtr<o_ + 512>(bm); } } while (0)
    OU_RD(0, 0); OU_RD(1, 1); OU_RD(2, 2);
    sfor<0, 22>([&](auto S) {
      constexpr int s = decltype(S)::v, p = s & 3;
      if constexpr (s % OU_G == 0) {
        constexpr int l = imin(s + OU_G, 22);
        WAITV(l < 22 ? 24 - l : 0);
        unsigned mk;
        sfor<s, l>([&](auto Q) { vmx<decltype(Q)::v == s>(mk, a[decltype(Q)::v]); });
        if constexpr (l == 22) { vmx<false>(mk, xq[0]); vmx<false>(mk, xq[1]); }
        if (HO_BAD(mk)) {
          WAITL(0); WAITV(0);
          HO_LOOP {
            sfor<s, 22>([&](auto Q) { constexpr int q = decltype(Q)::v; PIN(a[q]); rld128s<(q & 7) * 1024 - 4096>(a[q], mx4(0u, a[q]), ab + (q >> 3) * 8192, v0); });
            PIN(xq[0]); PIN(xq[1]); rld128s<0>(xq[0], mx4(0u, xq[0]), xb_, pl); rld128s<1024>(xq[1], mx4(0u, xq[1]), xb_, pl);
            WAITV(0);
            sfor<s, l>([&](auto Q) { vmx<decltype(Q)::v == s>(mk, a[decltype(Q)::v]); });
            if constexpr (l == 22) { vmx<false>(mk, xq[0]); vmx<false>(mk, xq[1]); }
            if (ho_ok(mk)) break;
            __builtin_amdgcn_s_sleep(OU_SL);
          }
        }
        _Pragma("unroll") for (int q = s; q < l; ++q) PIN(a[q]);
        if constexpr (s == 0) TS(11);
      }
      if constexpr (s + 3 < 22) OU_RD(s + 3, (s + 3) & 3);
      WAITL(imin(12, 4 * (21 - s)));
      PIN(lo[p][0]); PIN(hh[p][0]); PIN(lo[p][1]); PIN(hh[p][1]);
      acc[0] = mfma32(cat8(lo[p][0], hh[p][0]), a[s], s ? acc[0] : f32x16{});
      acc[1] = mfma32(cat8(lo[p][1], hh[p][1]), a[s], s ? acc[1] : f32x16{});
    });
    PIN(xq[0]); PIN(xq[1]);
#pragma unroll
    for (int i = 0; i < 4; ++i) PIN(gg[i]);
#undef OU_RD
    { char* d = smem + 65536 + 12288 + (wave ^ 2) * 16384 + lane * 16;
      const f32x16 e = acc[1];
      *(f32x4*)d = qt<0>(e); *(f32x4*)(d + 1024) = qt<1>(e); *(f32x4*)(d + 2048) = qt<2>(e); *(f32x4*)(d + 3072) = qt<3>(e); }
    const f32x16 k = acc[0];
    RAW_BAR();
    const char* own = smem + 65536 + 12288 + wave * 16384 + lane * 16;
    unsigned o[8];
    sfor<0, 4>([&](auto Q) { constexpr int q = decltype(Q)::v;
      const f32x4 t = qt<q>(k) + *(const f32x4*)(own + q * 1024);
      const u32x4 xx = __builtin_bit_cast(u32x4, xq[q >> 1]);
      const unsigned w0 = xx[2 * (q & 1)], w1 = xx[2 * (q & 1) + 1];
      o[2 * q] = pack2(bflo(w0) + gg[q][0] * t[0], bfhi(w0) + gg[q][1] * t[1]); o[2 * q + 1] = pack2(bflo(w1) + gg[q][2] * t[2], bfhi(w1) + gg[q][3] * t[3]); });
    asm volatile("s_nop 1\n\tv_permlane32_swap_b32 %0, %2\n\tv_permlane32_swap_b32 %1, %3\n\tv_permlane32_swap_b32 %4, %6\n\tv_permlane32_swap_b32 %5, %7\n\ts_nop 1"
                 : "+v"(o[0]), "+v"(o[1]), "+v"(o[2]), "+v"(o[3]), "+v"(o[4]), "+v"(o[5]), "+v"(o[6]), "+v"(o[7]));
    const unsigned ov = (em * DM + en + 4 * hi) * 2;
    asm volatile("global_store_dwordx4 %0, %1, %3\n\tglobal_store_dwordx4 %0, %2, %3 offset:32" :: "v"(ov), "v"(u32x4{o[0], o[1], o[2], o[3]}), "v"(u32x4{o[4], o[5], o[6], o[7]}), "s"(out) : "memory");
  }
  if constexpr (FUSED) asm volatile("LTAIL_END:");
}

__global__ __launch_bounds__(256, 1) void k_kv(const u16* __restrict__ ckv, const u16* __restrict__ wuk, const u16* __restrict__ wuv,
                                               u16* __restrict__ kimg, u16* __restrict__ vimg, const u16* __restrict__ x, u16* __restrict__ xn,
                                               const u16* __restrict__ c, const u16* __restrict__ wmod, float* __restrict__ mod, const u16* __restrict__ wq, unsigned* __restrict__ dbg, u16* __restrict__ pf) {
  const int bid = blockIdx.x;
  const unsigned head = (bid & 7) + 8 * (bid >> 6), mt = (bid >> 3) & 7;
  const unsigned wave = __builtin_amdgcn_readfirstlane(threadIdx.x >> 6);
  const unsigned long long pa = (unsigned long long)ckv + (mt * 4 + wave) * 65536ull, pwk = (unsigned long long)wuk + head * 256ull, pwv = (unsigned long long)wuv + head * 256ull,
                           pk = (unsigned long long)kimg + (head * 32 + mt * 4 + wave) * 16384ull, pv = (unsigned long long)vimg + (head * 32 + mt * 4 + wave) * 16384ull;
  const unsigned mb = bid < 96 ? (bid & ~15) | (bid & 7) << 1 | ((bid >> 3) & 1) : 0;
  if (threadIdx.x < 2) ((unsigned*)((char*)mod + WS_FL))[(bid & 63) * 32 + (bid >> 6) + 4 * threadIdx.x] = ~0u;
  const unsigned long long pwm = (unsigned long long)wmod + (wave * 128ull * 3072 + mb * 32) * 2, pmo = (unsigned long long)mod + mb * 128ull;
  asm volatile(
    "v_mbcnt_lo_u32_b32 v224, -1, 0\n\t"
    "v_mbcnt_hi_u32_b32 v224, -1, v224\n\t"
    "s_getpc_b64 s[62:63]\n\t"
    "LPC_%=:\n\t"
    "s_lshr_b32 s58, %21, 19\n\t"
    "s_lshl_b32 s58, s58, 2\n\t"
    "s_add_u32 s58, s58, %10\n\t"
    "s_lshl_b32 s58, s58, 7\n\t"
    "v_lshl_add_u32 v225, v224, 12, s58\n\t"
    "v_min_u32 v225, LKE_%=-LPC_%=-4, v225\n\t"
    "global_load_dword v255, v225, s[62:63]\n\t"
    "s_mov_b32 s36, %0\n\t"
    "s_mov_b32 s37, %1\n\t"
    "s_mov_b32 s38, 0x10000\n\t"
    "s_mov_b32 s39, 0x20000\n\t"
    "s_mov_b32 s40, %2\n\t"
    "s_mov_b32 s41, %3\n\t"
    "s_mov_b32 s42, 0x3fe100\n\t"
    "s_mov_b32 s43, 0x20000\n\t"
    "s_mov_b32 s44, %4\n\t"
    "s_mov_b32 s45, %5\n\t"
    "s_mov_b32 s46, 0x3fe100\n\t"
    "s_mov_b32 s47, 0x20000\n\t"
    "v_lshlrev_b32 v240, 4, v224\n\t"
    "v_lshrrev_b32 v226, 5, v224\n\t"
    "v_lshlrev_b32 v227, 2, v226\n\t"
    "v_bfe_u32 v225, v224, 2, 2\n\t"
    "v_add_u32 v227, v227, v225\n\t"
    "v_lshlrev_b32 v244, 13, v227\n\t"
    "v_bfe_u32 v227, v224, 4, 1\n\t"
    "v_lshl_add_u32 v244, v227, 6, v244\n\t"
    "v_and_b32 v245, 3, v224\n\t"
    "v_lshl_add_u32 v244, v245, 4, v244\n\t"
    "s_and_b32 s58, %10, 1\n\t"
    "s_lshl_b32 s58, s58, 16\n\t"
    "s_lshr_b32 s57, %10, 1\n\t"
    "s_lshl_b32 s57, s57, 7\n\t"
    "s_add_u32 s58, s58, s57\n\t"
    "v_add_u32 v243, s58, v244\n\t"
    "s_lshl_b32 s58, %10, 16\n\t"
    "v_add_u32 v244, s58, v244\n\t"
    "s_lshl_b32 s57, %10, 10\n\t"
    "s_mul_i32 s56, %10, 0x6000\n\t"
    "s_add_u32 s56, s56, 0x10000\n\t"
    "v_and_b32 v228, 7, v224\n\t"
    "v_lshrrev_b32 v229, 4, v224\n\t"
    "v_xor_b32 v228, v228, v229\n\t"
    "v_lshrrev_b32 v229, 3, v224\n\t"
    "v_lshlrev_b32 v229, 10, v229\n\t"
    "v_lshl_add_u32 v241, v228, 4, v229\n\t"
    "v_xor_b32 v242, 64, v241\n\t"
    "v_lshlrev_b32 v245, 3, v245\n\t"
    "v_lshl_add_u32 v245, v225, 6, v245\n\t"
    "v_lshl_add_u32 v245, v227, 5, v245\n\t"
    "v_lshl_add_u32 v245, v226, 10, v245\n\t"
    "v_add_u32 v246, 0x10000, v245\n\t"
    "v_add_u32 v251, 0x20000, v245\n\t"
    "s_add_u32 m0, s56, 0\n\t"
    "s_mov_b32 s58, 0\n\t"
    "buffer_load_dwordx4 v241, s[36:39], s58 offen offset:0 lds\n\t"
    "s_add_u32 m0, s56, 1024\n\t"
    "s_mov_b32 s58, 8192\n\t"
    "buffer_load_dwordx4 v242, s[36:39], s58 offen offset:0 lds\n\t"
    "s_add_u32 m0, s56, 2048\n\t"
    "s_mov_b32 s58, 16384\n\t"
    "buffer_load_dwordx4 v241, s[36:39], s58 offen offset:0 lds\n\t"
    "s_add_u32 m0, s56, 3072\n\t"
    "s_mov_b32 s58, 24576\n\t"
    "buffer_load_dwordx4 v242, s[36:39], s58 offen offset:0 lds\n\t"
    "s_add_u32 m0, s56, 4096\n\t"
    "s_mov_b32 s58, 32768\n\t"
    "buffer_load_dwordx4 v241, s[36:39], s58 offen offset:0 lds\n\t"
    "s_add_u32 m0, s56, 5120\n\t"
    "s_mov_b32 s58, 40960\n\t"
    "buffer_load_dwordx4 v242, s[36:39], s58 offen offset:0 lds\n\t"
    "s_add_u32 m0, s56, 6144\n\t"
    "s_mov_b32 s58, 49152\n\t"
    "buffer_load_dwordx4 v241, s[36:39], s58 offen offset:0 lds\n\t"
    "s_add_u32 m0, s56, 7168\n\t"
    "s_mov_b32 s58, 57344\n\t"
    "buffer_load_dwordx4 v242, s[36:39], s58 offen offset:0 lds\n\t"
    "s_add_u32 m0, s57, 0\n\t"
    "s_mov_b32 s58, 0\n\t"
    "buffer_load_dwordx4 v243, s[40:43], s58 offen offset:0 lds\n\t"
    "s_add_u32 m0, s57, 4096\n\t"
    "s_mov_b32 s58, 0\n\t"
    "buffer_load_dwordx4 v243, s[44:47], s58 offen offset:0 lds\n\t"
    "s_add_u32 m0, s57, 8192\n\t"
    "s_mov_b32 s58, 131072\n\t"
    "buffer_load_dwordx4 v243, s[40:43], s58 offen offset:0 lds\n\t"
    "s_add_u32 m0, s57, 12288\n\t"
    "s_mov_b32 s58, 131072\n\t"
    "buffer_load_dwordx4 v243, s[44:47], s58 offen offset:0 lds\n\t"
    "s_cmp_lt_u32 %10, 2\n\t"
    "s_cselect_b32 s72, s40, s44\n\t"
    "s_cselect_b32 s73, s41, s45\n\t"
    "s_mov_b32 s74, s42\n\t"
    "s_mov_b32 s75, s43\n\t"
    "v_lshrrev_b32 v230, 1, v224\n\t"
    "v_and_b32 v231, 1, v224\n\t"
    "v_lshlrev_b32 v230, 13, v230\n\t"
    "v_lshl_add_u32 v230, v231, 7, v230\n\t"
    "v_lshlrev_b32 v231, 10, v224\n\t"
    "buffer_load_dword v255, v230, s[72:75], %21 offen offset:0\n\t"
    "buffer_load_dword v255, v231, s[36:39], %22 offen offset:0\n\t"
    "buffer_load_dword v255, v231, s[36:39], %22 offen offset:128\n\t"
    "s_add_u32 m0, s57, 16384\n\t"
    "s_mov_b32 s58, 262144\n\t"
    "buffer_load_dwordx4 v243, s[40:43], s58 offen offset:0 lds\n\t"
    "s_add_u32 m0, s57, 20480\n\t"
    "s_mov_b32 s58, 262144\n\t"
    "buffer_load_dwordx4 v243, s[44:47], s58 offen offset:0 lds\n\t"
    "s_add_u32 m0, s56, 8192\n\t"
    "s_mov_b32 s58, 128\n\t"
    "buffer_load_dwordx4 v241, s[36:39], s58 offen offset:0 lds\n\t"
    "s_add_u32 m0, s56, 9216\n\t"
    "s_mov_b32 s58, 8320\n\t"
    "buffer_load_dwordx4 v242, s[36:39], s58 offen offset:0 lds\n\t"
    "s_mov_b32 s48, %6\n\t"
    "s_mov_b32 s49, %7\n\t"
    "s_mov_b32 s50, 0x4000\n\t"
    "s_mov_b32 s51, 0x20000\n\t"
    "s_mov_b32 s52, %8\n\t"
    "s_mov_b32 s53, %9\n\t"
    "s_mov_b32 s54, 0x4000\n\t"
    "s_mov_b32 s55, 0x20000\n\t"
    "s_mov_b32 s59, 0x1000\n\t"
    "s_mov_b32 s60, 0x2000\n\t"
    "s_mov_b32 s61, 0x3000\n\t"
    "v_and_b32 v228, 31, v224\n\t"
    "v_bfe_u32 v229, v224, 1, 3\n\t"
    "v_xor_b32 v229, v229, v226\n\t"
    "v_lshlrev_b32 v228, 7, v228\n\t"
    "v_lshl_add_u32 v228, v229, 4, v228\n\t"
    "v_add_u32 v247, s56, v228\n\t"
    "v_xor_b32 v248, 32, v247\n\t"
    "v_xor_b32 v249, 64, v247\n\t"
    "v_xor_b32 v250, 0x60, v247\n\t"
    "v_and_b32 v228, 31, v224\n\t"
    "v_lshlrev_b32 v252, 4, v228\n\t"
    "v_lshl_add_u32 v252, v226, 10, v252\n\t"
    "v_lshlrev_b32 v253, 9, v228\n\t"
    "v_lshl_add_u32 v253, v226, 4, v253\n\t"
    "v_lshrrev_b32 v228, 2, v224\n\t"
    "v_lshlrev_b32 v239, 4, v228\n\t"
    "v_mul_u32_u24 v238, 0xc000, v228\n\t"
    "v_and_b32 v228, 3, v224\n\t"
    "v_lshl_add_u32 v238, v228, 4, v238\n\t"
    "s_mov_b32 s64, %15\n\t"
    "s_mov_b32 s65, %16\n\t"
    "s_mov_b32 s66, %17\n\t"
    "s_mov_b32 s67, 0x20000\n\t"
    "s_mov_b32 s68, %18\n\t"
    "s_mov_b32 s69, %19\n\t"
    "s_mov_b32 s70, %20\n\t"
    "s_mov_b32 s71, 0x20000\n\t"
    "s_waitcnt vmcnt(11)\n\t"
    "ds_read_b128 v[0:3], v247 offset:0\n\t"
    "ds_read_b128 v[4:7], v247 offset:4096\n\t"
    "ds_read_b128 v[8:11], v248 offset:0\n\t"
    "ds_read_b128 v[12:15], v248 offset:4096\n\t"
    "ds_read_b128 v[16:19], v249 offset:0\n\t"
    "ds_read_b128 v[20:23], v249 offset:4096\n\t"
    "ds_read_b128 v[24:27], v250 offset:0\n\t"
    "ds_read_b128 v[28:31], v250 offset:4096\n\t"
    "s_waitcnt vmcnt(9)\n\t"
    "s_barrier\n\t"
    "ds_read_b64_tr_b16 v[192:193], v245 offset:0\n\t"
    "ds_read_b64_tr_b16 v[194:195], v245 offset:512\n\t"
    "ds_read_b64_tr_b16 v[196:197], v245 offset:256\n\t"
    "ds_read_b64_tr_b16 v[198:199], v245 offset:768\n\t"
    "ds_read_b64_tr_b16 v[200:201], v245 offset:2048\n\t"
    "ds_read_b64_tr_b16 v[202:203], v245 offset:2560\n\t"
    "ds_read_b64_tr_b16 v[204:205], v245 offset:2304\n\t"
    "ds_read_b64_tr_b16 v[206:207], v245 offset:2816\n\t"
    "s_waitcnt lgkmcnt(6)\n\t"
    "v_mfma_f32_32x32x16_bf16 a[0:15], v[192:195], v[0:3], 0\n\t"
    "ds_read_b64_tr_b16 v[208:209], v245 offset:4096\n\t"
    "v_mfma_f32_32x32x16_bf16 a[16:31], v[192:195], v[4:7], 0\n\t"
    "ds_read_b64_tr_b16 v[210:211], v245 offset:4608\n\t"
    "global_load_dwordx4 v[164:167], v252, %12 offset:0\n\t"
    "s_waitcnt lgkmcnt(6)\n\t"
    "v_mfma_f32_32x32x16_bf16 a[32:47], v[196:199], v[0:3], 0\n\t"
    "ds_read_b64_tr_b16 v[212:213], v245 offset:4352\n\t"
    "global_load_dwordx4 v[168:171], v252, %12 offset:512\n\t"
    "v_mfma_f32_32x32x16_bf16 a[48:63], v[196:199], v[4:7], 0\n\t"
    "ds_read_b64_tr_b16 v[214:215], v245 offset:4864\n\t"
    "global_load_dwordx4 v[160:163], v239, %14 offset:0\n\t"
    "s_waitcnt lgkmcnt(6)\n\t"
    "v_mfma_f32_32x32x16_bf16 a[64:79], v[200:203], v[0:3], 0\n\t"
    "ds_read_b64_tr_b16 v[216:217], v245 offset:6144\n\t"
    "v_mfma_f32_32x32x16_bf16 a[80:95], v[200:203], v[4:7], 0\n\t"
    "ds_read_b64_tr_b16 v[218:219], v245 offset:6656\n\t"
    "s_add_u32 m0, s56, 10240\n\t"
    "s_mov_b32 s58, 16512\n\t"
    "buffer_load_dwordx4 v241, s[36:39], s58 offen offset:0 lds\n\t"
    "s_waitcnt lgkmcnt(6)\n\t"
    "v_mfma_f32_32x32x16_bf16 a[96:111], v[204:207], v[0:3], 0\n\t"
    "ds_read_b64_tr_b16 v[220:221], v245 offset:6400\n\t"
    "v_mfma_f32_32x32x16_bf16 a[112:127], v[204:207], v[4:7], 0\n\t"
    "ds_read_b64_tr_b16 v[222:223], v245 offset:6912\n\t"
    "s_add_u32 m0, s56, 11264\n\t"
    "s_mov_b32 s58, 24704\n\t"
    "buffer_load_dwordx4 v242, s[36:39], s58 offen offset:0 lds\n\t"
    "s_waitcnt vmcnt(12)\n\t"
    "s_barrier\n\t"
    "s_waitcnt lgkmcnt(6)\n\t"
    "v_mfma_f32_32x32x16_bf16 a[128:143], v[0:3], v[208:211], 0\n\t"
    "ds_read_b64_tr_b16 v[192:193], v245 offset:8192\n\t"
    "v_mfma_f32_32x32x16_bf16 a[144:159], v[4:7], v[208:211], 0\n\t"
    "ds_read_b64_tr_b16 v[194:195], v245 offset:8704\n\t"
    "s_add_u32 m0, s56, 12288\n\t"
    "s_mov_b32 s58, 32896\n\t"
    "buffer_load_dwordx4 v241, s[36:39], s58 offen offset:0 lds\n\t"
    "s_waitcnt lgkmcnt(6)\n\t"
    "v_mfma_f32_32x32x16_bf16 a[160:175], v[0:3], v[212:215], 0\n\t"
    "ds_read_b64_tr_b16 v[196:197], v245 offset:8448\n\t"
    "s_add_u32 m0, s56, 13312\n\t"
    "s_mov_b32 s58, 41088\n\t"
    "buffer_load_dwordx4 v242, s[36:39], s58 offen offset:0 lds\n\t"
    "v_mfma_f32_32x32x16_bf16 a[176:191], v[4:7], v[212:215], 0\n\t"
    "ds_read_b64_tr_b16 v[198:199], v245 offset:8960\n\t"
    "s_add_u32 m0, s56, 14336\n\t"
    "s_mov_b32 s58, 49280\n\t"
    "buffer_load_dwordx4 v241, s[36:39], s58 offen offset:0 lds\n\t"
    "s_waitcnt lgkmcnt(6)\n\t"
    "v_mfma_f32_32x32x16_bf16 a[192:207], v[0:3], v[216:219], 0\n\t"
    "ds_read_b64_tr_b16 v[200:201], v245 offset:10240\n\t"
    "v_mfma_f32_32x32x16_bf16 a[208:223], v[4:7], v[216:219], 0\n\t"
    "ds_read_b64_tr_b16 v[202:203], v245 offset:10752\n\t"
    "s_add_u32 m0, s56, 15360\n\t"
    "s_mov_b32 s58, 57472\n\t"
    "buffer_load_dwordx4 v242, s[36:39], s58 offen offset:0 lds\n\t"
    "s_waitcnt lgkmcnt(6)\n\t"
    "v_mfma_f32_32x32x16_bf16 a[224:239], v[0:3], v[220:223], 0\n\t"
    "ds_read_b64_tr_b16 v[204:205], v245 offset:10496\n\t"
    "v_mfma_f32_32x32x16_bf16 a[240:255], v[4:7], v[220:223], 0\n\t"
    "ds_read_b64_tr_b16 v[206:207], v245 offset:11008\n\t"
    "s_add_u32 m0, s57, 24576\n\t"
    "s_mov_b32 s58, 393216\n\t"
    "buffer_load_dwordx4 v243, s[40:43], s58 offen offset:0 lds\n\t"
    "s_waitcnt lgkmcnt(6)\n\t"
    "v_mfma_f32_32x32x16_bf16 a[0:15], v[192:195], v[8:11], a[0:15]\n\t"
    "ds_read_b64_tr_b16 v[208:209], v245 offset:12288\n\t"
    "s_waitcnt vmcnt(8)\n\t"
    "v_lshlrev_b32 v172, 16, v164\n\t"
    "v_and_b32 v173, 0xffff0000, v164\n\t"
    "v_mfma_f32_32x32x16_bf16 a[16:31], v[192:195], v[12:15], a[16:31]\n\t"
    "ds_read_b64_tr_b16 v[210:211], v245 offset:12800\n\t"
    "s_add_u32 m0, s57, 28672\n\t"
    "s_mov_b32 s58, 393216\n\t"
    "buffer_load_dwordx4 v243, s[44:47], s58 offen offset:0 lds\n\t"
    "v_lshlrev_b32 v174, 16, v165\n\t"
    "v_and_b32 v175, 0xffff0000, v165\n\t"
    "v_lshlrev_b32 v176, 16, v166\n\t"
    "s_waitcnt lgkmcnt(6)\n\t"
    "v_mfma_f32_32x32x16_bf16 a[32:47], v[196:199], v[8:11], a[32:47]\n\t"
    "ds_read_b64_tr_b16 v[212:213], v245 offset:12544\n\t"
    "s_add_u32 m0, s57, 32768\n\t"
    "s_mov_b32 s58, 524288\n\t"
    "buffer_load_dwordx4 v243, s[40:43], s58 offen offset:0 lds\n\t"
    "v_and_b32 v177, 0xffff0000, v166\n\t"
    "v_lshlrev_b32 v178, 16, v167\n\t"
    "v_and_b32 v179, 0xffff0000, v167\n\t"
    "v_mfma_f32_32x32x16_bf16 a[48:63], v[196:199], v[12:15], a[48:63]\n\t"
    "ds_read_b64_tr_b16 v[214:215], v245 offset:13056\n\t"
    "s_add_u32 m0, s57, 36864\n\t"
    "s_mov_b32 s58, 524288\n\t"
    "buffer_load_dwordx4 v243, s[44:47], s58 offen offset:0 lds\n\t"
    "v_lshlrev_b32 v180, 16, v168\n\t"
    "v_and_b32 v181, 0xffff0000, v168\n\t"
    "v_lshlrev_b32 v182, 16, v169\n\t"
    "s_waitcnt lgkmcnt(6)\n\t"
    "v_mfma_f32_32x32x16_bf16 a[64:79], v[200:203], v[8:11], a[64:79]\n\t"
    "ds_read_b64_tr_b16 v[216:217], v245 offset:14336\n\t"
    "v_and_b32 v183, 0xffff0000, v169\n\t"
    "v_lshlrev_b32 v184, 16, v170\n\t"
    "v_and_b32 v185, 0xffff0000, v170\n\t"
    "v_mfma_f32_32x32x16_bf16 a[80:95], v[200:203], v[12:15], a[80:95]\n\t"
    "ds_read_b64_tr_b16 v[218:219], v245 offset:14848\n\t"
    "s_add_u32 m0, s57, 40960\n\t"
    "s_mov_b32 s58, 655360\n\t"
    "buffer_load_dwordx4 v243, s[40:43], s58 offen offset:0 lds\n\t"
    "v_lshlrev_b32 v186, 16, v171\n\t"
    "v_and_b32 v187, 0xffff0000, v171\n\t"
    "v_add_f32 v188, v172, v173\n\t"
    "s_waitcnt lgkmcnt(6)\n\t"
    "v_mfma_f32_32x32x16_bf16 a[96:111], v[204:207], v[8:11], a[96:111]\n\t"
    "ds_read_b64_tr_b16 v[220:221], v245 offset:14592\n\t"
    "v_add_f32 v189, v174, v175\n\t"
    "v_add_f32 v188, v188, v176\n\t"
    "v_add_f32 v189, v189, v177\n\t"
    "v_mfma_f32_32x32x16_bf16 a[112:127], v[204:207], v[12:15], a[112:127]\n\t"
    "ds_read_b64_tr_b16 v[222:223], v245 offset:15104\n\t"
    "s_add_u32 m0, s57, 45056\n\t"
    "s_mov_b32 s58, 655360\n\t"
    "buffer_load_dwordx4 v243, s[44:47], s58 offen offset:0 lds\n\t"
    "v_add_f32 v188, v188, v178\n\t"
    "v_add_f32 v189, v189, v179\n\t"
    "v_add_f32 v188, v188, v180\n\t"
    "s_barrier\n\t"
    "s_waitcnt lgkmcnt(6)\n\t"
    "v_mfma_f32_32x32x16_bf16 a[128:143], v[8:11], v[208:211], a[128:143]\n\t"
    "ds_read_b64_tr_b16 v[192:193], v245 offset:16384\n\t"
    "v_add_f32 v189, v189, v181\n\t"
    "v_add_f32 v188, v188, v182\n\t"
    "v_add_f32 v189, v189, v183\n\t"
    "v_mfma_f32_32x32x16_bf16 a[144:159], v[12:15], v[208:211], a[144:159]\n\t"
    "ds_read_b64_tr_b16 v[194:195], v245 offset:16896\n\t"
    "s_add_u32 m0, s56, 16384\n\t"
    "s_mov_b32 s58, 256\n\t"
    "buffer_load_dwordx4 v241, s[36:39], s58 offen offset:0 lds\n\t"
    "v_add_f32 v188, v188, v184\n\t"
    "v_add_f32 v189, v189, v185\n\t"
    "v_add_f32 v188, v188, v186\n\t"
    "s_waitcnt lgkmcnt(6)\n\t"
    "v_mfma_f32_32x32x16_bf16 a[160:175], v[8:11], v[212:215], a[160:175]\n\t"
    "ds_read_b64_tr_b16 v[196:197], v245 offset:16640\n\t"
    "s_add_u32 m0, s56, 17408\n\t"
    "s_mov_b32 s58, 8448\n\t"
    "buffer_load_dwordx4 v242, s[36:39], s58 offen offset:0 lds\n\t"
    "v_add_f32 v189, v189, v187\n\t"
    "v_add_f32 v188, v188, v189\n\t"
    "s_nop 1\n\t"
    "v_mfma_f32_32x32x16_bf16 a[176:191], v[12:15], v[212:215], a[176:191]\n\t"
    "ds_read_b64_tr_b16 v[198:199], v245 offset:17152\n\t"
    "s_add_u32 m0, s56, 18432\n\t"
    "s_mov_b32 s58, 16640\n\t"
    "buffer_load_dwordx4 v241, s[36:39], s58 offen offset:0 lds\n\t"
    "v_add_f32_dpp v188, v188, v188 quad_perm:[1,0,3,2] row_mask:0xf bank_mask:0xf\n\t"
    "s_nop 1\n\t"
    "v_add_f32_dpp v188, v188, v188 quad_perm:[2,3,0,1] row_mask:0xf bank_mask:0xf\n\t"
    "s_waitcnt lgkmcnt(6)\n\t"
    "v_mfma_f32_32x32x16_bf16 a[192:207], v[8:11], v[216:219], a[192:207]\n\t"
    "ds_read_b64_tr_b16 v[200:201], v245 offset:18432\n\t"
    "s_nop 1\n\t"
    "v_add_f32_dpp v188, v188, v188 row_half_mirror row_mask:0xf bank_mask:0xf\n\t"
    "s_nop 1\n\t"
    "v_mfma_f32_32x32x16_bf16 a[208:223], v[12:15], v[216:219], a[208:223]\n\t"
    "ds_read_b64_tr_b16 v[202:203], v245 offset:18944\n\t"
    "s_add_u32 m0, s56, 19456\n\t"
    "s_mov_b32 s58, 24832\n\t"
    "buffer_load_dwordx4 v242, s[36:39], s58 offen offset:0 lds\n\t"
    "v_add_f32_dpp v188, v188, v188 row_mirror row_mask:0xf bank_mask:0xf\n\t"
    "v_mov_b32 v189, v188\n\t"
    "s_nop 1\n\t"
    "s_waitcnt lgkmcnt(6)\n\t"
    "v_mfma_f32_32x32x16_bf16 a[224:239], v[8:11], v[220:223], a[224:239]\n\t"
    "ds_read_b64_tr_b16 v[204:205], v245 offset:18688\n\t"
    "v_permlane16_swap_b32 v188, v189\n\t"
    "s_nop 0\n\t"
    "v_add_f32 v188, v188, v189\n\t"
    "v_mfma_f32_32x32x16_bf16 a[240:255], v[12:15], v[220:223], a[240:255]\n\t"
    "ds_read_b64_tr_b16 v[206:207], v245 offset:19200\n\t"
    "s_add_u32 m0, s56, 20480\n\t"
    "s_mov_b32 s58, 33024\n\t"
    "buffer_load_dwordx4 v241, s[36:39], s58 offen offset:0 lds\n\t"
    "v_mul_f32 v188, 0x3b000000, v188\n\t"
    "v_sub_f32 v172, v172, v188\n\t"
    "v_sub_f32 v173, v173, v188\n\t"
    "s_waitcnt lgkmcnt(6)\n\t"
    "v_mfma_f32_32x32x16_bf16 a[0:15], v[192:195], v[16:19], a[0:15]\n\t"
    "ds_read_b64_tr_b16 v[208:209], v245 offset:20480\n\t"
    "v_sub_f32 v174, v174, v188\n\t"
    "v_sub_f32 v175, v175, v188\n\t"
    "v_sub_f32 v176, v176, v188\n\t"
    "v_mfma_f32_32x32x16_bf16 a[16:31], v[192:195], v[20:23], a[16:31]\n\t"
    "ds_read_b64_tr_b16 v[210:211], v245 offset:20992\n\t"
    "s_add_u32 m0, s56, 21504\n\t"
    "s_mov_b32 s58, 41216\n\t"
    "buffer_load_dwordx4 v242, s[36:39], s58 offen offset:0 lds\n\t"
    "v_sub_f32 v177, v177, v188\n\t"
    "v_sub_f32 v178, v178, v188\n\t"
    "v_sub_f32 v179, v179, v188\n\t"
    "s_waitcnt lgkmcnt(6)\n\t"
    "v_mfma_f32_32x32x16_bf16 a[32:47], v[196:199], v[16:19], a[32:47]\n\t"
    "ds_read_b64_tr_b16 v[212:213], v245 offset:20736\n\t"
    "s_add_u32 m0, s56, 22528\n\t"
    "s_mov_b32 s58, 49408\n\t"
    "buffer_load_dwordx4 v241, s[36:39], s58 offen offset:0 lds\n\t"
    "v_sub_f32 v180, v180, v188\n\t"
    "v_sub_f32 v181, v181, v188\n\t"
    "v_sub_f32 v182, v182, v188\n\t"
    "v_mfma_f32_32x32x16_bf16 a[48:63], v[196:199], v[20:23], a[48:63]\n\t"
    "ds_read_b64_tr_b16 v[214:215], v245 offset:21248\n\t"
    "s_add_u32 m0, s56, 23552\n\t"
    "s_mov_b32 s58, 57600\n\t"
    "buffer_load_dwordx4 v242, s[36:39], s58 offen offset:0 lds\n\t"
    "v_sub_f32 v183, v183, v188\n\t"
    "v_sub_f32 v184, v184, v188\n\t"
    "v_sub_f32 v185, v185, v188\n\t"
    "s_waitcnt lgkmcnt(6)\n\t"
    "v_mfma_f32_32x32x16_bf16 a[64:79], v[200:203], v[16:19], a[64:79]\n\t"
    "ds_read_b64_tr_b16 v[216:217], v245 offset:22528\n\t"
    "v_sub_f32 v186, v186, v188\n\t"
    "v_sub_f32 v187, v187, v188\n\t"
    "v_mul_f32 v188, v172, v172\n\t"
    "v_mfma_f32_32x32x16_bf16 a[80:95], v[200:203], v[20:23], a[80:95]\n\t"
    "ds_read_b64_tr_b16 v[218:219], v245 offset:23040\n\t"
    "s_add_u32 m0, s57, 49152\n\t"
    "s_mov_b32 s58, 786432\n\t"
    "buffer_load_dwordx4 v243, s[40:43], s58 offen offset:0 lds\n\t"
    "v_mul_f32 v189, v173, v173\n\t"
    "v_fmac_f32 v188, v174, v174\n\t"
    "v_fmac_f32 v189, v175, v175\n\t"
    "s_waitcnt lgkmcnt(6)\n\t"
    "v_mfma_f32_32x32x16_bf16 a[96:111], v[204:207], v[16:19], a[96:111]\n\t"
    "ds_read_b64_tr_b16 v[220:221], v245 offset:22784\n\t"
    "v_fmac_f32 v188, v176, v176\n\t"
    "v_fmac_f32 v189, v177, v177\n\t"
    "v_fmac_f32 v188, v178, v178\n\t"
    "v_mfma_f32_32x32x16_bf16 a[112:127], v[204:207], v[20:23], a[112:127]\n\t"
    "ds_read_b64_tr_b16 v[222:223], v245 offset:23296\n\t"
    "s_add_u32 m0, s57, 53248\n\t"
    "s_mov_b32 s58, 786432\n\t"
    "buffer_load_dwordx4 v243, s[44:47], s58 offen offset:0 lds\n\t"
    "v_fmac_f32 v189, v179, v179\n\t"
    "v_fmac_f32 v188, v180, v180\n\t"
    "v_fmac_f32 v189, v181, v181\n\t"
    "s_waitcnt vmcnt(14)\n\t"
    "s_barrier\n\t"
    "s_waitcnt lgkmcnt(6)\n\t"
    "v_mfma_f32_32x32x16_bf16 a[128:143], v[16:19], v[208:211], a[128:143]\n\t"
    "ds_read_b64_tr_b16 v[192:193], v245 offset:24576\n\t"
    "v_fmac_f32 v188, v182, v182\n\t"
    "v_fmac_f32 v189, v183, v183\n\t"
    "v_fmac_f32 v188, v184, v184\n\t"
    "v_mfma_f32_32x32x16_bf16 a[144:159], v[20:23], v[208:211], a[144:159]\n\t"
    "ds_read_b64_tr_b16 v[194:195], v245 offset:25088\n\t"
    "s_add_u32 m0, s56, 0\n\t"
    "s_mov_b32 s58, 384\n\t"
    "buffer_load_dwordx4 v241, s[36:39], s58 offen offset:0 lds\n\t"
    "v_fmac_f32 v189, v185, v185\n\t"
    "v_fmac_f32 v188, v186, v186\n\t"
    "v_fmac_f32 v189, v187, v187\n\t"
    "s_waitcnt lgkmcnt(6)\n\t"
    "v_mfma_f32_32x32x16_bf16 a[160:175], v[16:19], v[212:215], a[160:175]\n\t"
    "ds_read_b64_tr_b16 v[196:197], v245 offset:24832\n\t"
    "s_add_u32 m0, s56, 1024\n\t"
    "s_mov_b32 s58, 8576\n\t"
    "buffer_load_dwordx4 v242, s[36:39], s58 offen offset:0 lds\n\t"
    "v_add_f32 v188, v188, v189\n\t"
    "s_nop 1\n\t"
    "v_add_f32_dpp v188, v188, v188 quad_perm:[1,0,3,2] row_mask:0xf bank_mask:0xf\n\t"
    "v_mfma_f32_32x32x16_bf16 a[176:191], v[20:23], v[212:215], a[176:191]\n\t"
    "ds_read_b64_tr_b16 v[198:199], v245 offset:25344\n\t"
    "s_add_u32 m0, s56, 2048\n\t"
    "s_mov_b32 s58, 16768\n\t"
    "buffer_load_dwordx4 v241, s[36:39], s58 offen offset:0 lds\n\t"
    "s_nop 1\n\t"
    "v_add_f32_dpp v188, v188, v188 quad_perm:[2,3,0,1] row_mask:0xf bank_mask:0xf\n\t"
    "s_nop 1\n\t"
    "s_waitcnt lgkmcnt(6)\n\t"
    "v_mfma_f32_32x32x16_bf16 a[192:207], v[16:19], v[216:219], a[192:207]\n\t"
    "ds_read_b64_tr_b16 v[200:201], v245 offset:26624\n\t"
    "v_add_f32_dpp v188, v188, v188 row_half_mirror row_mask:0xf bank_mask:0xf\n\t"
    "s_nop 1\n\t"
    "v_add_f32_dpp v188, v188, v188 row_mirror row_mask:0xf bank_mask:0xf\n\t"
    "v_mfma_f32_32x32x16_bf16 a[208:223], v[20:23], v[216:219], a[208:223]\n\t"
    "ds_read_b64_tr_b16 v[202:203], v245 offset:27136\n\t"
    "s_add_u32 m0, s56, 3072\n\t"
    "s_mov_b32 s58, 24960\n\t"
    "buffer_load_dwordx4 v242, s[36:39], s58 offen offset:0 lds\n\t"
    "v_mov_b32 v189, v188\n\t"
    "s_nop 1\n\t"
    "v_permlane16_swap_b32 v188, v189\n\t"
    "s_waitcnt lgkmcnt(6)\n\t"
    "v_mfma_f32_32x32x16_bf16 a[224:239], v[16:19], v[220:223], a[224:239]\n\t"
    "ds_read_b64_tr_b16 v[204:205], v245 offset:26880\n\t"
    "s_nop 0\n\t"
    "v_add_f32 v188, v188, v189\n\t"
    "v_mov_b32 v189, 0x3727c5ac\n\t"
    "v_mfma_f32_32x32x16_bf16 a[240:255], v[20:23], v[220:223], a[240:255]\n\t"
    "ds_read_b64_tr_b16 v[206:207], v245 offset:27392\n\t"
    "s_add_u32 m0, s56, 4096\n\t"
    "s_mov_b32 s58, 33152\n\t"
    "buffer_load_dwordx4 v241, s[36:39], s58 offen offset:0 lds\n\t"
    "v_fmac_f32 v189, 0x3b000000, v188\n\t"
    "v_rsq_f32 v188, v189\n\t"
    "s_nop 0\n\t"
    "s_waitcnt lgkmcnt(6)\n\t"
    "v_mfma_f32_32x32x16_bf16 a[0:15], v[192:195], v[24:27], a[0:15]\n\t"
    "ds_read_b64_tr_b16 v[208:209], v245 offset:28672\n\t"
    "v_mul_f32 v172, v172, v188\n\t"
    "v_mul_f32 v173, v173, v188\n\t"
    "v_mul_f32 v174, v174, v188\n\t"
    "v_mfma_f32_32x32x16_bf16 a[16:31], v[192:195], v[28:31], a[16:31]\n\t"
    "ds_read_b64_tr_b16 v[210:211], v245 offset:29184\n\t"
    "s_mov_b32 s58, 0\n\t"
    "buffer_load_dwordx4 v[128:131], v238, s[64:67], s58 offen offset:0\n\t"
    "v_mul_f32 v175, v175, v188\n\t"
    "v_mul_f32 v176, v176, v188\n\t"
    "v_mul_f32 v177, v177, v188\n\t"
    "s_waitcnt lgkmcnt(6)\n\t"
    "v_mfma_f32_32x32x16_bf16 a[32:47], v[196:199], v[24:27], a[32:47]\n\t"
    "ds_read_b64_tr_b16 v[212:213], v245 offset:28928\n\t"
    "s_mov_b32 s58, 6144\n\t"
    "buffer_load_dwordx4 v[132:135], v238, s[64:67], s58 offen offset:0\n\t"
    "v_mul_f32 v178, v178, v188\n\t"
    "v_mul_f32 v179, v179, v188\n\t"
    "v_mul_f32 v180, v180, v188\n\t"
    "v_mfma_f32_32x32x16_bf16 a[48:63], v[196:199], v[28:31], a[48:63]\n\t"
    "ds_read_b64_tr_b16 v[214:215], v245 offset:29440\n\t"
    "s_mov_b32 s58, 12288\n\t"
    "buffer_load_dwordx4 v[136:139], v238, s[64:67], s58 offen offset:0\n\t"
    "v_mul_f32 v181, v181, v188\n\t"
    "v_mul_f32 v182, v182, v188\n\t"
    "v_mul_f32 v183, v183, v188\n\t"
    "s_waitcnt lgkmcnt(6)\n\t"
    "v_mfma_f32_32x32x16_bf16 a[64:79], v[200:203], v[24:27], a[64:79]\n\t"
    "ds_read_b64_tr_b16 v[216:217], v245 offset:30720\n\t"
    "v_mul_f32 v184, v184, v188\n\t"
    "v_mul_f32 v185, v185, v188\n\t"
    "v_mul_f32 v186, v186, v188\n\t"
    "v_mfma_f32_32x32x16_bf16 a[80:95], v[200:203], v[28:31], a[80:95]\n\t"
    "ds_read_b64_tr_b16 v[218:219], v245 offset:31232\n\t"
    "s_mov_b32 s58, 18432\n\t"
    "buffer_load_dwordx4 v[140:143], v238, s[64:67], s58 offen offset:0\n\t"
    "v_mul_f32 v187, v187, v188\n\t"
    "v_cvt_pk_f16_f32 v164, v172, v173\n\t"
    "v_cvt_pk_f16_f32 v165, v174, v175\n\t"
    "s_waitcnt lgkmcnt(6)\n\t"
    "v_mfma_f32_32x32x16_bf16 a[96:111], v[204:207], v[24:27], a[96:111]\n\t"
    "ds_read_b64_tr_b16 v[220:221], v245 offset:30976\n\t"
    "v_cvt_pk_f16_f32 v166, v176, v177\n\t"
    "v_cvt_pk_f16_f32 v167, v178, v179\n\t"
    "v_cvt_pk_f16_f32 v168, v180, v181\n\t"
    "v_mfma_f32_32x32x16_bf16 a[112:127], v[204:207], v[28:31], a[112:127]\n\t"
    "ds_read_b64_tr_b16 v[222:223], v245 offset:31488\n\t"
    "s_mov_b32 s58, 24576\n\t"
    "buffer_load_dwordx4 v[144:147], v238, s[64:67], s58 offen offset:0\n\t"
    "v_cvt_pk_f16_f32 v169, v182, v183\n\t"
    "v_cvt_pk_f16_f32 v170, v184, v185\n\t"
    "v_cvt_pk_f16_f32 v171, v186, v187\n\t"
    "s_waitcnt vmcnt(22)\n\t"
    "s_barrier\n\t"
    "s_waitcnt lgkmcnt(6)\n\t"
    "v_mfma_f32_32x32x16_bf16 a[128:143], v[24:27], v[208:211], a[128:143]\n\t"
    "ds_read_b64_tr_b16 v[192:193], v245 offset:32768\n\t"
    "ds_read_b128 v[32:35], v247 offset:8192\n\t"
    "v_add_u32 v239, 0x4000, v253\n\t"
    "global_store_dwordx4 v253, v[164:167], %13 offset:0\n\t"
    "global_store_dwordx4 v239, v[168:171], %13 offset:0\n\t"
    "v_mfma_f32_32x32x16_bf16 a[144:159], v[28:31], v[208:211], a[144:159]\n\t"
    "ds_read_b64_tr_b16 v[194:195], v245 offset:33280\n\t"
    "ds_read_b128 v[36:39], v247 offset:12288\n\t"
    "s_mov_b32 s58, 30720\n\t"
    "buffer_load_dwordx4 v[148:151], v238, s[64:67], s58 offen offset:0\n\t"
    "v_lshlrev_b32 v172, 16, v160\n\t"
    "v_and_b32 v173, 0xffff0000, v160\n\t"
    "s_waitcnt lgkmcnt(8)\n\t"
    "v_mfma_f32_32x32x16_bf16 a[160:175], v[24:27], v[212:215], a[160:175]\n\t"
    "ds_read_b64_tr_b16 v[196:197], v245 offset:33024\n\t"
    "ds_read_b128 v[40:43], v248 offset:8192\n\t"
    "s_mov_b32 s58, 36864\n\t"
    "buffer_load_dwordx4 v[152:155], v238, s[64:67], s58 offen offset:0\n\t"
    "v_lshlrev_b32 v174, 16, v161\n\t"
    "v_and_b32 v175, 0xffff0000, v161\n\t"
    "v_lshlrev_b32 v176, 16, v162\n\t"
    "v_mfma_f32_32x32x16_bf16 a[176:191], v[28:31], v[212:215], a[176:191]\n\t"
    "ds_read_b64_tr_b16 v[198:199], v245 offset:33536\n\t"
    "ds_read_b128 v[44:47], v248 offset:12288\n\t"
    "s_mov_b32 s58, 43008\n\t"
    "buffer_load_dwordx4 v[156:159], v238, s[64:67], s58 offen offset:0\n\t"
    "v_and_b32 v177, 0xffff0000, v162\n\t"
    "v_lshlrev_b32 v178, 16, v163\n\t"
    "v_and_b32 v179, 0xffff0000, v163\n\t"
    "s_waitcnt lgkmcnt(10)\n\t"
    "v_mfma_f32_32x32x16_bf16 a[192:207], v[24:27], v[216:219], a[192:207]\n\t"
    "ds_read_b64_tr_b16 v[200:201], v245 offset:34816\n\t"
    "ds_read_b128 v[48:51], v249 offset:8192\n\t"
    "v_mul_f32 v180, 0xbfb8aa3b, v172\n\t"
    "v_mul_f32 v181, 0xbfb8aa3b, v173\n\t"
    "v_mul_f32 v182, 0xbfb8aa3b, v174\n\t"
    "v_mfma_f32_32x32x16_bf16 a[208:223], v[28:31], v[216:219], a[208:223]\n\t"
    "ds_read_b64_tr_b16 v[202:203], v245 offset:35328\n\t"
    "ds_read_b128 v[52:55], v249 offset:12288\n\t"
    "s_add_u32 m0, s56, 5120\n\t"
    "s_mov_b32 s58, 41344\n\t"
    "buffer_load_dwordx4 v242, s[36:39], s58 offen offset:0 lds\n\t"
    "v_mul_f32 v183, 0xbfb8aa3b, v175\n\t"
    "v_mul_f32 v184, 0xbfb8aa3b, v176\n\t"
    "v_mul_f32 v185, 0xbfb8aa3b, v177\n\t"
    "s_waitcnt lgkmcnt(12)\n\t"
    "v_mfma_f32_32x32x16_bf16 a[224:239], v[24:27], v[220:223], a[224:239]\n\t"
    "ds_read_b64_tr_b16 v[204:205], v245 offset:35072\n\t"
    "ds_read_b128 v[56:59], v250 offset:8192\n\t"
    "v_mul_f32 v186, 0xbfb8aa3b, v178\n\t"
    "v_mul_f32 v187, 0xbfb8aa3b, v179\n\t"
    "v_exp_f32 v180, v180\n\t"
    "v_mfma_f32_32x32x16_bf16 a[240:255], v[28:31], v[220:223], a[240:255]\n\t"
    "ds_read_b64_tr_b16 v[206:207], v245 offset:35584\n\t"
    "ds_read_b128 v[60:63], v250 offset:12288\n\t"
    "s_add_u32 m0, s56, 6144\n\t"
    "s_mov_b32 s58, 49536\n\t"
    "buffer_load_dwordx4 v241, s[36:39], s58 offen offset:0 lds\n\t"
    "v_exp_f32 v181, v181\n\t"
    "v_exp_f32 v182, v182\n\t"
    "v_exp_f32 v183, v183\n\t"
    "s_waitcnt lgkmcnt(0)\n\t"
    "v_mfma_f32_32x32x16_bf16 a[0:15], v[192:195], v[32:35], a[0:15]\n\t"
    "ds_read_b64_tr_b16 v[208:209], v245 offset:36864\n\t"
    "v_exp_f32 v184, v184\n\t"
    "v_exp_f32 v185, v185\n\t"
    "v_exp_f32 v186, v186\n\t"
    "v_mfma_f32_32x32x16_bf16 a[16:31], v[192:195], v[36:39], a[16:31]\n\t"
    "ds_read_b64_tr_b16 v[210:211], v245 offset:37376\n\t"
    "s_add_u32 m0, s56, 7168\n\t"
    "s_mov_b32 s58, 57728\n\t"
    "buffer_load_dwordx4 v242, s[36:39], s58 offen offset:0 lds\n\t"
    "v_exp_f32 v187, v187\n\t"
    "v_add_f32 v180, 1.0, v180\n\t"
    "v_add_f32 v181, 1.0, v181\n\t"
    "v_mfma_f32_32x32x16_bf16 a[32:47], v[196:199], v[32:35], a[32:47]\n\t"
    "ds_read_b64_tr_b16 v[212:213], v245 offset:37120\n\t"
    "s_add_u32 m0, s57, 57344\n\t"
    "s_mov_b32 s58, 917504\n\t"
    "buffer_load_dwordx4 v243, s[40:43], s58 offen offset:0 lds\n\t"
    "v_add_f32 v182, 1.0, v182\n\t"
    "v_add_f32 v183, 1.0, v183\n\t"
    "v_add_f32 v184, 1.0, v184\n\t"
    "v_mfma_f32_32x32x16_bf16 a[48:63], v[196:199], v[36:39], a[48:63]\n\t"
    "ds_read_b64_tr_b16 v[214:215], v245 offset:37632\n\t"
    "s_add_u32 m0, s57, 61440\n\t"
    "s_mov_b32 s58, 917504\n\t"
    "buffer_load_dwordx4 v243, s[44:47], s58 offen offset:0 lds\n\t"
    "v_add_f32 v185, 1.0, v185\n\t"
    "v_add_f32 v186, 1.0, v186\n\t"
    "v_add_f32 v187, 1.0, v187\n\t"
    "v_mfma_f32_32x32x16_bf16 a[64:79], v[200:203], v[32:35], a[64:79]\n\t"
    "ds_read_b64_tr_b16 v[216:217], v245 offset:38912\n\t"
    "v_rcp_f32 v180, v180\n\t"
    "v_rcp_f32 v181, v181\n\t"
    "v_rcp_f32 v182, v182\n\t"
    "v_mfma_f32_32x32x16_bf16 a[80:95], v[200:203], v[36:39], a[80:95]\n\t"
    "ds_read_b64_tr_b16 v[218:219], v245 offset:39424\n\t"
    "s_add_u32 m0, s56, 8192\n\t"
    "s_mov_b32 s58, 512\n\t"
    "buffer_load_dwordx4 v241, s[36:39], s58 offen offset:0 lds\n\t"
    "v_rcp_f32 v183, v183\n\t"
    "v_rcp_f32 v184, v184\n\t"
    "v_rcp_f32 v185, v185\n\t"
    "v_mfma_f32_32x32x16_bf16 a[96:111], v[204:207], v[32:35], a[96:111]\n\t"
    "ds_read_b64_tr_b16 v[220:221], v245 offset:39168\n\t"
    "v_rcp_f32 v186, v186\n\t"
    "v_rcp_f32 v187, v187\n\t"
    "v_mul_f32 v172, v172, v180\n\t"
    "v_mfma_f32_32x32x16_bf16 a[112:127], v[204:207], v[36:39], a[112:127]\n\t"
    "ds_read_b64_tr_b16 v[222:223], v245 offset:39680\n\t"
    "s_add_u32 m0, s56, 9216\n\t"
    "s_mov_b32 s58, 8704\n\t"
    "buffer_load_dwordx4 v242, s[36:39], s58 offen offset:0 lds\n\t"
    "v_mul_f32 v173, v173, v181\n\t"
    "v_mul_f32 v174, v174, v182\n\t"
    "v_mul_f32 v175, v175, v183\n\t"
    "s_waitcnt vmcnt(32)\n\t"
    "s_barrier\n\t"
    "s_waitcnt lgkmcnt(6)\n\t"
    "v_mfma_f32_32x32x16_bf16 a[128:143], v[32:35], v[208:211], a[128:143]\n\t"
    "ds_read_b64_tr_b16 v[192:193], v245 offset:40960\n\t"
    "v_mul_f32 v176, v176, v184\n\t"
    "v_mul_f32 v177, v177, v185\n\t"
    "v_mul_f32 v178, v178, v186\n\t"
    "v_mfma_f32_32x32x16_bf16 a[144:159], v[36:39], v[208:211], a[144:159]\n\t"
    "ds_read_b64_tr_b16 v[194:195], v245 offset:41472\n\t"
    "s_add_u32 m0, s56, 10240\n\t"
    "s_mov_b32 s58, 16896\n\t"
    "buffer_load_dwordx4 v241, s[36:39], s58 offen offset:0 lds\n\t"
    "v_mul_f32 v179, v179, v187\n\t"
    "s_waitcnt lgkmcnt(6)\n\t"
    "v_mfma_f32_32x32x16_bf16 a[160:175], v[32:35], v[212:215], a[160:175]\n\t"
    "ds_read_b64_tr_b16 v[196:197], v245 offset:41216\n\t"
    "s_add_u32 m0, s56, 11264\n\t"
    "s_mov_b32 s58, 25088\n\t"
    "buffer_load_dwordx4 v242, s[36:39], s58 offen offset:0 lds\n\t"
    "v_mfma_f32_32x32x16_bf16 a[176:191], v[36:39], v[212:215], a[176:191]\n\t"
    "ds_read_b64_tr_b16 v[198:199], v245 offset:41728\n\t"
    "s_add_u32 m0, s56, 12288\n\t"
    "s_mov_b32 s58, 33280\n\t"
    "buffer_load_dwordx4 v241, s[36:39], s58 offen offset:0 lds\n\t"
    "s_waitcnt lgkmcnt(6)\n\t"
    "v_mfma_f32_32x32x16_bf16 a[192:207], v[32:35], v[216:219], a[192:207]\n\t"
    "ds_read_b64_tr_b16 v[200:201], v245 offset:43008\n\t"
    "v_mfma_f32_32x32x16_bf16 a[208:223], v[36:39], v[216:219], a[208:223]\n\t"
    "ds_read_b64_tr_b16 v[202:203], v245 offset:43520\n\t"
    "s_add_u32 m0, s56, 13312\n\t"
    "s_mov_b32 s58, 41472\n\t"
    "buffer_load_dwordx4 v242, s[36:39], s58 offen offset:0 lds\n\t"
    "s_waitcnt lgkmcnt(6)\n\t"
    "v_mfma_f32_32x32x16_bf16 a[224:239], v[32:35], v[220:223], a[224:239]\n\t"
    "ds_read_b64_tr_b16 v[204:205], v245 offset:43264\n\t"
    "v_mfma_f32_32x32x16_bf16 a[240:255], v[36:39], v[220:223], a[240:255]\n\t"
    "ds_read_b64_tr_b16 v[206:207], v245 offset:43776\n\t"
    "s_add_u32 m0, s56, 14336\n\t"
    "s_mov_b32 s58, 49664\n\t"
    "buffer_load_dwordx4 v241, s[36:39], s58 offen offset:0 lds\n\t"
    "s_waitcnt vmcnt(29)\n\t"
    "s_waitcnt lgkmcnt(6)\n\t"
    "v_mfma_f32_32x32x16_bf16 a[0:15], v[192:195], v[40:43], a[0:15]\n\t"
    "ds_read_b64_tr_b16 v[208:209], v245 offset:45056\n\t"
    "ds_read_b128 v[64:67], v247 offset:16384\n\t"
    "v_mfma_f32_32x32x16_bf16 a[16:31], v[192:195], v[44:47], a[16:31]\n\t"
    "ds_read_b64_tr_b16 v[210:211], v245 offset:45568\n\t"
    "ds_read_b128 v[68:71], v247 offset:20480\n\t"
    "s_add_u32 m0, s56, 15360\n\t"
    "s_mov_b32 s58, 57856\n\t"
    "buffer_load_dwordx4 v242, s[36:39], s58 offen offset:0 lds\n\t"
    "s_waitcnt lgkmcnt(8)\n\t"
    "v_mfma_f32_32x32x16_bf16 a[32:47], v[196:199], v[40:43], a[32:47]\n\t"
    "ds_read_b64_tr_b16 v[212:213], v245 offset:45312\n\t"
    "ds_read_b128 v[72:75], v248 offset:16384\n\t"
    "s_add_u32 m0, s57, 0\n\t"
    "s_mov_b32 s58, 1048576\n\t"
    "buffer_load_dwordx4 v244, s[40:43], s58 offen offset:0 lds\n\t"
    "v_mfma_f32_32x32x16_bf16 a[48:63], v[196:199], v[44:47], a[48:63]\n\t"
    "ds_read_b64_tr_b16 v[214:215], v245 offset:45824\n\t"
    "ds_read_b128 v[76:79], v248 offset:20480\n\t"
    "s_add_u32 m0, s57, 4096\n\t"
    "s_mov_b32 s58, 1310720\n\t"
    "buffer_load_dwordx4 v244, s[40:43], s58 offen offset:0 lds\n\t"
    "s_waitcnt lgkmcnt(10)\n\t"
    "v_mfma_f32_32x32x16_bf16 a[64:79], v[200:203], v[40:43], a[64:79]\n\t"
    "ds_read_b64_tr_b16 v[216:217], v245 offset:47104\n\t"
    "ds_read_b128 v[80:83], v249 offset:16384\n\t"
    "v_mfma_f32_32x32x16_bf16 a[80:95], v[200:203], v[44:47], a[80:95]\n\t"
    "ds_read_b64_tr_b16 v[218:219], v245 offset:47616\n\t"
    "ds_read_b128 v[84:87], v249 offset:20480\n\t"
    "s_add_u32 m0, s57, 8192\n\t"
    "s_mov_b32 s58, 1572864\n\t"
    "buffer_load_dwordx4 v244, s[40:43], s58 offen offset:0 lds\n\t"
    "s_waitcnt lgkmcnt(12)\n\t"
    "v_mfma_f32_32x32x16_bf16 a[96:111], v[204:207], v[40:43], a[96:111]\n\t"
    "ds_read_b64_tr_b16 v[220:221], v245 offset:47360\n\t"
    "ds_read_b128 v[88:91], v250 offset:16384\n\t"
    "v_mfma_f32_32x32x16_bf16 a[112:127], v[204:207], v[44:47], a[112:127]\n\t"
    "ds_read_b64_tr_b16 v[222:223], v245 offset:47872\n\t"
    "ds_read_b128 v[92:95], v250 offset:20480\n\t"
    "s_add_u32 m0, s57, 12288\n\t"
    "s_mov_b32 s58, 1835008\n\t"
    "buffer_load_dwordx4 v244, s[40:43], s58 offen offset:0 lds\n\t"
    "s_waitcnt vmcnt(32)\n\t"
    "s_barrier\n\t"
    "s_waitcnt lgkmcnt(13)\n\t"
    "v_mfma_f32_32x32x16_bf16 a[128:143], v[40:43], v[208:211], a[128:143]\n\t"
    "ds_read_b64_tr_b16 v[192:193], v245 offset:49152\n\t"
    "s_waitcnt vmcnt(17)\n\t"
    "v_lshlrev_b32 v180, 16, v128\n\t"
    "v_and_b32 v128, 0xffff0000, v128\n\t"
    "v_mfma_f32_32x32x16_bf16 a[144:159], v[44:47], v[208:211], a[144:159]\n\t"
    "ds_read_b64_tr_b16 v[194:195], v245 offset:49664\n\t"
    "s_add_u32 m0, s57, 16384\n\t"
    "s_mov_b32 s58, 2097152\n\t"
    "buffer_load_dwordx4 v244, s[40:43], s58 offen offset:0 lds\n\t"
    "v_mul_f32 v224, v172, v180\n\t"
    "v_mul_f32 v225, v172, v128\n\t"
    "v_lshlrev_b32 v181, 16, v129\n\t"
    "s_waitcnt lgkmcnt(11)\n\t"
    "v_mfma_f32_32x32x16_bf16 a[160:175], v[40:43], v[212:215], a[160:175]\n\t"
    "ds_read_b64_tr_b16 v[196:197], v245 offset:49408\n\t"
    "s_add_u32 m0, s57, 20480\n\t"
    "s_mov_b32 s58, 2359296\n\t"
    "buffer_load_dwordx4 v244, s[40:43], s58 offen offset:0 lds\n\t"
    "v_and_b32 v129, 0xffff0000, v129\n\t"
    "v_mul_f32 v226, v172, v181\n\t"
    "v_mul_f32 v227, v172, v129\n\t"
    "v_mfma_f32_32x32x16_bf16 a[176:191], v[44:47], v[212:215], a[176:191]\n\t"
    "ds_read_b64_tr_b16 v[198:199], v245 offset:49920\n\t"
    "s_add_u32 m0, s57, 24576\n\t"
    "s_mov_b32 s58, 2621440\n\t"
    "buffer_load_dwordx4 v244, s[40:43], s58 offen offset:0 lds\n\t"
    "v_lshlrev_b32 v180, 16, v130\n\t"
    "v_and_b32 v130, 0xffff0000, v130\n\t"
    "v_mul_f32 v228, v172, v180\n\t"
    "s_waitcnt lgkmcnt(9)\n\t"
    "v_mfma_f32_32x32x16_bf16 a[192:207], v[40:43], v[216:219], a[192:207]\n\t"
    "ds_read_b64_tr_b16 v[200:201], v245 offset:51200\n\t"
    "v_mul_f32 v229, v172, v130\n\t"
    "v_lshlrev_b32 v181, 16, v131\n\t"
    "v_and_b32 v131, 0xffff0000, v131\n\t"
    "v_mfma_f32_32x32x16_bf16 a[208:223], v[44:47], v[216:219], a[208:223]\n\t"
    "ds_read_b64_tr_b16 v[202:203], v245 offset:51712\n\t"
    "s_add_u32 m0, s57, 28672\n\t"
    "s_mov_b32 s58, 2883584\n\t"
    "buffer_load_dwordx4 v244, s[40:43], s58 offen offset:0 lds\n\t"
    "v_mul_f32 v230, v172, v181\n\t"
    "v_mul_f32 v231, v172, v131\n\t"
    "v_lshlrev_b32 v180, 16, v132\n\t"
    "s_waitcnt lgkmcnt(7)\n\t"
    "v_mfma_f32_32x32x16_bf16 a[224:239], v[40:43], v[220:223], a[224:239]\n\t"
    "ds_read_b64_tr_b16 v[204:205], v245 offset:51456\n\t"
    "v_and_b32 v132, 0xffff0000, v132\n\t"
    "v_fmac_f32 v224, v173, v180\n\t"
    "v_fmac_f32 v225, v173, v132\n\t"
    "v_mfma_f32_32x32x16_bf16 a[240:255], v[44:47], v[220:223], a[240:255]\n\t"
    "ds_read_b64_tr_b16 v[206:207], v245 offset:51968\n\t"
    "s_add_u32 m0, s57, 32768\n\t"
    "s_mov_b32 s58, 3145728\n\t"
    "buffer_load_dwordx4 v244, s[40:43], s58 offen offset:0 lds\n\t"
    "v_lshlrev_b32 v181, 16, v133\n\t"
    "v_and_b32 v133, 0xffff0000, v133\n\t"
    "v_fmac_f32 v226, v173, v181\n\t"
    "s_waitcnt lgkmcnt(6)\n\t"
    "v_mfma_f32_32x32x16_bf16 a[0:15], v[192:195], v[48:51], a[0:15]\n\t"
    "ds_read_b64_tr_b16 v[208:209], v245 offset:53248\n\t"
    "v_fmac_f32 v227, v173, v133\n\t"
    "v_lshlrev_b32 v180, 16, v134\n\t"
    "v_and_b32 v134, 0xffff0000, v134\n\t"
    "v_mfma_f32_32x32x16_bf16 a[16:31], v[192:195], v[52:55], a[16:31]\n\t"
    "ds_read_b64_tr_b16 v[210:211], v245 offset:53760\n\t"
    "s_add_u32 m0, s56, 16384\n\t"
    "s_mov_b32 s58, 640\n\t"
    "buffer_load_dwordx4 v241, s[36:39], s58 offen offset:0 lds\n\t"
    "v_fmac_f32 v228, v173, v180\n\t"
    "v_fmac_f32 v229, v173, v134\n\t"
    "v_lshlrev_b32 v181, 16, v135\n\t"
    "s_waitcnt lgkmcnt(6)\n\t"
    "v_mfma_f32_32x32x16_bf16 a[32:47], v[196:199], v[48:51], a[32:47]\n\t"
    "ds_read_b64_tr_b16 v[212:213], v245 offset:53504\n\t"
    "s_add_u32 m0, s56, 17408\n\t"
    "s_mov_b32 s58, 8832\n\t"
    "buffer_load_dwordx4 v242, s[36:39], s58 offen offset:0 lds\n\t"
    "v_and_b32 v135, 0xffff0000, v135\n\t"
    "v_fmac_f32 v230, v173, v181\n\t"
    "v_fmac_f32 v231, v173, v135\n\t"
    "v_mfma_f32_32x32x16_bf16 a[48:63], v[196:199], v[52:55], a[48:63]\n\t"
    "ds_read_b64_tr_b16 v[214:215], v245 offset:54016\n\t"
    "s_add_u32 m0, s56, 18432\n\t"
    "s_mov_b32 s58, 17024\n\t"
    "buffer_load_dwordx4 v241, s[36:39], s58 offen offset:0 lds\n\t"
    "v_lshlrev_b32 v180, 16, v136\n\t"
    "v_and_b32 v136, 0xffff0000, v136\n\t"
    "v_fmac_f32 v224, v174, v180\n\t"
    "s_waitcnt lgkmcnt(6)\n\t"
    "v_mfma_f32_32x32x16_bf16 a[64:79], v[200:203], v[48:51], a[64:79]\n\t"
    "ds_read_b64_tr_b16 v[216:217], v245 offset:55296\n\t"
    "v_fmac_f32 v225, v174, v136\n\t"
    "v_lshlrev_b32 v181, 16, v137\n\t"
    "v_and_b32 v137, 0xffff0000, v137\n\t"
    "v_mfma_f32_32x32x16_bf16 a[80:95], v[200:203], v[52:55], a[80:95]\n\t"
    "ds_read_b64_tr_b16 v[218:219], v245 offset:55808\n\t"
    "s_add_u32 m0, s56, 19456\n\t"
    "s_mov_b32 s58, 25216\n\t"
    "buffer_load_dwordx4 v242, s[36:39], s58 offen offset:0 lds\n\t"
    "v_fmac_f32 v226, v174, v181\n\t"
    "v_fmac_f32 v227, v174, v137\n\t"
    "v_lshlrev_b32 v180, 16, v138\n\t"
    "s_waitcnt lgkmcnt(6)\n\t"
    "v_mfma_f32_32x32x16_bf16 a[96:111], v[204:207], v[48:51], a[96:111]\n\t"
    "ds_read_b64_tr_b16 v[220:221], v245 offset:55552\n\t"
    "v_and_b32 v138, 0xffff0000, v138\n\t"
    "v_fmac_f32 v228, v174, v180\n\t"
    "v_fmac_f32 v229, v174, v138\n\t"
    "v_mfma_f32_32x32x16_bf16 a[112:127], v[204:207], v[52:55], a[112:127]\n\t"
    "ds_read_b64_tr_b16 v[222:223], v245 offset:56064\n\t"
    "s_add_u32 m0, s56, 20480\n\t"
    "s_mov_b32 s58, 33408\n\t"
    "buffer_load_dwordx4 v241, s[36:39], s58 offen offset:0 lds\n\t"
    "v_lshlrev_b32 v181, 16, v139\n\t"
    "v_and_b32 v139, 0xffff0000, v139\n\t"
    "v_fmac_f32 v230, v174, v181\n\t"
    "s_waitcnt vmcnt(22)\n\t"
    "s_barrier\n\t"
    "s_waitcnt lgkmcnt(6)\n\t"
    "v_mfma_f32_32x32x16_bf16 a[128:143], v[48:51], v[208:211], a[128:143]\n\t"
    "ds_read_b64_tr_b16 v[192:193], v245 offset:57344\n\t"
    "v_fmac_f32 v231, v174, v139\n\t"
    "v_lshlrev_b32 v180, 16, v140\n\t"
    "v_and_b32 v140, 0xffff0000, v140\n\t"
    "v_mfma_f32_32x32x16_bf16 a[144:159], v[52:55], v[208:211], a[144:159]\n\t"
    "ds_read_b64_tr_b16 v[194:195], v245 offset:57856\n\t"
    "s_add_u32 m0, s56, 21504\n\t"
    "s_mov_b32 s58, 41600\n\t"
    "buffer_load_dwordx4 v242, s[36:39], s58 offen offset:0 lds\n\t"
    "v_fmac_f32 v224, v175, v180\n\t"
    "v_fmac_f32 v225, v175, v140\n\t"
    "v_lshlrev_b32 v181, 16, v141\n\t"
    "s_waitcnt lgkmcnt(6)\n\t"
    "v_mfma_f32_32x32x16_bf16 a[160:175], v[48:51], v[212:215], a[160:175]\n\t"
    "ds_read_b64_tr_b16 v[196:197], v245 offset:57600\n\t"
    "s_add_u32 m0, s56, 22528\n\t"
    "s_mov_b32 s58, 49792\n\t"
    "buffer_load_dwordx4 v241, s[36:39], s58 offen offset:0 lds\n\t"
    "v_and_b32 v141, 0xffff0000, v141\n\t"
    "v_fmac_f32 v226, v175, v181\n\t"
    "v_fmac_f32 v227, v175, v141\n\t"
    "v_mfma_f32_32x32x16_bf16 a[176:191], v[52:55], v[212:215], a[176:191]\n\t"
    "ds_read_b64_tr_b16 v[198:199], v245 offset:58112\n\t"
    "s_add_u32 m0, s56, 23552\n\t"
    "s_mov_b32 s58, 57984\n\t"
    "buffer_load_dwordx4 v242, s[36:39], s58 offen offset:0 lds\n\t"
    "v_lshlrev_b32 v180, 16, v142\n\t"
    "v_and_b32 v142, 0xffff0000, v142\n\t"
    "v_fmac_f32 v228, v175, v180\n\t"
    "s_waitcnt lgkmcnt(6)\n\t"
    "v_mfma_f32_32x32x16_bf16 a[192:207], v[48:51], v[216:219], a[192:207]\n\t"
    "ds_read_b64_tr_b16 v[200:201], v245 offset:59392\n\t"
    "v_fmac_f32 v229, v175, v142\n\t"
    "v_lshlrev_b32 v181, 16, v143\n\t"
    "v_and_b32 v143, 0xffff0000, v143\n\t"
    "v_mfma_f32_32x32x16_bf16 a[208:223], v[52:55], v[216:219], a[208:223]\n\t"
    "ds_read_b64_tr_b16 v[202:203], v245 offset:59904\n\t"
    "s_add_u32 m0, s57, 36864\n\t"
    "s_mov_b32 s58, 3407872\n\t"
    "buffer_load_dwordx4 v244, s[40:43], s58 offen offset:0 lds\n\t"
    "v_fmac_f32 v230, v175, v181\n\t"
    "v_fmac_f32 v231, v175, v143\n\t"
    "v_lshlrev_b32 v180, 16, v144\n\t"
    "s_waitcnt lgkmcnt(6)\n\t"
    "v_mfma_f32_32x32x16_bf16 a[224:239], v[48:51], v[220:223], a[224:239]\n\t"
    "ds_read_b64_tr_b16 v[204:205], v245 offset:59648\n\t"
    "v_and_b32 v144, 0xffff0000, v144\n\t"
    "v_fmac_f32 v224, v176, v180\n\t"
    "v_fmac_f32 v225, v176, v144\n\t"
    "v_mfma_f32_32x32x16_bf16 a[240:255], v[52:55], v[220:223], a[240:255]\n\t"
    "ds_read_b64_tr_b16 v[206:207], v245 offset:60160\n\t"
    "s_add_u32 m0, s57, 40960\n\t"
    "s_mov_b32 s58, 3670016\n\t"
    "buffer_load_dwordx4 v244, s[40:43], s58 offen offset:0 lds\n\t"
    "v_lshlrev_b32 v181, 16, v145\n\t"
    "v_and_b32 v145, 0xffff0000, v145\n\t"
    "v_fmac_f32 v226, v176, v181\n\t"
    "s_waitcnt lgkmcnt(6)\n\t"
    "v_mfma_f32_32x32x16_bf16 a[0:15], v[192:195], v[56:59], a[0:15]\n\t"
    "ds_read_b64_tr_b16 v[208:209], v245 offset:61440\n\t"
    "ds_read_b128 v[96:99], v247 offset:0\n\t"
    "v_fmac_f32 v227, v176, v145\n\t"
    "v_lshlrev_b32 v180, 16, v146\n\t"
    "v_and_b32 v146, 0xffff0000, v146\n\t"
    "v_mfma_f32_32x32x16_bf16 a[16:31], v[192:195], v[60:63], a[16:31]\n\t"
    "ds_read_b64_tr_b16 v[210:211], v245 offset:61952\n\t"
    "ds_read_b128 v[100:103], v247 offset:4096\n\t"
    "s_add_u32 m0, s57, 45056\n\t"
    "s_mov_b32 s58, 3932160\n\t"
    "buffer_load_dwordx4 v244, s[40:43], s58 offen offset:0 lds\n\t"
    "v_fmac_f32 v228, v176, v180\n\t"
    "v_fmac_f32 v229, v176, v146\n\t"
    "v_lshlrev_b32 v181, 16, v147\n\t"
    "s_waitcnt lgkmcnt(8)\n\t"
    "v_mfma_f32_32x32x16_bf16 a[32:47], v[196:199], v[56:59], a[32:47]\n\t"
    "ds_read_b64_tr_b16 v[212:213], v245 offset:61696\n\t"
    "ds_read_b128 v[104:107], v248 offset:0\n\t"
    "v_and_b32 v147, 0xffff0000, v147\n\t"
    "v_fmac_f32 v230, v176, v181\n\t"
    "v_fmac_f32 v231, v176, v147\n\t"
    "v_mfma_f32_32x32x16_bf16 a[48:63], v[196:199], v[60:63], a[48:63]\n\t"
    "ds_read_b64_tr_b16 v[214:215], v245 offset:62208\n\t"
    "ds_read_b128 v[108:111], v248 offset:4096\n\t"
    "v_lshlrev_b32 v180, 16, v148\n\t"
    "v_and_b32 v148, 0xffff0000, v148\n\t"
    "v_fmac_f32 v224, v177, v180\n\t"
    "s_waitcnt lgkmcnt(10)\n\t"
    "v_mfma_f32_32x32x16_bf16 a[64:79], v[200:203], v[56:59], a[64:79]\n\t"
    "ds_read_b64_tr_b16 v[216:217], v245 offset:63488\n\t"
    "ds_read_b128 v[112:115], v249 offset:0\n\t"
    "v_fmac_f32 v225, v177, v148\n\t"
    "v_lshlrev_b32 v181, 16, v149\n\t"
    "v_and_b32 v149, 0xffff0000, v149\n\t"
    "v_mfma_f32_32x32x16_bf16 a[80:95], v[200:203], v[60:63], a[80:95]\n\t"
    "ds_read_b64_tr_b16 v[218:219], v245 offset:64000\n\t"
    "ds_read_b128 v[116:119], v249 offset:4096\n\t"
    "v_fmac_f32 v226, v177, v181\n\t"
    "v_fmac_f32 v227, v177, v149\n\t"
    "v_lshlrev_b32 v180, 16, v150\n\t"
    "s_waitcnt lgkmcnt(12)\n\t"
    "v_mfma_f32_32x32x16_bf16 a[96:111], v[204:207], v[56:59], a[96:111]\n\t"
    "ds_read_b64_tr_b16 v[220:221], v245 offset:63744\n\t"
    "ds_read_b128 v[120:123], v250 offset:0\n\t"
    "v_and_b32 v150, 0xffff0000, v150\n\t"
    "v_fmac_f32 v228, v177, v180\n\t"
    "v_fmac_f32 v229, v177, v150\n\t"
    "v_mfma_f32_32x32x16_bf16 a[112:127], v[204:207], v[60:63], a[112:127]\n\t"
    "ds_read_b64_tr_b16 v[222:223], v245 offset:64256\n\t"
    "ds_read_b128 v[124:127], v250 offset:4096\n\t"
    "v_lshlrev_b32 v181, 16, v151\n\t"
    "v_and_b32 v151, 0xffff0000, v151\n\t"
    "v_fmac_f32 v230, v177, v181\n\t"
    "s_waitcnt vmcnt(18)\n\t"
    "s_barrier\n\t"
    "s_waitcnt lgkmcnt(13)\n\t"
    "v_mfma_f32_32x32x16_bf16 a[128:143], v[56:59], v[208:211], a[128:143]\n\t"
    "ds_read_b64_tr_b16 v[192:193], v245 offset:0\n\t"
    "v_fmac_f32 v231, v177, v151\n\t"
    "v_lshlrev_b32 v180, 16, v152\n\t"
    "v_and_b32 v152, 0xffff0000, v152\n\t"
    "v_mfma_f32_32x32x16_bf16 a[144:159], v[60:63], v[208:211], a[144:159]\n\t"
    "ds_read_b64_tr_b16 v[194:195], v245 offset:512\n\t"
    "s_add_u32 m0, s57, 49152\n\t"
    "s_mov_b32 s58, 1048704\n\t"
    "buffer_load_dwordx4 v244, s[40:43], s58 offen offset:0 lds\n\t"
    "v_fmac_f32 v224, v178, v180\n\t"
    "v_fmac_f32 v225, v178, v152\n\t"
    "v_lshlrev_b32 v181, 16, v153\n\t"
    "s_waitcnt lgkmcnt(11)\n\t"
    "v_mfma_f32_32x32x16_bf16 a[160:175], v[56:59], v[212:215], a[160:175]\n\t"
    "ds_read_b64_tr_b16 v[196:197], v245 offset:256\n\t"
    "s_add_u32 m0, s57, 53248\n\t"
    "s_mov_b32 s58, 1310848\n\t"
    "buffer_load_dwordx4 v244, s[40:43], s58 offen offset:0 lds\n\t"
    "v_and_b32 v153, 0xffff0000, v153\n\t"
    "v_fmac_f32 v226, v178, v181\n\t"
    "v_fmac_f32 v227, v178, v153\n\t"
    "v_mfma_f32_32x32x16_bf16 a[176:191], v[60:63], v[212:215], a[176:191]\n\t"
    "ds_read_b64_tr_b16 v[198:199], v245 offset:768\n\t"
    "v_lshlrev_b32 v180, 16, v154\n\t"
    "v_and_b32 v154, 0xffff0000, v154\n\t"
    "v_fmac_f32 v228, v178, v180\n\t"
    "s_waitcnt lgkmcnt(9)\n\t"
    "v_mfma_f32_32x32x16_bf16 a[192:207], v[56:59], v[216:219], a[192:207]\n\t"
    "ds_read_b64_tr_b16 v[200:201], v245 offset:2048\n\t"
    "v_fmac_f32 v229, v178, v154\n\t"
    "v_lshlrev_b32 v181, 16, v155\n\t"
    "v_and_b32 v155, 0xffff0000, v155\n\t"
    "v_mfma_f32_32x32x16_bf16 a[208:223], v[60:63], v[216:219], a[208:223]\n\t"
    "ds_read_b64_tr_b16 v[202:203], v245 offset:2560\n\t"
    "v_fmac_f32 v230, v178, v181\n\t"
    "v_fmac_f32 v231, v178, v155\n\t"
    "v_lshlrev_b32 v180, 16, v156\n\t"
    "s_waitcnt lgkmcnt(7)\n\t"
    "v_mfma_f32_32x32x16_bf16 a[224:239], v[56:59], v[220:223], a[224:239]\n\t"
    "ds_read_b64_tr_b16 v[204:205], v245 offset:2304\n\t"
    "v_and_b32 v156, 0xffff0000, v156\n\t"
    "v_fmac_f32 v224, v179, v180\n\t"
    "v_fmac_f32 v225, v179, v156\n\t"
    "v_mfma_f32_32x32x16_bf16 a[240:255], v[60:63], v[220:223], a[240:255]\n\t"
    "ds_read_b64_tr_b16 v[206:207], v245 offset:2816\n\t"
    "v_lshlrev_b32 v181, 16, v157\n\t"
    "v_and_b32 v157, 0xffff0000, v157\n\t"
    "v_fmac_f32 v226, v179, v181\n\t"
    "s_waitcnt lgkmcnt(6)\n\t"
    "v_mfma_f32_32x32x16_bf16 a[0:15], v[192:195], v[64:67], a[0:15]\n\t"
    "ds_read_b64_tr_b16 v[208:209], v245 offset:4096\n\t"
    "v_fmac_f32 v227, v179, v157\n\t"
    "v_lshlrev_b32 v180, 16, v158\n\t"
    "v_and_b32 v158, 0xffff0000, v158\n\t"
    "v_mfma_f32_32x32x16_bf16 a[16:31], v[192:195], v[68:71], a[16:31]\n\t"
    "ds_read_b64_tr_b16 v[210:211], v245 offset:4608\n\t"
    "s_add_u32 m0, s56, 0\n\t"
    "s_mov_b32 s58, 768\n\t"
    "buffer_load_dwordx4 v241, s[36:39], s58 offen offset:0 lds\n\t"
    "v_fmac_f32 v228, v179, v180\n\t"
    "v_fmac_f32 v229, v179, v158\n\t"
    "v_lshlrev_b32 v181, 16, v159\n\t"
    "s_waitcnt lgkmcnt(6)\n\t"
    "v_mfma_f32_32x32x16_bf16 a[32:47], v[196:199], v[64:67], a[32:47]\n\t"
    "ds_read_b64_tr_b16 v[212:213], v245 offset:4352\n\t"
    "s_add_u32 m0, s56, 1024\n\t"
    "s_mov_b32 s58, 8960\n\t"
    "buffer_load_dwordx4 v242, s[36:39], s58 offen offset:0 lds\n\t"
    "v_and_b32 v159, 0xffff0000, v159\n\t"
    "v_fmac_f32 v230, v179, v181\n\t"
    "v_fmac_f32 v231, v179, v159\n\t"
    "v_mfma_f32_32x32x16_bf16 a[48:63], v[196:199], v[68:71], a[48:63]\n\t"
    "ds_read_b64_tr_b16 v[214:215], v245 offset:4864\n\t"
    "s_add_u32 m0, s56, 2048\n\t"
    "s_mov_b32 s58, 17152\n\t"
    "buffer_load_dwordx4 v241, s[36:39], s58 offen offset:0 lds\n\t"
    "s_nop 1\n\t"
    "v_permlane32_swap_b32 v224, v228\n\t"
    "v_permlane32_swap_b32 v225, v229\n\t"
    "s_waitcnt lgkmcnt(6)\n\t"
    "v_mfma_f32_32x32x16_bf16 a[0:15], v[200:203], v[72:75], a[0:15]\n\t"
    "ds_read_b64_tr_b16 v[216:217], v245 offset:6144\n\t"
    "v_permlane32_swap_b32 v226, v230\n\t"
    "v_permlane32_swap_b32 v227, v231\n\t"
    "v_add_f32 v224, v224, v228\n\t"
    "v_mfma_f32_32x32x16_bf16 a[16:31], v[200:203], v[76:79], a[16:31]\n\t"
    "ds_read_b64_tr_b16 v[218:219], v245 offset:6656\n\t"
    "s_add_u32 m0, s56, 3072\n\t"
    "s_mov_b32 s58, 25344\n\t"
    "buffer_load_dwordx4 v242, s[36:39], s58 offen offset:0 lds\n\t"
    "v_add_f32 v225, v225, v229\n\t"
    "v_add_f32 v226, v226, v230\n\t"
    "v_add_f32 v227, v227, v231\n\t"
    "s_waitcnt lgkmcnt(6)\n\t"
    "v_mfma_f32_32x32x16_bf16 a[32:47], v[204:207], v[72:75], a[32:47]\n\t"
    "ds_read_b64_tr_b16 v[220:221], v245 offset:6400\n\t"
    "s_nop 0\n\t"
    "v_permlane16_swap_b32 v224, v226\n\t"
    "v_permlane16_swap_b32 v225, v227\n\t"
    "v_mfma_f32_32x32x16_bf16 a[48:63], v[204:207], v[76:79], a[48:63]\n\t"
    "ds_read_b64_tr_b16 v[222:223], v245 offset:6912\n\t"
    "s_add_u32 m0, s56, 4096\n\t"
    "s_mov_b32 s58, 33536\n\t"
    "buffer_load_dwordx4 v241, s[36:39], s58 offen offset:0 lds\n\t"
    "s_nop 0\n\t"
    "v_add_f32 v252, v224, v226\n\t"
    "v_add_f32 v253, v225, v227\n\t"
    "s_waitcnt vmcnt(23)\n\t"
    "s_barrier\n\t"
    "s_waitcnt lgkmcnt(6)\n\t"
    "v_mfma_f32_32x32x16_bf16 a[0:15], v[208:211], v[80:83], a[0:15]\n\t"
    "ds_read_b64_tr_b16 v[192:193], v245 offset:8192\n\t"
    "s_nop 1\n\t"
    "v_add_f32_dpp v252, v252, v252 row_ror:8 row_mask:0xf bank_mask:0xf\n\t"
    "v_add_f32_dpp v253, v253, v253 row_ror:8 row_mask:0xf bank_mask:0xf\n\t"
    "v_mfma_f32_32x32x16_bf16 a[16:31], v[208:211], v[84:87], a[16:31]\n\t"
    "ds_read_b64_tr_b16 v[194:195], v245 offset:8704\n\t"
    "s_add_u32 m0, s56, 5120\n\t"
    "s_mov_b32 s58, 41728\n\t"
    "buffer_load_dwordx4 v242, s[36:39], s58 offen offset:0 lds\n\t"
    "s_nop 1\n\t"
    "v_add_f32_dpp v252, v252, v252 row_ror:4 row_mask:0xf bank_mask:0xf\n\t"
    "v_add_f32_dpp v253, v253, v253 row_ror:4 row_mask:0xf bank_mask:0xf\n\t"
    "s_waitcnt lgkmcnt(6)\n\t"
    "v_mfma_f32_32x32x16_bf16 a[32:47], v[212:215], v[80:83], a[32:47]\n\t"
    "ds_read_b64_tr_b16 v[196:197], v245 offset:8448\n\t"
    "s_add_u32 m0, s56, 6144\n\t"
    "s_mov_b32 s58, 49920\n\t"
    "buffer_load_dwordx4 v241, s[36:39], s58 offen offset:0 lds\n\t"
    "v_mfma_f32_32x32x16_bf16 a[48:63], v[212:215], v[84:87], a[48:63]\n\t"
    "ds_read_b64_tr_b16 v[198:199], v245 offset:8960\n\t"
    "s_add_u32 m0, s56, 7168\n\t"
    "s_mov_b32 s58, 58112\n\t"
    "buffer_load_dwordx4 v242, s[36:39], s58 offen offset:0 lds\n\t"
    "s_waitcnt lgkmcnt(6)\n\t"
    "v_mfma_f32_32x32x16_bf16 a[0:15], v[216:219], v[88:91], a[0:15]\n\t"
    "ds_read_b64_tr_b16 v[200:201], v245 offset:10240\n\t"
    "v_mfma_f32_32x32x16_bf16 a[16:31], v[216:219], v[92:95], a[16:31]\n\t"
    "ds_read_b64_tr_b16 v[202:203], v245 offset:10752\n\t"
    "s_add_u32 m0, s57, 57344\n\t"
    "s_mov_b32 s58, 1572992\n\t"
    "buffer_load_dwordx4 v244, s[40:43], s58 offen offset:0 lds\n\t"
    "s_waitcnt lgkmcnt(6)\n\t"
    "v_mfma_f32_32x32x16_bf16 a[32:47], v[220:223], v[88:91], a[32:47]\n\t"
    "ds_read_b64_tr_b16 v[204:205], v245 offset:10496\n\t"
    "v_mfma_f32_32x32x16_bf16 a[48:63], v[220:223], v[92:95], a[48:63]\n\t"
    "ds_read_b64_tr_b16 v[206:207], v245 offset:11008\n\t"
    "s_add_u32 m0, s57, 61440\n\t"
    "s_mov_b32 s58, 1835136\n\t"
    "buffer_load_dwordx4 v244, s[40:43], s58 offen offset:0 lds\n\t"
    "s_waitcnt lgkmcnt(6)\n\t"
    "v_mfma_f32_32x32x16_bf16 a[0:15], v[192:195], v[96:99], a[0:15]\n\t"
    "ds_read_b64_tr_b16 v[208:209], v245 offset:12288\n\t"
    "ds_read_b128 v[128:131], v247 offset:8192\n\t"
    "v_mfma_f32_32x32x16_bf16 a[16:31], v[192:195], v[100:103], a[16:31]\n\t"
    "ds_read_b64_tr_b16 v[210:211], v245 offset:12800\n\t"
    "ds_read_b128 v[132:135], v247 offset:12288\n\t"
    "s_waitcnt lgkmcnt(8)\n\t"
    "v_mfma_f32_32x32x16_bf16 a[32:47], v[196:199], v[96:99], a[32:47]\n\t"
    "ds_read_b64_tr_b16 v[212:213], v245 offset:12544\n\t"
    "ds_read_b128 v[136:139], v248 offset:8192\n\t"
    "v_mfma_f32_32x32x16_bf16 a[48:63], v[196:199], v[100:103], a[48:63]\n\t"
    "ds_read_b64_tr_b16 v[214:215], v245 offset:13056\n\t"
    "ds_read_b128 v[140:143], v248 offset:12288\n\t"
    "s_waitcnt lgkmcnt(10)\n\t"
    "v_mfma_f32_32x32x16_bf16 a[0:15], v[200:203], v[104:107], a[0:15]\n\t"
    "ds_read_b64_tr_b16 v[216:217], v245 offset:14336\n\t"
    "ds_read_b128 v[144:147], v249 offset:8192\n\t"
    "v_mfma_f32_32x32x16_bf16 a[16:31], v[200:203], v[108:111], a[16:31]\n\t"
    "ds_read_b64_tr_b16 v[218:219], v245 offset:14848\n\t"
    "ds_read_b128 v[148:151], v249 offset:12288\n\t"
    "s_waitcnt lgkmcnt(12)\n\t"
    "v_mfma_f32_32x32x16_bf16 a[32:47], v[204:207], v[104:107], a[32:47]\n\t"
    "ds_read_b64_tr_b16 v[220:221], v245 offset:14592\n\t"
    "ds_read_b128 v[152:155], v250 offset:8192\n\t"
    "v_mfma_f32_32x32x16_bf16 a[48:63], v[204:207], v[108:111], a[48:63]\n\t"
    "ds_read_b64_tr_b16 v[222:223], v245 offset:15104\n\t"
    "ds_read_b128 v[156:159], v250 offset:12288\n\t"
    "s_waitcnt vmcnt(26)\n\t"
    "s_barrier\n\t"
    "s_waitcnt vmcnt(15)\n\t"
    "s_waitcnt lgkmcnt(13)\n\t"
    "v_mfma_f32_32x32x16_bf16 a[0:15], v[208:211], v[112:115], a[0:15]\n\t"
    "ds_read_b64_tr_b16 v[192:193], v245 offset:16384\n\t"
    "ds_read_b128 v[160:163], v247 offset:16384\n\t"
    "v_mfma_f32_32x32x16_bf16 a[16:31], v[208:211], v[116:119], a[16:31]\n\t"
    "ds_read_b64_tr_b16 v[194:195], v245 offset:16896\n\t"
    "ds_read_b128 v[164:167], v247 offset:20480\n\t"
    "s_add_u32 m0, s57, 0\n\t"
    "s_mov_b32 s58, 2097280\n\t"
    "buffer_load_dwordx4 v244, s[40:43], s58 offen offset:0 lds\n\t"
    "s_waitcnt lgkmcnt(13)\n\t"
    "v_mfma_f32_32x32x16_bf16 a[32:47], v[212:215], v[112:115], a[32:47]\n\t"
    "ds_read_b64_tr_b16 v[196:197], v245 offset:16640\n\t"
    "ds_read_b128 v[168:171], v248 offset:16384\n\t"
    "s_add_u32 m0, s57, 4096\n\t"
    "s_mov_b32 s58, 2359424\n\t"
    "buffer_load_dwordx4 v244, s[40:43], s58 offen offset:0 lds\n\t"
    "v_mfma_f32_32x32x16_bf16 a[48:63], v[212:215], v[116:119], a[48:63]\n\t"
    "ds_read_b64_tr_b16 v[198:199], v245 offset:17152\n\t"
    "ds_read_b128 v[172:175], v248 offset:20480\n\t"
    "s_waitcnt lgkmcnt(13)\n\t"
    "v_mfma_f32_32x32x16_bf16 a[0:15], v[216:219], v[120:123], a[0:15]\n\t"
    "ds_read_b64_tr_b16 v[200:201], v245 offset:18432\n\t"
    "ds_read_b128 v[176:179], v249 offset:16384\n\t"
    "v_mfma_f32_32x32x16_bf16 a[16:31], v[216:219], v[124:127], a[16:31]\n\t"
    "ds_read_b64_tr_b16 v[202:203], v245 offset:18944\n\t"
    "ds_read_b128 v[180:183], v249 offset:20480\n\t"
    "s_waitcnt lgkmcnt(13)\n\t"
    "v_mfma_f32_32x32x16_bf16 a[32:47], v[220:223], v[120:123], a[32:47]\n\t"
    "ds_read_b64_tr_b16 v[204:205], v245 offset:18688\n\t"
    "ds_read_b128 v[184:187], v250 offset:16384\n\t"
    "v_mfma_f32_32x32x16_bf16 a[48:63], v[220:223], v[124:127], a[48:63]\n\t"
    "ds_read_b64_tr_b16 v[206:207], v245 offset:19200\n\t"
    "ds_read_b128 v[188:191], v250 offset:20480\n\t"
    "s_waitcnt lgkmcnt(13)\n\t"
    "v_mfma_f32_32x32x16_bf16 a[0:15], v[192:195], v[128:131], a[0:15]\n\t"
    "ds_read_b64_tr_b16 v[208:209], v245 offset:20480\n\t"
    "v_mfma_f32_32x32x16_bf16 a[16:31], v[192:195], v[132:135], a[16:31]\n\t"
    "ds_read_b64_tr_b16 v[210:211], v245 offset:20992\n\t"
    "s_add_u32 m0, s56, 8192\n\t"
    "s_mov_b32 s58, 896\n\t"
    "buffer_load_dwordx4 v241, s[36:39], s58 offen offset:0 lds\n\t"
    "s_waitcnt lgkmcnt(11)\n\t"
    "v_mfma_f32_32x32x16_bf16 a[32:47], v[196:199], v[128:131], a[32:47]\n\t"
    "ds_read_b64_tr_b16 v[212:213], v245 offset:20736\n\t"
    "s_add_u32 m0, s56, 9216\n\t"
    "s_mov_b32 s58, 9088\n\t"
    "buffer_load_dwordx4 v242, s[36:39], s58 offen offset:0 lds\n\t"
    "v_mfma_f32_32x32x16_bf16 a[48:63], v[196:199], v[132:135], a[48:63]\n\t"
    "ds_read_b64_tr_b16 v[214:215], v245 offset:21248\n\t"
    "s_add_u32 m0, s56, 10240\n\t"
    "s_mov_b32 s58, 17280\n\t"
    "buffer_load_dwordx4 v241, s[36:39], s58 offen offset:0 lds\n\t"
    "s_waitcnt lgkmcnt(9)\n\t"
    "v_mfma_f32_32x32x16_bf16 a[0:15], v[200:203], v[136:139], a[0:15]\n\t"
    "ds_read_b64_tr_b16 v[216:217], v245 offset:22528\n\t"
    "v_mfma_f32_32x32x16_bf16 a[16:31], v[200:203], v[140:143], a[16:31]\n\t"
    "ds_read_b64_tr_b16 v[218:219], v245 offset:23040\n\t"
    "s_add_u32 m0, s56, 11264\n\t"
    "s_mov_b32 s58, 25472\n\t"
    "buffer_load_dwordx4 v242, s[36:39], s58 offen offset:0 lds\n\t"
    "s_waitcnt lgkmcnt(7)\n\t"
    "v_mfma_f32_32x32x16_bf16 a[32:47], v[204:207], v[136:139], a[32:47]\n\t"
    "ds_read_b64_tr_b16 v[220:221], v245 offset:22784\n\t"
    "v_mfma_f32_32x32x16_bf16 a[48:63], v[204:207], v[140:143], a[48:63]\n\t"
    "ds_read_b64_tr_b16 v[222:223], v245 offset:23296\n\t"
    "s_add_u32 m0, s56, 12288\n\t"
    "s_mov_b32 s58, 33664\n\t"
    "buffer_load_dwordx4 v241, s[36:39], s58 offen offset:0 lds\n\t"
    "s_barrier\n\t"
    "s_waitcnt lgkmcnt(6)\n\t"
    "v_mfma_f32_32x32x16_bf16 a[0:15], v[208:211], v[144:147], a[0:15]\n\t"
    "ds_read_b64_tr_b16 v[192:193], v245 offset:24576\n\t"
    "v_mfma_f32_32x32x16_bf16 a[16:31], v[208:211], v[148:151], a[16:31]\n\t"
    "ds_read_b64_tr_b16 v[194:195], v245 offset:25088\n\t"
    "s_add_u32 m0, s56, 13312\n\t"
    "s_mov_b32 s58, 41856\n\t"
    "buffer_load_dwordx4 v242, s[36:39], s58 offen offset:0 lds\n\t"
    "s_waitcnt lgkmcnt(6)\n\t"
    "v_mfma_f32_32x32x16_bf16 a[32:47], v[212:215], v[144:147], a[32:47]\n\t"
    "ds_read_b64_tr_b16 v[196:197], v245 offset:24832\n\t"
    "s_add_u32 m0, s56, 14336\n\t"
    "s_mov_b32 s58, 50048\n\t"
    "buffer_load_dwordx4 v241, s[36:39], s58 offen offset:0 lds\n\t"
    "v_mfma_f32_32x32x16_bf16 a[48:63], v[212:215], v[148:151], a[48:63]\n\t"
    "ds_read_b64_tr_b16 v[198:199], v245 offset:25344\n\t"
    "s_add_u32 m0, s56, 15360\n\t"
    "s_mov_b32 s58, 58240\n\t"
    "buffer_load_dwordx4 v242, s[36:39], s58 offen offset:0 lds\n\t"
    "s_waitcnt lgkmcnt(6)\n\t"
    "v_mfma_f32_32x32x16_bf16 a[0:15], v[216:219], v[152:155], a[0:15]\n\t"
    "ds_read_b64_tr_b16 v[200:201], v245 offset:26624\n\t"
    "v_mfma_f32_32x32x16_bf16 a[16:31], v[216:219], v[156:159], a[16:31]\n\t"
    "ds_read_b64_tr_b16 v[202:203], v245 offset:27136\n\t"
    "s_add_u32 m0, s57, 8192\n\t"
    "s_mov_b32 s58, 2621568\n\t"
    "buffer_load_dwordx4 v244, s[40:43], s58 offen offset:0 lds\n\t"
    "s_waitcnt lgkmcnt(6)\n\t"
    "v_mfma_f32_32x32x16_bf16 a[32:47], v[220:223], v[152:155], a[32:47]\n\t"
    "ds_read_b64_tr_b16 v[204:205], v245 offset:26880\n\t"
    "v_mfma_f32_32x32x16_bf16 a[48:63], v[220:223], v[156:159], a[48:63]\n\t"
    "ds_read_b64_tr_b16 v[206:207], v245 offset:27392\n\t"
    "s_add_u32 m0, s57, 12288\n\t"
    "s_mov_b32 s58, 2883712\n\t"
    "buffer_load_dwordx4 v244, s[40:43], s58 offen offset:0 lds\n\t"
    "s_waitcnt lgkmcnt(6)\n\t"
    "v_mfma_f32_32x32x16_bf16 a[0:15], v[192:195], v[160:163], a[0:15]\n\t"
    "ds_read_b64_tr_b16 v[208:209], v245 offset:28672\n\t"
    "v_mfma_f32_32x32x16_bf16 a[16:31], v[192:195], v[164:167], a[16:31]\n\t"
    "ds_read_b64_tr_b16 v[210:211], v245 offset:29184\n\t"
    "s_waitcnt lgkmcnt(6)\n\t"
    "v_mfma_f32_32x32x16_bf16 a[32:47], v[196:199], v[160:163], a[32:47]\n\t"
    "ds_read_b64_tr_b16 v[212:213], v245 offset:28928\n\t"
    "v_mfma_f32_32x32x16_bf16 a[48:63], v[196:199], v[164:167], a[48:63]\n\t"
    "ds_read_b64_tr_b16 v[214:215], v245 offset:29440\n\t"
    "s_waitcnt lgkmcnt(6)\n\t"
    "v_mfma_f32_32x32x16_bf16 a[0:15], v[200:203], v[168:171], a[0:15]\n\t"
    "ds_read_b64_tr_b16 v[216:217], v245 offset:30720\n\t"
    "v_mfma_f32_32x32x16_bf16 a[16:31], v[200:203], v[172:175], a[16:31]\n\t"
    "ds_read_b64_tr_b16 v[218:219], v245 offset:31232\n\t"
    "s_waitcnt lgkmcnt(6)\n\t"
    "v_mfma_f32_32x32x16_bf16 a[32:47], v[204:207], v[168:171], a[32:47]\n\t"
    "ds_read_b64_tr_b16 v[220:221], v245 offset:30976\n\t"
    "v_mfma_f32_32x32x16_bf16 a[48:63], v[204:207], v[172:175], a[48:63]\n\t"
    "ds_read_b64_tr_b16 v[222:223], v245 offset:31488\n\t"
    "s_waitcnt vmcnt(26)\n\t"
    "s_barrier\n\t"
    "s_waitcnt vmcnt(14)\n\t"
    "s_waitcnt lgkmcnt(6)\n\t"
    "v_mfma_f32_32x32x16_bf16 a[0:15], v[208:211], v[176:179], a[0:15]\n\t"
    "ds_read_b64_tr_b16 v[192:193], v245 offset:32768\n\t"
    "ds_read_b128 v[0:3], v247 offset:0\n\t"
    "v_mfma_f32_32x32x16_bf16 a[16:31], v[208:211], v[180:183], a[16:31]\n\t"
    "ds_read_b64_tr_b16 v[194:195], v245 offset:33280\n\t"
    "ds_read_b128 v[4:7], v247 offset:4096\n\t"
    "s_add_u32 m0, s57, 16384\n\t"
    "s_mov_b32 s58, 3145856\n\t"
    "buffer_load_dwordx4 v244, s[40:43], s58 offen offset:0 lds\n\t"
    "s_waitcnt lgkmcnt(8)\n\t"
    "v_mfma_f32_32x32x16_bf16 a[32:47], v[212:215], v[176:179], a[32:47]\n\t"
    "ds_read_b64_tr_b16 v[196:197], v245 offset:33024\n\t"
    "ds_read_b128 v[8:11], v248 offset:0\n\t"
    "s_add_u32 m0, s57, 20480\n\t"
    "s_mov_b32 s58, 3408000\n\t"
    "buffer_load_dwordx4 v244, s[40:43], s58 offen offset:0 lds\n\t"
    "v_mfma_f32_32x32x16_bf16 a[48:63], v[212:215], v[180:183], a[48:63]\n\t"
    "ds_read_b64_tr_b16 v[198:199], v245 offset:33536\n\t"
    "ds_read_b128 v[12:15], v248 offset:4096\n\t"
    "s_add_u32 m0, s57, 81920\n\t"
    "s_mov_b32 s58, 1048576\n\t"
    "buffer_load_dwordx4 v244, s[44:47], s58 offen offset:0 lds\n\t"
    "s_waitcnt lgkmcnt(10)\n\t"
    "v_mfma_f32_32x32x16_bf16 a[0:15], v[216:219], v[184:187], a[0:15]\n\t"
    "ds_read_b64_tr_b16 v[200:201], v245 offset:34816\n\t"
    "ds_read_b128 v[16:19], v249 offset:0\n\t"
    "v_mfma_f32_32x32x16_bf16 a[16:31], v[216:219], v[188:191], a[16:31]\n\t"
    "ds_read_b64_tr_b16 v[202:203], v245 offset:35328\n\t"
    "ds_read_b128 v[20:23], v249 offset:4096\n\t"
    "s_add_u32 m0, s57, 86016\n\t"
    "s_mov_b32 s58, 1310720\n\t"
    "buffer_load_dwordx4 v244, s[44:47], s58 offen offset:0 lds\n\t"
    "s_waitcnt lgkmcnt(12)\n\t"
    "v_mfma_f32_32x32x16_bf16 a[32:47], v[220:223], v[184:187], a[32:47]\n\t"
    "ds_read_b64_tr_b16 v[204:205], v245 offset:35072\n\t"
    "ds_read_b128 v[24:27], v250 offset:0\n\t"
    "v_mfma_f32_32x32x16_bf16 a[48:63], v[220:223], v[188:191], a[48:63]\n\t"
    "ds_read_b64_tr_b16 v[206:207], v245 offset:35584\n\t"
    "ds_read_b128 v[28:31], v250 offset:4096\n\t"
    "s_add_u32 m0, s57, 106496\n\t"
    "s_mov_b32 s58, 1572864\n\t"
    "buffer_load_dwordx4 v244, s[44:47], s58 offen offset:0 lds\n\t"
    "s_waitcnt lgkmcnt(0)\n\t"
    "v_mfma_f32_32x32x16_bf16 a[0:15], v[192:195], v[0:3], a[0:15]\n\t"
    "ds_read_b64_tr_b16 v[208:209], v245 offset:36864\n\t"
    "v_mfma_f32_32x32x16_bf16 a[16:31], v[192:195], v[4:7], a[16:31]\n\t"
    "ds_read_b64_tr_b16 v[210:211], v245 offset:37376\n\t"
    "s_add_u32 m0, s57, 110592\n\t"
    "s_mov_b32 s58, 1835008\n\t"
    "buffer_load_dwordx4 v244, s[44:47], s58 offen offset:0 lds\n\t"
    "v_mfma_f32_32x32x16_bf16 a[32:47], v[196:199], v[0:3], a[32:47]\n\t"
    "ds_read_b64_tr_b16 v[212:213], v245 offset:37120\n\t"
    "s_add_u32 m0, s57, 131072\n\t"
    "s_mov_b32 s58, 2097152\n\t"
    "buffer_load_dwordx4 v244, s[44:47], s58 offen offset:0 lds\n\t"
    "v_mfma_f32_32x32x16_bf16 a[48:63], v[196:199], v[4:7], a[48:63]\n\t"
    "ds_read_b64_tr_b16 v[214:215], v245 offset:37632\n\t"
    "s_add_u32 m0, s57, 135168\n\t"
    "s_mov_b32 s58, 2359296\n\t"
    "buffer_load_dwordx4 v244, s[44:47], s58 offen offset:0 lds\n\t"
    "v_mfma_f32_32x32x16_bf16 a[0:15], v[200:203], v[8:11], a[0:15]\n\t"
    "ds_read_b64_tr_b16 v[216:217], v245 offset:38912\n\t"
    "v_mfma_f32_32x32x16_bf16 a[16:31], v[200:203], v[12:15], a[16:31]\n\t"
    "ds_read_b64_tr_b16 v[218:219], v245 offset:39424\n\t"
    "s_add_u32 m0, s57, 155648\n\t"
    "s_mov_b32 s58, 2621440\n\t"
    "buffer_load_dwordx4 v244, s[44:47], s58 offen offset:0 lds\n\t"
    "v_mfma_f32_32x32x16_bf16 a[32:47], v[204:207], v[8:11], a[32:47]\n\t"
    "ds_read_b64_tr_b16 v[220:221], v245 offset:39168\n\t"
    "v_mfma_f32_32x32x16_bf16 a[48:63], v[204:207], v[12:15], a[48:63]\n\t"
    "ds_read_b64_tr_b16 v[222:223], v245 offset:39680\n\t"
    "s_add_u32 m0, s57, 159744\n\t"
    "s_mov_b32 s58, 2883584\n\t"
    "buffer_load_dwordx4 v244, s[44:47], s58 offen offset:0 lds\n\t"
    "s_barrier\n\t"
    "s_waitcnt vmcnt(12)\n\t"
    "s_waitcnt lgkmcnt(6)\n\t"
    "v_mfma_f32_32x32x16_bf16 a[0:15], v[208:211], v[16:19], a[0:15]\n\t"
    "ds_read_b64_tr_b16 v[192:193], v245 offset:40960\n\t"
    "ds_read_b128 v[32:35], v247 offset:8192\n\t"
    "v_mfma_f32_32x32x16_bf16 a[16:31], v[208:211], v[20:23], a[16:31]\n\t"
    "ds_read_b64_tr_b16 v[194:195], v245 offset:41472\n\t"
    "ds_read_b128 v[36:39], v247 offset:12288\n\t"
    "s_add_u32 m0, s57, 24576\n\t"
    "s_mov_b32 s58, 3670144\n\t"
    "buffer_load_dwordx4 v244, s[40:43], s58 offen offset:0 lds\n\t"
    "s_waitcnt lgkmcnt(8)\n\t"
    "v_mfma_f32_32x32x16_bf16 a[32:47], v[212:215], v[16:19], a[32:47]\n\t"
    "ds_read_b64_tr_b16 v[196:197], v245 offset:41216\n\t"
    "ds_read_b128 v[40:43], v248 offset:8192\n\t"
    "s_add_u32 m0, s57, 28672\n\t"
    "s_mov_b32 s58, 3932288\n\t"
    "buffer_load_dwordx4 v244, s[40:43], s58 offen offset:0 lds\n\t"
    "v_mfma_f32_32x32x16_bf16 a[48:63], v[212:215], v[20:23], a[48:63]\n\t"
    "ds_read_b64_tr_b16 v[198:199], v245 offset:41728\n\t"
    "ds_read_b128 v[44:47], v248 offset:12288\n\t"
    "s_add_u32 m0, s57, 65536\n\t"
    "s_mov_b32 s58, 3145728\n\t"
    "buffer_load_dwordx4 v244, s[44:47], s58 offen offset:0 lds\n\t"
    "s_waitcnt lgkmcnt(10)\n\t"
    "v_mfma_f32_32x32x16_bf16 a[0:15], v[216:219], v[24:27], a[0:15]\n\t"
    "ds_read_b64_tr_b16 v[200:201], v245 offset:43008\n\t"
    "ds_read_b128 v[48:51], v249 offset:8192\n\t"
    "v_mfma_f32_32x32x16_bf16 a[16:31], v[216:219], v[28:31], a[16:31]\n\t"
    "ds_read_b64_tr_b16 v[202:203], v245 offset:43520\n\t"
    "ds_read_b128 v[52:55], v249 offset:12288\n\t"
    "s_add_u32 m0, s57, 69632\n\t"
    "s_mov_b32 s58, 3407872\n\t"
    "buffer_load_dwordx4 v244, s[44:47], s58 offen offset:0 lds\n\t"
    "s_waitcnt lgkmcnt(12)\n\t"
    "v_mfma_f32_32x32x16_bf16 a[32:47], v[220:223], v[24:27], a[32:47]\n\t"
    "ds_read_b64_tr_b16 v[204:205], v245 offset:43264\n\t"
    "ds_read_b128 v[56:59], v250 offset:8192\n\t"
    "v_mfma_f32_32x32x16_bf16 a[48:63], v[220:223], v[28:31], a[48:63]\n\t"
    "ds_read_b64_tr_b16 v[206:207], v245 offset:43776\n\t"
    "ds_read_b128 v[60:63], v250 offset:12288\n\t"
    "s_add_u32 m0, s57, 90112\n\t"
    "s_mov_b32 s58, 3670016\n\t"
    "buffer_load_dwordx4 v244, s[44:47], s58 offen offset:0 lds\n\t"
    "s_waitcnt lgkmcnt(0)\n\t"
    "v_mfma_f32_32x32x16_bf16 a[0:15], v[192:195], v[32:35], a[0:15]\n\t"
    "ds_read_b64_tr_b16 v[208:209], v245 offset:45056\n\t"
    "v_mfma_f32_32x32x16_bf16 a[16:31], v[192:195], v[36:39], a[16:31]\n\t"
    "ds_read_b64_tr_b16 v[210:211], v245 offset:45568\n\t"
    "s_add_u32 m0, s57, 94208\n\t"
    "s_mov_b32 s58, 3932160\n\t"
    "buffer_load_dwordx4 v244, s[44:47], s58 offen offset:0 lds\n\t"
    "v_mfma_f32_32x32x16_bf16 a[32:47], v[196:199], v[32:35], a[32:47]\n\t"
    "ds_read_b64_tr_b16 v[212:213], v245 offset:45312\n\t"
    "s_add_u32 m0, s57, 114688\n\t"
    "s_mov_b32 s58, 1048704\n\t"
    "buffer_load_dwordx4 v244, s[44:47], s58 offen offset:0 lds\n\t"
    "v_mfma_f32_32x32x16_bf16 a[48:63], v[196:199], v[36:39], a[48:63]\n\t"
    "ds_read_b64_tr_b16 v[214:215], v245 offset:45824\n\t"
    "s_add_u32 m0, s57, 118784\n\t"
    "s_mov_b32 s58, 1310848\n\t"
    "buffer_load_dwordx4 v244, s[44:47], s58 offen offset:0 lds\n\t"
    "v_mfma_f32_32x32x16_bf16 a[0:15], v[200:203], v[40:43], a[0:15]\n\t"
    "ds_read_b64_tr_b16 v[216:217], v245 offset:47104\n\t"
    "v_mfma_f32_32x32x16_bf16 a[16:31], v[200:203], v[44:47], a[16:31]\n\t"
    "ds_read_b64_tr_b16 v[218:219], v245 offset:47616\n\t"
    "s_add_u32 m0, s57, 139264\n\t"
    "s_mov_b32 s58, 1572992\n\t"
    "buffer_load_dwordx4 v244, s[44:47], s58 offen offset:0 lds\n\t"
    "v_mfma_f32_32x32x16_bf16 a[32:47], v[204:207], v[40:43], a[32:47]\n\t"
    "ds_read_b64_tr_b16 v[220:221], v245 offset:47360\n\t"
    "v_mfma_f32_32x32x16_bf16 a[48:63], v[204:207], v[44:47], a[48:63]\n\t"
    "ds_read_b64_tr_b16 v[222:223], v245 offset:47872\n\t"
    "s_add_u32 m0, s57, 143360\n\t"
    "s_mov_b32 s58, 1835136\n\t"
    "buffer_load_dwordx4 v244, s[44:47], s58 offen offset:0 lds\n\t"
    "s_barrier\n\t"
    "s_waitcnt lgkmcnt(6)\n\t"
    "v_mfma_f32_32x32x16_bf16 a[0:15], v[208:211], v[48:51], a[0:15]\n\t"
    "ds_read_b64_tr_b16 v[192:193], v245 offset:49152\n\t"
    "v_mfma_f32_32x32x16_bf16 a[16:31], v[208:211], v[52:55], a[16:31]\n\t"
    "ds_read_b64_tr_b16 v[194:195], v245 offset:49664\n\t"
    "s_add_u32 m0, s57, 73728\n\t"
    "s_mov_b32 s58, 2097280\n\t"
    "buffer_load_dwordx4 v244, s[44:47], s58 offen offset:0 lds\n\t"
    "s_waitcnt lgkmcnt(6)\n\t"
    "v_mfma_f32_32x32x16_bf16 a[32:47], v[212:215], v[48:51], a[32:47]\n\t"
    "ds_read_b64_tr_b16 v[196:197], v245 offset:49408\n\t"
    "s_add_u32 m0, s57, 77824\n\t"
    "s_mov_b32 s58, 2359424\n\t"
    "buffer_load_dwordx4 v244, s[44:47], s58 offen offset:0 lds\n\t"
    "v_mfma_f32_32x32x16_bf16 a[48:63], v[212:215], v[52:55], a[48:63]\n\t"
    "ds_read_b64_tr_b16 v[198:199], v245 offset:49920\n\t"
    "s_add_u32 m0, s57, 98304\n\t"
    "s_mov_b32 s58, 2621568\n\t"
    "buffer_load_dwordx4 v244, s[44:47], s58 offen offset:0 lds\n\t"
    "s_waitcnt lgkmcnt(6)\n\t"
    "v_mfma_f32_32x32x16_bf16 a[0:15], v[216:219], v[56:59], a[0:15]\n\t"
    "ds_read_b64_tr_b16 v[200:201], v245 offset:51200\n\t"
    "v_mfma_f32_32x32x16_bf16 a[16:31], v[216:219], v[60:63], a[16:31]\n\t"
    "ds_read_b64_tr_b16 v[202:203], v245 offset:51712\n\t"
    "s_add_u32 m0, s57, 102400\n\t"
    "s_mov_b32 s58, 2883712\n\t"
    "buffer_load_dwordx4 v244, s[44:47], s58 offen offset:0 lds\n\t"
    "s_waitcnt lgkmcnt(6)\n\t"
    "v_mfma_f32_32x32x16_bf16 a[32:47], v[220:223], v[56:59], a[32:47]\n\t"
    "ds_read_b64_tr_b16 v[204:205], v245 offset:51456\n\t"
    "v_mfma_f32_32x32x16_bf16 a[48:63], v[220:223], v[60:63], a[48:63]\n\t"
    "ds_read_b64_tr_b16 v[206:207], v245 offset:51968\n\t"
    "s_add_u32 m0, s57, 122880\n\t"
    "s_mov_b32 s58, 3145856\n\t"
    "buffer_load_dwordx4 v244, s[44:47], s58 offen offset:0 lds\n\t"
    "s_waitcnt lgkmcnt(6)\n\t"
    "v_mfma_f32_32x32x16_bf16 a[64:79], v[192:195], v[64:67], a[64:79]\n\t"
    "ds_read_b64_tr_b16 v[208:209], v245 offset:53248\n\t"
    "v_mfma_f32_32x32x16_bf16 a[80:95], v[192:195], v[68:71], a[80:95]\n\t"
    "ds_read_b64_tr_b16 v[210:211], v245 offset:53760\n\t"
    "s_add_u32 m0, s57, 126976\n\t"
    "s_mov_b32 s58, 3408000\n\t"
    "buffer_load_dwordx4 v244, s[44:47], s58 offen offset:0 lds\n\t"
    "s_waitcnt lgkmcnt(6)\n\t"
    "v_mfma_f32_32x32x16_bf16 a[96:111], v[196:199], v[64:67], a[96:111]\n\t"
    "ds_read_b64_tr_b16 v[212:213], v245 offset:53504\n\t"
    "s_add_u32 m0, s57, 147456\n\t"
    "s_mov_b32 s58, 3670144\n\t"
    "buffer_load_dwordx4 v244, s[44:47], s58 offen offset:0 lds\n\t"
    "v_mfma_f32_32x32x16_bf16 a[112:127], v[196:199], v[68:71], a[112:127]\n\t"
    "ds_read_b64_tr_b16 v[214:215], v245 offset:54016\n\t"
    "s_add_u32 m0, s57, 151552\n\t"
    "s_mov_b32 s58, 3932288\n\t"
    "buffer_load_dwordx4 v244, s[44:47], s58 offen offset:0 lds\n\t"
    "v_accvgpr_read_b32 v224, a0\n\t"
    "s_waitcnt lgkmcnt(6)\n\t"
    "v_mfma_f32_32x32x16_bf16 a[64:79], v[200:203], v[72:75], a[64:79]\n\t"
    "ds_read_b64_tr_b16 v[216:217], v245 offset:55296\n\t"
    "v_accvgpr_read_b32 v225, a1\n\t"
    "v_mfma_f32_32x32x16_bf16 a[80:95], v[200:203], v[76:79], a[80:95]\n\t"
    "ds_read_b64_tr_b16 v[218:219], v245 offset:55808\n\t"
    "v_accvgpr_read_b32 v226, a2\n\t"
    "s_waitcnt lgkmcnt(6)\n\t"
    "v_mfma_f32_32x32x16_bf16 a[96:111], v[204:207], v[72:75], a[96:111]\n\t"
    "ds_read_b64_tr_b16 v[220:221], v245 offset:55552\n\t"
    "v_accvgpr_read_b32 v227, a3\n\t"
    "v_mfma_f32_32x32x16_bf16 a[112:127], v[204:207], v[76:79], a[112:127]\n\t"
    "ds_read_b64_tr_b16 v[222:223], v245 offset:56064\n\t"
    "v_accvgpr_read_b32 v228, a4\n\t"
    "v_accvgpr_read_b32 v229, a5\n\t"
    "s_barrier\n\t"
    "s_waitcnt lgkmcnt(6)\n\t"
    "v_mfma_f32_32x32x16_bf16 a[64:79], v[208:211], v[80:83], a[64:79]\n\t"
    "ds_read_b64_tr_b16 v[192:193], v245 offset:57344\n\t"
    "v_accvgpr_read_b32 v230, a6\n\t"
    "v_mfma_f32_32x32x16_bf16 a[80:95], v[208:211], v[84:87], a[80:95]\n\t"
    "ds_read_b64_tr_b16 v[194:195], v245 offset:57856\n\t"
    "v_accvgpr_read_b32 v231, a7\n\t"
    "s_waitcnt lgkmcnt(6)\n\t"
    "v_mfma_f32_32x32x16_bf16 a[96:111], v[212:215], v[80:83], a[96:111]\n\t"
    "ds_read_b64_tr_b16 v[196:197], v245 offset:57600\n\t"
    "v_cvt_pk_bf16_f32 v224, v224, v225\n\t"
    "v_mfma_f32_32x32x16_bf16 a[112:127], v[212:215], v[84:87], a[112:127]\n\t"
    "ds_read_b64_tr_b16 v[198:199], v245 offset:58112\n\t"
    "v_cvt_pk_bf16_f32 v225, v226, v227\n\t"
    "s_waitcnt lgkmcnt(6)\n\t"
    "v_mfma_f32_32x32x16_bf16 a[64:79], v[216:219], v[88:91], a[64:79]\n\t"
    "ds_read_b64_tr_b16 v[200:201], v245 offset:59392\n\t"
    "v_cvt_pk_bf16_f32 v226, v228, v229\n\t"
    "v_cvt_pk_bf16_f32 v227, v230, v231\n\t"
    "v_mfma_f32_32x32x16_bf16 a[80:95], v[216:219], v[92:95], a[80:95]\n\t"
    "ds_read_b64_tr_b16 v[202:203], v245 offset:59904\n\t"
    "buffer_store_dwordx4 v[224:227], v240, s[48:51], 0 offen offset:0 sc1\n\t"
    "s_waitcnt lgkmcnt(6)\n\t"
    "v_mfma_f32_32x32x16_bf16 a[96:111], v[220:223], v[88:91], a[96:111]\n\t"
    "ds_read_b64_tr_b16 v[204:205], v245 offset:59648\n\t"
    "v_accvgpr_read_b32 v232, a8\n\t"
    "v_mfma_f32_32x32x16_bf16 a[112:127], v[220:223], v[92:95], a[112:127]\n\t"
    "ds_read_b64_tr_b16 v[206:207], v245 offset:60160\n\t"
    "v_accvgpr_read_b32 v233, a9\n\t"
    "s_waitcnt lgkmcnt(6)\n\t"
    "v_mfma_f32_32x32x16_bf16 a[64:79], v[192:195], v[96:99], a[64:79]\n\t"
    "ds_read_b64_tr_b16 v[208:209], v245 offset:61440\n\t"
    "v_accvgpr_read_b32 v234, a10\n\t"
    "v_mfma_f32_32x32x16_bf16 a[80:95], v[192:195], v[100:103], a[80:95]\n\t"
    "ds_read_b64_tr_b16 v[210:211], v245 offset:61952\n\t"
    "v_accvgpr_read_b32 v235, a11\n\t"
    "v_accvgpr_read_b32 v236, a12\n\t"
    "s_waitcnt lgkmcnt(6)\n\t"
    "v_mfma_f32_32x32x16_bf16 a[96:111], v[196:199], v[96:99], a[96:111]\n\t"
    "ds_read_b64_tr_b16 v[212:213], v245 offset:61696\n\t"
    "v_accvgpr_read_b32 v237, a13\n\t"
    "v_mfma_f32_32x32x16_bf16 a[112:127], v[196:199], v[100:103], a[112:127]\n\t"
    "ds_read_b64_tr_b16 v[214:215], v245 offset:62208\n\t"
    "v_accvgpr_read_b32 v238, a14\n\t"
    "s_waitcnt lgkmcnt(6)\n\t"
    "v_mfma_f32_32x32x16_bf16 a[64:79], v[200:203], v[104:107], a[64:79]\n\t"
    "ds_read_b64_tr_b16 v[216:217], v245 offset:63488\n\t"
    "v_accvgpr_read_b32 v239, a15\n\t"
    "v_mfma_f32_32x32x16_bf16 a[80:95], v[200:203], v[108:111], a[80:95]\n\t"
    "ds_read_b64_tr_b16 v[218:219], v245 offset:64000\n\t"
    "v_cvt_pk_bf16_f32 v232, v232, v233\n\t"
    "s_waitcnt lgkmcnt(6)\n\t"
    "v_mfma_f32_32x32x16_bf16 a[96:111], v[204:207], v[104:107], a[96:111]\n\t"
    "ds_read_b64_tr_b16 v[220:221], v245 offset:63744\n\t"
    "v_cvt_pk_bf16_f32 v233, v234, v235\n\t"
    "v_cvt_pk_bf16_f32 v234, v236, v237\n\t"
    "v_mfma_f32_32x32x16_bf16 a[112:127], v[204:207], v[108:111], a[112:127]\n\t"
    "ds_read_b64_tr_b16 v[222:223], v245 offset:64256\n\t"
    "v_cvt_pk_bf16_f32 v235, v238, v239\n\t"
    "s_barrier\n\t"
    "s_waitcnt lgkmcnt(6)\n\t"
    "v_mfma_f32_32x32x16_bf16 a[64:79], v[208:211], v[112:115], a[64:79]\n\t"
    "ds_read_b64_tr_b16 v[192:193], v245 offset:0\n\t"
    "buffer_store_dwordx4 v[232:235], v240, s[48:51], 0 offen offset:1024 sc1\n\t"
    "v_mfma_f32_32x32x16_bf16 a[80:95], v[208:211], v[116:119], a[80:95]\n\t"
    "ds_read_b64_tr_b16 v[194:195], v245 offset:512\n\t"
    "v_accvgpr_read_b32 v224, a16\n\t"
    "s_waitcnt lgkmcnt(6)\n\t"
    "v_mfma_f32_32x32x16_bf16 a[96:111], v[212:215], v[112:115], a[96:111]\n\t"
    "ds_read_b64_tr_b16 v[196:197], v245 offset:256\n\t"
    "v_accvgpr_read_b32 v225, a17\n\t"
    "v_mfma_f32_32x32x16_bf16 a[112:127], v[212:215], v[116:119], a[112:127]\n\t"
    "ds_read_b64_tr_b16 v[198:199], v245 offset:768\n\t"
    "v_accvgpr_read_b32 v226, a18\n\t"
    "v_accvgpr_read_b32 v227, a19\n\t"
    "s_waitcnt lgkmcnt(6)\n\t"
    "v_mfma_f32_32x32x16_bf16 a[64:79], v[216:219], v[120:123], a[64:79]\n\t"
    "ds_read_b64_tr_b16 v[200:201], v245 offset:2048\n\t"
    "v_accvgpr_read_b32 v228, a20\n\t"
    "v_mfma_f32_32x32x16_bf16 a[80:95], v[216:219], v[124:127], a[80:95]\n\t"
    "ds_read_b64_tr_b16 v[202:203], v245 offset:2560\n\t"
    "v_accvgpr_read_b32 v229, a21\n\t"
    "s_waitcnt lgkmcnt(6)\n\t"
    "v_mfma_f32_32x32x16_bf16 a[96:111], v[220:223], v[120:123], a[96:111]\n\t"
    "ds_read_b64_tr_b16 v[204:205], v245 offset:2304\n\t"
    "v_accvgpr_read_b32 v230, a22\n\t"
    "v_mfma_f32_32x32x16_bf16 a[112:127], v[220:223], v[124:127], a[112:127]\n\t"
    "ds_read_b64_tr_b16 v[206:207], v245 offset:2816\n\t"
    "v_accvgpr_read_b32 v231, a23\n\t"
    "s_waitcnt lgkmcnt(6)\n\t"
    "v_mfma_f32_32x32x16_bf16 a[64:79], v[192:195], v[128:131], a[64:79]\n\t"
    "ds_read_b64_tr_b16 v[208:209], v245 offset:4096\n\t"
    "v_cvt_pk_bf16_f32 v224, v224, v225\n\t"
    "v_cvt_pk_bf16_f32 v225, v226, v227\n\t"
    "v_mfma_f32_32x32x16_bf16 a[80:95], v[192:195], v[132:135], a[80:95]\n\t"
    "ds_read_b64_tr_b16 v[210:211], v245 offset:4608\n\t"
    "v_cvt_pk_bf16_f32 v226, v228, v229\n\t"
    "s_waitcnt lgkmcnt(6)\n\t"
    "v_mfma_f32_32x32x16_bf16 a[96:111], v[196:199], v[128:131], a[96:111]\n\t"
    "ds_read_b64_tr_b16 v[212:213], v245 offset:4352\n\t"
    "v_cvt_pk_bf16_f32 v227, v230, v231\n\t"
    "v_mfma_f32_32x32x16_bf16 a[112:127], v[196:199], v[132:135], a[112:127]\n\t"
    "ds_read_b64_tr_b16 v[214:215], v245 offset:4864\n\t"
    "buffer_store_dwordx4 v[224:227], v240, s[48:51], s60 offen offset:0 sc1\n\t"
    "s_waitcnt lgkmcnt(6)\n\t"
    "v_mfma_f32_32x32x16_bf16 a[64:79], v[200:203], v[136:139], a[64:79]\n\t"
    "ds_read_b64_tr_b16 v[216:217], v245 offset:6144\n\t"
    "v_accvgpr_read_b32 v232, a24\n\t"
    "v_mfma_f32_32x32x16_bf16 a[80:95], v[200:203], v[140:143], a[80:95]\n\t"
    "ds_read_b64_tr_b16 v[218:219], v245 offset:6656\n\t"
    "v_accvgpr_read_b32 v233, a25\n\t"
    "v_accvgpr_read_b32 v234, a26\n\t"
    "s_waitcnt lgkmcnt(6)\n\t"
    "v_mfma_f32_32x32x16_bf16 a[96:111], v[204:207], v[136:139], a[96:111]\n\t"
    "ds_read_b64_tr_b16 v[220:221], v245 offset:6400\n\t"
    "v_accvgpr_read_b32 v235, a27\n\t"
    "v_mfma_f32_32x32x16_bf16 a[112:127], v[204:207], v[140:143], a[112:127]\n\t"
    "ds_read_b64_tr_b16 v[222:223], v245 offset:6912\n\t"
    "v_accvgpr_read_b32 v236, a28\n\t"
    "s_waitcnt vmcnt(31)\n\t"
    "s_barrier\n\t"
    "s_waitcnt lgkmcnt(6)\n\t"
    "v_mfma_f32_32x32x16_bf16 a[64:79], v[208:211], v[144:147], a[64:79]\n\t"
    "ds_read_b64_tr_b16 v[192:193], v245 offset:8192\n\t"
    "v_accvgpr_read_b32 v237, a29\n\t"
    "v_mfma_f32_32x32x16_bf16 a[80:95], v[208:211], v[148:151], a[80:95]\n\t"
    "ds_read_b64_tr_b16 v[194:195], v245 offset:8704\n\t"
    "v_accvgpr_read_b32 v238, a30\n\t"
    "s_waitcnt lgkmcnt(6)\n\t"
    "v_mfma_f32_32x32x16_bf16 a[96:111], v[212:215], v[144:147], a[96:111]\n\t"
    "ds_read_b64_tr_b16 v[196:197], v245 offset:8448\n\t"
    "v_accvgpr_read_b32 v239, a31\n\t"
    "v_cvt_pk_bf16_f32 v232, v232, v233\n\t"
    "v_mfma_f32_32x32x16_bf16 a[112:127], v[212:215], v[148:151], a[112:127]\n\t"
    "ds_read_b64_tr_b16 v[198:199], v245 offset:8960\n\t"
    "v_cvt_pk_bf16_f32 v233, v234, v235\n\t"
    "s_waitcnt lgkmcnt(6)\n\t"
    "v_mfma_f32_32x32x16_bf16 a[64:79], v[216:219], v[152:155], a[64:79]\n\t"
    "ds_read_b64_tr_b16 v[200:201], v245 offset:10240\n\t"
    "v_cvt_pk_bf16_f32 v234, v236, v237\n\t"
    "v_mfma_f32_32x32x16_bf16 a[80:95], v[216:219], v[156:159], a[80:95]\n\t"
    "ds_read_b64_tr_b16 v[202:203], v245 offset:10752\n\t"
    "v_cvt_pk_bf16_f32 v235, v238, v239\n\t"
    "s_waitcnt lgkmcnt(6)\n\t"
    "v_mfma_f32_32x32x16_bf16 a[96:111], v[220:223], v[152:155], a[96:111]\n\t"
    "ds_read_b64_tr_b16 v[204:205], v245 offset:10496\n\t"
    "buffer_store_dwordx4 v[232:235], v240, s[48:51], s60 offen offset:1024 sc1\n\t"
    "v_mfma_f32_32x32x16_bf16 a[112:127], v[220:223], v[156:159], a[112:127]\n\t"
    "ds_read_b64_tr_b16 v[206:207], v245 offset:11008\n\t"
    "v_accvgpr_read_b32 v224, a32\n\t"
    "v_accvgpr_read_b32 v225, a33\n\t"
    "s_waitcnt lgkmcnt(6)\n\t"
    "v_mfma_f32_32x32x16_bf16 a[64:79], v[192:195], v[160:163], a[64:79]\n\t"
    "ds_read_b64_tr_b16 v[208:209], v245 offset:12288\n\t"
    "v_lshlrev_b32 v250, 2, v240\n\t"
    "global_load_dword v255, v250, %23 offset:0\n\t"
    "v_accvgpr_read_b32 v226, a34\n\t"
    "v_mfma_f32_32x32x16_bf16 a[80:95], v[192:195], v[164:167], a[80:95]\n\t"
    "ds_read_b64_tr_b16 v[210:211], v245 offset:12800\n\t"
    "v_accvgpr_read_b32 v227, a35\n\t"
    "s_waitcnt lgkmcnt(6)\n\t"
    "v_mfma_f32_32x32x16_bf16 a[96:111], v[196:199], v[160:163], a[96:111]\n\t"
    "ds_read_b64_tr_b16 v[212:213], v245 offset:12544\n\t"
    "v_accvgpr_read_b32 v228, a36\n\t"
    "v_mfma_f32_32x32x16_bf16 a[112:127], v[196:199], v[164:167], a[112:127]\n\t"
    "ds_read_b64_tr_b16 v[214:215], v245 offset:13056\n\t"
    "v_accvgpr_read_b32 v229, a37\n\t"
    "s_waitcnt lgkmcnt(6)\n\t"
    "v_mfma_f32_32x32x16_bf16 a[64:79], v[200:203], v[168:171], a[64:79]\n\t"
    "ds_read_b64_tr_b16 v[216:217], v245 offset:14336\n\t"
    "v_accvgpr_read_b32 v230, a38\n\t"
    "v_accvgpr_read_b32 v231, a39\n\t"
    "v_mfma_f32_32x32x16_bf16 a[80:95], v[200:203], v[172:175], a[80:95]\n\t"
    "ds_read_b64_tr_b16 v[218:219], v245 offset:14848\n\t"
    "v_cvt_pk_bf16_f32 v224, v224, v225\n\t"
    "s_waitcnt lgkmcnt(6)\n\t"
    "v_mfma_f32_32x32x16_bf16 a[96:111], v[204:207], v[168:171], a[96:111]\n\t"
    "ds_read_b64_tr_b16 v[220:221], v245 offset:14592\n\t"
    "v_cvt_pk_bf16_f32 v225, v226, v227\n\t"
    "v_mfma_f32_32x32x16_bf16 a[112:127], v[204:207], v[172:175], a[112:127]\n\t"
    "ds_read_b64_tr_b16 v[222:223], v245 offset:15104\n\t"
    "v_cvt_pk_bf16_f32 v226, v228, v229\n\t"
    "s_waitcnt vmcnt(31)\n\t"
    "s_barrier\n\t"
    "s_waitcnt lgkmcnt(6)\n\t"
    "v_mfma_f32_32x32x16_bf16 a[64:79], v[208:211], v[176:179], a[64:79]\n\t"
    "ds_read_b64_tr_b16 v[192:193], v245 offset:16384\n\t"
    "v_cvt_pk_bf16_f32 v227, v230, v231\n\t"
    "v_mfma_f32_32x32x16_bf16 a[80:95], v[208:211], v[180:183], a[80:95]\n\t"
    "ds_read_b64_tr_b16 v[194:195], v245 offset:16896\n\t"
    "buffer_store_dwordx4 v[224:227], v240, s[48:51], 0 offen offset:2048 sc1\n\t"
    "v_accvgpr_read_b32 v232, a40\n\t"
    "s_waitcnt lgkmcnt(6)\n\t"
    "v_mfma_f32_32x32x16_bf16 a[96:111], v[212:215], v[176:179], a[96:111]\n\t"
    "ds_read_b64_tr_b16 v[196:197], v245 offset:16640\n\t"
    "v_accvgpr_read_b32 v233, a41\n\t"
    "v_mfma_f32_32x32x16_bf16 a[112:127], v[212:215], v[180:183], a[112:127]\n\t"
    "ds_read_b64_tr_b16 v[198:199], v245 offset:17152\n\t"
    "v_accvgpr_read_b32 v234, a42\n\t"
    "s_waitcnt lgkmcnt(6)\n\t"
    "v_mfma_f32_32x32x16_bf16 a[64:79], v[216:219], v[184:187], a[64:79]\n\t"
    "ds_read_b64_tr_b16 v[200:201], v245 offset:18432\n\t"
    "v_accvgpr_read_b32 v235, a43\n\t"
    "v_mfma_f32_32x32x16_bf16 a[80:95], v[216:219], v[188:191], a[80:95]\n\t"
    "ds_read_b64_tr_b16 v[202:203], v245 offset:18944\n\t"
    "v_accvgpr_read_b32 v236, a44\n\t"
    "s_waitcnt lgkmcnt(6)\n\t"
    "v_mfma_f32_32x32x16_bf16 a[96:111], v[220:223], v[184:187], a[96:111]\n\t"
    "ds_read_b64_tr_b16 v[204:205], v245 offset:18688\n\t"
    "v_accvgpr_read_b32 v237, a45\n\t"
    "v_accvgpr_read_b32 v238, a46\n\t"
    "v_mfma_f32_32x32x16_bf16 a[112:127], v[220:223], v[188:191], a[112:127]\n\t"
    "ds_read_b64_tr_b16 v[206:207], v245 offset:19200\n\t"
    "v_accvgpr_read_b32 v239, a47\n\t"
    "s_waitcnt lgkmcnt(6)\n\t"
    "v_mfma_f32_32x32x16_bf16 a[64:79], v[192:195], v[0:3], a[64:79]\n\t"
    "ds_read_b64_tr_b16 v[208:209], v245 offset:20480\n\t"
    "v_cvt_pk_bf16_f32 v232, v232, v233\n\t"
    "v_mfma_f32_32x32x16_bf16 a[80:95], v[192:195], v[4:7], a[80:95]\n\t"
    "ds_read_b64_tr_b16 v[210:211], v245 offset:20992\n\t"
    "v_cvt_pk_bf16_f32 v233, v234, v235\n\t"
    "s_waitcnt lgkmcnt(6)\n\t"
    "v_mfma_f32_32x32x16_bf16 a[96:111], v[196:199], v[0:3], a[96:111]\n\t"
    "ds_read_b64_tr_b16 v[212:213], v245 offset:20736\n\t"
    "v_cvt_pk_bf16_f32 v234, v236, v237\n\t"
    "v_mfma_f32_32x32x16_bf16 a[112:127], v[196:199], v[4:7], a[112:127]\n\t"
    "ds_read_b64_tr_b16 v[214:215], v245 offset:21248\n\t"
    "v_cvt_pk_bf16_f32 v235, v238, v239\n\t"
    "buffer_store_dwordx4 v[232:235], v240, s[48:51], 0 offen offset:3072 sc1\n\t"
    "s_waitcnt lgkmcnt(6)\n\t"
    "v_mfma_f32_32x32x16_bf16 a[64:79], v[200:203], v[8:11], a[64:79]\n\t"
    "ds_read_b64_tr_b16 v[216:217], v245 offset:22528\n\t"
    "v_accvgpr_read_b32 v224, a48\n\t"
    "v_mfma_f32_32x32x16_bf16 a[80:95], v[200:203], v[12:15], a[80:95]\n\t"
    "ds_read_b64_tr_b16 v[218:219], v245 offset:23040\n\t"
    "v_accvgpr_read_b32 v225, a49\n\t"
    "s_waitcnt lgkmcnt(6)\n\t"
    "v_mfma_f32_32x32x16_bf16 a[96:111], v[204:207], v[8:11], a[96:111]\n\t"
    "ds_read_b64_tr_b16 v[220:221], v245 offset:22784\n\t"
    "v_accvgpr_read_b32 v226, a50\n\t"
    "v_mfma_f32_32x32x16_bf16 a[112:127], v[204:207], v[12:15], a[112:127]\n\t"
    "ds_read_b64_tr_b16 v[222:223], v245 offset:23296\n\t"
    "v_accvgpr_read_b32 v227, a51\n\t"
    "s_waitcnt vmcnt(23)\n\t"
    "s_barrier\n\t"
    "s_waitcnt lgkmcnt(6)\n\t"
    "v_mfma_f32_32x32x16_bf16 a[64:79], v[208:211], v[16:19], a[64:79]\n\t"
    "ds_read_b64_tr_b16 v[192:193], v245 offset:24576\n\t"
    "v_accvgpr_read_b32 v228, a52\n\t"
    "v_accvgpr_read_b32 v229, a53\n\t"
    "v_mfma_f32_32x32x16_bf16 a[80:95], v[208:211], v[20:23], a[80:95]\n\t"
    "ds_read_b64_tr_b16 v[194:195], v245 offset:25088\n\t"
    "v_accvgpr_read_b32 v230, a54\n\t"
    "s_waitcnt lgkmcnt(6)\n\t"
    "v_mfma_f32_32x32x16_bf16 a[96:111], v[212:215], v[16:19], a[96:111]\n\t"
    "ds_read_b64_tr_b16 v[196:197], v245 offset:24832\n\t"
    "v_accvgpr_read_b32 v231, a55\n\t"
    "v_mfma_f32_32x32x16_bf16 a[112:127], v[212:215], v[20:23], a[112:127]\n\t"
    "ds_read_b64_tr_b16 v[198:199], v245 offset:25344\n\t"
    "v_cvt_pk_bf16_f32 v224, v224, v225\n\t"
    "s_waitcnt lgkmcnt(6)\n\t"
    "v_mfma_f32_32x32x16_bf16 a[64:79], v[216:219], v[24:27], a[64:79]\n\t"
    "ds_read_b64_tr_b16 v[200:201], v245 offset:26624\n\t"
    "v_cvt_pk_bf16_f32 v225, v226, v227\n\t"
    "v_mfma_f32_32x32x16_bf16 a[80:95], v[216:219], v[28:31], a[80:95]\n\t"
    "ds_read_b64_tr_b16 v[202:203], v245 offset:27136\n\t"
    "v_cvt_pk_bf16_f32 v226, v228, v229\n\t"
    "v_cvt_pk_bf16_f32 v227, v230, v231\n\t"
    "s_waitcnt lgkmcnt(6)\n\t"
    "v_mfma_f32_32x32x16_bf16 a[96:111], v[220:223], v[24:27], a[96:111]\n\t"
    "ds_read_b64_tr_b16 v[204:205], v245 offset:26880\n\t"
    "buffer_store_dwordx4 v[224:227], v240, s[48:51], s60 offen offset:2048 sc1\n\t"
    "v_mfma_f32_32x32x16_bf16 a[112:127], v[220:223], v[28:31], a[112:127]\n\t"
    "ds_read_b64_tr_b16 v[206:207], v245 offset:27392\n\t"
    "v_accvgpr_read_b32 v232, a56\n\t"
    "s_waitcnt lgkmcnt(6)\n\t"
    "v_mfma_f32_32x32x16_bf16 a[64:79], v[192:195], v[32:35], a[64:79]\n\t"
    "ds_read_b64_tr_b16 v[208:209], v245 offset:28672\n\t"
    "v_accvgpr_read_b32 v233, a57\n\t"
    "v_mfma_f32_32x32x16_bf16 a[80:95], v[192:195], v[36:39], a[80:95]\n\t"
    "ds_read_b64_tr_b16 v[210:211], v245 offset:29184\n\t"
    "v_accvgpr_read_b32 v234, a58\n\t"
    "s_waitcnt lgkmcnt(6)\n\t"
    "v_mfma_f32_32x32x16_bf16 a[96:111], v[196:199], v[32:35], a[96:111]\n\t"
    "ds_read_b64_tr_b16 v[212:213], v245 offset:28928\n\t"
    "v_accvgpr_read_b32 v235, a59\n\t"
    "v_accvgpr_read_b32 v236, a60\n\t"
    "v_mfma_f32_32x32x16_bf16 a[112:127], v[196:199], v[36:39], a[112:127]\n\t"
    "ds_read_b64_tr_b16 v[214:215], v245 offset:29440\n\t"
    "v_accvgpr_read_b32 v237, a61\n\t"
    "s_waitcnt lgkmcnt(6)\n\t"
    "v_mfma_f32_32x32x16_bf16 a[64:79], v[200:203], v[40:43], a[64:79]\n\t"
    "ds_read_b64_tr_b16 v[216:217], v245 offset:30720\n\t"
    "v_accvgpr_read_b32 v238, a62\n\t"
    "v_mfma_f32_32x32x16_bf16 a[80:95], v[200:203], v[44:47], a[80:95]\n\t"
    "ds_read_b64_tr_b16 v[218:219], v245 offset:31232\n\t"
    "v_accvgpr_read_b32 v239, a63\n\t"
    "s_waitcnt lgkmcnt(6)\n\t"
    "v_mfma_f32_32x32x16_bf16 a[96:111], v[204:207], v[40:43], a[96:111]\n\t"
    "ds_read_b64_tr_b16 v[220:221], v245 offset:30976\n\t"
    "v_cvt_pk_bf16_f32 v232, v232, v233\n\t"
    "v_mfma_f32_32x32x16_bf16 a[112:127], v[204:207], v[44:47], a[112:127]\n\t"
    "ds_read_b64_tr_b16 v[222:223], v245 offset:31488\n\t"
    "v_cvt_pk_bf16_f32 v233, v234, v235\n\t"
    "v_cvt_pk_bf16_f32 v234, v236, v237\n\t"
    "s_barrier\n\t"
    "s_waitcnt lgkmcnt(6)\n\t"
    "v_mfma_f32_32x32x16_bf16 a[64:79], v[208:211], v[48:51], a[64:79]\n\t"
    "ds_read_b64_tr_b16 v[192:193], v246 offset:16384\n\t"
    "v_cvt_pk_bf16_f32 v235, v238, v239\n\t"
    "v_mfma_f32_32x32x16_bf16 a[80:95], v[208:211], v[52:55], a[80:95]\n\t"
    "ds_read_b64_tr_b16 v[194:195], v246 offset:16896\n\t"
    "buffer_store_dwordx4 v[232:235], v240, s[48:51], s60 offen offset:3072 sc1\n\t"
    "s_waitcnt lgkmcnt(6)\n\t"
    "v_mfma_f32_32x32x16_bf16 a[96:111], v[212:215], v[48:51], a[96:111]\n\t"
    "ds_read_b64_tr_b16 v[196:197], v246 offset:16640\n\t"
    "v_mfma_f32_32x32x16_bf16 a[112:127], v[212:215], v[52:55], a[112:127]\n\t"
    "ds_read_b64_tr_b16 v[198:199], v246 offset:17152\n\t"
    "s_waitcnt lgkmcnt(6)\n\t"
    "v_mfma_f32_32x32x16_bf16 a[64:79], v[216:219], v[56:59], a[64:79]\n\t"
    "ds_read_b64_tr_b16 v[200:201], v246 offset:18432\n\t"
    "v_mfma_f32_32x32x16_bf16 a[80:95], v[216:219], v[60:63], a[80:95]\n\t"
    "ds_read_b64_tr_b16 v[202:203], v246 offset:18944\n\t"
    "s_waitcnt lgkmcnt(6)\n\t"
    "v_mfma_f32_32x32x16_bf16 a[96:111], v[220:223], v[56:59], a[96:111]\n\t"
    "ds_read_b64_tr_b16 v[204:205], v246 offset:18688\n\t"
    "v_mfma_f32_32x32x16_bf16 a[112:127], v[220:223], v[60:63], a[112:127]\n\t"
    "ds_read_b64_tr_b16 v[206:207], v246 offset:19200\n\t"
    "s_waitcnt lgkmcnt(6)\n\t"
    "v_mfma_f32_32x32x16_bf16 a[128:143], v[64:67], v[192:195], a[128:143]\n\t"
    "ds_read_b64_tr_b16 v[208:209], v246 offset:20480\n\t"
    "v_mfma_f32_32x32x16_bf16 a[144:159], v[68:71], v[192:195], a[144:159]\n\t"
    "ds_read_b64_tr_b16 v[210:211], v246 offset:20992\n\t"
    "s_waitcnt lgkmcnt(6)\n\t"
    "v_mfma_f32_32x32x16_bf16 a[160:175], v[64:67], v[196:199], a[160:175]\n\t"
    "ds_read_b64_tr_b16 v[212:213], v246 offset:20736\n\t"
    "v_accvgpr_read_b32 v224, a64\n\t"
    "v_mfma_f32_32x32x16_bf16 a[176:191], v[68:71], v[196:199], a[176:191]\n\t"
    "ds_read_b64_tr_b16 v[214:215], v246 offset:21248\n\t"
    "v_accvgpr_read_b32 v225, a65\n\t"
    "s_waitcnt lgkmcnt(6)\n\t"
    "v_mfma_f32_32x32x16_bf16 a[128:143], v[72:75], v[200:203], a[128:143]\n\t"
    "ds_read_b64_tr_b16 v[216:217], v246 offset:22528\n\t"
    "v_accvgpr_read_b32 v226, a66\n\t"
    "v_mfma_f32_32x32x16_bf16 a[144:159], v[76:79], v[200:203], a[144:159]\n\t"
    "ds_read_b64_tr_b16 v[218:219], v246 offset:23040\n\t"
    "v_accvgpr_read_b32 v227, a67\n\t"
    "s_waitcnt lgkmcnt(6)\n\t"
    "v_mfma_f32_32x32x16_bf16 a[160:175], v[72:75], v[204:207], a[160:175]\n\t"
    "ds_read_b64_tr_b16 v[220:221], v246 offset:22784\n\t"
    "v_accvgpr_read_b32 v228, a68\n\t"
    "v_accvgpr_read_b32 v229, a69\n\t"
    "v_mfma_f32_32x32x16_bf16 a[176:191], v[76:79], v[204:207], a[176:191]\n\t"
    "ds_read_b64_tr_b16 v[222:223], v246 offset:23296\n\t"
    "v_accvgpr_read_b32 v230, a70\n\t"
    "s_barrier\n\t"
    "s_waitcnt lgkmcnt(6)\n\t"
    "v_mfma_f32_32x32x16_bf16 a[128:143], v[80:83], v[208:211], a[128:143]\n\t"
    "ds_read_b64_tr_b16 v[192:193], v246 offset:40960\n\t"
    "v_accvgpr_read_b32 v231, a71\n\t"
    "v_mfma_f32_32x32x16_bf16 a[144:159], v[84:87], v[208:211], a[144:159]\n\t"
    "ds_read_b64_tr_b16 v[194:195], v246 offset:41472\n\t"
    "v_cvt_pk_bf16_f32 v224, v224, v225\n\t"
    "s_waitcnt lgkmcnt(6)\n\t"
    "v_mfma_f32_32x32x16_bf16 a[160:175], v[80:83], v[212:215], a[160:175]\n\t"
    "ds_read_b64_tr_b16 v[196:197], v246 offset:41216\n\t"
    "v_cvt_pk_bf16_f32 v225, v226, v227\n\t"
    "v_mfma_f32_32x32x16_bf16 a[176:191], v[84:87], v[212:215], a[176:191]\n\t"
    "ds_read_b64_tr_b16 v[198:199], v246 offset:41728\n\t"
    "v_cvt_pk_bf16_f32 v226, v228, v229\n\t"
    "v_cvt_pk_bf16_f32 v227, v230, v231\n\t"
    "s_waitcnt lgkmcnt(6)\n\t"
    "v_mfma_f32_32x32x16_bf16 a[128:143], v[88:91], v[216:219], a[128:143]\n\t"
    "ds_read_b64_tr_b16 v[200:201], v246 offset:43008\n\t"
    "buffer_store_dwordx4 v[224:227], v240, s[48:51], s59 offen offset:0 sc1\n\t"
    "v_mfma_f32_32x32x16_bf16 a[144:159], v[92:95], v[216:219], a[144:159]\n\t"
    "ds_read_b64_tr_b16 v[202:203], v246 offset:43520\n\t"
    "v_accvgpr_read_b32 v232, a72\n\t"
    "s_waitcnt lgkmcnt(6)\n\t"
    "v_mfma_f32_32x32x16_bf16 a[160:175], v[88:91], v[220:223], a[160:175]\n\t"
    "ds_read_b64_tr_b16 v[204:205], v246 offset:43264\n\t"
    "v_accvgpr_read_b32 v233, a73\n\t"
    "v_mfma_f32_32x32x16_bf16 a[176:191], v[92:95], v[220:223], a[176:191]\n\t"
    "ds_read_b64_tr_b16 v[206:207], v246 offset:43776\n\t"
    "v_accvgpr_read_b32 v234, a74\n\t"
    "s_waitcnt lgkmcnt(6)\n\t"
    "v_mfma_f32_32x32x16_bf16 a[128:143], v[96:99], v[192:195], a[128:143]\n\t"
    "ds_read_b64_tr_b16 v[208:209], v246 offset:45056\n\t"
    "v_mbcnt_lo_u32_b32 v241, -1, 0\n\t"
    "v_mbcnt_hi_u32_b32 v241, -1, v241\n\t"
    "v_accvgpr_read_b32 v235, a75\n\t"
    "v_accvgpr_read_b32 v236, a76\n\t"
    "v_mfma_f32_32x32x16_bf16 a[144:159], v[100:103], v[192:195], a[144:159]\n\t"
    "ds_read_b64_tr_b16 v[210:211], v246 offset:45568\n\t"
    "v_and_b32 v247, 3, v241\n\t"
    "v_lshrrev_b32 v241, 4, v241\n\t"
    "v_accvgpr_read_b32 v237, a77\n\t"
    "s_waitcnt lgkmcnt(6)\n\t"
    "v_mfma_f32_32x32x16_bf16 a[160:175], v[96:99], v[196:199], a[160:175]\n\t"
    "ds_read_b64_tr_b16 v[212:213], v246 offset:45312\n\t"
    "v_lshlrev_b32 v247, 5, v247\n\t"
    "v_lshl_add_u32 v247, v241, 3, v247\n\t"
    "v_accvgpr_read_b32 v238, a78\n\t"
    "v_mfma_f32_32x32x16_bf16 a[176:191], v[100:103], v[196:199], a[176:191]\n\t"
    "ds_read_b64_tr_b16 v[214:215], v246 offset:45824\n\t"
    "s_lshr_b32 s58, s57, 3\n\t"
    "v_add_u32 v241, s58, v247\n\t"
    "v_accvgpr_read_b32 v239, a79\n\t"
    "s_waitcnt lgkmcnt(6)\n\t"
    "v_mfma_f32_32x32x16_bf16 a[128:143], v[104:107], v[200:203], a[128:143]\n\t"
    "ds_read_b64_tr_b16 v[216:217], v246 offset:47104\n\t"
    "ds_write_b64 v241, v[252:253] offset:0\n\t"
    "s_waitcnt lgkmcnt(0)\n\t"
    "v_cvt_pk_bf16_f32 v232, v232, v233\n\t"
    "v_mfma_f32_32x32x16_bf16 a[144:159], v[108:111], v[200:203], a[144:159]\n\t"
    "ds_read_b64_tr_b16 v[218:219], v246 offset:47616\n\t"
    "v_cvt_pk_bf16_f32 v233, v234, v235\n\t"
    "v_cvt_pk_bf16_f32 v234, v236, v237\n\t"
    "v_mfma_f32_32x32x16_bf16 a[160:175], v[104:107], v[204:207], a[160:175]\n\t"
    "ds_read_b64_tr_b16 v[220:221], v246 offset:47360\n\t"
    "v_cvt_pk_bf16_f32 v235, v238, v239\n\t"
    "v_mfma_f32_32x32x16_bf16 a[176:191], v[108:111], v[204:207], a[176:191]\n\t"
    "ds_read_b64_tr_b16 v[222:223], v246 offset:47872\n\t"
    "buffer_store_dwordx4 v[232:235], v240, s[48:51], s59 offen offset:1024 sc1\n\t"
    "s_barrier\n\t"
    "v_mfma_f32_32x32x16_bf16 a[128:143], v[112:115], v[208:211], a[128:143]\n\t"
    "ds_read_b64_tr_b16 v[192:193], v251 offset:0\n\t"
    "v_accvgpr_read_b32 v224, a80\n\t"
    "v_mfma_f32_32x32x16_bf16 a[144:159], v[116:119], v[208:211], a[144:159]\n\t"
    "ds_read_b64_tr_b16 v[194:195], v251 offset:512\n\t"
    "v_accvgpr_read_b32 v225, a81\n\t"
    "v_mfma_f32_32x32x16_bf16 a[160:175], v[112:115], v[212:215], a[160:175]\n\t"
    "ds_read_b64_tr_b16 v[196:197], v251 offset:256\n\t"
    "v_accvgpr_read_b32 v226, a82\n\t"
    "v_accvgpr_read_b32 v227, a83\n\t"
    "v_mfma_f32_32x32x16_bf16 a[176:191], v[116:119], v[212:215], a[176:191]\n\t"
    "ds_read_b64_tr_b16 v[198:199], v251 offset:768\n\t"
    "v_accvgpr_read_b32 v228, a84\n\t"
    "s_waitcnt lgkmcnt(6)\n\t"
    "v_mfma_f32_32x32x16_bf16 a[128:143], v[120:123], v[216:219], a[128:143]\n\t"
    "ds_read_b64_tr_b16 v[200:201], v251 offset:2048\n\t"
    "v_accvgpr_read_b32 v229, a85\n\t"
    "v_mfma_f32_32x32x16_bf16 a[144:159], v[124:127], v[216:219], a[144:159]\n\t"
    "ds_read_b64_tr_b16 v[202:203], v251 offset:2560\n\t"
    "v_accvgpr_read_b32 v230, a86\n\t"
    "s_waitcnt lgkmcnt(6)\n\t"
    "v_mfma_f32_32x32x16_bf16 a[160:175], v[120:123], v[220:223], a[160:175]\n\t"
    "ds_read_b64_tr_b16 v[204:205], v251 offset:2304\n\t"
    "v_accvgpr_read_b32 v231, a87\n\t"
    "v_mfma_f32_32x32x16_bf16 a[176:191], v[124:127], v[220:223], a[176:191]\n\t"
    "ds_read_b64_tr_b16 v[206:207], v251 offset:2816\n\t"
    "v_cvt_pk_bf16_f32 v224, v224, v225\n\t"
    "v_cvt_pk_bf16_f32 v225, v226, v227\n\t"
    "s_waitcnt lgkmcnt(6)\n\t"
    "v_mfma_f32_32x32x16_bf16 a[128:143], v[128:131], v[192:195], a[128:143]\n\t"
    "ds_read_b64_tr_b16 v[208:209], v251 offset:4096\n\t"
    "ds_read_b64 v[252:253], v247 offset:0\n\t"
    "ds_read_b64 v[248:249], v247 offset:128\n\t"
    "v_cvt_pk_bf16_f32 v226, v228, v229\n\t"
    "v_mfma_f32_32x32x16_bf16 a[144:159], v[132:135], v[192:195], a[144:159]\n\t"
    "ds_read_b64_tr_b16 v[210:211], v251 offset:4608\n\t"
    "ds_read_b64 v[242:243], v247 offset:256\n\t"
    "ds_read_b64 v[244:245], v247 offset:384\n\t"
    "v_cvt_pk_bf16_f32 v227, v230, v231\n\t"
    "s_waitcnt lgkmcnt(10)\n\t"
    "v_mfma_f32_32x32x16_bf16 a[160:175], v[128:131], v[196:199], a[160:175]\n\t"
    "ds_read_b64_tr_b16 v[212:213], v251 offset:4352\n\t"
    "buffer_store_dwordx4 v[224:227], v240, s[48:51], s61 offen offset:0 sc1\n\t"
    "v_mfma_f32_32x32x16_bf16 a[176:191], v[132:135], v[196:199], a[176:191]\n\t"
    "ds_read_b64_tr_b16 v[214:215], v251 offset:4864\n\t"
    "v_accvgpr_read_b32 v232, a88\n\t"
    "s_waitcnt lgkmcnt(10)\n\t"
    "v_mfma_f32_32x32x16_bf16 a[128:143], v[136:139], v[200:203], a[128:143]\n\t"
    "ds_read_b64_tr_b16 v[216:217], v251 offset:6144\n\t"
    "v_accvgpr_read_b32 v233, a89\n\t"
    "v_accvgpr_read_b32 v234, a90\n\t"
    "v_mfma_f32_32x32x16_bf16 a[144:159], v[140:143], v[200:203], a[144:159]\n\t"
    "ds_read_b64_tr_b16 v[218:219], v251 offset:6656\n\t"
    "v_accvgpr_read_b32 v235, a91\n\t"
    "s_waitcnt lgkmcnt(10)\n\t"
    "v_mfma_f32_32x32x16_bf16 a[160:175], v[136:139], v[204:207], a[160:175]\n\t"
    "ds_read_b64_tr_b16 v[220:221], v251 offset:6400\n\t"
    "v_accvgpr_read_b32 v236, a92\n\t"
    "v_mfma_f32_32x32x16_bf16 a[176:191], v[140:143], v[204:207], a[176:191]\n\t"
    "ds_read_b64_tr_b16 v[222:223], v251 offset:6912\n\t"
    "v_accvgpr_read_b32 v237, a93\n\t"
    "s_barrier\n\t"
    "s_waitcnt lgkmcnt(8)\n\t"
    "v_mfma_f32_32x32x16_bf16 a[128:143], v[144:147], v[208:211], a[128:143]\n\t"
    "ds_read_b64_tr_b16 v[192:193], v251 offset:24576\n\t"
    "s_waitcnt lgkmcnt(7)\n\t"
    "v_add_f32 v252, v252, v248\n\t"
    "v_accvgpr_read_b32 v238, a94\n\t"
    "v_mfma_f32_32x32x16_bf16 a[144:159], v[148:151], v[208:211], a[144:159]\n\t"
    "ds_read_b64_tr_b16 v[194:195], v251 offset:25088\n\t"
    "v_add_f32 v253, v253, v249\n\t"
    "v_add_f32 v242, v242, v244\n\t"
    "v_accvgpr_read_b32 v239, a95\n\t"
    "v_cvt_pk_bf16_f32 v232, v232, v233\n\t"
    "s_waitcnt lgkmcnt(6)\n\t"
    "v_mfma_f32_32x32x16_bf16 a[160:175], v[144:147], v[212:215], a[160:175]\n\t"
    "ds_read_b64_tr_b16 v[196:197], v251 offset:24832\n\t"
    "v_add_f32 v243, v243, v245\n\t"
    "v_add_f32 v252, v252, v242\n\t"
    "v_cvt_pk_bf16_f32 v233, v234, v235\n\t"
    "v_mfma_f32_32x32x16_bf16 a[176:191], v[148:151], v[212:215], a[176:191]\n\t"
    "ds_read_b64_tr_b16 v[198:199], v251 offset:25344\n\t"
    "v_add_f32 v253, v253, v243\n\t"
    "buffer_store_dwordx2 v[252:253], v247, s[68:71], 0 offen\n\t"
    "v_cvt_pk_bf16_f32 v234, v236, v237\n\t"
    "s_waitcnt lgkmcnt(6)\n\t"
    "v_mfma_f32_32x32x16_bf16 a[128:143], v[152:155], v[216:219], a[128:143]\n\t"
    "ds_read_b64_tr_b16 v[200:201], v251 offset:26624\n\t"
    "v_cvt_pk_bf16_f32 v235, v238, v239\n\t"
    "v_mfma_f32_32x32x16_bf16 a[144:159], v[156:159], v[216:219], a[144:159]\n\t"
    "ds_read_b64_tr_b16 v[202:203], v251 offset:27136\n\t"
    "buffer_store_dwordx4 v[232:235], v240, s[48:51], s61 offen offset:1024 sc1\n\t"
    "s_waitcnt lgkmcnt(6)\n\t"
    "v_mfma_f32_32x32x16_bf16 a[160:175], v[152:155], v[220:223], a[160:175]\n\t"
    "ds_read_b64_tr_b16 v[204:205], v251 offset:26880\n\t"
    "v_accvgpr_read_b32 v224, a96\n\t"
    "v_accvgpr_read_b32 v225, a97\n\t"
    "v_mfma_f32_32x32x16_bf16 a[176:191], v[156:159], v[220:223], a[176:191]\n\t"
    "ds_read_b64_tr_b16 v[206:207], v251 offset:27392\n\t"
    "v_accvgpr_read_b32 v226, a98\n\t"
    "s_waitcnt lgkmcnt(6)\n\t"
    "v_mfma_f32_32x32x16_bf16 a[128:143], v[160:163], v[192:195], a[128:143]\n\t"
    "ds_read_b64_tr_b16 v[208:209], v251 offset:28672\n\t"
    "v_accvgpr_read_b32 v227, a99\n\t"
    "v_mfma_f32_32x32x16_bf16 a[144:159], v[164:167], v[192:195], a[144:159]\n\t"
    "ds_read_b64_tr_b16 v[210:211], v251 offset:29184\n\t"
    "v_accvgpr_read_b32 v228, a100\n\t"
    "s_waitcnt lgkmcnt(6)\n\t"
    "v_mfma_f32_32x32x16_bf16 a[160:175], v[160:163], v[196:199], a[160:175]\n\t"
    "ds_read_b64_tr_b16 v[212:213], v251 offset:28928\n\t"
    "v_accvgpr_read_b32 v229, a101\n\t"
    "v_mfma_f32_32x32x16_bf16 a[176:191], v[164:167], v[196:199], a[176:191]\n\t"
    "ds_read_b64_tr_b16 v[214:215], v251 offset:29440\n\t"
    "v_accvgpr_read_b32 v230, a102\n\t"
    "v_accvgpr_read_b32 v231, a103\n\t"
    "s_waitcnt lgkmcnt(6)\n\t"
    "v_mfma_f32_32x32x16_bf16 a[128:143], v[168:171], v[200:203], a[128:143]\n\t"
    "ds_read_b64_tr_b16 v[216:217], v251 offset:30720\n\t"
    "v_cvt_pk_bf16_f32 v224, v224, v225\n\t"
    "v_mfma_f32_32x32x16_bf16 a[144:159], v[172:175], v[200:203], a[144:159]\n\t"
    "ds_read_b64_tr_b16 v[218:219], v251 offset:31232\n\t"
    "v_cvt_pk_bf16_f32 v225, v226, v227\n\t"
    "s_waitcnt lgkmcnt(6)\n\t"
    "v_mfma_f32_32x32x16_bf16 a[160:175], v[168:171], v[204:207], a[160:175]\n\t"
    "ds_read_b64_tr_b16 v[220:221], v251 offset:30976\n\t"
    "v_cvt_pk_bf16_f32 v226, v228, v229\n\t"
    "v_mfma_f32_32x32x16_bf16 a[176:191], v[172:175], v[204:207], a[176:191]\n\t"
    "ds_read_b64_tr_b16 v[222:223], v251 offset:31488\n\t"
    "v_cvt_pk_bf16_f32 v227, v230, v231\n\t"
    "s_waitcnt vmcnt(28)\n\t"
    "s_barrier\n\t"
    "s_waitcnt lgkmcnt(6)\n\t"
    "v_mfma_f32_32x32x16_bf16 a[128:143], v[176:179], v[208:211], a[128:143]\n\t"
    "ds_read_b64_tr_b16 v[192:193], v246 offset:0\n\t"
    "buffer_store_dwordx4 v[224:227], v240, s[48:51], s59 offen offset:2048 sc1\n\t"
    "v_accvgpr_read_b32 v232, a104\n\t"
    "v_mfma_f32_32x32x16_bf16 a[144:159], v[180:183], v[208:211], a[144:159]\n\t"
    "ds_read_b64_tr_b16 v[194:195], v246 offset:512\n\t"
    "v_accvgpr_read_b32 v233, a105\n\t"
    "s_waitcnt lgkmcnt(6)\n\t"
    "v_mfma_f32_32x32x16_bf16 a[160:175], v[176:179], v[212:215], a[160:175]\n\t"
    "ds_read_b64_tr_b16 v[196:197], v246 offset:256\n\t"
    "v_accvgpr_read_b32 v234, a106\n\t"
    "v_mfma_f32_32x32x16_bf16 a[176:191], v[180:183], v[212:215], a[176:191]\n\t"
    "ds_read_b64_tr_b16 v[198:199], v246 offset:768\n\t"
    "v_accvgpr_read_b32 v235, a107\n\t"
    "s_waitcnt lgkmcnt(6)\n\t"
    "v_mfma_f32_32x32x16_bf16 a[128:143], v[184:187], v[216:219], a[128:143]\n\t"
    "ds_read_b64_tr_b16 v[200:201], v246 offset:2048\n\t"
    "v_accvgpr_read_b32 v236, a108\n\t"
    "v_mfma_f32_32x32x16_bf16 a[144:159], v[188:191], v[216:219], a[144:159]\n\t"
    "ds_read_b64_tr_b16 v[202:203], v246 offset:2560\n\t"
    "v_accvgpr_read_b32 v237, a109\n\t"
    "v_accvgpr_read_b32 v238, a110\n\t"
    "s_waitcnt lgkmcnt(6)\n\t"
    "v_mfma_f32_32x32x16_bf16 a[160:175], v[184:187], v[220:223], a[160:175]\n\t"
    "ds_read_b64_tr_b16 v[204:205], v246 offset:2304\n\t"
    "v_accvgpr_read_b32 v239, a111\n\t"
    "v_mfma_f32_32x32x16_bf16 a[176:191], v[188:191], v[220:223], a[176:191]\n\t"
    "ds_read_b64_tr_b16 v[206:207], v246 offset:2816\n\t"
    "v_cvt_pk_bf16_f32 v232, v232, v233\n\t"
    "s_waitcnt lgkmcnt(6)\n\t"
    "v_mfma_f32_32x32x16_bf16 a[128:143], v[0:3], v[192:195], a[128:143]\n\t"
    "ds_read_b64_tr_b16 v[208:209], v246 offset:4096\n\t"
    "v_cvt_pk_bf16_f32 v233, v234, v235\n\t"
    "v_mfma_f32_32x32x16_bf16 a[144:159], v[4:7], v[192:195], a[144:159]\n\t"
    "ds_read_b64_tr_b16 v[210:211], v246 offset:4608\n\t"
    "v_cvt_pk_bf16_f32 v234, v236, v237\n\t"
    "s_waitcnt lgkmcnt(6)\n\t"
    "v_mfma_f32_32x32x16_bf16 a[160:175], v[0:3], v[196:199], a[160:175]\n\t"
    "ds_read_b64_tr_b16 v[212:213], v246 offset:4352\n\t"
    "v_cvt_pk_bf16_f32 v235, v238, v239\n\t"
    "buffer_store_dwordx4 v[232:235], v240, s[48:51], s59 offen offset:3072 sc1\n\t"
    "v_mfma_f32_32x32x16_bf16 a[176:191], v[4:7], v[196:199], a[176:191]\n\t"
    "ds_read_b64_tr_b16 v[214:215], v246 offset:4864\n\t"
    "v_accvgpr_read_b32 v224, a112\n\t"
    "s_waitcnt lgkmcnt(6)\n\t"
    "v_mfma_f32_32x32x16_bf16 a[128:143], v[8:11], v[200:203], a[128:143]\n\t"
    "ds_read_b64_tr_b16 v[216:217], v246 offset:6144\n\t"
    "v_accvgpr_read_b32 v225, a113\n\t"
    "v_mfma_f32_32x32x16_bf16 a[144:159], v[12:15], v[200:203], a[144:159]\n\t"
    "ds_read_b64_tr_b16 v[218:219], v246 offset:6656\n\t"
    "v_accvgpr_read_b32 v226, a114\n\t"
    "s_waitcnt lgkmcnt(6)\n\t"
    "v_mfma_f32_32x32x16_bf16 a[160:175], v[8:11], v[204:207], a[160:175]\n\t"
    "ds_read_b64_tr_b16 v[220:221], v246 offset:6400\n\t"
    "v_accvgpr_read_b32 v227, a115\n\t"
    "v_mfma_f32_32x32x16_bf16 a[176:191], v[12:15], v[204:207], a[176:191]\n\t"
    "ds_read_b64_tr_b16 v[222:223], v246 offset:6912\n\t"
    "v_accvgpr_read_b32 v228, a116\n\t"
    "v_accvgpr_read_b32 v229, a117\n\t"
    "s_waitcnt vmcnt(28)\n\t"
    "s_barrier\n\t"
    "s_waitcnt lgkmcnt(6)\n\t"
    "v_mfma_f32_32x32x16_bf16 a[128:143], v[16:19], v[208:211], a[128:143]\n\t"
    "ds_read_b64_tr_b16 v[192:193], v246 offset:24576\n\t"
    "v_accvgpr_read_b32 v230, a118\n\t"
    "v_mfma_f32_32x32x16_bf16 a[144:159], v[20:23], v[208:211], a[144:159]\n\t"
    "ds_read_b64_tr_b16 v[194:195], v246 offset:25088\n\t"
    "v_accvgpr_read_b32 v231, a119\n\t"
    "s_waitcnt lgkmcnt(6)\n\t"
    "v_mfma_f32_32x32x16_bf16 a[160:175], v[16:19], v[212:215], a[160:175]\n\t"
    "ds_read_b64_tr_b16 v[196:197], v246 offset:24832\n\t"
    "v_cvt_pk_bf16_f32 v224, v224, v225\n\t"
    "v_mfma_f32_32x32x16_bf16 a[176:191], v[20:23], v[212:215], a[176:191]\n\t"
    "ds_read_b64_tr_b16 v[198:199], v246 offset:25344\n\t"
    "v_cvt_pk_bf16_f32 v225, v226, v227\n\t"
    "s_waitcnt lgkmcnt(6)\n\t"
    "v_mfma_f32_32x32x16_bf16 a[128:143], v[24:27], v[216:219], a[128:143]\n\t"
    "ds_read_b64_tr_b16 v[200:201], v246 offset:26624\n\t"
    "v_cvt_pk_bf16_f32 v226, v228, v229\n\t"
    "v_cvt_pk_bf16_f32 v227, v230, v231\n\t"
    "v_mfma_f32_32x32x16_bf16 a[144:159], v[28:31], v[216:219], a[144:159]\n\t"
    "ds_read_b64_tr_b16 v[202:203], v246 offset:27136\n\t"
    "buffer_store_dwordx4 v[224:227], v240, s[48:51], s61 offen offset:2048 sc1\n\t"
    "s_waitcnt lgkmcnt(6)\n\t"
    "v_mfma_f32_32x32x16_bf16 a[160:175], v[24:27], v[220:223], a[160:175]\n\t"
    "ds_read_b64_tr_b16 v[204:205], v246 offset:26880\n\t"
    "v_accvgpr_read_b32 v232, a120\n\t"
    "v_mfma_f32_32x32x16_bf16 a[176:191], v[28:31], v[220:223], a[176:191]\n\t"
    "ds_read_b64_tr_b16 v[206:207], v246 offset:27392\n\t"
    "v_accvgpr_read_b32 v233, a121\n\t"
    "s_waitcnt lgkmcnt(6)\n\t"
    "v_mfma_f32_32x32x16_bf16 a[128:143], v[32:35], v[192:195], a[128:143]\n\t"
    "ds_read_b64_tr_b16 v[208:209], v246 offset:28672\n\t"
    "v_accvgpr_read_b32 v234, a122\n\t"
    "v_mfma_f32_32x32x16_bf16 a[144:159], v[36:39], v[192:195], a[144:159]\n\t"
    "ds_read_b64_tr_b16 v[210:211], v246 offset:29184\n\t"
    "v_accvgpr_read_b32 v235, a123\n\t"
    "v_accvgpr_read_b32 v236, a124\n\t"
    "s_waitcnt lgkmcnt(6)\n\t"
    "v_mfma_f32_32x32x16_bf16 a[160:175], v[32:35], v[196:199], a[160:175]\n\t"
    "ds_read_b64_tr_b16 v[212:213], v246 offset:28928\n\t"
    "v_accvgpr_read_b32 v237, a125\n\t"
    "v_mfma_f32_32x32x16_bf16 a[176:191], v[36:39], v[196:199], a[176:191]\n\t"
    "ds_read_b64_tr_b16 v[214:215], v246 offset:29440\n\t"
    "v_accvgpr_read_b32 v238, a126\n\t"
    "s_waitcnt lgkmcnt(6)\n\t"
    "v_mfma_f32_32x32x16_bf16 a[128:143], v[40:43], v[200:203], a[128:143]\n\t"
    "ds_read_b64_tr_b16 v[216:217], v246 offset:30720\n\t"
    "v_accvgpr_read_b32 v239, a127\n\t"
    "v_mfma_f32_32x32x16_bf16 a[144:159], v[44:47], v[200:203], a[144:159]\n\t"
    "ds_read_b64_tr_b16 v[218:219], v246 offset:31232\n\t"
    "v_cvt_pk_bf16_f32 v232, v232, v233\n\t"
    "s_waitcnt lgkmcnt(6)\n\t"
    "v_mfma_f32_32x32x16_bf16 a[160:175], v[40:43], v[204:207], a[160:175]\n\t"
    "ds_read_b64_tr_b16 v[220:221], v246 offset:30976\n\t"
    "v_cvt_pk_bf16_f32 v233, v234, v235\n\t"
    "v_cvt_pk_bf16_f32 v234, v236, v237\n\t"
    "v_mfma_f32_32x32x16_bf16 a[176:191], v[44:47], v[204:207], a[176:191]\n\t"
    "ds_read_b64_tr_b16 v[222:223], v246 offset:31488\n\t"
    "v_cvt_pk_bf16_f32 v235, v238, v239\n\t"
    "s_waitcnt vmcnt(27)\n\t"
    "s_barrier\n\t"
    "s_waitcnt lgkmcnt(6)\n\t"
    "v_mfma_f32_32x32x16_bf16 a[128:143], v[48:51], v[208:211], a[128:143]\n\t"
    "ds_read_b64_tr_b16 v[192:193], v246 offset:49152\n\t"
    "buffer_store_dwordx4 v[232:235], v240, s[48:51], s61 offen offset:3072 sc1\n\t"
    "v_mfma_f32_32x32x16_bf16 a[144:159], v[52:55], v[208:211], a[144:159]\n\t"
    "ds_read_b64_tr_b16 v[194:195], v246 offset:49664\n\t"
    "s_waitcnt lgkmcnt(6)\n\t"
    "v_mfma_f32_32x32x16_bf16 a[160:175], v[48:51], v[212:215], a[160:175]\n\t"
    "ds_read_b64_tr_b16 v[196:197], v246 offset:49408\n\t"
    "v_mfma_f32_32x32x16_bf16 a[176:191], v[52:55], v[212:215], a[176:191]\n\t"
    "ds_read_b64_tr_b16 v[198:199], v246 offset:49920\n\t"
    "s_waitcnt lgkmcnt(6)\n\t"
    "v_mfma_f32_32x32x16_bf16 a[128:143], v[56:59], v[216:219], a[128:143]\n\t"
    "ds_read_b64_tr_b16 v[200:201], v246 offset:51200\n\t"
    "v_mfma_f32_32x32x16_bf16 a[144:159], v[60:63], v[216:219], a[144:159]\n\t"
    "ds_read_b64_tr_b16 v[202:203], v246 offset:51712\n\t"
    "s_waitcnt lgkmcnt(6)\n\t"
    "v_mfma_f32_32x32x16_bf16 a[160:175], v[56:59], v[220:223], a[160:175]\n\t"
    "ds_read_b64_tr_b16 v[204:205], v246 offset:51456\n\t"
    "v_mfma_f32_32x32x16_bf16 a[176:191], v[60:63], v[220:223], a[176:191]\n\t"
    "ds_read_b64_tr_b16 v[206:207], v246 offset:51968\n\t"
    "s_waitcnt lgkmcnt(6)\n\t"
    "v_mfma_f32_32x32x16_bf16 a[192:207], v[64:67], v[192:195], a[192:207]\n\t"
    "ds_read_b64_tr_b16 v[208:209], v246 offset:53248\n\t"
    "v_mfma_f32_32x32x16_bf16 a[208:223], v[68:71], v[192:195], a[208:223]\n\t"
    "ds_read_b64_tr_b16 v[210:211], v246 offset:53760\n\t"
    "s_waitcnt lgkmcnt(6)\n\t"
    "v_mfma_f32_32x32x16_bf16 a[224:239], v[64:67], v[196:199], a[224:239]\n\t"
    "ds_read_b64_tr_b16 v[212:213], v246 offset:53504\n\t"
    "v_accvgpr_read_b32 v224, a128\n\t"
    "v_mfma_f32_32x32x16_bf16 a[240:255], v[68:71], v[196:199], a[240:255]\n\t"
    "ds_read_b64_tr_b16 v[214:215], v246 offset:54016\n\t"
    "v_accvgpr_read_b32 v225, a129\n\t"
    "s_waitcnt lgkmcnt(6)\n\t"
    "v_mfma_f32_32x32x16_bf16 a[192:207], v[72:75], v[200:203], a[192:207]\n\t"
    "ds_read_b64_tr_b16 v[216:217], v246 offset:55296\n\t"
    "v_accvgpr_read_b32 v226, a130\n\t"
    "v_mfma_f32_32x32x16_bf16 a[208:223], v[76:79], v[200:203], a[208:223]\n\t"
    "ds_read_b64_tr_b16 v[218:219], v246 offset:55808\n\t"
    "v_accvgpr_read_b32 v227, a131\n\t"
    "s_waitcnt lgkmcnt(6)\n\t"
    "v_mfma_f32_32x32x16_bf16 a[224:239], v[72:75], v[204:207], a[224:239]\n\t"
    "ds_read_b64_tr_b16 v[220:221], v246 offset:55552\n\t"
    "v_accvgpr_read_b32 v228, a132\n\t"
    "v_accvgpr_read_b32 v229, a133\n\t"
    "v_mfma_f32_32x32x16_bf16 a[240:255], v[76:79], v[204:207], a[240:255]\n\t"
    "ds_read_b64_tr_b16 v[222:223], v246 offset:56064\n\t"
    "v_accvgpr_read_b32 v230, a134\n\t"
    "s_waitcnt vmcnt(26)\n\t"
    "s_barrier\n\t"
    "s_waitcnt lgkmcnt(6)\n\t"
    "v_mfma_f32_32x32x16_bf16 a[192:207], v[80:83], v[208:211], a[192:207]\n\t"
    "ds_read_b64_tr_b16 v[192:193], v251 offset:8192\n\t"
    "v_accvgpr_read_b32 v231, a135\n\t"
    "v_mfma_f32_32x32x16_bf16 a[208:223], v[84:87], v[208:211], a[208:223]\n\t"
    "ds_read_b64_tr_b16 v[194:195], v251 offset:8704\n\t"
    "v_cvt_pk_bf16_f32 v224, v224, v225\n\t"
    "s_waitcnt lgkmcnt(6)\n\t"
    "v_mfma_f32_32x32x16_bf16 a[224:239], v[80:83], v[212:215], a[224:239]\n\t"
    "ds_read_b64_tr_b16 v[196:197], v251 offset:8448\n\t"
    "v_cvt_pk_bf16_f32 v225, v226, v227\n\t"
    "v_mfma_f32_32x32x16_bf16 a[240:255], v[84:87], v[212:215], a[240:255]\n\t"
    "ds_read_b64_tr_b16 v[198:199], v251 offset:8960\n\t"
    "v_cvt_pk_bf16_f32 v226, v228, v229\n\t"
    "v_cvt_pk_bf16_f32 v227, v230, v231\n\t"
    "s_waitcnt lgkmcnt(6)\n\t"
    "v_mfma_f32_32x32x16_bf16 a[192:207], v[88:91], v[216:219], a[192:207]\n\t"
    "ds_read_b64_tr_b16 v[200:201], v251 offset:10240\n\t"
    "buffer_store_dwordx4 v[224:227], v240, s[52:55], 0 offen offset:0 sc1\n\t"
    "v_mfma_f32_32x32x16_bf16 a[208:223], v[92:95], v[216:219], a[208:223]\n\t"
    "ds_read_b64_tr_b16 v[202:203], v251 offset:10752\n\t"
    "v_accvgpr_read_b32 v232, a136\n\t"
    "s_waitcnt lgkmcnt(6)\n\t"
    "v_mfma_f32_32x32x16_bf16 a[224:239], v[88:91], v[220:223], a[224:239]\n\t"
    "ds_read_b64_tr_b16 v[204:205], v251 offset:10496\n\t"
    "v_accvgpr_read_b32 v233, a137\n\t"
    "v_mfma_f32_32x32x16_bf16 a[240:255], v[92:95], v[220:223], a[240:255]\n\t"
    "ds_read_b64_tr_b16 v[206:207], v251 offset:11008\n\t"
    "v_accvgpr_read_b32 v234, a138\n\t"
    "s_waitcnt lgkmcnt(6)\n\t"
    "v_mfma_f32_32x32x16_bf16 a[192:207], v[96:99], v[192:195], a[192:207]\n\t"
    "ds_read_b64_tr_b16 v[208:209], v251 offset:12288\n\t"
    "v_accvgpr_read_b32 v235, a139\n\t"
    "v_accvgpr_read_b32 v236, a140\n\t"
    "v_mfma_f32_32x32x16_bf16 a[208:223], v[100:103], v[192:195], a[208:223]\n\t"
    "ds_read_b64_tr_b16 v[210:211], v251 offset:12800\n\t"
    "v_accvgpr_read_b32 v237, a141\n\t"
    "s_waitcnt lgkmcnt(6)\n\t"
    "v_mfma_f32_32x32x16_bf16 a[224:239], v[96:99], v[196:199], a[224:239]\n\t"
    "ds_read_b64_tr_b16 v[212:213], v251 offset:12544\n\t"
    "v_accvgpr_read_b32 v238, a142\n\t"
    "v_mfma_f32_32x32x16_bf16 a[240:255], v[100:103], v[196:199], a[240:255]\n\t"
    "ds_read_b64_tr_b16 v[214:215], v251 offset:13056\n\t"
    "v_accvgpr_read_b32 v239, a143\n\t"
    "s_waitcnt lgkmcnt(6)\n\t"
    "v_mfma_f32_32x32x16_bf16 a[192:207], v[104:107], v[200:203], a[192:207]\n\t"
    "ds_read_b64_tr_b16 v[216:217], v251 offset:14336\n\t"
    "v_cvt_pk_bf16_f32 v232, v232, v233\n\t"
    "v_mfma_f32_32x32x16_bf16 a[208:223], v[108:111], v[200:203], a[208:223]\n\t"
    "ds_read_b64_tr_b16 v[218:219], v251 offset:14848\n\t"
    "v_cvt_pk_bf16_f32 v233, v234, v235\n\t"
    "v_cvt_pk_bf16_f32 v234, v236, v237\n\t"
    "s_waitcnt lgkmcnt(6)\n\t"
    "v_mfma_f32_32x32x16_bf16 a[224:239], v[104:107], v[204:207], a[224:239]\n\t"
    "ds_read_b64_tr_b16 v[220:221], v251 offset:14592\n\t"
    "v_cvt_pk_bf16_f32 v235, v238, v239\n\t"
    "v_mfma_f32_32x32x16_bf16 a[240:255], v[108:111], v[204:207], a[240:255]\n\t"
    "ds_read_b64_tr_b16 v[222:223], v251 offset:15104\n\t"
    "buffer_store_dwordx4 v[232:235], v240, s[52:55], 0 offen offset:1024 sc1\n\t"
    "s_waitcnt vmcnt(26)\n\t"
    "s_barrier\n\t"
    "s_waitcnt lgkmcnt(6)\n\t"
    "v_mfma_f32_32x32x16_bf16 a[192:207], v[112:115], v[208:211], a[192:207]\n\t"
    "ds_read_b64_tr_b16 v[192:193], v246 offset:8192\n\t"
    "v_accvgpr_read_b32 v224, a144\n\t"
    "v_mfma_f32_32x32x16_bf16 a[208:223], v[116:119], v[208:211], a[208:223]\n\t"
    "ds_read_b64_tr_b16 v[194:195], v246 offset:8704\n\t"
    "v_accvgpr_read_b32 v225, a145\n\t"
    "s_waitcnt lgkmcnt(6)\n\t"
    "v_mfma_f32_32x32x16_bf16 a[224:239], v[112:115], v[212:215], a[224:239]\n\t"
    "ds_read_b64_tr_b16 v[196:197], v246 offset:8448\n\t"
    "v_accvgpr_read_b32 v226, a146\n\t"
    "v_accvgpr_read_b32 v227, a147\n\t"
    "v_mfma_f32_32x32x16_bf16 a[240:255], v[116:119], v[212:215], a[240:255]\n\t"
    "ds_read_b64_tr_b16 v[198:199], v246 offset:8960\n\t"
    "v_accvgpr_read_b32 v228, a148\n\t"
    "s_waitcnt lgkmcnt(6)\n\t"
    "v_mfma_f32_32x32x16_bf16 a[192:207], v[120:123], v[216:219], a[192:207]\n\t"
    "ds_read_b64_tr_b16 v[200:201], v246 offset:10240\n\t"
    "v_accvgpr_read_b32 v229, a149\n\t"
    "v_mfma_f32_32x32x16_bf16 a[208:223], v[124:127], v[216:219], a[208:223]\n\t"
    "ds_read_b64_tr_b16 v[202:203], v246 offset:10752\n\t"
    "v_accvgpr_read_b32 v230, a150\n\t"
    "s_waitcnt lgkmcnt(6)\n\t"
    "v_mfma_f32_32x32x16_bf16 a[224:239], v[120:123], v[220:223], a[224:239]\n\t"
    "ds_read_b64_tr_b16 v[204:205], v246 offset:10496\n\t"
    "v_accvgpr_read_b32 v231, a151\n\t"
    "v_mfma_f32_32x32x16_bf16 a[240:255], v[124:127], v[220:223], a[240:255]\n\t"
    "ds_read_b64_tr_b16 v[206:207], v246 offset:11008\n\t"
    "v_cvt_pk_bf16_f32 v224, v224, v225\n\t"
    "v_cvt_pk_bf16_f32 v225, v226, v227\n\t"
    "s_waitcnt lgkmcnt(6)\n\t"
    "v_mfma_f32_32x32x16_bf16 a[192:207], v[128:131], v[192:195], a[192:207]\n\t"
    "ds_read_b64_tr_b16 v[208:209], v246 offset:12288\n\t"
    "v_cvt_pk_bf16_f32 v226, v228, v229\n\t"
    "v_mfma_f32_32x32x16_bf16 a[208:223], v[132:135], v[192:195], a[208:223]\n\t"
    "ds_read_b64_tr_b16 v[210:211], v246 offset:12800\n\t"
    "v_cvt_pk_bf16_f32 v227, v230, v231\n\t"
    "s_waitcnt lgkmcnt(6)\n\t"
    "v_mfma_f32_32x32x16_bf16 a[224:239], v[128:131], v[196:199], a[224:239]\n\t"
    "ds_read_b64_tr_b16 v[212:213], v246 offset:12544\n\t"
    "buffer_store_dwordx4 v[224:227], v240, s[52:55], s60 offen offset:0 sc1\n\t"
    "v_mfma_f32_32x32x16_bf16 a[240:255], v[132:135], v[196:199], a[240:255]\n\t"
    "ds_read_b64_tr_b16 v[214:215], v246 offset:13056\n\t"
    "v_accvgpr_read_b32 v232, a152\n\t"
    "s_waitcnt lgkmcnt(6)\n\t"
    "v_mfma_f32_32x32x16_bf16 a[192:207], v[136:139], v[200:203], a[192:207]\n\t"
    "ds_read_b64_tr_b16 v[216:217], v246 offset:14336\n\t"
    "v_accvgpr_read_b32 v233, a153\n\t"
    "v_accvgpr_read_b32 v234, a154\n\t"
    "v_mfma_f32_32x32x16_bf16 a[208:223], v[140:143], v[200:203], a[208:223]\n\t"
    "ds_read_b64_tr_b16 v[218:219], v246 offset:14848\n\t"
    "v_accvgpr_read_b32 v235, a155\n\t"
    "s_waitcnt lgkmcnt(6)\n\t"
    "v_mfma_f32_32x32x16_bf16 a[224:239], v[136:139], v[204:207], a[224:239]\n\t"
    "ds_read_b64_tr_b16 v[220:221], v246 offset:14592\n\t"
    "v_accvgpr_read_b32 v236, a156\n\t"
    "v_mfma_f32_32x32x16_bf16 a[240:255], v[140:143], v[204:207], a[240:255]\n\t"
    "ds_read_b64_tr_b16 v[222:223], v246 offset:15104\n\t"
    "v_accvgpr_read_b32 v237, a157\n\t"
    "s_waitcnt vmcnt(25)\n\t"
    "s_barrier\n\t"
    "s_waitcnt lgkmcnt(6)\n\t"
    "v_mfma_f32_32x32x16_bf16 a[192:207], v[144:147], v[208:211], a[192:207]\n\t"
    "ds_read_b64_tr_b16 v[192:193], v246 offset:32768\n\t"
    "v_accvgpr_read_b32 v238, a158\n\t"
    "v_mfma_f32_32x32x16_bf16 a[208:223], v[148:151], v[208:211], a[208:223]\n\t"
    "ds_read_b64_tr_b16 v[194:195], v246 offset:33280\n\t"
    "v_accvgpr_read_b32 v239, a159\n\t"
    "v_cvt_pk_bf16_f32 v232, v232, v233\n\t"
    "s_waitcnt lgkmcnt(6)\n\t"
    "v_mfma_f32_32x32x16_bf16 a[224:239], v[144:147], v[212:215], a[224:239]\n\t"
    "ds_read_b64_tr_b16 v[196:197], v246 offset:33024\n\t"
    "v_cvt_pk_bf16_f32 v233, v234, v235\n\t"
    "v_mfma_f32_32x32x16_bf16 a[240:255], v[148:151], v[212:215], a[240:255]\n\t"
    "ds_read_b64_tr_b16 v[198:199], v246 offset:33536\n\t"
    "v_cvt_pk_bf16_f32 v234, v236, v237\n\t"
    "s_waitcnt lgkmcnt(6)\n\t"
    "v_mfma_f32_32x32x16_bf16 a[192:207], v[152:155], v[216:219], a[192:207]\n\t"
    "ds_read_b64_tr_b16 v[200:201], v246 offset:34816\n\t"
    "v_cvt_pk_bf16_f32 v235, v238, v239\n\t"
    "v_mfma_f32_32x32x16_bf16 a[208:223], v[156:159], v[216:219], a[208:223]\n\t"
    "ds_read_b64_tr_b16 v[202:203], v246 offset:35328\n\t"
    "buffer_store_dwordx4 v[232:235], v240, s[52:55], s60 offen offset:1024 sc1\n\t"
    "s_waitcnt lgkmcnt(6)\n\t"
    "v_mfma_f32_32x32x16_bf16 a[224:239], v[152:155], v[220:223], a[224:239]\n\t"
    "ds_read_b64_tr_b16 v[204:205], v246 offset:35072\n\t"
    "v_accvgpr_read_b32 v224, a160\n\t"
    "v_accvgpr_read_b32 v225, a161\n\t"
    "v_mfma_f32_32x32x16_bf16 a[240:255], v[156:159], v[220:223], a[240:255]\n\t"
    "ds_read_b64_tr_b16 v[206:207], v246 offset:35584\n\t"
    "v_accvgpr_read_b32 v226, a162\n\t"
    "s_waitcnt lgkmcnt(6)\n\t"
    "v_mfma_f32_32x32x16_bf16 a[192:207], v[160:163], v[192:195], a[192:207]\n\t"
    "ds_read_b64_tr_b16 v[208:209], v246 offset:36864\n\t"
    "v_accvgpr_read_b32 v227, a163\n\t"
    "v_mfma_f32_32x32x16_bf16 a[208:223], v[164:167], v[192:195], a[208:223]\n\t"
    "ds_read_b64_tr_b16 v[210:211], v246 offset:37376\n\t"
    "v_accvgpr_read_b32 v228, a164\n\t"
    "s_waitcnt lgkmcnt(6)\n\t"
    "v_mfma_f32_32x32x16_bf16 a[224:239], v[160:163], v[196:199], a[224:239]\n\t"
    "ds_read_b64_tr_b16 v[212:213], v246 offset:37120\n\t"
    "v_accvgpr_read_b32 v229, a165\n\t"
    "v_mfma_f32_32x32x16_bf16 a[240:255], v[164:167], v[196:199], a[240:255]\n\t"
    "ds_read_b64_tr_b16 v[214:215], v246 offset:37632\n\t"
    "v_accvgpr_read_b32 v230, a166\n\t"
    "v_accvgpr_read_b32 v231, a167\n\t"
    "s_waitcnt lgkmcnt(6)\n\t"
    "v_mfma_f32_32x32x16_bf16 a[192:207], v[168:171], v[200:203], a[192:207]\n\t"
    "ds_read_b64_tr_b16 v[216:217], v246 offset:38912\n\t"
    "v_cvt_pk_bf16_f32 v224, v224, v225\n\t"
    "v_mfma_f32_32x32x16_bf16 a[208:223], v[172:175], v[200:203], a[208:223]\n\t"
    "ds_read_b64_tr_b16 v[218:219], v246 offset:39424\n\t"
    "v_cvt_pk_bf16_f32 v225, v226, v227\n\t"
    "s_waitcnt lgkmcnt(6)\n\t"
    "v_mfma_f32_32x32x16_bf16 a[224:239], v[168:171], v[204:207], a[224:239]\n\t"
    "ds_read_b64_tr_b16 v[220:221], v246 offset:39168\n\t"
    "v_cvt_pk_bf16_f32 v226, v228, v229\n\t"
    "v_mfma_f32_32x32x16_bf16 a[240:255], v[172:175], v[204:207], a[240:255]\n\t"
    "ds_read_b64_tr_b16 v[222:223], v246 offset:39680\n\t"
    "v_cvt_pk_bf16_f32 v227, v230, v231\n\t"
    "s_waitcnt vmcnt(24)\n\t"
    "s_barrier\n\t"
    "s_waitcnt lgkmcnt(6)\n\t"
    "v_mfma_f32_32x32x16_bf16 a[192:207], v[176:179], v[208:211], a[192:207]\n\t"
    "ds_read_b64_tr_b16 v[192:193], v246 offset:57344\n\t"
    "buffer_store_dwordx4 v[224:227], v240, s[52:55], 0 offen offset:2048 sc1\n\t"
    "v_accvgpr_read_b32 v232, a168\n\t"
    "v_mfma_f32_32x32x16_bf16 a[208:223], v[180:183], v[208:211], a[208:223]\n\t"
    "ds_read_b64_tr_b16 v[194:195], v246 offset:57856\n\t"
    "v_accvgpr_read_b32 v233, a169\n\t"
    "s_waitcnt lgkmcnt(6)\n\t"
    "v_mfma_f32_32x32x16_bf16 a[224:239], v[176:179], v[212:215], a[224:239]\n\t"
    "ds_read_b64_tr_b16 v[196:197], v246 offset:57600\n\t"
    "v_accvgpr_read_b32 v234, a170\n\t"
    "v_mfma_f32_32x32x16_bf16 a[240:255], v[180:183], v[212:215], a[240:255]\n\t"
    "ds_read_b64_tr_b16 v[198:199], v246 offset:58112\n\t"
    "v_accvgpr_read_b32 v235, a171\n\t"
    "s_waitcnt lgkmcnt(6)\n\t"
    "v_mfma_f32_32x32x16_bf16 a[192:207], v[184:187], v[216:219], a[192:207]\n\t"
    "ds_read_b64_tr_b16 v[200:201], v246 offset:59392\n\t"
    "v_accvgpr_read_b32 v236, a172\n\t"
    "v_mfma_f32_32x32x16_bf16 a[208:223], v[188:191], v[216:219], a[208:223]\n\t"
    "ds_read_b64_tr_b16 v[202:203], v246 offset:59904\n\t"
    "v_accvgpr_read_b32 v237, a173\n\t"
    "v_accvgpr_read_b32 v238, a174\n\t"
    "s_waitcnt lgkmcnt(6)\n\t"
    "v_mfma_f32_32x32x16_bf16 a[224:239], v[184:187], v[220:223], a[224:239]\n\t"
    "ds_read_b64_tr_b16 v[204:205], v246 offset:59648\n\t"
    "v_accvgpr_read_b32 v239, a175\n\t"
    "v_mfma_f32_32x32x16_bf16 a[240:255], v[188:191], v[220:223], a[240:255]\n\t"
    "ds_read_b64_tr_b16 v[206:207], v246 offset:60160\n\t"
    "v_cvt_pk_bf16_f32 v232, v232, v233\n\t"
    "s_waitcnt lgkmcnt(6)\n\t"
    "v_mfma_f32_32x32x16_bf16 a[192:207], v[0:3], v[192:195], a[192:207]\n\t"
    "ds_read_b64_tr_b16 v[208:209], v246 offset:61440\n\t"
    "v_cvt_pk_bf16_f32 v233, v234, v235\n\t"
    "v_mfma_f32_32x32x16_bf16 a[208:223], v[4:7], v[192:195], a[208:223]\n\t"
    "ds_read_b64_tr_b16 v[210:211], v246 offset:61952\n\t"
    "v_cvt_pk_bf16_f32 v234, v236, v237\n\t"
    "s_waitcnt lgkmcnt(6)\n\t"
    "v_mfma_f32_32x32x16_bf16 a[224:239], v[0:3], v[196:199], a[224:239]\n\t"
    "ds_read_b64_tr_b16 v[212:213], v246 offset:61696\n\t"
    "v_cvt_pk_bf16_f32 v235, v238, v239\n\t"
    "buffer_store_dwordx4 v[232:235], v240, s[52:55], 0 offen offset:3072 sc1\n\t"
    "v_mfma_f32_32x32x16_bf16 a[240:255], v[4:7], v[196:199], a[240:255]\n\t"
    "ds_read_b64_tr_b16 v[214:215], v246 offset:62208\n\t"
    "v_accvgpr_read_b32 v224, a176\n\t"
    "s_waitcnt lgkmcnt(6)\n\t"
    "v_mfma_f32_32x32x16_bf16 a[192:207], v[8:11], v[200:203], a[192:207]\n\t"
    "ds_read_b64_tr_b16 v[216:217], v246 offset:63488\n\t"
    "v_accvgpr_read_b32 v225, a177\n\t"
    "v_mfma_f32_32x32x16_bf16 a[208:223], v[12:15], v[200:203], a[208:223]\n\t"
    "ds_read_b64_tr_b16 v[218:219], v246 offset:64000\n\t"
    "v_accvgpr_read_b32 v226, a178\n\t"
    "s_waitcnt lgkmcnt(6)\n\t"
    "v_mfma_f32_32x32x16_bf16 a[224:239], v[8:11], v[204:207], a[224:239]\n\t"
    "ds_read_b64_tr_b16 v[220:221], v246 offset:63744\n\t"
    "v_accvgpr_read_b32 v227, a179\n\t"
    "v_mfma_f32_32x32x16_bf16 a[240:255], v[12:15], v[204:207], a[240:255]\n\t"
    "ds_read_b64_tr_b16 v[222:223], v246 offset:64256\n\t"
    "v_accvgpr_read_b32 v228, a180\n\t"
    "v_accvgpr_read_b32 v229, a181\n\t"
    "s_waitcnt vmcnt(24)\n\t"
    "s_barrier\n\t"
    "s_waitcnt lgkmcnt(6)\n\t"
    "v_mfma_f32_32x32x16_bf16 a[192:207], v[16:19], v[208:211], a[192:207]\n\t"
    "ds_read_b64_tr_b16 v[192:193], v251 offset:16384\n\t"
    "v_accvgpr_read_b32 v230, a182\n\t"
    "v_mfma_f32_32x32x16_bf16 a[208:223], v[20:23], v[208:211], a[208:223]\n\t"
    "ds_read_b64_tr_b16 v[194:195], v251 offset:16896\n\t"
    "v_accvgpr_read_b32 v231, a183\n\t"
    "s_waitcnt lgkmcnt(6)\n\t"
    "v_mfma_f32_32x32x16_bf16 a[224:239], v[16:19], v[212:215], a[224:239]\n\t"
    "ds_read_b64_tr_b16 v[196:197], v251 offset:16640\n\t"
    "v_cvt_pk_bf16_f32 v224, v224, v225\n\t"
    "v_mfma_f32_32x32x16_bf16 a[240:255], v[20:23], v[212:215], a[240:255]\n\t"
    "ds_read_b64_tr_b16 v[198:199], v251 offset:17152\n\t"
    "v_cvt_pk_bf16_f32 v225, v226, v227\n\t"
    "s_waitcnt lgkmcnt(6)\n\t"
    "v_mfma_f32_32x32x16_bf16 a[192:207], v[24:27], v[216:219], a[192:207]\n\t"
    "ds_read_b64_tr_b16 v[200:201], v251 offset:18432\n\t"
    "v_cvt_pk_bf16_f32 v226, v228, v229\n\t"
    "v_cvt_pk_bf16_f32 v227, v230, v231\n\t"
    "v_mfma_f32_32x32x16_bf16 a[208:223], v[28:31], v[216:219], a[208:223]\n\t"
    "ds_read_b64_tr_b16 v[202:203], v251 offset:18944\n\t"
    "buffer_store_dwordx4 v[224:227], v240, s[52:55], s60 offen offset:2048 sc1\n\t"
    "s_waitcnt lgkmcnt(6)\n\t"
    "v_mfma_f32_32x32x16_bf16 a[224:239], v[24:27], v[220:223], a[224:239]\n\t"
    "ds_read_b64_tr_b16 v[204:205], v251 offset:18688\n\t"
    "v_accvgpr_read_b32 v232, a184\n\t"
    "v_mfma_f32_32x32x16_bf16 a[240:255], v[28:31], v[220:223], a[240:255]\n\t"
    "ds_read_b64_tr_b16 v[206:207], v251 offset:19200\n\t"
    "v_accvgpr_read_b32 v233, a185\n\t"
    "s_waitcnt lgkmcnt(6)\n\t"
    "v_mfma_f32_32x32x16_bf16 a[192:207], v[32:35], v[192:195], a[192:207]\n\t"
    "ds_read_b64_tr_b16 v[208:209], v251 offset:20480\n\t"
    "v_accvgpr_read_b32 v234, a186\n\t"
    "v_mfma_f32_32x32x16_bf16 a[208:223], v[36:39], v[192:195], a[208:223]\n\t"
    "ds_read_b64_tr_b16 v[210:211], v251 offset:20992\n\t"
    "v_accvgpr_read_b32 v235, a187\n\t"
    "v_accvgpr_read_b32 v236, a188\n\t"
    "s_waitcnt lgkmcnt(6)\n\t"
    "v_mfma_f32_32x32x16_bf16 a[224:239], v[32:35], v[196:199], a[224:239]\n\t"
    "ds_read_b64_tr_b16 v[212:213], v251 offset:20736\n\t"
    "v_accvgpr_read_b32 v237, a189\n\t"
    "v_mfma_f32_32x32x16_bf16 a[240:255], v[36:39], v[196:199], a[240:255]\n\t"
    "ds_read_b64_tr_b16 v[214:215], v251 offset:21248\n\t"
    "v_accvgpr_read_b32 v238, a190\n\t"
    "s_waitcnt lgkmcnt(6)\n\t"
    "v_mfma_f32_32x32x16_bf16 a[192:207], v[40:43], v[200:203], a[192:207]\n\t"
    "ds_read_b64_tr_b16 v[216:217], v251 offset:22528\n\t"
    "v_accvgpr_read_b32 v239, a191\n\t"
    "v_mfma_f32_32x32x16_bf16 a[208:223], v[44:47], v[200:203], a[208:223]\n\t"
    "ds_read_b64_tr_b16 v[218:219], v251 offset:23040\n\t"
    "v_cvt_pk_bf16_f32 v232, v232, v233\n\t"
    "s_waitcnt lgkmcnt(6)\n\t"
    "v_mfma_f32_32x32x16_bf16 a[224:239], v[40:43], v[204:207], a[224:239]\n\t"
    "ds_read_b64_tr_b16 v[220:221], v251 offset:22784\n\t"
    "v_cvt_pk_bf16_f32 v233, v234, v235\n\t"
    "v_cvt_pk_bf16_f32 v234, v236, v237\n\t"
    "v_mfma_f32_32x32x16_bf16 a[240:255], v[44:47], v[204:207], a[240:255]\n\t"
    "ds_read_b64_tr_b16 v[222:223], v251 offset:23296\n\t"
    "v_cvt_pk_bf16_f32 v235, v238, v239\n\t"
    "s_waitcnt lgkmcnt(6)\n\t"
    "v_mfma_f32_32x32x16_bf16 a[192:207], v[48:51], v[208:211], a[192:207]\n\t"
    "buffer_store_dwordx4 v[232:235], v240, s[52:55], s60 offen offset:3072 sc1\n\t"
    "v_mfma_f32_32x32x16_bf16 a[208:223], v[52:55], v[208:211], a[208:223]\n\t"
    "s_waitcnt lgkmcnt(4)\n\t"
    "v_mfma_f32_32x32x16_bf16 a[224:239], v[48:51], v[212:215], a[224:239]\n\t"
    "v_mfma_f32_32x32x16_bf16 a[240:255], v[52:55], v[212:215], a[240:255]\n\t"
    "s_waitcnt lgkmcnt(2)\n\t"
    "v_mfma_f32_32x32x16_bf16 a[192:207], v[56:59], v[216:219], a[192:207]\n\t"
    "v_mfma_f32_32x32x16_bf16 a[208:223], v[60:63], v[216:219], a[208:223]\n\t"
    "s_waitcnt lgkmcnt(0)\n\t"
    "v_mfma_f32_32x32x16_bf16 a[224:239], v[56:59], v[220:223], a[224:239]\n\t"
    "v_mfma_f32_32x32x16_bf16 a[240:255], v[60:63], v[220:223], a[240:255]\n\t"
    "s_nop 7\n\t"
    "s_nop 1\n\t"
    "v_accvgpr_read_b32 v224, a192\n\t"
    "v_accvgpr_read_b32 v225, a193\n\t"
    "v_accvgpr_read_b32 v226, a194\n\t"
    "v_accvgpr_read_b32 v227, a195\n\t"
    "v_accvgpr_read_b32 v228, a196\n\t"
    "v_accvgpr_read_b32 v229, a197\n\t"
    "v_accvgpr_read_b32 v230, a198\n\t"
    "v_accvgpr_read_b32 v231, a199\n\t"
    "v_cvt_pk_bf16_f32 v224, v224, v225\n\t"
    "v_cvt_pk_bf16_f32 v225, v226, v227\n\t"
    "v_cvt_pk_bf16_f32 v226, v228, v229\n\t"
    "v_cvt_pk_bf16_f32 v227, v230, v231\n\t"
    "buffer_store_dwordx4 v[224:227], v240, s[52:55], s59 offen offset:0 sc1\n\t"
    "v_accvgpr_read_b32 v232, a200\n\t"
    "v_accvgpr_read_b32 v233, a201\n\t"
    "v_accvgpr_read_b32 v234, a202\n\t"
    "v_accvgpr_read_b32 v235, a203\n\t"
    "v_accvgpr_read_b32 v236, a204\n\t"
    "v_accvgpr_read_b32 v237, a205\n\t"
    "v_accvgpr_read_b32 v238, a206\n\t"
    "v_accvgpr_read_b32 v239, a207\n\t"
    "v_cvt_pk_bf16_f32 v232, v232, v233\n\t"
    "v_cvt_pk_bf16_f32 v233, v234, v235\n\t"
    "v_cvt_pk_bf16_f32 v234, v236, v237\n\t"
    "v_cvt_pk_bf16_f32 v235, v238, v239\n\t"
    "buffer_store_dwordx4 v[232:235], v240, s[52:55], s59 offen offset:1024 sc1\n\t"
    "v_accvgpr_read_b32 v224, a208\n\t"
    "v_accvgpr_read_b32 v225, a209\n\t"
    "v_accvgpr_read_b32 v226, a210\n\t"
    "v_accvgpr_read_b32 v227, a211\n\t"
    "v_accvgpr_read_b32 v228, a212\n\t"
    "v_accvgpr_read_b32 v229, a213\n\t"
    "v_accvgpr_read_b32 v230, a214\n\t"
    "v_accvgpr_read_b32 v231, a215\n\t"
    "v_cvt_pk_bf16_f32 v224, v224, v225\n\t"
    "v_cvt_pk_bf16_f32 v225, v226, v227\n\t"
    "v_cvt_pk_bf16_f32 v226, v228, v229\n\t"
    "v_cvt_pk_bf16_f32 v227, v230, v231\n\t"
    "buffer_store_dwordx4 v[224:227], v240, s[52:55], s61 offen offset:0 sc1\n\t"
    "v_accvgpr_read_b32 v232, a216\n\t"
    "v_accvgpr_read_b32 v233, a217\n\t"
    "v_accvgpr_read_b32 v234, a218\n\t"
    "v_accvgpr_read_b32 v235, a219\n\t"
    "v_accvgpr_read_b32 v236, a220\n\t"
    "v_accvgpr_read_b32 v237, a221\n\t"
    "v_accvgpr_read_b32 v238, a222\n\t"
    "v_accvgpr_read_b32 v239, a223\n\t"
    "v_cvt_pk_bf16_f32 v232, v232, v233\n\t"
    "v_cvt_pk_bf16_f32 v233, v234, v235\n\t"
    "v_cvt_pk_bf16_f32 v234, v236, v237\n\t"
    "v_cvt_pk_bf16_f32 v235, v238, v239\n\t"
    "buffer_store_dwordx4 v[232:235], v240, s[52:55], s61 offen offset:1024 sc1\n\t"
    "v_accvgpr_read_b32 v224, a224\n\t"
    "v_accvgpr_read_b32 v225, a225\n\t"
    "v_accvgpr_read_b32 v226, a226\n\t"
    "v_accvgpr_read_b32 v227, a227\n\t"
    "v_accvgpr_read_b32 v228, a228\n\t"
    "v_accvgpr_read_b32 v229, a229\n\t"
    "v_accvgpr_read_b32 v230, a230\n\t"
    "v_accvgpr_read_b32 v231, a231\n\t"
    "v_cvt_pk_bf16_f32 v224, v224, v225\n\t"
    "v_cvt_pk_bf16_f32 v225, v226, v227\n\t"
    "v_cvt_pk_bf16_f32 v226, v228, v229\n\t"
    "v_cvt_pk_bf16_f32 v227, v230, v231\n\t"
    "buffer_store_dwordx4 v[224:227], v240, s[52:55], s59 offen offset:2048 sc1\n\t"
    "v_accvgpr_read_b32 v232, a232\n\t"
    "v_accvgpr_read_b32 v233, a233\n\t"
    "v_accvgpr_read_b32 v234, a234\n\t"
    "v_accvgpr_read_b32 v235, a235\n\t"
    "v_accvgpr_read_b32 v236, a236\n\t"
    "v_accvgpr_read_b32 v237, a237\n\t"
    "v_accvgpr_read_b32 v238, a238\n\t"
    "v_accvgpr_read_b32 v239, a239\n\t"
    "v_cvt_pk_bf16_f32 v232, v232, v233\n\t"
    "v_cvt_pk_bf16_f32 v233, v234, v235\n\t"
    "v_cvt_pk_bf16_f32 v234, v236, v237\n\t"
    "v_cvt_pk_bf16_f32 v235, v238, v239\n\t"
    "buffer_store_dwordx4 v[232:235], v240, s[52:55], s59 offen offset:3072 sc1\n\t"
    "v_accvgpr_read_b32 v224, a240\n\t"
    "v_accvgpr_read_b32 v225, a241\n\t"
    "v_accvgpr_read_b32 v226, a242\n\t"
    "v_accvgpr_read_b32 v227, a243\n\t"
    "v_accvgpr_read_b32 v228, a244\n\t"
    "v_accvgpr_read_b32 v229, a245\n\t"
    "v_accvgpr_read_b32 v230, a246\n\t"
    "v_accvgpr_read_b32 v231, a247\n\t"
    "v_cvt_pk_bf16_f32 v224, v224, v225\n\t"
    "v_cvt_pk_bf16_f32 v225, v226, v227\n\t"
    "v_cvt_pk_bf16_f32 v226, v228, v229\n\t"
    "v_cvt_pk_bf16_f32 v227, v230, v231\n\t"
    "buffer_store_dwordx4 v[224:227], v240, s[52:55], s61 offen offset:2048 sc1\n\t"
    "v_accvgpr_read_b32 v232, a248\n\t"
    "v_accvgpr_read_b32 v233, a249\n\t"
    "v_accvgpr_read_b32 v234, a250\n\t"
    "v_accvgpr_read_b32 v235, a251\n\t"
    "v_accvgpr_read_b32 v236, a252\n\t"
    "v_accvgpr_read_b32 v237, a253\n\t"
    "v_accvgpr_read_b32 v238, a254\n\t"
    "v_accvgpr_read_b32 v239, a255\n\t"
    "v_cvt_pk_bf16_f32 v232, v232, v233\n\t"
    "v_cvt_pk_bf16_f32 v233, v234, v235\n\t"
    "v_cvt_pk_bf16_f32 v234, v236, v237\n\t"
    "v_cvt_pk_bf16_f32 v235, v238, v239\n\t"
    "buffer_store_dwordx4 v[232:235], v240, s[52:55], s61 offen offset:3072 sc1\n\t"
    "LKE_%=:\n\t"
    "LKV_END:\n\t"
    :: "s"((unsigned)pa), "s"((unsigned)(pa >> 32)), "s"((unsigned)pwk), "s"((unsigned)(pwk >> 32)), "s"((unsigned)pwv), "s"((unsigned)(pwv >> 32)),
       "s"((unsigned)pk), "s"((unsigned)(pk >> 32)), "s"((unsigned)pv), "s"((unsigned)(pv >> 32)), "s"(wave),
       "s"((unsigned long long)dbg + (bid * 4 + wave) * 256ull),
       "s"((unsigned long long)x + (bid * 8 + wave * 2) * 1024ull), "s"((unsigned long long)xn + (bid >> 2) * 32768ull + ((bid & 3) * 8 + wave * 2) * 16ull),
       "s"((unsigned long long)c + wave * 256ull), "s"((unsigned)pwm), "s"((unsigned)(pwm >> 32)), "s"(bid < 96 ? 127u * 6144u + 64u : 0u),
       "s"((unsigned)pmo), "s"((unsigned)(pmo >> 32)), "s"(bid < 96 ? 128u : 0u),
       "s"((mt * 64 + (wave & 1) * 32) * 8192u), "s"((unsigned)(bid >> 6) * 256u), "s"((unsigned long long)wq + (bid * 4 + wave) * 4096ull),
       "s"((unsigned long long)wmod + (((bid >> 3) & 15) * 4 + wave) * 49152u + (bid & 7) * 128u), "s"((wave + 4 * (bid & 1)) * 8192u)
    : "v0", "v1", "v2", "v3", "v4", "v5", "v6", "v7", "v8", "v9", "v10", "v11", "v12", "v13", "v14", "v15", "v16", "v17", "v18", "v19", "v20", "v21", "v22", "v23", "v24", "v25", "v26", "v27", "v28", "v29", "v30", "v31", "v32", "v33", "v34", "v35", "v36", "v37", "v38", "v39", "v40", "v41", "v42", "v43", "v44", "v45", "v46", "v47", "v48", "v49", "v50", "v51", "v52", "v53", "v54", "v55", "v56", "v57", "v58", "v59", "v60", "v61", "v62", "v63", "v64", "v65", "v66", "v67", "v68", "v69", "v70", "v71", "v72", "v73", "v74", "v75", "v76", "v77", "v78", "v79", "v80", "v81", "v82", "v83", "v84", "v85", "v86", "v87", "v88", "v89", "v90", "v91", "v92", "v93", "v94", "v95", "v96", "v97", "v98", "v99", "v100", "v101", "v102", "v103", "v104", "v105", "v106", "v107", "v108", "v109", "v110", "v111", "v112", "v113", "v114", "v115", "v116", "v117", "v118", "v119", "v120", "v121", "v122", "v123", "v124", "v125", "v126", "v127", "v128", "v129", "v130", "v131", "v132", "v133", "v134", "v135", "v136", "v137", "v138", "v139", "v140", "v141", "v142", "v143", "v144", "v145", "v146", "v147", "v148", "v149", "v150", "v151", "v152", "v153", "v154", "v155", "v156", "v157", "v158", "v159", "v160", "v161", "v162", "v163", "v164", "v165", "v166", "v167", "v168", "v169", "v170", "v171", "v172", "v173", "v174", "v175", "v176", "v177", "v178", "v179", "v180", "v181", "v182", "v183", "v184", "v185", "v186", "v187", "v188", "v189", "v190", "v191", "v192", "v193", "v194", "v195", "v196", "v197", "v198", "v199", "v200", "v201", "v202", "v203", "v204", "v205", "v206", "v207", "v208", "v209", "v210", "v211", "v212", "v213", "v214", "v215", "v216", "v217", "v218", "v219", "v220", "v221", "v222", "v223", "v224", "v225", "v226", "v227", "v228", "v229", "v230", "v231", "v232", "v233", "v234", "v235", "v236", "v237", "v238", "v239", "v240", "v241", "v242", "v243", "v244", "v245", "v246", "v247", "v248", "v249", "v250", "v251", "v252", "v253", "v254", "v255", "a0", "a1", "a2", "a3", "a4", "a5", "a6", "a7", "a8", "a9", "a10", "a11", "a12", "a13", "a14", "a15", "a16", "a17", "a18", "a19", "a20", "a21", "a22", "a23", "a24", "a25", "a26", "a27", "a28", "a29", "a30", "a31", "a32", "a33", "a34", "a35", "a36", "a37", "a38", "a39", "a40", "a41", "a42", "a43", "a44", "a45", "a46", "a47", "a48", "a49", "a50", "a51", "a52", "a53", "a54", "a55", "a56", "a57", "a58", "a59", "a60", "a61", "a62", "a63", "a64", "a65", "a66", "a67", "a68", "a69", "a70", "a71", "a72", "a73", "a74", "a75", "a76", "a77", "a78", "a79", "a80", "a81", "a82", "a83", "a84", "a85", "a86", "a87", "a88", "a89", "a90", "a91", "a92", "a93", "a94", "a95", "a96", "a97", "a98", "a99", "a100", "a101", "a102", "a103", "a104", "a105", "a106", "a107", "a108", "a109", "a110", "a111", "a112", "a113", "a114", "a115", "a116", "a117", "a118", "a119", "a120", "a121", "a122", "a123", "a124", "a125", "a126", "a127", "a128", "a129", "a130", "a131", "a132", "a133", "a134", "a135", "a136", "a137", "a138", "a139", "a140", "a141", "a142", "a143", "a144", "a145", "a146", "a147", "a148", "a149", "a150", "a151", "a152", "a153", "a154", "a155", "a156", "a157", "a158", "a159", "a160", "a161", "a162", "a163", "a164", "a165", "a166", "a167", "a168", "a169", "a170", "a171", "a172", "a173", "a174", "a175", "a176", "a177", "a178", "a179", "a180", "a181", "a182", "a183", "a184", "a185", "a186", "a187", "a188", "a189", "a190", "a191", "a192", "a193", "a194", "a195", "a196", "a197", "a198", "a199", "a200", "a201", "a202", "a203", "a204", "a205", "a206", "a207", "a208", "a209", "a210", "a211", "a212", "a213", "a214", "a215", "a216", "a217", "a218", "a219", "a220", "a221", "a222", "a223", "a224", "a225", "a226", "a227", "a228", "a229", "a230", "a231", "a232", "a233", "a234", "a235", "a236", "a237", "a238", "a239", "a240", "a241", "a242", "a243", "a244", "a245", "a246", "a247", "a248", "a249", "a250", "a251", "a252", "a253", "a254", "a255", "s36", "s37", "s38", "s39", "s40", "s41", "s42", "s43", "s44", "s45", "s46", "s47", "s48", "s49", "s50", "s51", "s52", "s53", "s54", "s55", "s56", "s57", "s58", "s59", "s60", "s61", "s62", "s63", "s64", "s65", "s66", "s67", "s68", "s69", "s70", "s71", "s72", "s73", "s74", "s75", "s76", "s77", "s96", "s97", "vcc", "m0", "scc", "memory");
}

__global__ __launch_bounds__(256, 1) void k_attn(const u16* __restrict__ h, const u16* __restrict__ wq, const u16* __restrict__ kimg, const u16* __restrict__ vimg, u16* __restrict__ ctx,
                                                 const u16* __restrict__ wo, const u16* __restrict__ win, const u16* __restrict__ wsp, const u16* __restrict__ wout, const float* __restrict__ mod, unsigned* __restrict__ dbg, u16* __restrict__ pf, const u16* x, const u16* bsp, u16* out, char* ws) {
  const int bid = blockIdx.x;
  const unsigned head = (bid & 7) + 8 * (bid >> 6), pr = (bid >> 3) & 7;
  const unsigned wave = __builtin_amdgcn_readfirstlane(threadIdx.x >> 6);
  const unsigned long long pq = (unsigned long long)h, pw = (unsigned long long)wq + head * 256ull, pk = (unsigned long long)kimg + head * 524288ull,
                           pv = (unsigned long long)vimg + head * 524288ull, pc = (unsigned long long)ctx + head * 8192ull;
  const int wn = 4 + pr, wi = (head * 4 + wave) * wn + (int)((0x000b001509001d00ull >> (8 * pr)) & 255) * 128;
  const unsigned long long w0 = ((const unsigned long long __attribute__((address_space(4)))*)__builtin_amdgcn_kernarg_segment_ptr())[(0x77575678u >> (4 * pr)) & 15]
                                + (unsigned)min(wi, ((pr & 5) ? 4096 : 344 << (pr >> 1)) - wn) * 2048u;
  const unsigned long long w1 = (unsigned long long)wo + ((bid & 7) * 128 + wave * 16) * 1024ull + (bid >> 6) * 256;
  const unsigned wc = WS_FL + (bid & 63) * 128 + 16 + (bid >> 6) * 4;
  unsigned wl;
  asm volatile(
    "v_mbcnt_lo_u32_b32 v240, -1, 0\n\t"
    "v_mbcnt_hi_u32_b32 v240, -1, v240\n\t"
    "s_getpc_b64 s[62:63]\n\t"
    "LPC_%=:\n\t"
    "LATT_BEG:\n\t"
    "s_and_b32 s66, %10, 7\n\t"
    "s_lshl_b32 s66, s66, 2\n\t"
    "s_add_u32 s66, s66, %9\n\t"
    "s_lshl_b32 s66, s66, 7\n\t"
    "v_min_u32 v241, 17, v240\n\t"
    "v_lshl_add_u32 v241, v241, 12, s66\n\t"
    "global_load_dword v253, v241, s[62:63]\n\t"
    "v_lshlrev_b32 v224, 4, v240\n\t"
    "v_add_u32 v227, 0x10000, v224\n\t"
    "v_and_b32 v241, 31, v240\n\t"
    "v_lshrrev_b32 v242, 5, v240\n\t"
    "v_lshlrev_b32 v243, 2, v242\n\t"
    "v_sub_u32 v239, v241, v243\n\t"
    "v_bfe_u32 v241, v240, 2, 2\n\t"
    "v_add_u32 v243, v243, v241\n\t"
    "v_lshlrev_b32 v246, 13, v243\n\t"
    "v_bfe_u32 v243, v240, 4, 1\n\t"
    "v_lshl_add_u32 v246, v243, 6, v246\n\t"
    "v_and_b32 v244, 3, v240\n\t"
    "v_lshl_add_u32 v246, v244, 4, v246\n\t"
    "v_lshlrev_b32 v244, 3, v244\n\t"
    "v_lshl_add_u32 v244, v241, 6, v244\n\t"
    "v_lshl_add_u32 v244, v243, 5, v244\n\t"
    "v_lshl_add_u32 v244, v242, 12, v244\n\t"
    "v_add_u32 v245, 0x10000, v244\n\t"
    "v_mov_b32 v252, 0xf149f2ca\n\t"
    "v_lshlrev_b32 v255, 7, v240\n\t"
    "s_mov_b64 s[98:99], %19\n\t"
    "s_lshl_b32 s94, %9, 1\n\t"
    "s_add_u32 s94, s94, 1\n\t"
    "s_mov_b32 s44, %1\n\t"
    "s_mov_b32 s45, %2\n\t"
    "s_mov_b32 s46, 0x200000\n\t"
    "s_mov_b32 s47, 0x20000\n\t"
    "s_mov_b32 s72, %16\n\t"
    "s_mov_b32 s73, %17\n\t"
    "s_mov_b32 s74, 0x3fe100\n\t"
    "s_mov_b32 s75, 0x20000\n\t"
    "s_mov_b32 s36, %3\n\t"
    "s_mov_b32 s37, %4\n\t"
    "s_mov_b32 s38, 0x80000\n\t"
    "s_mov_b32 s39, 0x20000\n\t"
    "s_mov_b32 s40, %5\n\t"
    "s_mov_b32 s41, %6\n\t"
    "s_mov_b32 s42, 0x80000\n\t"
    "s_mov_b32 s43, 0x20000\n\t"
    "s_mov_b32 s48, %7\n\t"
    "s_mov_b32 s49, %8\n\t"
    "s_mov_b32 s50, %11\n\t"
    "s_mov_b32 s51, 0x20000\n\t"
    "s_mov_b32 s89, %9\n\t"
    "s_lshl_b32 s56, s89, 12\n\t"
    "s_add_u32 s57, s56, 0x4000\n\t"
    "s_lshl_b32 s53, %10, 1\n\t"
    "s_and_b32 s66, s89, 1\n\t"
    "s_sub_u32 s67, 15, %10\n\t"
    "s_lshl_b32 s67, s67, 1\n\t"
    "s_add_u32 s67, s67, s66\n\t"
    "s_add_u32 s68, s53, s66\n\t"
    "s_lshl_b32 s92, s67, 14\n\t"
    "s_lshl_b32 s69, s68, 14\n\t"
    "s_add_u32 s64, s53, 1\n\t"
    "s_add_u32 s65, s53, 2\n\t"
    "s_cmp_lt_u32 s89, 2\n\t"
    "s_cselect_b32 s63, s92, s69\n\t"
    "s_cselect_b32 s64, -1, s64\n\t"
    "s_cmp_eq_u32 s89, 2\n\t"
    "s_cselect_b32 s94, s65, 99\n\t"
    "s_lshl_b32 s76, s63, 2\n\t"
    "s_lshl_b32 s78, s89, 12\n\t"
    "s_lshl_b32 s77, s66, 16\n\t"
    "s_lshr_b32 s79, s89, 1\n\t"
    "s_lshl_b32 s79, s79, 7\n\t"
    "s_add_u32 s77, s77, s79\n\t"
    "s_lshl_b32 s86, s66, 14\n\t"
    "s_add_u32 s86, s86, 0x20000\n\t"
    "s_lshl_b32 s88, s67, 19\n\t"
    "s_lshl_b32 s87, s68, 19\n\t"
    "s_lshl_b32 s66, s89, 9\n\t"
    "v_lshlrev_b32 v241, 3, v240\n\t"
    "v_add_u32 v241, s66, v241\n\t"
    "global_load_dwordx2 v[160:161], v241, %18 offset:0\n\t"
    "global_load_dwordx2 v[162:163], v241, %18 offset:2048\n\t"
    "v_add_u32 v220, 0, v246\n\t"
    "v_add_u32 v221, 130048, v246\n\t"
    "v_add_u32 v222, 260096, v246\n\t"
    "v_add_u32 v223, 390144, v246\n\t"
    "s_add_u32 m0, s78, 0\n\t"
    "s_add_u32 s67, s77, 0\n\t"
    "buffer_load_dwordx4 v220, s[72:75], s67 offen offset:0 lds\n\t"
    "buffer_load_dwordx4 v221, s[72:75], s67 offen offset:1024 lds\n\t"
    "buffer_load_dwordx4 v222, s[72:75], s67 offen offset:2048 lds\n\t"
    "buffer_load_dwordx4 v223, s[72:75], s67 offen offset:3072 lds\n\t"
    "s_add_u32 s66, s76, 0\n\t"
    "buffer_load_dwordx4 v[0:3], v224, s[44:47], s66 offen offset:0\n\t"
    "buffer_load_dwordx4 v[8:11], v224, s[44:47], s66 offen offset:1024\n\t"
    "buffer_load_dwordx4 v[16:19], v224, s[44:47], s66 offen offset:2048\n\t"
    "buffer_load_dwordx4 v[24:27], v224, s[44:47], s66 offen offset:3072\n\t"
    "s_add_u32 s66, s76, 32768\n\t"
    "buffer_load_dwordx4 v[4:7], v224, s[44:47], s66 offen offset:0\n\t"
    "buffer_load_dwordx4 v[12:15], v224, s[44:47], s66 offen offset:1024\n\t"
    "buffer_load_dwordx4 v[20:23], v224, s[44:47], s66 offen offset:2048\n\t"
    "buffer_load_dwordx4 v[28:31], v224, s[44:47], s66 offen offset:3072\n\t"
    "s_add_u32 m0, s78, 16384\n\t"
    "s_add_u32 s67, s77, 524288\n\t"
    "buffer_load_dwordx4 v220, s[72:75], s67 offen offset:0 lds\n\t"
    "buffer_load_dwordx4 v221, s[72:75], s67 offen offset:1024 lds\n\t"
    "buffer_load_dwordx4 v222, s[72:75], s67 offen offset:2048 lds\n\t"
    "buffer_load_dwordx4 v223, s[72:75], s67 offen offset:3072 lds\n\t"
    "s_add_u32 s66, s76, 4096\n\t"
    "buffer_load_dwordx4 v[32:35], v224, s[44:47], s66 offen offset:0\n\t"
    "buffer_load_dwordx4 v[40:43], v224, s[44:47], s66 offen offset:1024\n\t"
    "s_sub_u32 s54, s53, 1\n\t"
    "s_add_u32 s55, s56, 0x4000\n\t"
    "s_mov_b32 s59, 0x4000\n\t"
    "s_add_u32 s58, s56, 0x8000\n\t"
    "s_add_u32 s69, s58, s56\n\t"
    "s_cmp_eq_u32 s53, 0\n\t"
    "s_cselect_b32 s58, s69, s58\n\t"
    "s_mov_b32 s52, 0\n\t"
    "s_mov_b32 s62, 0\n\t"
    "s_mov_b32 s93, 1\n\t"
    "s_mov_b32 s60, 0xff7fffff\n\t"
    "s_mov_b32 s61, 0xff7fffff\n\t"
    "v_mov_b32 v228, 0\n\t"
    "v_mov_b32 v229, 0\n\t"
    "v_mov_b32 v230, 0\n\t"
    "v_mov_b32 v231, 0\n\t"
    "v_mov_b32 v232, 0\n\t"
    "v_mov_b32 v233, 0\n\t"
    "v_mov_b32 v225, v224\n\t"
    "v_and_b32 v243, 24, v241\n\t"
    "v_lshrrev_b32 v241, 5, v241\n\t"
    "v_lshl_add_u32 v241, v241, 6, v243\n\t"
    "v_add_u32 v241, 0x20000, v241\n\t"
    "v_lshlrev_b32 v243, 6, v242\n\t"
    "v_add_u32 v243, 0x20000, v243\n\t"
    "v_mov_b32 v164, 0x3e0293ee\n\t"
    "s_waitcnt vmcnt(18)\n\t"
    "v_fma_f32 v162, v162, v164, v164\n\t"
    "v_fma_f32 v163, v163, v164, v164\n\t"
    "v_mul_f32 v160, v160, v164\n\t"
    "v_mul_f32 v161, v161, v164\n\t"
    "ds_write_b64 v241, v[162:163] offset:0\n\t"
    "ds_write_b64 v241, v[160:161] offset:32\n\t"
    "s_waitcnt vmcnt(14) lgkmcnt(0)\n\t"
    "s_barrier\n\t"
    "ds_read_b128 v[160:163], v243 offset:0\n\t"
    "ds_read_b128 v[164:167], v243 offset:16\n\t"
    "ds_read_b128 v[168:171], v243 offset:32\n\t"
    "ds_read_b128 v[172:175], v243 offset:48\n\t"
    "ds_read_b128 v[176:179], v243 offset:128\n\t"
    "ds_read_b128 v[180:183], v243 offset:144\n\t"
    "ds_read_b128 v[184:187], v243 offset:160\n\t"
    "ds_read_b128 v[188:191], v243 offset:176\n\t"
    "ds_read_b64_tr_b16 v[128:129], v244 offset:0\n\t"
    "ds_read_b64_tr_b16 v[130:131], v244 offset:512\n\t"
    "ds_read_b64_tr_b16 v[132:133], v244 offset:256\n\t"
    "ds_read_b64_tr_b16 v[134:135], v244 offset:768\n\t"
    "ds_read_b64_tr_b16 v[136:137], v244 offset:8192\n\t"
    "ds_read_b64_tr_b16 v[138:139], v244 offset:8704\n\t"
    "ds_read_b64_tr_b16 v[140:141], v244 offset:8448\n\t"
    "ds_read_b64_tr_b16 v[142:143], v244 offset:8960\n\t"
    "s_waitcnt vmcnt(6) lgkmcnt(12)\n\t"
    "v_fma_mix_f32 v192, v0, v160, v168 op_sel_hi:[1,0,0]\n\t"
    "v_fma_mix_f32 v193, v0, v161, v169 op_sel:[1,0,0] op_sel_hi:[1,0,0]\n\t"
    "v_fma_mix_f32 v194, v1, v162, v170 op_sel_hi:[1,0,0]\n\t"
    "v_fma_mix_f32 v195, v1, v163, v171 op_sel:[1,0,0] op_sel_hi:[1,0,0]\n\t"
    "v_fma_mix_f32 v196, v2, v164, v172 op_sel_hi:[1,0,0]\n\t"
    "v_fma_mix_f32 v197, v2, v165, v173 op_sel:[1,0,0] op_sel_hi:[1,0,0]\n\t"
    "v_fma_mix_f32 v198, v3, v166, v174 op_sel_hi:[1,0,0]\n\t"
    "v_fma_mix_f32 v199, v3, v167, v175 op_sel:[1,0,0] op_sel_hi:[1,0,0]\n\t"
    "v_cvt_pk_bf16_f32 v0, v192, v193\n\t"
    "v_cvt_pk_bf16_f32 v1, v194, v195\n\t"
    "v_cvt_pk_bf16_f32 v2, v196, v197\n\t"
    "v_cvt_pk_bf16_f32 v3, v198, v199\n\t"
    "v_fma_mix_f32 v200, v4, v160, v168 op_sel_hi:[1,0,0]\n\t"
    "v_fma_mix_f32 v201, v4, v161, v169 op_sel:[1,0,0] op_sel_hi:[1,0,0]\n\t"
    "v_fma_mix_f32 v202, v5, v162, v170 op_sel_hi:[1,0,0]\n\t"
    "v_fma_mix_f32 v203, v5, v163, v171 op_sel:[1,0,0] op_sel_hi:[1,0,0]\n\t"
    "v_fma_mix_f32 v204, v6, v164, v172 op_sel_hi:[1,0,0]\n\t"
    "v_fma_mix_f32 v205, v6, v165, v173 op_sel:[1,0,0] op_sel_hi:[1,0,0]\n\t"
    "v_fma_mix_f32 v206, v7, v166, v174 op_sel_hi:[1,0,0]\n\t"
    "v_fma_mix_f32 v207, v7, v167, v175 op_sel:[1,0,0] op_sel_hi:[1,0,0]\n\t"
    "v_cvt_pk_bf16_f32 v4, v200, v201\n\t"
    "v_cvt_pk_bf16_f32 v5, v202, v203\n\t"
    "v_cvt_pk_bf16_f32 v6, v204, v205\n\t"
    "v_cvt_pk_bf16_f32 v7, v206, v207\n\t"
    "s_nop 1\n\t"
    "s_waitcnt lgkmcnt(6)\n\t"
    "v_mfma_f32_32x32x16_bf16 a[0:15], v[128:131], v[0:3], 0\n\t"
    "ds_read_b64_tr_b16 v[144:145], v244 offset:1024\n\t"
    "ds_read_b128 v[160:163], v243 offset:256\n\t"
    "ds_read_b128 v[164:167], v243 offset:272\n\t"
    "ds_read_b128 v[168:171], v243 offset:288\n\t"
    "ds_read_b128 v[172:175], v243 offset:304\n\t"
    "v_mfma_f32_32x32x16_bf16 a[16:31], v[128:131], v[4:7], 0\n\t"
    "ds_read_b64_tr_b16 v[146:147], v244 offset:1536\n\t"
    "v_fma_mix_f32 v192, v8, v176, v184 op_sel_hi:[1,0,0]\n\t"
    "v_fma_mix_f32 v193, v8, v177, v185 op_sel:[1,0,0] op_sel_hi:[1,0,0]\n\t"
    "v_fma_mix_f32 v194, v9, v178, v186 op_sel_hi:[1,0,0]\n\t"
    "buffer_load_dwordx4 v[48:51], v224, s[44:47], s66 offen offset:2048\n\t"
    "s_waitcnt lgkmcnt(10)\n\t"
    "v_mfma_f32_32x32x16_bf16 a[32:47], v[132:135], v[0:3], 0\n\t"
    "ds_read_b64_tr_b16 v[148:149], v244 offset:1280\n\t"
    "v_fma_mix_f32 v195, v9, v179, v187 op_sel:[1,0,0] op_sel_hi:[1,0,0]\n\t"
    "v_fma_mix_f32 v196, v10, v180, v188 op_sel_hi:[1,0,0]\n\t"
    "v_fma_mix_f32 v197, v10, v181, v189 op_sel:[1,0,0] op_sel_hi:[1,0,0]\n\t"
    "v_fma_mix_f32 v198, v11, v182, v190 op_sel_hi:[1,0,0]\n\t"
    "v_mfma_f32_32x32x16_bf16 a[48:63], v[132:135], v[4:7], 0\n\t"
    "ds_read_b64_tr_b16 v[150:151], v244 offset:1792\n\t"
    "v_fma_mix_f32 v199, v11, v183, v191 op_sel:[1,0,0] op_sel_hi:[1,0,0]\n\t"
    "v_cvt_pk_bf16_f32 v8, v192, v193\n\t"
    "v_cvt_pk_bf16_f32 v9, v194, v195\n\t"
    "v_cvt_pk_bf16_f32 v10, v196, v197\n\t"
    "buffer_load_dwordx4 v[56:59], v224, s[44:47], s66 offen offset:3072\n\t"
    "s_waitcnt lgkmcnt(10)\n\t"
    "v_mfma_f32_32x32x16_bf16 a[64:79], v[136:139], v[0:3], 0\n\t"
    "ds_read_b64_tr_b16 v[152:153], v244 offset:9216\n\t"
    "v_cvt_pk_bf16_f32 v11, v198, v199\n\t"
    "v_fma_mix_f32 v200, v12, v176, v184 op_sel_hi:[1,0,0]\n\t"
    "v_fma_mix_f32 v201, v12, v177, v185 op_sel:[1,0,0] op_sel_hi:[1,0,0]\n\t"
    "v_fma_mix_f32 v202, v13, v178, v186 op_sel_hi:[1,0,0]\n\t"
    "v_mfma_f32_32x32x16_bf16 a[80:95], v[136:139], v[4:7], 0\n\t"
    "ds_read_b64_tr_b16 v[154:155], v244 offset:9728\n\t"
    "v_fma_mix_f32 v203, v13, v179, v187 op_sel:[1,0,0] op_sel_hi:[1,0,0]\n\t"
    "v_fma_mix_f32 v204, v14, v180, v188 op_sel_hi:[1,0,0]\n\t"
    "v_fma_mix_f32 v205, v14, v181, v189 op_sel:[1,0,0] op_sel_hi:[1,0,0]\n\t"
    "v_fma_mix_f32 v206, v15, v182, v190 op_sel_hi:[1,0,0]\n\t"
    "s_add_u32 s66, s76, 36864\n\t"
    "buffer_load_dwordx4 v[36:39], v224, s[44:47], s66 offen offset:0\n\t"
    "s_waitcnt lgkmcnt(10)\n\t"
    "v_mfma_f32_32x32x16_bf16 a[96:111], v[140:143], v[0:3], 0\n\t"
    "ds_read_b64_tr_b16 v[156:157], v244 offset:9472\n\t"
    "v_fma_mix_f32 v207, v15, v183, v191 op_sel:[1,0,0] op_sel_hi:[1,0,0]\n\t"
    "v_cvt_pk_bf16_f32 v12, v200, v201\n\t"
    "v_cvt_pk_bf16_f32 v13, v202, v203\n\t"
    "v_cvt_pk_bf16_f32 v14, v204, v205\n\t"
    "v_mfma_f32_32x32x16_bf16 a[112:127], v[140:143], v[4:7], 0\n\t"
    "ds_read_b64_tr_b16 v[158:159], v244 offset:9984\n\t"
    "v_cvt_pk_bf16_f32 v15, v206, v207\n\t"
    "ds_read_b128 v[176:179], v243 offset:384\n\t"
    "ds_read_b128 v[180:183], v243 offset:400\n\t"
    "ds_read_b128 v[184:187], v243 offset:416\n\t"
    "buffer_load_dwordx4 v[44:47], v224, s[44:47], s66 offen offset:1024\n\t"
    "s_waitcnt lgkmcnt(9)\n\t"
    "v_mfma_f32_32x32x16_bf16 a[0:15], v[144:147], v[8:11], a[0:15]\n\t"
    "ds_read_b64_tr_b16 v[128:129], v244 offset:2048\n\t"
    "ds_read_b128 v[188:191], v243 offset:432\n\t"
    "v_fma_mix_f32 v192, v16, v160, v168 op_sel_hi:[1,0,0]\n\t"
    "v_fma_mix_f32 v193, v16, v161, v169 op_sel:[1,0,0] op_sel_hi:[1,0,0]\n\t"
    "v_mfma_f32_32x32x16_bf16 a[16:31], v[144:147], v[12:15], a[16:31]\n\t"
    "ds_read_b64_tr_b16 v[130:131], v244 offset:2560\n\t"
    "v_fma_mix_f32 v194, v17, v162, v170 op_sel_hi:[1,0,0]\n\t"
    "v_fma_mix_f32 v195, v17, v163, v171 op_sel:[1,0,0] op_sel_hi:[1,0,0]\n\t"
    "v_fma_mix_f32 v196, v18, v164, v172 op_sel_hi:[1,0,0]\n\t"
    "v_fma_mix_f32 v197, v18, v165, v173 op_sel:[1,0,0] op_sel_hi:[1,0,0]\n\t"
    "buffer_load_dwordx4 v[52:55], v224, s[44:47], s66 offen offset:2048\n\t"
    "s_waitcnt lgkmcnt(10)\n\t"
    "v_mfma_f32_32x32x16_bf16 a[32:47], v[148:151], v[8:11], a[32:47]\n\t"
    "ds_read_b64_tr_b16 v[132:133], v244 offset:2304\n\t"
    "v_fma_mix_f32 v198, v19, v166, v174 op_sel_hi:[1,0,0]\n\t"
    "v_fma_mix_f32 v199, v19, v167, v175 op_sel:[1,0,0] op_sel_hi:[1,0,0]\n\t"
    "v_cvt_pk_bf16_f32 v16, v192, v193\n\t"
    "v_cvt_pk_bf16_f32 v17, v194, v195\n\t"
    "v_mfma_f32_32x32x16_bf16 a[48:63], v[148:151], v[12:15], a[48:63]\n\t"
    "ds_read_b64_tr_b16 v[134:135], v244 offset:2816\n\t"
    "v_cvt_pk_bf16_f32 v18, v196, v197\n\t"
    "v_cvt_pk_bf16_f32 v19, v198, v199\n\t"
    "v_fma_mix_f32 v200, v20, v160, v168 op_sel_hi:[1,0,0]\n\t"
    "v_fma_mix_f32 v201, v20, v161, v169 op_sel:[1,0,0] op_sel_hi:[1,0,0]\n\t"
    "buffer_load_dwordx4 v[60:63], v224, s[44:47], s66 offen offset:3072\n\t"
    "s_waitcnt lgkmcnt(10)\n\t"
    "v_mfma_f32_32x32x16_bf16 a[64:79], v[152:155], v[8:11], a[64:79]\n\t"
    "ds_read_b64_tr_b16 v[136:137], v244 offset:10240\n\t"
    "v_fma_mix_f32 v202, v21, v162, v170 op_sel_hi:[1,0,0]\n\t"
    "v_fma_mix_f32 v203, v21, v163, v171 op_sel:[1,0,0] op_sel_hi:[1,0,0]\n\t"
    "v_fma_mix_f32 v204, v22, v164, v172 op_sel_hi:[1,0,0]\n\t"
    "v_fma_mix_f32 v205, v22, v165, v173 op_sel:[1,0,0] op_sel_hi:[1,0,0]\n\t"
    "v_mfma_f32_32x32x16_bf16 a[80:95], v[152:155], v[12:15], a[80:95]\n\t"
    "ds_read_b64_tr_b16 v[138:139], v244 offset:10752\n\t"
    "v_fma_mix_f32 v206, v23, v166, v174 op_sel_hi:[1,0,0]\n\t"
    "v_fma_mix_f32 v207, v23, v167, v175 op_sel:[1,0,0] op_sel_hi:[1,0,0]\n\t"
    "v_cvt_pk_bf16_f32 v20, v200, v201\n\t"
    "v_cvt_pk_bf16_f32 v21, v202, v203\n\t"
    "s_add_u32 m0, s78, 32768\n\t"
    "s_add_u32 s67, s77, 1048576\n\t"
    "buffer_load_dwordx4 v220, s[72:75], s67 offen offset:0 lds\n\t"
    "s_waitcnt lgkmcnt(10)\n\t"
    "v_mfma_f32_32x32x16_bf16 a[96:111], v[156:159], v[8:11], a[96:111]\n\t"
    "ds_read_b64_tr_b16 v[140:141], v244 offset:10496\n\t"
    "v_cvt_pk_bf16_f32 v22, v204, v205\n\t"
    "v_cvt_pk_bf16_f32 v23, v206, v207\n\t"
    "ds_read_b128 v[160:163], v243 offset:512\n\t"
    "ds_read_b128 v[164:167], v243 offset:528\n\t"
    "v_mfma_f32_32x32x16_bf16 a[112:127], v[156:159], v[12:15], a[112:127]\n\t"
    "ds_read_b64_tr_b16 v[142:143], v244 offset:11008\n\t"
    "ds_read_b128 v[168:171], v243 offset:544\n\t"
    "ds_read_b128 v[172:175], v243 offset:560\n\t"
    "buffer_load_dwordx4 v221, s[72:75], s67 offen offset:1024 lds\n\t"
    "s_waitcnt lgkmcnt(10)\n\t"
    "v_mfma_f32_32x32x16_bf16 a[0:15], v[128:131], v[16:19], a[0:15]\n\t"
    "ds_read_b64_tr_b16 v[144:145], v244 offset:3072\n\t"
    "v_fma_mix_f32 v192, v24, v176, v184 op_sel_hi:[1,0,0]\n\t"
    "v_fma_mix_f32 v193, v24, v177, v185 op_sel:[1,0,0] op_sel_hi:[1,0,0]\n\t"
    "v_fma_mix_f32 v194, v25, v178, v186 op_sel_hi:[1,0,0]\n\t"
    "v_mfma_f32_32x32x16_bf16 a[16:31], v[128:131], v[20:23], a[16:31]\n\t"
    "ds_read_b64_tr_b16 v[146:147], v244 offset:3584\n\t"
    "v_fma_mix_f32 v195, v25, v179, v187 op_sel:[1,0,0] op_sel_hi:[1,0,0]\n\t"
    "v_fma_mix_f32 v196, v26, v180, v188 op_sel_hi:[1,0,0]\n\t"
    "v_fma_mix_f32 v197, v26, v181, v189 op_sel:[1,0,0] op_sel_hi:[1,0,0]\n\t"
    "v_fma_mix_f32 v198, v27, v182, v190 op_sel_hi:[1,0,0]\n\t"
    "buffer_load_dwordx4 v222, s[72:75], s67 offen offset:2048 lds\n\t"
    "s_waitcnt lgkmcnt(10)\n\t"
    "v_mfma_f32_32x32x16_bf16 a[32:47], v[132:135], v[16:19], a[32:47]\n\t"
    "ds_read_b64_tr_b16 v[148:149], v244 offset:3328\n\t"
    "v_fma_mix_f32 v199, v27, v183, v191 op_sel:[1,0,0] op_sel_hi:[1,0,0]\n\t"
    "v_cvt_pk_bf16_f32 v24, v192, v193\n\t"
    "v_cvt_pk_bf16_f32 v25, v194, v195\n\t"
    "v_cvt_pk_bf16_f32 v26, v196, v197\n\t"
    "v_mfma_f32_32x32x16_bf16 a[48:63], v[132:135], v[20:23], a[48:63]\n\t"
    "ds_read_b64_tr_b16 v[150:151], v244 offset:3840\n\t"
    "v_cvt_pk_bf16_f32 v27, v198, v199\n\t"
    "v_fma_mix_f32 v200, v28, v176, v184 op_sel_hi:[1,0,0]\n\t"
    "v_fma_mix_f32 v201, v28, v177, v185 op_sel:[1,0,0] op_sel_hi:[1,0,0]\n\t"
    "v_fma_mix_f32 v202, v29, v178, v186 op_sel_hi:[1,0,0]\n\t"
    "buffer_load_dwordx4 v223, s[72:75], s67 offen offset:3072 lds\n\t"
    "s_waitcnt lgkmcnt(10)\n\t"
    "v_mfma_f32_32x32x16_bf16 a[64:79], v[136:139], v[16:19], a[64:79]\n\t"
    "ds_read_b64_tr_b16 v[152:153], v244 offset:11264\n\t"
    "v_fma_mix_f32 v203, v29, v179, v187 op_sel:[1,0,0] op_sel_hi:[1,0,0]\n\t"
    "v_fma_mix_f32 v204, v30, v180, v188 op_sel_hi:[1,0,0]\n\t"
    "v_fma_mix_f32 v205, v30, v181, v189 op_sel:[1,0,0] op_sel_hi:[1,0,0]\n\t"
    "v_fma_mix_f32 v206, v31, v182, v190 op_sel_hi:[1,0,0]\n\t"
    "v_mfma_f32_32x32x16_bf16 a[80:95], v[136:139], v[20:23], a[80:95]\n\t"
    "ds_read_b64_tr_b16 v[154:155], v244 offset:11776\n\t"
    "v_fma_mix_f32 v207, v31, v183, v191 op_sel:[1,0,0] op_sel_hi:[1,0,0]\n\t"
    "v_cvt_pk_bf16_f32 v28, v200, v201\n\t"
    "v_cvt_pk_bf16_f32 v29, v202, v203\n\t"
    "v_cvt_pk_bf16_f32 v30, v204, v205\n\t"
    "s_add_u32 s66, s76, 8192\n\t"
    "buffer_load_dwordx4 v[64:67], v224, s[44:47], s66 offen offset:0\n\t"
    "s_waitcnt lgkmcnt(8)\n\t"
    "v_mfma_f32_32x32x16_bf16 a[96:111], v[140:143], v[16:19], a[96:111]\n\t"
    "ds_read_b64_tr_b16 v[156:157], v244 offset:11520\n\t"
    "v_cvt_pk_bf16_f32 v31, v206, v207\n\t"
    "ds_read_b128 v[176:179], v243 offset:640\n\t"
    "ds_read_b128 v[180:183], v243 offset:656\n\t"
    "ds_read_b128 v[184:187], v243 offset:672\n\t"
    "v_mfma_f32_32x32x16_bf16 a[112:127], v[140:143], v[20:23], a[112:127]\n\t"
    "ds_read_b64_tr_b16 v[158:159], v244 offset:12032\n\t"
    "ds_read_b128 v[188:191], v243 offset:688\n\t"
    "buffer_load_dwordx4 v[72:75], v224, s[44:47], s66 offen offset:1024\n\t"
    "s_waitcnt vmcnt(14)\n\t"
    "s_barrier\n\t"
    "s_waitcnt lgkmcnt(10)\n\t"
    "v_mfma_f32_32x32x16_bf16 a[0:15], v[144:147], v[24:27], a[0:15]\n\t"
    "ds_read_b64_tr_b16 v[128:129], v244 offset:16384\n\t"
    "s_waitcnt vmcnt(6)\n\t"
    "v_fma_mix_f32 v192, v32, v160, v168 op_sel_hi:[1,0,0]\n\t"
    "v_fma_mix_f32 v193, v32, v161, v169 op_sel:[1,0,0] op_sel_hi:[1,0,0]\n\t"
    "v_fma_mix_f32 v194, v33, v162, v170 op_sel_hi:[1,0,0]\n\t"
    "v_mfma_f32_32x32x16_bf16 a[16:31], v[144:147], v[28:31], a[16:31]\n\t"
    "ds_read_b64_tr_b16 v[130:131], v244 offset:16896\n\t"
    "v_fma_mix_f32 v195, v33, v163, v171 op_sel:[1,0,0] op_sel_hi:[1,0,0]\n\t"
    "v_fma_mix_f32 v196, v34, v164, v172 op_sel_hi:[1,0,0]\n\t"
    "v_fma_mix_f32 v197, v34, v165, v173 op_sel:[1,0,0] op_sel_hi:[1,0,0]\n\t"
    "v_fma_mix_f32 v198, v35, v166, v174 op_sel_hi:[1,0,0]\n\t"
    "buffer_load_dwordx4 v[80:83], v224, s[44:47], s66 offen offset:2048\n\t"
    "s_waitcnt lgkmcnt(10)\n\t"
    "v_mfma_f32_32x32x16_bf16 a[32:47], v[148:151], v[24:27], a[32:47]\n\t"
    "ds_read_b64_tr_b16 v[132:133], v244 offset:16640\n\t"
    "v_fma_mix_f32 v199, v35, v167, v175 op_sel:[1,0,0] op_sel_hi:[1,0,0]\n\t"
    "v_cvt_pk_bf16_f32 v32, v192, v193\n\t"
    "v_cvt_pk_bf16_f32 v33, v194, v195\n\t"
    "v_cvt_pk_bf16_f32 v34, v196, v197\n\t"
    "v_mfma_f32_32x32x16_bf16 a[48:63], v[148:151], v[28:31], a[48:63]\n\t"
    "ds_read_b64_tr_b16 v[134:135], v244 offset:17152\n\t"
    "v_cvt_pk_bf16_f32 v35, v198, v199\n\t"
    "v_fma_mix_f32 v200, v36, v160, v168 op_sel_hi:[1,0,0]\n\t"
    "v_fma_mix_f32 v201, v36, v161, v169 op_sel:[1,0,0] op_sel_hi:[1,0,0]\n\t"
    "v_fma_mix_f32 v202, v37, v162, v170 op_sel_hi:[1,0,0]\n\t"
    "buffer_load_dwordx4 v[88:91], v224, s[44:47], s66 offen offset:3072\n\t"
    "s_waitcnt lgkmcnt(10)\n\t"
    "v_mfma_f32_32x32x16_bf16 a[64:79], v[152:155], v[24:27], a[64:79]\n\t"
    "ds_read_b64_tr_b16 v[136:137], v244 offset:24576\n\t"
    "v_fma_mix_f32 v203, v37, v163, v171 op_sel:[1,0,0] op_sel_hi:[1,0,0]\n\t"
    "v_fma_mix_f32 v204, v38, v164, v172 op_sel_hi:[1,0,0]\n\t"
    "v_fma_mix_f32 v205, v38, v165, v173 op_sel:[1,0,0] op_sel_hi:[1,0,0]\n\t"
    "v_fma_mix_f32 v206, v39, v166, v174 op_sel_hi:[1,0,0]\n\t"
    "v_mfma_f32_32x32x16_bf16 a[80:95], v[152:155], v[28:31], a[80:95]\n\t"
    "ds_read_b64_tr_b16 v[138:139], v244 offset:25088\n\t"
    "v_fma_mix_f32 v207, v39, v167, v175 op_sel:[1,0,0] op_sel_hi:[1,0,0]\n\t"
    "v_cvt_pk_bf16_f32 v36, v200, v201\n\t"
    "v_cvt_pk_bf16_f32 v37, v202, v203\n\t"
    "v_cvt_pk_bf16_f32 v38, v204, v205\n\t"
    "s_add_u32 s66, s76, 40960\n\t"
    "buffer_load_dwordx4 v[68:71], v224, s[44:47], s66 offen offset:0\n\t"
    "s_waitcnt lgkmcnt(7)\n\t"
    "v_mfma_f32_32x32x16_bf16 a[96:111], v[156:159], v[24:27], a[96:111]\n\t"
    "ds_read_b64_tr_b16 v[140:141], v244 offset:24832\n\t"
    "v_cvt_pk_bf16_f32 v39, v206, v207\n\t"
    "ds_read_b128 v[160:163], v243 offset:768\n\t"
    "ds_read_b128 v[164:167], v243 offset:784\n\t"
    "ds_read_b128 v[168:171], v243 offset:800\n\t"
    "v_mfma_f32_32x32x16_bf16 a[112:127], v[156:159], v[28:31], a[112:127]\n\t"
    "ds_read_b64_tr_b16 v[142:143], v244 offset:25344\n\t"
    "ds_read_b128 v[172:175], v243 offset:816\n\t"
    "buffer_load_dwordx4 v[76:79], v224, s[44:47], s66 offen offset:1024\n\t"
    "s_waitcnt lgkmcnt(10)\n\t"
    "v_mfma_f32_32x32x16_bf16 a[0:15], v[128:131], v[32:35], a[0:15]\n\t"
    "ds_read_b64_tr_b16 v[144:145], v244 offset:17408\n\t"
    "v_fma_mix_f32 v192, v40, v176, v184 op_sel_hi:[1,0,0]\n\t"
    "v_fma_mix_f32 v193, v40, v177, v185 op_sel:[1,0,0] op_sel_hi:[1,0,0]\n\t"
    "v_fma_mix_f32 v194, v41, v178, v186 op_sel_hi:[1,0,0]\n\t"
    "v_mfma_f32_32x32x16_bf16 a[16:31], v[128:131], v[36:39], a[16:31]\n\t"
    "ds_read_b64_tr_b16 v[146:147], v244 offset:17920\n\t"
    "v_fma_mix_f32 v195, v41, v179, v187 op_sel:[1,0,0] op_sel_hi:[1,0,0]\n\t"
    "v_fma_mix_f32 v196, v42, v180, v188 op_sel_hi:[1,0,0]\n\t"
    "v_fma_mix_f32 v197, v42, v181, v189 op_sel:[1,0,0] op_sel_hi:[1,0,0]\n\t"
    "v_fma_mix_f32 v198, v43, v182, v190 op_sel_hi:[1,0,0]\n\t"
    "buffer_load_dwordx4 v[84:87], v224, s[44:47], s66 offen offset:2048\n\t"
    "s_waitcnt lgkmcnt(10)\n\t"
    "v_mfma_f32_32x32x16_bf16 a[32:47], v[132:135], v[32:35], a[32:47]\n\t"
    "ds_read_b64_tr_b16 v[148:149], v244 offset:17664\n\t"
    "v_fma_mix_f32 v199, v43, v183, v191 op_sel:[1,0,0] op_sel_hi:[1,0,0]\n\t"
    "v_cvt_pk_bf16_f32 v40, v192, v193\n\t"
    "v_cvt_pk_bf16_f32 v41, v194, v195\n\t"
    "v_cvt_pk_bf16_f32 v42, v196, v197\n\t"
    "v_mfma_f32_32x32x16_bf16 a[48:63], v[132:135], v[36:39], a[48:63]\n\t"
    "ds_read_b64_tr_b16 v[150:151], v244 offset:18176\n\t"
    "v_cvt_pk_bf16_f32 v43, v198, v199\n\t"
    "v_fma_mix_f32 v200, v44, v176, v184 op_sel_hi:[1,0,0]\n\t"
    "v_fma_mix_f32 v201, v44, v177, v185 op_sel:[1,0,0] op_sel_hi:[1,0,0]\n\t"
    "v_fma_mix_f32 v202, v45, v178, v186 op_sel_hi:[1,0,0]\n\t"
    "buffer_load_dwordx4 v[92:95], v224, s[44:47], s66 offen offset:3072\n\t"
    "s_waitcnt lgkmcnt(10)\n\t"
    "v_mfma_f32_32x32x16_bf16 a[64:79], v[136:139], v[32:35], a[64:79]\n\t"
    "ds_read_b64_tr_b16 v[152:153], v244 offset:25600\n\t"
    "v_fma_mix_f32 v203, v45, v179, v187 op_sel:[1,0,0] op_sel_hi:[1,0,0]\n\t"
    "v_fma_mix_f32 v204, v46, v180, v188 op_sel_hi:[1,0,0]\n\t"
    "v_fma_mix_f32 v205, v46, v181, v189 op_sel:[1,0,0] op_sel_hi:[1,0,0]\n\t"
    "v_fma_mix_f32 v206, v47, v182, v190 op_sel_hi:[1,0,0]\n\t"
    "v_mfma_f32_32x32x16_bf16 a[80:95], v[136:139], v[36:39], a[80:95]\n\t"
    "ds_read_b64_tr_b16 v[154:155], v244 offset:26112\n\t"
    "v_fma_mix_f32 v207, v47, v183, v191 op_sel:[1,0,0] op_sel_hi:[1,0,0]\n\t"
    "v_cvt_pk_bf16_f32 v44, v200, v201\n\t"
    "v_cvt_pk_bf16_f32 v45, v202, v203\n\t"
    "v_cvt_pk_bf16_f32 v46, v204, v205\n\t"
    "s_add_u32 m0, s78, 49152\n\t"
    "s_add_u32 s67, s77, 1572864\n\t"
    "buffer_load_dwordx4 v220, s[72:75], s67 offen offset:0 lds\n\t"
    "s_waitcnt lgkmcnt(7)\n\t"
    "v_mfma_f32_32x32x16_bf16 a[96:111], v[140:143], v[32:35], a[96:111]\n\t"
    "ds_read_b64_tr_b16 v[156:157], v244 offset:25856\n\t"
    "v_cvt_pk_bf16_f32 v47, v206, v207\n\t"
    "ds_read_b128 v[176:179], v243 offset:896\n\t"
    "ds_read_b128 v[180:183], v243 offset:912\n\t"
    "ds_read_b128 v[184:187], v243 offset:928\n\t"
    "v_mfma_f32_32x32x16_bf16 a[112:127], v[140:143], v[36:39], a[112:127]\n\t"
    "ds_read_b64_tr_b16 v[158:159], v244 offset:26368\n\t"
    "ds_read_b128 v[188:191], v243 offset:944\n\t"
    "buffer_load_dwordx4 v221, s[72:75], s67 offen offset:1024 lds\n\t"
    "s_waitcnt lgkmcnt(10)\n\t"
    "v_mfma_f32_32x32x16_bf16 a[0:15], v[144:147], v[40:43], a[0:15]\n\t"
    "ds_read_b64_tr_b16 v[128:129], v244 offset:18432\n\t"
    "v_fma_mix_f32 v192, v48, v160, v168 op_sel_hi:[1,0,0]\n\t"
    "v_fma_mix_f32 v193, v48, v161, v169 op_sel:[1,0,0] op_sel_hi:[1,0,0]\n\t"
    "v_fma_mix_f32 v194, v49, v162, v170 op_sel_hi:[1,0,0]\n\t"
    "v_mfma_f32_32x32x16_bf16 a[16:31], v[144:147], v[44:47], a[16:31]\n\t"
    "ds_read_b64_tr_b16 v[130:131], v244 offset:18944\n\t"
    "v_fma_mix_f32 v195, v49, v163, v171 op_sel:[1,0,0] op_sel_hi:[1,0,0]\n\t"
    "v_fma_mix_f32 v196, v50, v164, v172 op_sel_hi:[1,0,0]\n\t"
    "v_fma_mix_f32 v197, v50, v165, v173 op_sel:[1,0,0] op_sel_hi:[1,0,0]\n\t"
    "v_fma_mix_f32 v198, v51, v166, v174 op_sel_hi:[1,0,0]\n\t"
    "buffer_load_dwordx4 v222, s[72:75], s67 offen offset:2048 lds\n\t"
    "s_waitcnt lgkmcnt(10)\n\t"
    "v_mfma_f32_32x32x16_bf16 a[32:47], v[148:151], v[40:43], a[32:47]\n\t"
    "ds_read_b64_tr_b16 v[132:133], v244 offset:18688\n\t"
    "v_fma_mix_f32 v199, v51, v167, v175 op_sel:[1,0,0] op_sel_hi:[1,0,0]\n\t"
    "v_cvt_pk_bf16_f32 v48, v192, v193\n\t"
    "v_cvt_pk_bf16_f32 v49, v194, v195\n\t"
    "v_cvt_pk_bf16_f32 v50, v196, v197\n\t"
    "v_mfma_f32_32x32x16_bf16 a[48:63], v[148:151], v[44:47], a[48:63]\n\t"
    "ds_read_b64_tr_b16 v[134:135], v244 offset:19200\n\t"
    "v_cvt_pk_bf16_f32 v51, v198, v199\n\t"
    "v_fma_mix_f32 v200, v52, v160, v168 op_sel_hi:[1,0,0]\n\t"
    "v_fma_mix_f32 v201, v52, v161, v169 op_sel:[1,0,0] op_sel_hi:[1,0,0]\n\t"
    "v_fma_mix_f32 v202, v53, v162, v170 op_sel_hi:[1,0,0]\n\t"
    "buffer_load_dwordx4 v223, s[72:75], s67 offen offset:3072 lds\n\t"
    "s_waitcnt lgkmcnt(10)\n\t"
    "v_mfma_f32_32x32x16_bf16 a[64:79], v[152:155], v[40:43], a[64:79]\n\t"
    "ds_read_b64_tr_b16 v[136:137], v244 offset:26624\n\t"
    "v_fma_mix_f32 v203, v53, v163, v171 op_sel:[1,0,0] op_sel_hi:[1,0,0]\n\t"
    "v_fma_mix_f32 v204, v54, v164, v172 op_sel_hi:[1,0,0]\n\t"
    "v_fma_mix_f32 v205, v54, v165, v173 op_sel:[1,0,0] op_sel_hi:[1,0,0]\n\t"
    "v_fma_mix_f32 v206, v55, v166, v174 op_sel_hi:[1,0,0]\n\t"
    "v_mfma_f32_32x32x16_bf16 a[80:95], v[152:155], v[44:47], a[80:95]\n\t"
    "ds_read_b64_tr_b16 v[138:139], v244 offset:27136\n\t"
    "v_fma_mix_f32 v207, v55, v167, v175 op_sel:[1,0,0] op_sel_hi:[1,0,0]\n\t"
    "v_cvt_pk_bf16_f32 v52, v200, v201\n\t"
    "v_cvt_pk_bf16_f32 v53, v202, v203\n\t"
    "v_cvt_pk_bf16_f32 v54, v204, v205\n\t"
    "s_add_u32 s66, s76, 12288\n\t"
    "buffer_load_dwordx4 v[96:99], v224, s[44:47], s66 offen offset:0\n\t"
    "s_waitcnt lgkmcnt(7)\n\t"
    "v_mfma_f32_32x32x16_bf16 a[96:111], v[156:159], v[40:43], a[96:111]\n\t"
    "ds_read_b64_tr_b16 v[140:141], v244 offset:26880\n\t"
    "v_cvt_pk_bf16_f32 v55, v206, v207\n\t"
    "ds_read_b128 v[160:163], v243 offset:1024\n\t"
    "ds_read_b128 v[164:167], v243 offset:1040\n\t"
    "ds_read_b128 v[168:171], v243 offset:1056\n\t"
    "v_mfma_f32_32x32x16_bf16 a[112:127], v[156:159], v[44:47], a[112:127]\n\t"
    "ds_read_b64_tr_b16 v[142:143], v244 offset:27392\n\t"
    "ds_read_b128 v[172:175], v243 offset:1072\n\t"
    "buffer_load_dwordx4 v[104:107], v224, s[44:47], s66 offen offset:1024\n\t"
    "s_waitcnt lgkmcnt(10)\n\t"
    "v_mfma_f32_32x32x16_bf16 a[0:15], v[128:131], v[48:51], a[0:15]\n\t"
    "ds_read_b64_tr_b16 v[144:145], v244 offset:19456\n\t"
    "v_fma_mix_f32 v192, v56, v176, v184 op_sel_hi:[1,0,0]\n\t"
    "v_fma_mix_f32 v193, v56, v177, v185 op_sel:[1,0,0] op_sel_hi:[1,0,0]\n\t"
    "v_fma_mix_f32 v194, v57, v178, v186 op_sel_hi:[1,0,0]\n\t"
    "v_mfma_f32_32x32x16_bf16 a[16:31], v[128:131], v[52:55], a[16:31]\n\t"
    "ds_read_b64_tr_b16 v[146:147], v244 offset:19968\n\t"
    "v_fma_mix_f32 v195, v57, v179, v187 op_sel:[1,0,0] op_sel_hi:[1,0,0]\n\t"
    "v_fma_mix_f32 v196, v58, v180, v188 op_sel_hi:[1,0,0]\n\t"
    "v_fma_mix_f32 v197, v58, v181, v189 op_sel:[1,0,0] op_sel_hi:[1,0,0]\n\t"
    "v_fma_mix_f32 v198, v59, v182, v190 op_sel_hi:[1,0,0]\n\t"
    "buffer_load_dwordx4 v[112:115], v224, s[44:47], s66 offen offset:2048\n\t"
    "s_waitcnt lgkmcnt(10)\n\t"
    "v_mfma_f32_32x32x16_bf16 a[32:47], v[132:135], v[48:51], a[32:47]\n\t"
    "ds_read_b64_tr_b16 v[148:149], v244 offset:19712\n\t"
    "v_fma_mix_f32 v199, v59, v183, v191 op_sel:[1,0,0] op_sel_hi:[1,0,0]\n\t"
    "v_cvt_pk_bf16_f32 v56, v192, v193\n\t"
    "v_cvt_pk_bf16_f32 v57, v194, v195\n\t"
    "v_cvt_pk_bf16_f32 v58, v196, v197\n\t"
    "v_mfma_f32_32x32x16_bf16 a[48:63], v[132:135], v[52:55], a[48:63]\n\t"
    "ds_read_b64_tr_b16 v[150:151], v244 offset:20224\n\t"
    "v_cvt_pk_bf16_f32 v59, v198, v199\n\t"
    "v_fma_mix_f32 v200, v60, v176, v184 op_sel_hi:[1,0,0]\n\t"
    "v_fma_mix_f32 v201, v60, v177, v185 op_sel:[1,0,0] op_sel_hi:[1,0,0]\n\t"
    "v_fma_mix_f32 v202, v61, v178, v186 op_sel_hi:[1,0,0]\n\t"
    "buffer_load_dwordx4 v[120:123], v224, s[44:47], s66 offen offset:3072\n\t"
    "s_waitcnt lgkmcnt(10)\n\t"
    "v_mfma_f32_32x32x16_bf16 a[64:79], v[136:139], v[48:51], a[64:79]\n\t"
    "ds_read_b64_tr_b16 v[152:153], v244 offset:27648\n\t"
    "v_fma_mix_f32 v203, v61, v179, v187 op_sel:[1,0,0] op_sel_hi:[1,0,0]\n\t"
    "v_fma_mix_f32 v204, v62, v180, v188 op_sel_hi:[1,0,0]\n\t"
    "v_fma_mix_f32 v205, v62, v181, v189 op_sel:[1,0,0] op_sel_hi:[1,0,0]\n\t"
    "v_fma_mix_f32 v206, v63, v182, v190 op_sel_hi:[1,0,0]\n\t"
    "v_mfma_f32_32x32x16_bf16 a[80:95], v[136:139], v[52:55], a[80:95]\n\t"
    "ds_read_b64_tr_b16 v[154:155], v244 offset:28160\n\t"
    "v_fma_mix_f32 v207, v63, v183, v191 op_sel:[1,0,0] op_sel_hi:[1,0,0]\n\t"
    "v_cvt_pk_bf16_f32 v60, v200, v201\n\t"
    "v_cvt_pk_bf16_f32 v61, v202, v203\n\t"
    "v_cvt_pk_bf16_f32 v62, v204, v205\n\t"
    "s_add_u32 s66, s76, 45056\n\t"
    "buffer_load_dwordx4 v[100:103], v224, s[44:47], s66 offen offset:0\n\t"
    "s_waitcnt lgkmcnt(7)\n\t"
    "v_mfma_f32_32x32x16_bf16 a[96:111], v[140:143], v[48:51], a[96:111]\n\t"
    "ds_read_b64_tr_b16 v[156:157], v244 offset:27904\n\t"
    "v_cvt_pk_bf16_f32 v63, v206, v207\n\t"
    "ds_read_b128 v[176:179], v243 offset:1152\n\t"
    "ds_read_b128 v[180:183], v243 offset:1168\n\t"
    "ds_read_b128 v[184:187], v243 offset:1184\n\t"
    "v_mfma_f32_32x32x16_bf16 a[112:127], v[140:143], v[52:55], a[112:127]\n\t"
    "ds_read_b64_tr_b16 v[158:159], v244 offset:28416\n\t"
    "ds_read_b128 v[188:191], v243 offset:1200\n\t"
    "buffer_load_dwordx4 v[108:111], v224, s[44:47], s66 offen offset:1024\n\t"
    "s_waitcnt vmcnt(18)\n\t"
    "s_barrier\n\t"
    "s_waitcnt lgkmcnt(10)\n\t"
    "v_mfma_f32_32x32x16_bf16 a[0:15], v[144:147], v[56:59], a[0:15]\n\t"
    "ds_read_b64_tr_b16 v[128:129], v244 offset:32768\n\t"
    "s_waitcnt vmcnt(10)\n\t"
    "v_fma_mix_f32 v192, v64, v160, v168 op_sel_hi:[1,0,0]\n\t"
    "v_fma_mix_f32 v193, v64, v161, v169 op_sel:[1,0,0] op_sel_hi:[1,0,0]\n\t"
    "v_fma_mix_f32 v194, v65, v162, v170 op_sel_hi:[1,0,0]\n\t"
    "v_mfma_f32_32x32x16_bf16 a[16:31], v[144:147], v[60:63], a[16:31]\n\t"
    "ds_read_b64_tr_b16 v[130:131], v244 offset:33280\n\t"
    "v_fma_mix_f32 v195, v65, v163, v171 op_sel:[1,0,0] op_sel_hi:[1,0,0]\n\t"
    "v_fma_mix_f32 v196, v66, v164, v172 op_sel_hi:[1,0,0]\n\t"
    "v_fma_mix_f32 v197, v66, v165, v173 op_sel:[1,0,0] op_sel_hi:[1,0,0]\n\t"
    "v_fma_mix_f32 v198, v67, v166, v174 op_sel_hi:[1,0,0]\n\t"
    "buffer_load_dwordx4 v[116:119], v224, s[44:47], s66 offen offset:2048\n\t"
    "s_waitcnt lgkmcnt(10)\n\t"
    "v_mfma_f32_32x32x16_bf16 a[32:47], v[148:151], v[56:59], a[32:47]\n\t"
    "ds_read_b64_tr_b16 v[132:133], v244 offset:33024\n\t"
    "v_fma_mix_f32 v199, v67, v167, v175 op_sel:[1,0,0] op_sel_hi:[1,0,0]\n\t"
    "v_cvt_pk_bf16_f32 v64, v192, v193\n\t"
    "v_cvt_pk_bf16_f32 v65, v194, v195\n\t"
    "v_cvt_pk_bf16_f32 v66, v196, v197\n\t"
    "v_mfma_f32_32x32x16_bf16 a[48:63], v[148:151], v[60:63], a[48:63]\n\t"
    "ds_read_b64_tr_b16 v[134:135], v244 offset:33536\n\t"
    "v_cvt_pk_bf16_f32 v67, v198, v199\n\t"
    "v_fma_mix_f32 v200, v68, v160, v168 op_sel_hi:[1,0,0]\n\t"
    "v_fma_mix_f32 v201, v68, v161, v169 op_sel:[1,0,0] op_sel_hi:[1,0,0]\n\t"
    "v_fma_mix_f32 v202, v69, v162, v170 op_sel_hi:[1,0,0]\n\t"
    "buffer_load_dwordx4 v[124:127], v224, s[44:47], s66 offen offset:3072\n\t"
    "s_waitcnt lgkmcnt(10)\n\t"
    "v_mfma_f32_32x32x16_bf16 a[64:79], v[152:155], v[56:59], a[64:79]\n\t"
    "ds_read_b64_tr_b16 v[136:137], v244 offset:40960\n\t"
    "v_fma_mix_f32 v203, v69, v163, v171 op_sel:[1,0,0] op_sel_hi:[1,0,0]\n\t"
    "v_fma_mix_f32 v204, v70, v164, v172 op_sel_hi:[1,0,0]\n\t"
    "v_fma_mix_f32 v205, v70, v165, v173 op_sel:[1,0,0] op_sel_hi:[1,0,0]\n\t"
    "v_fma_mix_f32 v206, v71, v166, v174 op_sel_hi:[1,0,0]\n\t"
    "v_mfma_f32_32x32x16_bf16 a[80:95], v[152:155], v[60:63], a[80:95]\n\t"
    "ds_read_b64_tr_b16 v[138:139], v244 offset:41472\n\t"
    "v_fma_mix_f32 v207, v71, v167, v175 op_sel:[1,0,0] op_sel_hi:[1,0,0]\n\t"
    "v_cvt_pk_bf16_f32 v68, v200, v201\n\t"
    "v_cvt_pk_bf16_f32 v69, v202, v203\n\t"
    "v_cvt_pk_bf16_f32 v70, v204, v205\n\t"
    "s_add_u32 m0, s78, 65536\n\t"
    "s_add_u32 s67, s77, 2097152\n\t"
    "buffer_load_dwordx4 v220, s[72:75], s67 offen offset:0 lds\n\t"
    "s_waitcnt lgkmcnt(7)\n\t"
    "v_mfma_f32_32x32x16_bf16 a[96:111], v[156:159], v[56:59], a[96:111]\n\t"
    "ds_read_b64_tr_b16 v[140:141], v244 offset:41216\n\t"
    "v_cvt_pk_bf16_f32 v71, v206, v207\n\t"
    "ds_read_b128 v[160:163], v243 offset:1280\n\t"
    "ds_read_b128 v[164:167], v243 offset:1296\n\t"
    "ds_read_b128 v[168:171], v243 offset:1312\n\t"
    "v_mfma_f32_32x32x16_bf16 a[112:127], v[156:159], v[60:63], a[112:127]\n\t"
    "ds_read_b64_tr_b16 v[142:143], v244 offset:41728\n\t"
    "ds_read_b128 v[172:175], v243 offset:1328\n\t"
    "buffer_load_dwordx4 v221, s[72:75], s67 offen offset:1024 lds\n\t"
    "s_waitcnt lgkmcnt(10)\n\t"
    "v_mfma_f32_32x32x16_bf16 a[0:15], v[128:131], v[64:67], a[0:15]\n\t"
    "ds_read_b64_tr_b16 v[144:145], v244 offset:33792\n\t"
    "v_fma_mix_f32 v192, v72, v176, v184 op_sel_hi:[1,0,0]\n\t"
    "v_fma_mix_f32 v193, v72, v177, v185 op_sel:[1,0,0] op_sel_hi:[1,0,0]\n\t"
    "v_fma_mix_f32 v194, v73, v178, v186 op_sel_hi:[1,0,0]\n\t"
    "v_mfma_f32_32x32x16_bf16 a[16:31], v[128:131], v[68:71], a[16:31]\n\t"
    "ds_read_b64_tr_b16 v[146:147], v244 offset:34304\n\t"
    "v_fma_mix_f32 v195, v73, v179, v187 op_sel:[1,0,0] op_sel_hi:[1,0,0]\n\t"
    "v_fma_mix_f32 v196, v74, v180, v188 op_sel_hi:[1,0,0]\n\t"
    "v_fma_mix_f32 v197, v74, v181, v189 op_sel:[1,0,0] op_sel_hi:[1,0,0]\n\t"
    "v_fma_mix_f32 v198, v75, v182, v190 op_sel_hi:[1,0,0]\n\t"
    "buffer_load_dwordx4 v222, s[72:75], s67 offen offset:2048 lds\n\t"
    "s_waitcnt lgkmcnt(10)\n\t"
    "v_mfma_f32_32x32x16_bf16 a[32:47], v[132:135], v[64:67], a[32:47]\n\t"
    "ds_read_b64_tr_b16 v[148:149], v244 offset:34048\n\t"
    "v_fma_mix_f32 v199, v75, v183, v191 op_sel:[1,0,0] op_sel_hi:[1,0,0]\n\t"
    "v_cvt_pk_bf16_f32 v72, v192, v193\n\t"
    "v_cvt_pk_bf16_f32 v73, v194, v195\n\t"
    "v_cvt_pk_bf16_f32 v74, v196, v197\n\t"
    "v_mfma_f32_32x32x16_bf16 a[48:63], v[132:135], v[68:71], a[48:63]\n\t"
    "ds_read_b64_tr_b16 v[150:151], v244 offset:34560\n\t"
    "v_cvt_pk_bf16_f32 v75, v198, v199\n\t"
    "v_fma_mix_f32 v200, v76, v176, v184 op_sel_hi:[1,0,0]\n\t"
    "v_fma_mix_f32 v201, v76, v177, v185 op_sel:[1,0,0] op_sel_hi:[1,0,0]\n\t"
    "v_fma_mix_f32 v202, v77, v178, v186 op_sel_hi:[1,0,0]\n\t"
    "buffer_load_dwordx4 v223, s[72:75], s67 offen offset:3072 lds\n\t"
    "s_waitcnt lgkmcnt(10)\n\t"
    "v_mfma_f32_32x32x16_bf16 a[64:79], v[136:139], v[64:67], a[64:79]\n\t"
    "ds_read_b64_tr_b16 v[152:153], v244 offset:41984\n\t"
    "v_fma_mix_f32 v203, v77, v179, v187 op_sel:[1,0,0] op_sel_hi:[1,0,0]\n\t"
    "v_fma_mix_f32 v204, v78, v180, v188 op_sel_hi:[1,0,0]\n\t"
    "v_fma_mix_f32 v205, v78, v181, v189 op_sel:[1,0,0] op_sel_hi:[1,0,0]\n\t"
    "v_fma_mix_f32 v206, v79, v182, v190 op_sel_hi:[1,0,0]\n\t"
    "v_mfma_f32_32x32x16_bf16 a[80:95], v[136:139], v[68:71], a[80:95]\n\t"
    "ds_read_b64_tr_b16 v[154:155], v244 offset:42496\n\t"
    "v_fma_mix_f32 v207, v79, v183, v191 op_sel:[1,0,0] op_sel_hi:[1,0,0]\n\t"
    "v_cvt_pk_bf16_f32 v76, v200, v201\n\t"
    "v_cvt_pk_bf16_f32 v77, v202, v203\n\t"
    "v_cvt_pk_bf16_f32 v78, v204, v205\n\t"
    "s_add_u32 s66, s76, 16384\n\t"
    "buffer_load_dwordx4 v[0:3], v224, s[44:47], s66 offen offset:0\n\t"
    "s_waitcnt lgkmcnt(7)\n\t"
    "v_mfma_f32_32x32x16_bf16 a[96:111], v[140:143], v[64:67], a[96:111]\n\t"
    "ds_read_b64_tr_b16 v[156:157], v244 offset:42240\n\t"
    "v_cvt_pk_bf16_f32 v79, v206, v207\n\t"
    "ds_read_b128 v[176:179], v243 offset:1408\n\t"
    "ds_read_b128 v[180:183], v243 offset:1424\n\t"
    "ds_read_b128 v[184:187], v243 offset:1440\n\t"
    "v_mfma_f32_32x32x16_bf16 a[112:127], v[140:143], v[68:71], a[112:127]\n\t"
    "ds_read_b64_tr_b16 v[158:159], v244 offset:42752\n\t"
    "ds_read_b128 v[188:191], v243 offset:1456\n\t"
    "buffer_load_dwordx4 v[8:11], v224, s[44:47], s66 offen offset:1024\n\t"
    "s_waitcnt lgkmcnt(10)\n\t"
    "v_mfma_f32_32x32x16_bf16 a[0:15], v[144:147], v[72:75], a[0:15]\n\t"
    "ds_read_b64_tr_b16 v[128:129], v244 offset:34816\n\t"
    "v_fma_mix_f32 v192, v80, v160, v168 op_sel_hi:[1,0,0]\n\t"
    "v_fma_mix_f32 v193, v80, v161, v169 op_sel:[1,0,0] op_sel_hi:[1,0,0]\n\t"
    "v_fma_mix_f32 v194, v81, v162, v170 op_sel_hi:[1,0,0]\n\t"
    "v_mfma_f32_32x32x16_bf16 a[16:31], v[144:147], v[76:79], a[16:31]\n\t"
    "ds_read_b64_tr_b16 v[130:131], v244 offset:35328\n\t"
    "v_fma_mix_f32 v195, v81, v163, v171 op_sel:[1,0,0] op_sel_hi:[1,0,0]\n\t"
    "v_fma_mix_f32 v196, v82, v164, v172 op_sel_hi:[1,0,0]\n\t"
    "v_fma_mix_f32 v197, v82, v165, v173 op_sel:[1,0,0] op_sel_hi:[1,0,0]\n\t"
    "v_fma_mix_f32 v198, v83, v166, v174 op_sel_hi:[1,0,0]\n\t"
    "buffer_load_dwordx4 v[16:19], v224, s[44:47], s66 offen offset:2048\n\t"
    "s_waitcnt lgkmcnt(10)\n\t"
    "v_mfma_f32_32x32x16_bf16 a[32:47], v[148:151], v[72:75], a[32:47]\n\t"
    "ds_read_b64_tr_b16 v[132:133], v244 offset:35072\n\t"
    "v_fma_mix_f32 v199, v83, v167, v175 op_sel:[1,0,0] op_sel_hi:[1,0,0]\n\t"
    "v_cvt_pk_bf16_f32 v80, v192, v193\n\t"
    "v_cvt_pk_bf16_f32 v81, v194, v195\n\t"
    "v_cvt_pk_bf16_f32 v82, v196, v197\n\t"
    "v_mfma_f32_32x32x16_bf16 a[48:63], v[148:151], v[76:79], a[48:63]\n\t"
    "ds_read_b64_tr_b16 v[134:135], v244 offset:35584\n\t"
    "v_cvt_pk_bf16_f32 v83, v198, v199\n\t"
    "v_fma_mix_f32 v200, v84, v160, v168 op_sel_hi:[1,0,0]\n\t"
    "v_fma_mix_f32 v201, v84, v161, v169 op_sel:[1,0,0] op_sel_hi:[1,0,0]\n\t"
    "v_fma_mix_f32 v202, v85, v162, v170 op_sel_hi:[1,0,0]\n\t"
    "buffer_load_dwordx4 v[24:27], v224, s[44:47], s66 offen offset:3072\n\t"
    "s_waitcnt lgkmcnt(10)\n\t"
    "v_mfma_f32_32x32x16_bf16 a[64:79], v[152:155], v[72:75], a[64:79]\n\t"
    "ds_read_b64_tr_b16 v[136:137], v244 offset:43008\n\t"
    "v_fma_mix_f32 v203, v85, v163, v171 op_sel:[1,0,0] op_sel_hi:[1,0,0]\n\t"
    "v_fma_mix_f32 v204, v86, v164, v172 op_sel_hi:[1,0,0]\n\t"
    "v_fma_mix_f32 v205, v86, v165, v173 op_sel:[1,0,0] op_sel_hi:[1,0,0]\n\t"
    "v_fma_mix_f32 v206, v87, v166, v174 op_sel_hi:[1,0,0]\n\t"
    "v_mfma_f32_32x32x16_bf16 a[80:95], v[152:155], v[76:79], a[80:95]\n\t"
    "ds_read_b64_tr_b16 v[138:139], v244 offset:43520\n\t"
    "v_fma_mix_f32 v207, v87, v167, v175 op_sel:[1,0,0] op_sel_hi:[1,0,0]\n\t"
    "v_cvt_pk_bf16_f32 v84, v200, v201\n\t"
    "v_cvt_pk_bf16_f32 v85, v202, v203\n\t"
    "v_cvt_pk_bf16_f32 v86, v204, v205\n\t"
    "s_add_u32 s66, s76, 49152\n\t"
    "buffer_load_dwordx4 v[4:7], v224, s[44:47], s66 offen offset:0\n\t"
    "s_waitcnt lgkmcnt(7)\n\t"
    "v_mfma_f32_32x32x16_bf16 a[96:111], v[156:159], v[72:75], a[96:111]\n\t"
    "ds_read_b64_tr_b16 v[140:141], v244 offset:43264\n\t"
    "v_cvt_pk_bf16_f32 v87, v206, v207\n\t"
    "ds_read_b128 v[160:163], v243 offset:1536\n\t"
    "ds_read_b128 v[164:167], v243 offset:1552\n\t"
    "ds_read_b128 v[168:171], v243 offset:1568\n\t"
    "v_mfma_f32_32x32x16_bf16 a[112:127], v[156:159], v[76:79], a[112:127]\n\t"
    "ds_read_b64_tr_b16 v[142:143], v244 offset:43776\n\t"
    "ds_read_b128 v[172:175], v243 offset:1584\n\t"
    "buffer_load_dwordx4 v[12:15], v224, s[44:47], s66 offen offset:1024\n\t"
    "s_waitcnt lgkmcnt(10)\n\t"
    "v_mfma_f32_32x32x16_bf16 a[0:15], v[128:131], v[80:83], a[0:15]\n\t"
    "ds_read_b64_tr_b16 v[144:145], v244 offset:35840\n\t"
    "v_fma_mix_f32 v192, v88, v176, v184 op_sel_hi:[1,0,0]\n\t"
    "v_fma_mix_f32 v193, v88, v177, v185 op_sel:[1,0,0] op_sel_hi:[1,0,0]\n\t"
    "v_fma_mix_f32 v194, v89, v178, v186 op_sel_hi:[1,0,0]\n\t"
    "v_mfma_f32_32x32x16_bf16 a[16:31], v[128:131], v[84:87], a[16:31]\n\t"
    "ds_read_b64_tr_b16 v[146:147], v244 offset:36352\n\t"
    "v_fma_mix_f32 v195, v89, v179, v187 op_sel:[1,0,0] op_sel_hi:[1,0,0]\n\t"
    "v_fma_mix_f32 v196, v90, v180, v188 op_sel_hi:[1,0,0]\n\t"
    "v_fma_mix_f32 v197, v90, v181, v189 op_sel:[1,0,0] op_sel_hi:[1,0,0]\n\t"
    "v_fma_mix_f32 v198, v91, v182, v190 op_sel_hi:[1,0,0]\n\t"
    "buffer_load_dwordx4 v[20:23], v224, s[44:47], s66 offen offset:2048\n\t"
    "s_waitcnt lgkmcnt(10)\n\t"
    "v_mfma_f32_32x32x16_bf16 a[32:47], v[132:135], v[80:83], a[32:47]\n\t"
    "ds_read_b64_tr_b16 v[148:149], v244 offset:36096\n\t"
    "v_fma_mix_f32 v199, v91, v183, v191 op_sel:[1,0,0] op_sel_hi:[1,0,0]\n\t"
    "v_cvt_pk_bf16_f32 v88, v192, v193\n\t"
    "v_cvt_pk_bf16_f32 v89, v194, v195\n\t"
    "v_cvt_pk_bf16_f32 v90, v196, v197\n\t"
    "v_mfma_f32_32x32x16_bf16 a[48:63], v[132:135], v[84:87], a[48:63]\n\t"
    "ds_read_b64_tr_b16 v[150:151], v244 offset:36608\n\t"
    "v_cvt_pk_bf16_f32 v91, v198, v199\n\t"
    "v_fma_mix_f32 v200, v92, v176, v184 op_sel_hi:[1,0,0]\n\t"
    "v_fma_mix_f32 v201, v92, v177, v185 op_sel:[1,0,0] op_sel_hi:[1,0,0]\n\t"
    "v_fma_mix_f32 v202, v93, v178, v186 op_sel_hi:[1,0,0]\n\t"
    "buffer_load_dwordx4 v[28:31], v224, s[44:47], s66 offen offset:3072\n\t"
    "s_waitcnt lgkmcnt(10)\n\t"
    "v_mfma_f32_32x32x16_bf16 a[64:79], v[136:139], v[80:83], a[64:79]\n\t"
    "ds_read_b64_tr_b16 v[152:153], v244 offset:44032\n\t"
    "v_fma_mix_f32 v203, v93, v179, v187 op_sel:[1,0,0] op_sel_hi:[1,0,0]\n\t"
    "v_fma_mix_f32 v204, v94, v180, v188 op_sel_hi:[1,0,0]\n\t"
    "v_fma_mix_f32 v205, v94, v181, v189 op_sel:[1,0,0] op_sel_hi:[1,0,0]\n\t"
    "v_fma_mix_f32 v206, v95, v182, v190 op_sel_hi:[1,0,0]\n\t"
    "v_mfma_f32_32x32x16_bf16 a[80:95], v[136:139], v[84:87], a[80:95]\n\t"
    "ds_read_b64_tr_b16 v[154:155], v244 offset:44544\n\t"
    "v_fma_mix_f32 v207, v95, v183, v191 op_sel:[1,0,0] op_sel_hi:[1,0,0]\n\t"
    "v_cvt_pk_bf16_f32 v92, v200, v201\n\t"
    "v_cvt_pk_bf16_f32 v93, v202, v203\n\t"
    "v_cvt_pk_bf16_f32 v94, v204, v205\n\t"
    "s_add_u32 m0, s78, 81920\n\t"
    "s_add_u32 s67, s77, 2621440\n\t"
    "buffer_load_dwordx4 v220, s[72:75], s67 offen offset:0 lds\n\t"
    "s_waitcnt lgkmcnt(7)\n\t"
    "v_mfma_f32_32x32x16_bf16 a[96:111], v[140:143], v[80:83], a[96:111]\n\t"
    "ds_read_b64_tr_b16 v[156:157], v244 offset:44288\n\t"
    "v_cvt_pk_bf16_f32 v95, v206, v207\n\t"
    "ds_read_b128 v[176:179], v243 offset:1664\n\t"
    "ds_read_b128 v[180:183], v243 offset:1680\n\t"
    "ds_read_b128 v[184:187], v243 offset:1696\n\t"
    "v_mfma_f32_32x32x16_bf16 a[112:127], v[140:143], v[84:87], a[112:127]\n\t"
    "ds_read_b64_tr_b16 v[158:159], v244 offset:44800\n\t"
    "ds_read_b128 v[188:191], v243 offset:1712\n\t"
    "buffer_load_dwordx4 v221, s[72:75], s67 offen offset:1024 lds\n\t"
    "s_waitcnt vmcnt(22)\n\t"
    "s_barrier\n\t"
    "s_waitcnt lgkmcnt(10)\n\t"
    "v_mfma_f32_32x32x16_bf16 a[0:15], v[144:147], v[88:91], a[0:15]\n\t"
    "ds_read_b64_tr_b16 v[128:129], v244 offset:49152\n\t"
    "s_waitcnt vmcnt(14)\n\t"
    "v_fma_mix_f32 v192, v96, v160, v168 op_sel_hi:[1,0,0]\n\t"
    "v_fma_mix_f32 v193, v96, v161, v169 op_sel:[1,0,0] op_sel_hi:[1,0,0]\n\t"
    "v_fma_mix_f32 v194, v97, v162, v170 op_sel_hi:[1,0,0]\n\t"
    "v_mfma_f32_32x32x16_bf16 a[16:31], v[144:147], v[92:95], a[16:31]\n\t"
    "ds_read_b64_tr_b16 v[130:131], v244 offset:49664\n\t"
    "v_fma_mix_f32 v195, v97, v163, v171 op_sel:[1,0,0] op_sel_hi:[1,0,0]\n\t"
    "v_fma_mix_f32 v196, v98, v164, v172 op_sel_hi:[1,0,0]\n\t"
    "v_fma_mix_f32 v197, v98, v165, v173 op_sel:[1,0,0] op_sel_hi:[1,0,0]\n\t"
    "v_fma_mix_f32 v198, v99, v166, v174 op_sel_hi:[1,0,0]\n\t"
    "buffer_load_dwordx4 v222, s[72:75], s67 offen offset:2048 lds\n\t"
    "s_waitcnt lgkmcnt(10)\n\t"
    "v_mfma_f32_32x32x16_bf16 a[32:47], v[148:151], v[88:91], a[32:47]\n\t"
    "ds_read_b64_tr_b16 v[132:133], v244 offset:49408\n\t"
    "v_fma_mix_f32 v199, v99, v167, v175 op_sel:[1,0,0] op_sel_hi:[1,0,0]\n\t"
    "v_cvt_pk_bf16_f32 v96, v192, v193\n\t"
    "v_cvt_pk_bf16_f32 v97, v194, v195\n\t"
    "v_cvt_pk_bf16_f32 v98, v196, v197\n\t"
    "v_mfma_f32_32x32x16_bf16 a[48:63], v[148:151], v[92:95], a[48:63]\n\t"
    "ds_read_b64_tr_b16 v[134:135], v244 offset:49920\n\t"
    "v_cvt_pk_bf16_f32 v99, v198, v199\n\t"
    "v_fma_mix_f32 v200, v100, v160, v168 op_sel_hi:[1,0,0]\n\t"
    "v_fma_mix_f32 v201, v100, v161, v169 op_sel:[1,0,0] op_sel_hi:[1,0,0]\n\t"
    "v_fma_mix_f32 v202, v101, v162, v170 op_sel_hi:[1,0,0]\n\t"
    "buffer_load_dwordx4 v223, s[72:75], s67 offen offset:3072 lds\n\t"
    "s_waitcnt lgkmcnt(10)\n\t"
    "v_mfma_f32_32x32x16_bf16 a[64:79], v[152:155], v[88:91], a[64:79]\n\t"
    "ds_read_b64_tr_b16 v[136:137], v244 offset:57344\n\t"
    "v_fma_mix_f32 v203, v101, v163, v171 op_sel:[1,0,0] op_sel_hi:[1,0,0]\n\t"
    "v_fma_mix_f32 v204, v102, v164, v172 op_sel_hi:[1,0,0]\n\t"
    "v_fma_mix_f32 v205, v102, v165, v173 op_sel:[1,0,0] op_sel_hi:[1,0,0]\n\t"
    "v_fma_mix_f32 v206, v103, v166, v174 op_sel_hi:[1,0,0]\n\t"
    "v_mfma_f32_32x32x16_bf16 a[80:95], v[152:155], v[92:95], a[80:95]\n\t"
    "ds_read_b64_tr_b16 v[138:139], v244 offset:57856\n\t"
    "v_fma_mix_f32 v207, v103, v167, v175 op_sel:[1,0,0] op_sel_hi:[1,0,0]\n\t"
    "v_cvt_pk_bf16_f32 v100, v200, v201\n\t"
    "v_cvt_pk_bf16_f32 v101, v202, v203\n\t"
    "v_cvt_pk_bf16_f32 v102, v204, v205\n\t"
    "s_add_u32 s66, s76, 20480\n\t"
    "buffer_load_dwordx4 v[32:35], v224, s[44:47], s66 offen offset:0\n\t"
    "s_waitcnt lgkmcnt(7)\n\t"
    "v_mfma_f32_32x32x16_bf16 a[96:111], v[156:159], v[88:91], a[96:111]\n\t"
    "ds_read_b64_tr_b16 v[140:141], v244 offset:57600\n\t"
    "v_cvt_pk_bf16_f32 v103, v206, v207\n\t"
    "ds_read_b128 v[160:163], v243 offset:1792\n\t"
    "ds_read_b128 v[164:167], v243 offset:1808\n\t"
    "ds_read_b128 v[168:171], v243 offset:1824\n\t"
    "v_mfma_f32_32x32x16_bf16 a[112:127], v[156:159], v[92:95], a[112:127]\n\t"
    "ds_read_b64_tr_b16 v[142:143], v244 offset:58112\n\t"
    "ds_read_b128 v[172:175], v243 offset:1840\n\t"
    "buffer_load_dwordx4 v[40:43], v224, s[44:47], s66 offen offset:1024\n\t"
    "s_waitcnt lgkmcnt(10)\n\t"
    "v_mfma_f32_32x32x16_bf16 a[0:15], v[128:131], v[96:99], a[0:15]\n\t"
    "ds_read_b64_tr_b16 v[144:145], v244 offset:50176\n\t"
    "v_fma_mix_f32 v192, v104, v176, v184 op_sel_hi:[1,0,0]\n\t"
    "v_fma_mix_f32 v193, v104, v177, v185 op_sel:[1,0,0] op_sel_hi:[1,0,0]\n\t"
    "v_fma_mix_f32 v194, v105, v178, v186 op_sel_hi:[1,0,0]\n\t"
    "v_mfma_f32_32x32x16_bf16 a[16:31], v[128:131], v[100:103], a[16:31]\n\t"
    "ds_read_b64_tr_b16 v[146:147], v244 offset:50688\n\t"
    "v_fma_mix_f32 v195, v105, v179, v187 op_sel:[1,0,0] op_sel_hi:[1,0,0]\n\t"
    "v_fma_mix_f32 v196, v106, v180, v188 op_sel_hi:[1,0,0]\n\t"
    "v_fma_mix_f32 v197, v106, v181, v189 op_sel:[1,0,0] op_sel_hi:[1,0,0]\n\t"
    "v_fma_mix_f32 v198, v107, v182, v190 op_sel_hi:[1,0,0]\n\t"
    "buffer_load_dwordx4 v[48:51], v224, s[44:47], s66 offen offset:2048\n\t"
    "s_waitcnt lgkmcnt(10)\n\t"
    "v_mfma_f32_32x32x16_bf16 a[32:47], v[132:135], v[96:99], a[32:47]\n\t"
    "ds_read_b64_tr_b16 v[148:149], v244 offset:50432\n\t"
    "v_fma_mix_f32 v199, v107, v183, v191 op_sel:[1,0,0] op_sel_hi:[1,0,0]\n\t"
    "v_cvt_pk_bf16_f32 v104, v192, v193\n\t"
    "v_cvt_pk_bf16_f32 v105, v194, v195\n\t"
    "v_cvt_pk_bf16_f32 v106, v196, v197\n\t"
    "v_mfma_f32_32x32x16_bf16 a[48:63], v[132:135], v[100:103], a[48:63]\n\t"
    "ds_read_b64_tr_b16 v[150:151], v244 offset:50944\n\t"
    "v_cvt_pk_bf16_f32 v107, v198, v199\n\t"
    "v_fma_mix_f32 v200, v108, v176, v184 op_sel_hi:[1,0,0]\n\t"
    "v_fma_mix_f32 v201, v108, v177, v185 op_sel:[1,0,0] op_sel_hi:[1,0,0]\n\t"
    "v_fma_mix_f32 v202, v109, v178, v186 op_sel_hi:[1,0,0]\n\t"
    "buffer_load_dwordx4 v[56:59], v224, s[44:47], s66 offen offset:3072\n\t"
    "s_waitcnt lgkmcnt(10)\n\t"
    "v_mfma_f32_32x32x16_bf16 a[64:79], v[136:139], v[96:99], a[64:79]\n\t"
    "ds_read_b64_tr_b16 v[152:153], v244 offset:58368\n\t"
    "v_fma_mix_f32 v203, v109, v179, v187 op_sel:[1,0,0] op_sel_hi:[1,0,0]\n\t"
    "v_fma_mix_f32 v204, v110, v180, v188 op_sel_hi:[1,0,0]\n\t"
    "v_fma_mix_f32 v205, v110, v181, v189 op_sel:[1,0,0] op_sel_hi:[1,0,0]\n\t"
    "v_fma_mix_f32 v206, v111, v182, v190 op_sel_hi:[1,0,0]\n\t"
    "v_mfma_f32_32x32x16_bf16 a[80:95], v[136:139], v[100:103], a[80:95]\n\t"
    "ds_read_b64_tr_b16 v[154:155], v244 offset:58880\n\t"
    "v_fma_mix_f32 v207, v111, v183, v191 op_sel:[1,0,0] op_sel_hi:[1,0,0]\n\t"
    "v_cvt_pk_bf16_f32 v108, v200, v201\n\t"
    "v_cvt_pk_bf16_f32 v109, v202, v203\n\t"
    "v_cvt_pk_bf16_f32 v110, v204, v205\n\t"
    "s_add_u32 s66, s76, 53248\n\t"
    "buffer_load_dwordx4 v[36:39], v224, s[44:47], s66 offen offset:0\n\t"
    "s_waitcnt lgkmcnt(7)\n\t"
    "v_mfma_f32_32x32x16_bf16 a[96:111], v[140:143], v[96:99], a[96:111]\n\t"
    "ds_read_b64_tr_b16 v[156:157], v244 offset:58624\n\t"
    "v_cvt_pk_bf16_f32 v111, v206, v207\n\t"
    "ds_read_b128 v[176:179], v243 offset:1920\n\t"
    "ds_read_b128 v[180:183], v243 offset:1936\n\t"
    "ds_read_b128 v[184:187], v243 offset:1952\n\t"
    "v_mfma_f32_32x32x16_bf16 a[112:127], v[140:143], v[100:103], a[112:127]\n\t"
    "ds_read_b64_tr_b16 v[158:159], v244 offset:59136\n\t"
    "ds_read_b128 v[188:191], v243 offset:1968\n\t"
    "buffer_load_dwordx4 v[44:47], v224, s[44:47], s66 offen offset:1024\n\t"
    "s_waitcnt lgkmcnt(10)\n\t"
    "v_mfma_f32_32x32x16_bf16 a[0:15], v[144:147], v[104:107], a[0:15]\n\t"
    "ds_read_b64_tr_b16 v[128:129], v244 offset:51200\n\t"
    "v_fma_mix_f32 v192, v112, v160, v168 op_sel_hi:[1,0,0]\n\t"
    "v_fma_mix_f32 v193, v112, v161, v169 op_sel:[1,0,0] op_sel_hi:[1,0,0]\n\t"
    "v_fma_mix_f32 v194, v113, v162, v170 op_sel_hi:[1,0,0]\n\t"
    "v_mfma_f32_32x32x16_bf16 a[16:31], v[144:147], v[108:111], a[16:31]\n\t"
    "ds_read_b64_tr_b16 v[130:131], v244 offset:51712\n\t"
    "v_fma_mix_f32 v195, v113, v163, v171 op_sel:[1,0,0] op_sel_hi:[1,0,0]\n\t"
    "v_fma_mix_f32 v196, v114, v164, v172 op_sel_hi:[1,0,0]\n\t"
    "v_fma_mix_f32 v197, v114, v165, v173 op_sel:[1,0,0] op_sel_hi:[1,0,0]\n\t"
    "v_fma_mix_f32 v198, v115, v166, v174 op_sel_hi:[1,0,0]\n\t"
    "buffer_load_dwordx4 v[52:55], v224, s[44:47], s66 offen offset:2048\n\t"
    "s_waitcnt lgkmcnt(10)\n\t"
    "v_mfma_f32_32x32x16_bf16 a[32:47], v[148:151], v[104:107], a[32:47]\n\t"
    "ds_read_b64_tr_b16 v[132:133], v244 offset:51456\n\t"
    "v_fma_mix_f32 v199, v115, v167, v175 op_sel:[1,0,0] op_sel_hi:[1,0,0]\n\t"
    "v_cvt_pk_bf16_f32 v112, v192, v193\n\t"
    "v_cvt_pk_bf16_f32 v113, v194, v195\n\t"
    "v_cvt_pk_bf16_f32 v114, v196, v197\n\t"
    "v_mfma_f32_32x32x16_bf16 a[48:63], v[148:151], v[108:111], a[48:63]\n\t"
    "ds_read_b64_tr_b16 v[134:135], v244 offset:51968\n\t"
    "v_cvt_pk_bf16_f32 v115, v198, v199\n\t"
    "v_fma_mix_f32 v200, v116, v160, v168 op_sel_hi:[1,0,0]\n\t"
    "v_fma_mix_f32 v201, v116, v161, v169 op_sel:[1,0,0] op_sel_hi:[1,0,0]\n\t"
    "v_fma_mix_f32 v202, v117, v162, v170 op_sel_hi:[1,0,0]\n\t"
    "buffer_load_dwordx4 v[60:63], v224, s[44:47], s66 offen offset:3072\n\t"
    "s_waitcnt lgkmcnt(10)\n\t"
    "v_mfma_f32_32x32x16_bf16 a[64:79], v[152:155], v[104:107], a[64:79]\n\t"
    "ds_read_b64_tr_b16 v[136:137], v244 offset:59392\n\t"
    "v_fma_mix_f32 v203, v117, v163, v171 op_sel:[1,0,0] op_sel_hi:[1,0,0]\n\t"
    "v_fma_mix_f32 v204, v118, v164, v172 op_sel_hi:[1,0,0]\n\t"
    "v_fma_mix_f32 v205, v118, v165, v173 op_sel:[1,0,0] op_sel_hi:[1,0,0]\n\t"
    "v_fma_mix_f32 v206, v119, v166, v174 op_sel_hi:[1,0,0]\n\t"
    "v_mfma_f32_32x32x16_bf16 a[80:95], v[152:155], v[108:111], a[80:95]\n\t"
    "ds_read_b64_tr_b16 v[138:139], v244 offset:59904\n\t"
    "v_fma_mix_f32 v207, v119, v167, v175 op_sel:[1,0,0] op_sel_hi:[1,0,0]\n\t"
    "v_cvt_pk_bf16_f32 v116, v200, v201\n\t"
    "v_cvt_pk_bf16_f32 v117, v202, v203\n\t"
    "v_cvt_pk_bf16_f32 v118, v204, v205\n\t"
    "s_add_u32 m0, s78, 98304\n\t"
    "s_add_u32 s67, s77, 3145728\n\t"
    "buffer_load_dwordx4 v220, s[72:75], s67 offen offset:0 lds\n\t"
    "s_waitcnt lgkmcnt(7)\n\t"
    "v_mfma_f32_32x32x16_bf16 a[96:111], v[156:159], v[104:107], a[96:111]\n\t"
    "ds_read_b64_tr_b16 v[140:141], v244 offset:59648\n\t"
    "v_cvt_pk_bf16_f32 v119, v206, v207\n\t"
    "ds_read_b128 v[160:163], v243 offset:2048\n\t"
    "ds_read_b128 v[164:167], v243 offset:2064\n\t"
    "ds_read_b128 v[168:171], v243 offset:2080\n\t"
    "v_mfma_f32_32x32x16_bf16 a[112:127], v[156:159], v[108:111], a[112:127]\n\t"
    "ds_read_b64_tr_b16 v[142:143], v244 offset:60160\n\t"
    "ds_read_b128 v[172:175], v243 offset:2096\n\t"
    "buffer_load_dwordx4 v221, s[72:75], s67 offen offset:1024 lds\n\t"
    "s_waitcnt lgkmcnt(10)\n\t"
    "v_mfma_f32_32x32x16_bf16 a[0:15], v[128:131], v[112:115], a[0:15]\n\t"
    "ds_read_b64_tr_b16 v[144:145], v244 offset:52224\n\t"
    "v_fma_mix_f32 v192, v120, v176, v184 op_sel_hi:[1,0,0]\n\t"
    "v_fma_mix_f32 v193, v120, v177, v185 op_sel:[1,0,0] op_sel_hi:[1,0,0]\n\t"
    "v_fma_mix_f32 v194, v121, v178, v186 op_sel_hi:[1,0,0]\n\t"
    "v_mfma_f32_32x32x16_bf16 a[16:31], v[128:131], v[116:119], a[16:31]\n\t"
    "ds_read_b64_tr_b16 v[146:147], v244 offset:52736\n\t"
    "v_fma_mix_f32 v195, v121, v179, v187 op_sel:[1,0,0] op_sel_hi:[1,0,0]\n\t"
    "v_fma_mix_f32 v196, v122, v180, v188 op_sel_hi:[1,0,0]\n\t"
    "v_fma_mix_f32 v197, v122, v181, v189 op_sel:[1,0,0] op_sel_hi:[1,0,0]\n\t"
    "v_fma_mix_f32 v198, v123, v182, v190 op_sel_hi:[1,0,0]\n\t"
    "buffer_load_dwordx4 v222, s[72:75], s67 offen offset:2048 lds\n\t"
    "s_waitcnt lgkmcnt(10)\n\t"
    "v_mfma_f32_32x32x16_bf16 a[32:47], v[132:135], v[112:115], a[32:47]\n\t"
    "ds_read_b64_tr_b16 v[148:149], v244 offset:52480\n\t"
    "v_fma_mix_f32 v199, v123, v183, v191 op_sel:[1,0,0] op_sel_hi:[1,0,0]\n\t"
    "v_cvt_pk_bf16_f32 v120, v192, v193\n\t"
    "v_cvt_pk_bf16_f32 v121, v194, v195\n\t"
    "v_cvt_pk_bf16_f32 v122, v196, v197\n\t"
    "v_mfma_f32_32x32x16_bf16 a[48:63], v[132:135], v[116:119], a[48:63]\n\t"
    "ds_read_b64_tr_b16 v[150:151], v244 offset:52992\n\t"
    "v_cvt_pk_bf16_f32 v123, v198, v199\n\t"
    "v_fma_mix_f32 v200, v124, v176, v184 op_sel_hi:[1,0,0]\n\t"
    "v_fma_mix_f32 v201, v124, v177, v185 op_sel:[1,0,0] op_sel_hi:[1,0,0]\n\t"
    "v_fma_mix_f32 v202, v125, v178, v186 op_sel_hi:[1,0,0]\n\t"
    "buffer_load_dwordx4 v223, s[72:75], s67 offen offset:3072 lds\n\t"
    "s_waitcnt lgkmcnt(10)\n\t"
    "v_mfma_f32_32x32x16_bf16 a[64:79], v[136:139], v[112:115], a[64:79]\n\t"
    "ds_read_b64_tr_b16 v[152:153], v244 offset:60416\n\t"
    "v_fma_mix_f32 v203, v125, v179, v187 op_sel:[1,0,0] op_sel_hi:[1,0,0]\n\t"
    "v_fma_mix_f32 v204, v126, v180, v188 op_sel_hi:[1,0,0]\n\t"
    "v_fma_mix_f32 v205, v126, v181, v189 op_sel:[1,0,0] op_sel_hi:[1,0,0]\n\t"
    "v_fma_mix_f32 v206, v127, v182, v190 op_sel_hi:[1,0,0]\n\t"
    "v_mfma_f32_32x32x16_bf16 a[80:95], v[136:139], v[116:119], a[80:95]\n\t"
    "ds_read_b64_tr_b16 v[154:155], v244 offset:60928\n\t"
    "v_fma_mix_f32 v207, v127, v183, v191 op_sel:[1,0,0] op_sel_hi:[1,0,0]\n\t"
    "v_cvt_pk_bf16_f32 v124, v200, v201\n\t"
    "v_cvt_pk_bf16_f32 v125, v202, v203\n\t"
    "v_cvt_pk_bf16_f32 v126, v204, v205\n\t"
    "s_add_u32 s66, s76, 24576\n\t"
    "buffer_load_dwordx4 v[64:67], v224, s[44:47], s66 offen offset:0\n\t"
    "s_waitcnt lgkmcnt(7)\n\t"
    "v_mfma_f32_32x32x16_bf16 a[96:111], v[140:143], v[112:115], a[96:111]\n\t"
    "ds_read_b64_tr_b16 v[156:157], v244 offset:60672\n\t"
    "v_cvt_pk_bf16_f32 v127, v206, v207\n\t"
    "ds_read_b128 v[176:179], v243 offset:2176\n\t"
    "ds_read_b128 v[180:183], v243 offset:2192\n\t"
    "ds_read_b128 v[184:187], v243 offset:2208\n\t"
    "v_mfma_f32_32x32x16_bf16 a[112:127], v[140:143], v[116:119], a[112:127]\n\t"
    "ds_read_b64_tr_b16 v[158:159], v244 offset:61184\n\t"
    "ds_read_b128 v[188:191], v243 offset:2224\n\t"
    "buffer_load_dwordx4 v[72:75], v224, s[44:47], s66 offen offset:1024\n\t"
    "s_waitcnt vmcnt(26)\n\t"
    "s_barrier\n\t"
    "s_waitcnt lgkmcnt(10)\n\t"
    "v_mfma_f32_32x32x16_bf16 a[0:15], v[144:147], v[120:123], a[0:15]\n\t"
    "ds_read_b64_tr_b16 v[128:129], v245 offset:0\n\t"
    "s_waitcnt vmcnt(18)\n\t"
    "v_fma_mix_f32 v192, v0, v160, v168 op_sel_hi:[1,0,0]\n\t"
    "v_fma_mix_f32 v193, v0, v161, v169 op_sel:[1,0,0] op_sel_hi:[1,0,0]\n\t"
    "v_fma_mix_f32 v194, v1, v162, v170 op_sel_hi:[1,0,0]\n\t"
    "v_mfma_f32_32x32x16_bf16 a[16:31], v[144:147], v[124:127], a[16:31]\n\t"
    "ds_read_b64_tr_b16 v[130:131], v245 offset:512\n\t"
    "v_fma_mix_f32 v195, v1, v163, v171 op_sel:[1,0,0] op_sel_hi:[1,0,0]\n\t"
    "v_fma_mix_f32 v196, v2, v164, v172 op_sel_hi:[1,0,0]\n\t"
    "v_fma_mix_f32 v197, v2, v165, v173 op_sel:[1,0,0] op_sel_hi:[1,0,0]\n\t"
    "v_fma_mix_f32 v198, v3, v166, v174 op_sel_hi:[1,0,0]\n\t"
    "buffer_load_dwordx4 v[80:83], v224, s[44:47], s66 offen offset:2048\n\t"
    "s_waitcnt lgkmcnt(10)\n\t"
    "v_mfma_f32_32x32x16_bf16 a[32:47], v[148:151], v[120:123], a[32:47]\n\t"
    "ds_read_b64_tr_b16 v[132:133], v245 offset:256\n\t"
    "v_fma_mix_f32 v199, v3, v167, v175 op_sel:[1,0,0] op_sel_hi:[1,0,0]\n\t"
    "v_cvt_pk_bf16_f32 v0, v192, v193\n\t"
    "v_cvt_pk_bf16_f32 v1, v194, v195\n\t"
    "v_cvt_pk_bf16_f32 v2, v196, v197\n\t"
    "v_mfma_f32_32x32x16_bf16 a[48:63], v[148:151], v[124:127], a[48:63]\n\t"
    "ds_read_b64_tr_b16 v[134:135], v245 offset:768\n\t"
    "v_cvt_pk_bf16_f32 v3, v198, v199\n\t"
    "v_fma_mix_f32 v200, v4, v160, v168 op_sel_hi:[1,0,0]\n\t"
    "v_fma_mix_f32 v201, v4, v161, v169 op_sel:[1,0,0] op_sel_hi:[1,0,0]\n\t"
    "v_fma_mix_f32 v202, v5, v162, v170 op_sel_hi:[1,0,0]\n\t"
    "buffer_load_dwordx4 v[88:91], v224, s[44:47], s66 offen offset:3072\n\t"
    "s_waitcnt lgkmcnt(10)\n\t"
    "v_mfma_f32_32x32x16_bf16 a[64:79], v[152:155], v[120:123], a[64:79]\n\t"
    "ds_read_b64_tr_b16 v[136:137], v245 offset:8192\n\t"
    "v_fma_mix_f32 v203, v5, v163, v171 op_sel:[1,0,0] op_sel_hi:[1,0,0]\n\t"
    "v_fma_mix_f32 v204, v6, v164, v172 op_sel_hi:[1,0,0]\n\t"
    "v_fma_mix_f32 v205, v6, v165, v173 op_sel:[1,0,0] op_sel_hi:[1,0,0]\n\t"
    "v_fma_mix_f32 v206, v7, v166, v174 op_sel_hi:[1,0,0]\n\t"
    "v_mfma_f32_32x32x16_bf16 a[80:95], v[152:155], v[124:127], a[80:95]\n\t"
    "ds_read_b64_tr_b16 v[138:139], v245 offset:8704\n\t"
    "v_fma_mix_f32 v207, v7, v167, v175 op_sel:[1,0,0] op_sel_hi:[1,0,0]\n\t"
    "v_cvt_pk_bf16_f32 v4, v200, v201\n\t"
    "v_cvt_pk_bf16_f32 v5, v202, v203\n\t"
    "v_cvt_pk_bf16_f32 v6, v204, v205\n\t"
    "s_add_u32 s66, s76, 57344\n\t"
    "buffer_load_dwordx4 v[68:71], v224, s[44:47], s66 offen offset:0\n\t"
    "s_waitcnt lgkmcnt(7)\n\t"
    "v_mfma_f32_32x32x16_bf16 a[96:111], v[156:159], v[120:123], a[96:111]\n\t"
    "ds_read_b64_tr_b16 v[140:141], v245 offset:8448\n\t"
    "v_cvt_pk_bf16_f32 v7, v206, v207\n\t"
    "ds_read_b128 v[160:163], v243 offset:2304\n\t"
    "ds_read_b128 v[164:167], v243 offset:2320\n\t"
    "ds_read_b128 v[168:171], v243 offset:2336\n\t"
    "v_mfma_f32_32x32x16_bf16 a[112:127], v[156:159], v[124:127], a[112:127]\n\t"
    "ds_read_b64_tr_b16 v[142:143], v245 offset:8960\n\t"
    "ds_read_b128 v[172:175], v243 offset:2352\n\t"
    "buffer_load_dwordx4 v[76:79], v224, s[44:47], s66 offen offset:1024\n\t"
    "s_waitcnt lgkmcnt(10)\n\t"
    "v_mfma_f32_32x32x16_bf16 a[0:15], v[128:131], v[0:3], a[0:15]\n\t"
    "ds_read_b64_tr_b16 v[144:145], v245 offset:1024\n\t"
    "v_fma_mix_f32 v192, v8, v176, v184 op_sel_hi:[1,0,0]\n\t"
    "v_fma_mix_f32 v193, v8, v177, v185 op_sel:[1,0,0] op_sel_hi:[1,0,0]\n\t"
    "v_fma_mix_f32 v194, v9, v178, v186 op_sel_hi:[1,0,0]\n\t"
    "v_mfma_f32_32x32x16_bf16 a[16:31], v[128:131], v[4:7], a[16:31]\n\t"
    "ds_read_b64_tr_b16 v[146:147], v245 offset:1536\n\t"
    "v_fma_mix_f32 v195, v9, v179, v187 op_sel:[1,0,0] op_sel_hi:[1,0,0]\n\t"
    "v_fma_mix_f32 v196, v10, v180, v188 op_sel_hi:[1,0,0]\n\t"
    "v_fma_mix_f32 v197, v10, v181, v189 op_sel:[1,0,0] op_sel_hi:[1,0,0]\n\t"
    "v_fma_mix_f32 v198, v11, v182, v190 op_sel_hi:[1,0,0]\n\t"
    "buffer_load_dwordx4 v[84:87], v224, s[44:47], s66 offen offset:2048\n\t"
    "s_waitcnt lgkmcnt(10)\n\t"
    "v_mfma_f32_32x32x16_bf16 a[32:47], v[132:135], v[0:3], a[32:47]\n\t"
    "ds_read_b64_tr_b16 v[148:149], v245 offset:1280\n\t"
    "v_fma_mix_f32 v199, v11, v183, v191 op_sel:[1,0,0] op_sel_hi:[1,0,0]\n\t"
    "v_cvt_pk_bf16_f32 v8, v192, v193\n\t"
    "v_cvt_pk_bf16_f32 v9, v194, v195\n\t"
    "v_cvt_pk_bf16_f32 v10, v196, v197\n\t"
    "v_mfma_f32_32x32x16_bf16 a[48:63], v[132:135], v[4:7], a[48:63]\n\t"
    "ds_read_b64_tr_b16 v[150:151], v245 offset:1792\n\t"
    "v_cvt_pk_bf16_f32 v11, v198, v199\n\t"
    "v_fma_mix_f32 v200, v12, v176, v184 op_sel_hi:[1,0,0]\n\t"
    "v_fma_mix_f32 v201, v12, v177, v185 op_sel:[1,0,0] op_sel_hi:[1,0,0]\n\t"
    "v_fma_mix_f32 v202, v13, v178, v186 op_sel_hi:[1,0,0]\n\t"
    "buffer_load_dwordx4 v[92:95], v224, s[44:47], s66 offen offset:3072\n\t"
    "s_waitcnt lgkmcnt(10)\n\t"
    "v_mfma_f32_32x32x16_bf16 a[64:79], v[136:139], v[0:3], a[64:79]\n\t"
    "ds_read_b64_tr_b16 v[152:153], v245 offset:9216\n\t"
    "v_fma_mix_f32 v203, v13, v179, v187 op_sel:[1,0,0] op_sel_hi:[1,0,0]\n\t"
    "v_fma_mix_f32 v204, v14, v180, v188 op_sel_hi:[1,0,0]\n\t"
    "v_fma_mix_f32 v205, v14, v181, v189 op_sel:[1,0,0] op_sel_hi:[1,0,0]\n\t"
    "v_fma_mix_f32 v206, v15, v182, v190 op_sel_hi:[1,0,0]\n\t"
    "v_mfma_f32_32x32x16_bf16 a[80:95], v[136:139], v[4:7], a[80:95]\n\t"
    "ds_read_b64_tr_b16 v[154:155], v245 offset:9728\n\t"
    "v_fma_mix_f32 v207, v15, v183, v191 op_sel:[1,0,0] op_sel_hi:[1,0,0]\n\t"
    "v_cvt_pk_bf16_f32 v12, v200, v201\n\t"
    "v_cvt_pk_bf16_f32 v13, v202, v203\n\t"
    "v_cvt_pk_bf16_f32 v14, v204, v205\n\t"
    "s_add_u32 m0, s78, 114688\n\t"
    "s_add_u32 s67, s77, 3670016\n\t"
    "buffer_load_dwordx4 v220, s[72:75], s67 offen offset:0 lds\n\t"
    "s_waitcnt lgkmcnt(7)\n\t"
    "v_mfma_f32_32x32x16_bf16 a[96:111], v[140:143], v[0:3], a[96:111]\n\t"
    "ds_read_b64_tr_b16 v[156:157], v245 offset:9472\n\t"
    "v_cvt_pk_bf16_f32 v15, v206, v207\n\t"
    "ds_read_b128 v[176:179], v243 offset:2432\n\t"
    "ds_read_b128 v[180:183], v243 offset:2448\n\t"
    "ds_read_b128 v[184:187], v243 offset:2464\n\t"
    "v_mfma_f32_32x32x16_bf16 a[112:127], v[140:143], v[4:7], a[112:127]\n\t"
    "ds_read_b64_tr_b16 v[158:159], v245 offset:9984\n\t"
    "ds_read_b128 v[188:191], v243 offset:2480\n\t"
    "buffer_load_dwordx4 v221, s[72:75], s67 offen offset:1024 lds\n\t"
    "s_waitcnt lgkmcnt(10)\n\t"
    "v_mfma_f32_32x32x16_bf16 a[0:15], v[144:147], v[8:11], a[0:15]\n\t"
    "ds_read_b64_tr_b16 v[128:129], v245 offset:2048\n\t"
    "v_fma_mix_f32 v192, v16, v160, v168 op_sel_hi:[1,0,0]\n\t"
    "v_fma_mix_f32 v193, v16, v161, v169 op_sel:[1,0,0] op_sel_hi:[1,0,0]\n\t"
    "v_fma_mix_f32 v194, v17, v162, v170 op_sel_hi:[1,0,0]\n\t"
    "v_mfma_f32_32x32x16_bf16 a[16:31], v[144:147], v[12:15], a[16:31]\n\t"
    "ds_read_b64_tr_b16 v[130:131], v245 offset:2560\n\t"
    "v_fma_mix_f32 v195, v17, v163, v171 op_sel:[1,0,0] op_sel_hi:[1,0,0]\n\t"
    "v_fma_mix_f32 v196, v18, v164, v172 op_sel_hi:[1,0,0]\n\t"
    "v_fma_mix_f32 v197, v18, v165, v173 op_sel:[1,0,0] op_sel_hi:[1,0,0]\n\t"
    "v_fma_mix_f32 v198, v19, v166, v174 op_sel_hi:[1,0,0]\n\t"
    "buffer_load_dwordx4 v222, s[72:75], s67 offen offset:2048 lds\n\t"
    "s_waitcnt lgkmcnt(10)\n\t"
    "v_mfma_f32_32x32x16_bf16 a[32:47], v[148:151], v[8:11], a[32:47]\n\t"
    "ds_read_b64_tr_b16 v[132:133], v245 offset:2304\n\t"
    "v_fma_mix_f32 v199, v19, v167, v175 op_sel:[1,0,0] op_sel_hi:[1,0,0]\n\t"
    "v_cvt_pk_bf16_f32 v16, v192, v193\n\t"
    "v_cvt_pk_bf16_f32 v17, v194, v195\n\t"
    "v_cvt_pk_bf16_f32 v18, v196, v197\n\t"
    "v_mfma_f32_32x32x16_bf16 a[48:63], v[148:151], v[12:15], a[48:63]\n\t"
    "ds_read_b64_tr_b16 v[134:135], v245 offset:2816\n\t"
    "v_cvt_pk_bf16_f32 v19, v198, v199\n\t"
    "v_fma_mix_f32 v200, v20, v160, v168 op_sel_hi:[1,0,0]\n\t"
    "v_fma_mix_f32 v201, v20, v161, v169 op_sel:[1,0,0] op_sel_hi:[1,0,0]\n\t"
    "v_fma_mix_f32 v202, v21, v162, v170 op_sel_hi:[1,0,0]\n\t"
    "buffer_load_dwordx4 v223, s[72:75], s67 offen offset:3072 lds\n\t"
    "s_waitcnt lgkmcnt(10)\n\t"
    "v_mfma_f32_32x32x16_bf16 a[64:79], v[152:155], v[8:11], a[64:79]\n\t"
    "ds_read_b64_tr_b16 v[136:137], v245 offset:10240\n\t"
    "v_fma_mix_f32 v203, v21, v163, v171 op_sel:[1,0,0] op_sel_hi:[1,0,0]\n\t"
    "v_fma_mix_f32 v204, v22, v164, v172 op_sel_hi:[1,0,0]\n\t"
    "v_fma_mix_f32 v205, v22, v165, v173 op_sel:[1,0,0] op_sel_hi:[1,0,0]\n\t"
    "v_fma_mix_f32 v206, v23, v166, v174 op_sel_hi:[1,0,0]\n\t"
    "v_mfma_f32_32x32x16_bf16 a[80:95], v[152:155], v[12:15], a[80:95]\n\t"
    "ds_read_b64_tr_b16 v[138:139], v245 offset:10752\n\t"
    "v_fma_mix_f32 v207, v23, v167, v175 op_sel:[1,0,0] op_sel_hi:[1,0,0]\n\t"
    "v_cvt_pk_bf16_f32 v20, v200, v201\n\t"
    "v_cvt_pk_bf16_f32 v21, v202, v203\n\t"
    "v_cvt_pk_bf16_f32 v22, v204, v205\n\t"
    "s_add_u32 s66, s76, 28672\n\t"
    "buffer_load_dwordx4 v[96:99], v224, s[44:47], s66 offen offset:0\n\t"
    "s_waitcnt lgkmcnt(7)\n\t"
    "v_mfma_f32_32x32x16_bf16 a[96:111], v[156:159], v[8:11], a[96:111]\n\t"
    "ds_read_b64_tr_b16 v[140:141], v245 offset:10496\n\t"
    "v_cvt_pk_bf16_f32 v23, v206, v207\n\t"
    "ds_read_b128 v[160:163], v243 offset:2560\n\t"
    "ds_read_b128 v[164:167], v243 offset:2576\n\t"
    "ds_read_b128 v[168:171], v243 offset:2592\n\t"
    "v_mfma_f32_32x32x16_bf16 a[112:127], v[156:159], v[12:15], a[112:127]\n\t"
    "ds_read_b64_tr_b16 v[142:143], v245 offset:11008\n\t"
    "ds_read_b128 v[172:175], v243 offset:2608\n\t"
    "buffer_load_dwordx4 v[104:107], v224, s[44:47], s66 offen offset:1024\n\t"
    "s_waitcnt lgkmcnt(10)\n\t"
    "v_mfma_f32_32x32x16_bf16 a[0:15], v[128:131], v[16:19], a[0:15]\n\t"
    "ds_read_b64_tr_b16 v[144:145], v245 offset:3072\n\t"
    "v_fma_mix_f32 v192, v24, v176, v184 op_sel_hi:[1,0,0]\n\t"
    "v_fma_mix_f32 v193, v24, v177, v185 op_sel:[1,0,0] op_sel_hi:[1,0,0]\n\t"
    "v_fma_mix_f32 v194, v25, v178, v186 op_sel_hi:[1,0,0]\n\t"
    "v_mfma_f32_32x32x16_bf16 a[16:31], v[128:131], v[20:23], a[16:31]\n\t"
    "ds_read_b64_tr_b16 v[146:147], v245 offset:3584\n\t"
    "v_fma_mix_f32 v195, v25, v179, v187 op_sel:[1,0,0] op_sel_hi:[1,0,0]\n\t"
    "v_fma_mix_f32 v196, v26, v180, v188 op_sel_hi:[1,0,0]\n\t"
    "v_fma_mix_f32 v197, v26, v181, v189 op_sel:[1,0,0] op_sel_hi:[1,0,0]\n\t"
    "v_fma_mix_f32 v198, v27, v182, v190 op_sel_hi:[1,0,0]\n\t"
    "buffer_load_dwordx4 v[112:115], v224, s[44:47], s66 offen offset:2048\n\t"
    "s_waitcnt lgkmcnt(10)\n\t"
    "v_mfma_f32_32x32x16_bf16 a[32:47], v[132:135], v[16:19], a[32:47]\n\t"
    "ds_read_b64_tr_b16 v[148:149], v245 offset:3328\n\t"
    "v_fma_mix_f32 v199, v27, v183, v191 op_sel:[1,0,0] op_sel_hi:[1,0,0]\n\t"
    "v_cvt_pk_bf16_f32 v24, v192, v193\n\t"
    "v_cvt_pk_bf16_f32 v25, v194, v195\n\t"
    "v_cvt_pk_bf16_f32 v26, v196, v197\n\t"
    "v_mfma_f32_32x32x16_bf16 a[48:63], v[132:135], v[20:23], a[48:63]\n\t"
    "ds_read_b64_tr_b16 v[150:151], v245 offset:3840\n\t"
    "v_cvt_pk_bf16_f32 v27, v198, v199\n\t"
    "v_fma_mix_f32 v200, v28, v176, v184 op_sel_hi:[1,0,0]\n\t"
    "v_fma_mix_f32 v201, v28, v177, v185 op_sel:[1,0,0] op_sel_hi:[1,0,0]\n\t"
    "v_fma_mix_f32 v202, v29, v178, v186 op_sel_hi:[1,0,0]\n\t"
    "buffer_load_dwordx4 v[120:123], v224, s[44:47], s66 offen offset:3072\n\t"
    "s_waitcnt lgkmcnt(10)\n\t"
    "v_mfma_f32_32x32x16_bf16 a[64:79], v[136:139], v[16:19], a[64:79]\n\t"
    "ds_read_b64_tr_b16 v[152:153], v245 offset:11264\n\t"
    "v_fma_mix_f32 v203, v29, v179, v187 op_sel:[1,0,0] op_sel_hi:[1,0,0]\n\t"
    "v_fma_mix_f32 v204, v30, v180, v188 op_sel_hi:[1,0,0]\n\t"
    "v_fma_mix_f32 v205, v30, v181, v189 op_sel:[1,0,0] op_sel_hi:[1,0,0]\n\t"
    "v_fma_mix_f32 v206, v31, v182, v190 op_sel_hi:[1,0,0]\n\t"
    "v_mfma_f32_32x32x16_bf16 a[80:95], v[136:139], v[20:23], a[80:95]\n\t"
    "ds_read_b64_tr_b16 v[154:155], v245 offset:11776\n\t"
    "v_fma_mix_f32 v207, v31, v183, v191 op_sel:[1,0,0] op_sel_hi:[1,0,0]\n\t"
    "v_cvt_pk_bf16_f32 v28, v200, v201\n\t"
    "v_cvt_pk_bf16_f32 v29, v202, v203\n\t"
    "v_cvt_pk_bf16_f32 v30, v204, v205\n\t"
    "s_add_u32 s66, s76, 61440\n\t"
    "buffer_load_dwordx4 v[100:103], v224, s[44:47], s66 offen offset:0\n\t"
    "s_waitcnt lgkmcnt(7)\n\t"
    "v_mfma_f32_32x32x16_bf16 a[96:111], v[140:143], v[16:19], a[96:111]\n\t"
    "ds_read_b64_tr_b16 v[156:157], v245 offset:11520\n\t"
    "v_cvt_pk_bf16_f32 v31, v206, v207\n\t"
    "ds_read_b128 v[176:179], v243 offset:2688\n\t"
    "ds_read_b128 v[180:183], v243 offset:2704\n\t"
    "ds_read_b128 v[184:187], v243 offset:2720\n\t"
    "v_mfma_f32_32x32x16_bf16 a[112:127], v[140:143], v[20:23], a[112:127]\n\t"
    "ds_read_b64_tr_b16 v[158:159], v245 offset:12032\n\t"
    "ds_read_b128 v[188:191], v243 offset:2736\n\t"
    "buffer_load_dwordx4 v[108:111], v224, s[44:47], s66 offen offset:1024\n\t"
    "s_waitcnt vmcnt(30)\n\t"
    "s_barrier\n\t"
    "s_waitcnt lgkmcnt(10)\n\t"
    "v_mfma_f32_32x32x16_bf16 a[0:15], v[144:147], v[24:27], a[0:15]\n\t"
    "ds_read_b64_tr_b16 v[128:129], v245 offset:16384\n\t"
    "s_waitcnt vmcnt(22)\n\t"
    "v_fma_mix_f32 v192, v32, v160, v168 op_sel_hi:[1,0,0]\n\t"
    "v_fma_mix_f32 v193, v32, v161, v169 op_sel:[1,0,0] op_sel_hi:[1,0,0]\n\t"
    "v_fma_mix_f32 v194, v33, v162, v170 op_sel_hi:[1,0,0]\n\t"
    "v_mfma_f32_32x32x16_bf16 a[16:31], v[144:147], v[28:31], a[16:31]\n\t"
    "ds_read_b64_tr_b16 v[130:131], v245 offset:16896\n\t"
    "v_fma_mix_f32 v195, v33, v163, v171 op_sel:[1,0,0] op_sel_hi:[1,0,0]\n\t"
    "v_fma_mix_f32 v196, v34, v164, v172 op_sel_hi:[1,0,0]\n\t"
    "v_fma_mix_f32 v197, v34, v165, v173 op_sel:[1,0,0] op_sel_hi:[1,0,0]\n\t"
    "v_fma_mix_f32 v198, v35, v166, v174 op_sel_hi:[1,0,0]\n\t"
    "buffer_load_dwordx4 v[116:119], v224, s[44:47], s66 offen offset:2048\n\t"
    "s_waitcnt lgkmcnt(10)\n\t"
    "v_mfma_f32_32x32x16_bf16 a[32:47], v[148:151], v[24:27], a[32:47]\n\t"
    "ds_read_b64_tr_b16 v[132:133], v245 offset:16640\n\t"
    "v_fma_mix_f32 v199, v35, v167, v175 op_sel:[1,0,0] op_sel_hi:[1,0,0]\n\t"
    "v_cvt_pk_bf16_f32 v32, v192, v193\n\t"
    "v_cvt_pk_bf16_f32 v33, v194, v195\n\t"
    "v_cvt_pk_bf16_f32 v34, v196, v197\n\t"
    "v_mfma_f32_32x32x16_bf16 a[48:63], v[148:151], v[28:31], a[48:63]\n\t"
    "ds_read_b64_tr_b16 v[134:135], v245 offset:17152\n\t"
    "v_cvt_pk_bf16_f32 v35, v198, v199\n\t"
    "v_fma_mix_f32 v200, v36, v160, v168 op_sel_hi:[1,0,0]\n\t"
    "v_fma_mix_f32 v201, v36, v161, v169 op_sel:[1,0,0] op_sel_hi:[1,0,0]\n\t"
    "v_fma_mix_f32 v202, v37, v162, v170 op_sel_hi:[1,0,0]\n\t"
    "buffer_load_dwordx4 v[124:127], v224, s[44:47], s66 offen offset:3072\n\t"
    "s_waitcnt lgkmcnt(10)\n\t"
    "v_mfma_f32_32x32x16_bf16 a[64:79], v[152:155], v[24:27], a[64:79]\n\t"
    "ds_read_b64_tr_b16 v[136:137], v245 offset:24576\n\t"
    "v_fma_mix_f32 v203, v37, v163, v171 op_sel:[1,0,0] op_sel_hi:[1,0,0]\n\t"
    "v_fma_mix_f32 v204, v38, v164, v172 op_sel_hi:[1,0,0]\n\t"
    "v_fma_mix_f32 v205, v38, v165, v173 op_sel:[1,0,0] op_sel_hi:[1,0,0]\n\t"
    "v_fma_mix_f32 v206, v39, v166, v174 op_sel_hi:[1,0,0]\n\t"
    "v_mfma_f32_32x32x16_bf16 a[80:95], v[152:155], v[28:31], a[80:95]\n\t"
    "ds_read_b64_tr_b16 v[138:139], v245 offset:25088\n\t"
    "v_fma_mix_f32 v207, v39, v167, v175 op_sel:[1,0,0] op_sel_hi:[1,0,0]\n\t"
    "v_cvt_pk_bf16_f32 v36, v200, v201\n\t"
    "v_cvt_pk_bf16_f32 v37, v202, v203\n\t"
    "v_cvt_pk_bf16_f32 v38, v204, v205\n\t"
    "s_mov_b32 m0, s56\n\t"
    "s_nop 0\n\t"
    "buffer_load_dwordx4 v224, s[36:39], s56 offen offset:0 lds\n\t"
    "s_waitcnt lgkmcnt(7)\n\t"
    "v_mfma_f32_32x32x16_bf16 a[96:111], v[156:159], v[24:27], a[96:111]\n\t"
    "ds_read_b64_tr_b16 v[140:141], v245 offset:24832\n\t"
    "v_cvt_pk_bf16_f32 v39, v206, v207\n\t"
    "ds_read_b128 v[160:163], v243 offset:2816\n\t"
    "ds_read_b128 v[164:167], v243 offset:2832\n\t"
    "ds_read_b128 v[168:171], v243 offset:2848\n\t"
    "v_mfma_f32_32x32x16_bf16 a[112:127], v[156:159], v[28:31], a[112:127]\n\t"
    "ds_read_b64_tr_b16 v[142:143], v245 offset:25344\n\t"
    "ds_read_b128 v[172:175], v243 offset:2864\n\t"
    "s_mov_b32 m0, s56\n\t"
    "s_nop 0\n\t"
    "buffer_load_dwordx4 v224, s[36:39], s56 offen offset:1024 lds\n\t"
    "s_waitcnt lgkmcnt(10)\n\t"
    "v_mfma_f32_32x32x16_bf16 a[0:15], v[128:131], v[32:35], a[0:15]\n\t"
    "ds_read_b64_tr_b16 v[144:145], v245 offset:17408\n\t"
    "v_fma_mix_f32 v192, v40, v176, v184 op_sel_hi:[1,0,0]\n\t"
    "v_fma_mix_f32 v193, v40, v177, v185 op_sel:[1,0,0] op_sel_hi:[1,0,0]\n\t"
    "v_fma_mix_f32 v194, v41, v178, v186 op_sel_hi:[1,0,0]\n\t"
    "v_mfma_f32_32x32x16_bf16 a[16:31], v[128:131], v[36:39], a[16:31]\n\t"
    "ds_read_b64_tr_b16 v[146:147], v245 offset:17920\n\t"
    "v_fma_mix_f32 v195, v41, v179, v187 op_sel:[1,0,0] op_sel_hi:[1,0,0]\n\t"
    "v_fma_mix_f32 v196, v42, v180, v188 op_sel_hi:[1,0,0]\n\t"
    "v_fma_mix_f32 v197, v42, v181, v189 op_sel:[1,0,0] op_sel_hi:[1,0,0]\n\t"
    "v_fma_mix_f32 v198, v43, v182, v190 op_sel_hi:[1,0,0]\n\t"
    "s_mov_b32 m0, s56\n\t"
    "s_nop 0\n\t"
    "buffer_load_dwordx4 v224, s[36:39], s56 offen offset:2048 lds\n\t"
    "s_waitcnt lgkmcnt(10)\n\t"
    "v_mfma_f32_32x32x16_bf16 a[32:47], v[132:135], v[32:35], a[32:47]\n\t"
    "ds_read_b64_tr_b16 v[148:149], v245 offset:17664\n\t"
    "v_fma_mix_f32 v199, v43, v183, v191 op_sel:[1,0,0] op_sel_hi:[1,0,0]\n\t"
    "v_cvt_pk_bf16_f32 v40, v192, v193\n\t"
    "v_cvt_pk_bf16_f32 v41, v194, v195\n\t"
    "v_cvt_pk_bf16_f32 v42, v196, v197\n\t"
    "v_mfma_f32_32x32x16_bf16 a[48:63], v[132:135], v[36:39], a[48:63]\n\t"
    "ds_read_b64_tr_b16 v[150:151], v245 offset:18176\n\t"
    "v_cvt_pk_bf16_f32 v43, v198, v199\n\t"
    "v_fma_mix_f32 v200, v44, v176, v184 op_sel_hi:[1,0,0]\n\t"
    "v_fma_mix_f32 v201, v44, v177, v185 op_sel:[1,0,0] op_sel_hi:[1,0,0]\n\t"
    "v_fma_mix_f32 v202, v45, v178, v186 op_sel_hi:[1,0,0]\n\t"
    "s_mov_b32 m0, s56\n\t"
    "s_nop 0\n\t"
    "buffer_load_dwordx4 v224, s[36:39], s56 offen offset:3072 lds\n\t"
    "s_waitcnt lgkmcnt(10)\n\t"
    "v_mfma_f32_32x32x16_bf16 a[64:79], v[136:139], v[32:35], a[64:79]\n\t"
    "ds_read_b64_tr_b16 v[152:153], v245 offset:25600\n\t"
    "v_fma_mix_f32 v203, v45, v179, v187 op_sel:[1,0,0] op_sel_hi:[1,0,0]\n\t"
    "v_fma_mix_f32 v204, v46, v180, v188 op_sel_hi:[1,0,0]\n\t"
    "v_fma_mix_f32 v205, v46, v181, v189 op_sel:[1,0,0] op_sel_hi:[1,0,0]\n\t"
    "v_fma_mix_f32 v206, v47, v182, v190 op_sel_hi:[1,0,0]\n\t"
    "v_mfma_f32_32x32x16_bf16 a[80:95], v[136:139], v[36:39], a[80:95]\n\t"
    "ds_read_b64_tr_b16 v[154:155], v245 offset:26112\n\t"
    "v_fma_mix_f32 v207, v47, v183, v191 op_sel:[1,0,0] op_sel_hi:[1,0,0]\n\t"
    "v_cvt_pk_bf16_f32 v44, v200, v201\n\t"
    "v_cvt_pk_bf16_f32 v45, v202, v203\n\t"
    "v_cvt_pk_bf16_f32 v46, v204, v205\n\t"
    "s_mov_b32 m0, s57\n\t"
    "s_nop 0\n\t"
    "buffer_load_dwordx4 v224, s[36:39], s57 offen offset:0 lds\n\t"
    "s_waitcnt lgkmcnt(7)\n\t"
    "v_mfma_f32_32x32x16_bf16 a[96:111], v[140:143], v[32:35], a[96:111]\n\t"
    "ds_read_b64_tr_b16 v[156:157], v245 offset:25856\n\t"
    "v_cvt_pk_bf16_f32 v47, v206, v207\n\t"
    "ds_read_b128 v[176:179], v243 offset:2944\n\t"
    "ds_read_b128 v[180:183], v243 offset:2960\n\t"
    "ds_read_b128 v[184:187], v243 offset:2976\n\t"
    "v_mfma_f32_32x32x16_bf16 a[112:127], v[140:143], v[36:39], a[112:127]\n\t"
    "ds_read_b64_tr_b16 v[158:159], v245 offset:26368\n\t"
    "ds_read_b128 v[188:191], v243 offset:2992\n\t"
    "s_mov_b32 m0, s57\n\t"
    "s_nop 0\n\t"
    "buffer_load_dwordx4 v224, s[36:39], s57 offen offset:1024 lds\n\t"
    "s_waitcnt lgkmcnt(10)\n\t"
    "v_mfma_f32_32x32x16_bf16 a[0:15], v[144:147], v[40:43], a[0:15]\n\t"
    "ds_read_b64_tr_b16 v[128:129], v245 offset:18432\n\t"
    "v_fma_mix_f32 v192, v48, v160, v168 op_sel_hi:[1,0,0]\n\t"
    "v_fma_mix_f32 v193, v48, v161, v169 op_sel:[1,0,0] op_sel_hi:[1,0,0]\n\t"
    "v_fma_mix_f32 v194, v49, v162, v170 op_sel_hi:[1,0,0]\n\t"
    "v_mfma_f32_32x32x16_bf16 a[16:31], v[144:147], v[44:47], a[16:31]\n\t"
    "ds_read_b64_tr_b16 v[130:131], v245 offset:18944\n\t"
    "v_fma_mix_f32 v195, v49, v163, v171 op_sel:[1,0,0] op_sel_hi:[1,0,0]\n\t"
    "v_fma_mix_f32 v196, v50, v164, v172 op_sel_hi:[1,0,0]\n\t"
    "v_fma_mix_f32 v197, v50, v165, v173 op_sel:[1,0,0] op_sel_hi:[1,0,0]\n\t"
    "v_fma_mix_f32 v198, v51, v166, v174 op_sel_hi:[1,0,0]\n\t"
    "s_mov_b32 m0, s57\n\t"
    "s_nop 0\n\t"
    "buffer_load_dwordx4 v224, s[36:39], s57 offen offset:2048 lds\n\t"
    "s_waitcnt lgkmcnt(10)\n\t"
    "v_mfma_f32_32x32x16_bf16 a[32:47], v[148:151], v[40:43], a[32:47]\n\t"
    "ds_read_b64_tr_b16 v[132:133], v245 offset:18688\n\t"
    "v_fma_mix_f32 v199, v51, v167, v175 op_sel:[1,0,0] op_sel_hi:[1,0,0]\n\t"
    "v_cvt_pk_bf16_f32 v48, v192, v193\n\t"
    "v_cvt_pk_bf16_f32 v49, v194, v195\n\t"
    "v_cvt_pk_bf16_f32 v50, v196, v197\n\t"
    "v_mfma_f32_32x32x16_bf16 a[48:63], v[148:151], v[44:47], a[48:63]\n\t"
    "ds_read_b64_tr_b16 v[134:135], v245 offset:19200\n\t"
    "v_cvt_pk_bf16_f32 v51, v198, v199\n\t"
    "v_fma_mix_f32 v200, v52, v160, v168 op_sel_hi:[1,0,0]\n\t"
    "v_fma_mix_f32 v201, v52, v161, v169 op_sel:[1,0,0] op_sel_hi:[1,0,0]\n\t"
    "v_fma_mix_f32 v202, v53, v162, v170 op_sel_hi:[1,0,0]\n\t"
    "s_mov_b32 m0, s57\n\t"
    "s_nop 0\n\t"
    "buffer_load_dwordx4 v224, s[36:39], s57 offen offset:3072 lds\n\t"
    "s_waitcnt lgkmcnt(10)\n\t"
    "v_mfma_f32_32x32x16_bf16 a[64:79], v[152:155], v[40:43], a[64:79]\n\t"
    "ds_read_b64_tr_b16 v[136:137], v245 offset:26624\n\t"
    "v_fma_mix_f32 v203, v53, v163, v171 op_sel:[1,0,0] op_sel_hi:[1,0,0]\n\t"
    "v_fma_mix_f32 v204, v54, v164, v172 op_sel_hi:[1,0,0]\n\t"
    "v_fma_mix_f32 v205, v54, v165, v173 op_sel:[1,0,0] op_sel_hi:[1,0,0]\n\t"
    "v_fma_mix_f32 v206, v55, v166, v174 op_sel_hi:[1,0,0]\n\t"
    "v_mfma_f32_32x32x16_bf16 a[80:95], v[152:155], v[44:47], a[80:95]\n\t"
    "ds_read_b64_tr_b16 v[138:139], v245 offset:27136\n\t"
    "v_fma_mix_f32 v207, v55, v167, v175 op_sel:[1,0,0] op_sel_hi:[1,0,0]\n\t"
    "v_cvt_pk_bf16_f32 v52, v200, v201\n\t"
    "v_cvt_pk_bf16_f32 v53, v202, v203\n\t"
    "v_cvt_pk_bf16_f32 v54, v204, v205\n\t"
    "s_waitcnt lgkmcnt(7)\n\t"
    "v_mfma_f32_32x32x16_bf16 a[96:111], v[156:159], v[40:43], a[96:111]\n\t"
    "ds_read_b64_tr_b16 v[140:141], v245 offset:26880\n\t"
    "v_cvt_pk_bf16_f32 v55, v206, v207\n\t"
    "ds_read_b128 v[160:163], v243 offset:3072\n\t"
    "ds_read_b128 v[164:167], v243 offset:3088\n\t"
    "ds_read_b128 v[168:171], v243 offset:3104\n\t"
    "v_mfma_f32_32x32x16_bf16 a[112:127], v[156:159], v[44:47], a[112:127]\n\t"
    "ds_read_b64_tr_b16 v[142:143], v245 offset:27392\n\t"
    "ds_read_b128 v[172:175], v243 offset:3120\n\t"
    "s_waitcnt lgkmcnt(10)\n\t"
    "v_mfma_f32_32x32x16_bf16 a[0:15], v[128:131], v[48:51], a[0:15]\n\t"
    "ds_read_b64_tr_b16 v[144:145], v245 offset:19456\n\t"
    "v_fma_mix_f32 v192, v56, v176, v184 op_sel_hi:[1,0,0]\n\t"
    "v_fma_mix_f32 v193, v56, v177, v185 op_sel:[1,0,0] op_sel_hi:[1,0,0]\n\t"
    "v_fma_mix_f32 v194, v57, v178, v186 op_sel_hi:[1,0,0]\n\t"
    "v_mfma_f32_32x32x16_bf16 a[16:31], v[128:131], v[52:55], a[16:31]\n\t"
    "ds_read_b64_tr_b16 v[146:147], v245 offset:19968\n\t"
    "v_fma_mix_f32 v195, v57, v179, v187 op_sel:[1,0,0] op_sel_hi:[1,0,0]\n\t"
    "v_fma_mix_f32 v196, v58, v180, v188 op_sel_hi:[1,0,0]\n\t"
    "v_fma_mix_f32 v197, v58, v181, v189 op_sel:[1,0,0] op_sel_hi:[1,0,0]\n\t"
    "v_fma_mix_f32 v198, v59, v182, v190 op_sel_hi:[1,0,0]\n\t"
    "s_waitcnt lgkmcnt(10)\n\t"
    "v_mfma_f32_32x32x16_bf16 a[32:47], v[132:135], v[48:51], a[32:47]\n\t"
    "ds_read_b64_tr_b16 v[148:149], v245 offset:19712\n\t"
    "v_fma_mix_f32 v199, v59, v183, v191 op_sel:[1,0,0] op_sel_hi:[1,0,0]\n\t"
    "v_cvt_pk_bf16_f32 v56, v192, v193\n\t"
    "v_cvt_pk_bf16_f32 v57, v194, v195\n\t"
    "v_cvt_pk_bf16_f32 v58, v196, v197\n\t"
    "v_mfma_f32_32x32x16_bf16 a[48:63], v[132:135], v[52:55], a[48:63]\n\t"
    "ds_read_b64_tr_b16 v[150:151], v245 offset:20224\n\t"
    "v_cvt_pk_bf16_f32 v59, v198, v199\n\t"
    "v_fma_mix_f32 v200, v60, v176, v184 op_sel_hi:[1,0,0]\n\t"
    "v_fma_mix_f32 v201, v60, v177, v185 op_sel:[1,0,0] op_sel_hi:[1,0,0]\n\t"
    "v_fma_mix_f32 v202, v61, v178, v186 op_sel_hi:[1,0,0]\n\t"
    "s_waitcnt lgkmcnt(10)\n\t"
    "v_mfma_f32_32x32x16_bf16 a[64:79], v[136:139], v[48:51], a[64:79]\n\t"
    "ds_read_b64_tr_b16 v[152:153], v245 offset:27648\n\t"
    "v_fma_mix_f32 v203, v61, v179, v187 op_sel:[1,0,0] op_sel_hi:[1,0,0]\n\t"
    "v_fma_mix_f32 v204, v62, v180, v188 op_sel_hi:[1,0,0]\n\t"
    "v_fma_mix_f32 v205, v62, v181, v189 op_sel:[1,0,0] op_sel_hi:[1,0,0]\n\t"
    "v_fma_mix_f32 v206, v63, v182, v190 op_sel_hi:[1,0,0]\n\t"
    "v_mfma_f32_32x32x16_bf16 a[80:95], v[136:139], v[52:55], a[80:95]\n\t"
    "ds_read_b64_tr_b16 v[154:155], v245 offset:28160\n\t"
    "v_fma_mix_f32 v207, v63, v183, v191 op_sel:[1,0,0] op_sel_hi:[1,0,0]\n\t"
    "v_cvt_pk_bf16_f32 v60, v200, v201\n\t"
    "v_cvt_pk_bf16_f32 v61, v202, v203\n\t"
    "v_cvt_pk_bf16_f32 v62, v204, v205\n\t"
    "s_waitcnt lgkmcnt(7)\n\t"
    "v_mfma_f32_32x32x16_bf16 a[96:111], v[140:143], v[48:51], a[96:111]\n\t"
    "ds_read_b64_tr_b16 v[156:157], v245 offset:27904\n\t"
    "v_cvt_pk_bf16_f32 v63, v206, v207\n\t"
    "ds_read_b128 v[176:179], v243 offset:3200\n\t"
    "ds_read_b128 v[180:183], v243 offset:3216\n\t"
    "ds_read_b128 v[184:187], v243 offset:3232\n\t"
    "v_mfma_f32_32x32x16_bf16 a[112:127], v[140:143], v[52:55], a[112:127]\n\t"
    "ds_read_b64_tr_b16 v[158:159], v245 offset:28416\n\t"
    "ds_read_b128 v[188:191], v243 offset:3248\n\t"
    "s_waitcnt vmcnt(20) lgkmcnt(12)\n\t"
    "v_fma_mix_f32 v192, v64, v160, v168 op_sel_hi:[1,0,0]\n\t"
    "v_fma_mix_f32 v193, v64, v161, v169 op_sel:[1,0,0] op_sel_hi:[1,0,0]\n\t"
    "s_barrier\n\t"
    "s_waitcnt lgkmcnt(10)\n\t"
    "v_mfma_f32_32x32x16_bf16 a[0:15], v[144:147], v[56:59], a[0:15]\n\t"
    "ds_read_b64_tr_b16 v[128:129], v245 offset:32768\n\t"
    "v_fma_mix_f32 v194, v65, v162, v170 op_sel_hi:[1,0,0]\n\t"
    "v_fma_mix_f32 v195, v65, v163, v171 op_sel:[1,0,0] op_sel_hi:[1,0,0]\n\t"
    "v_fma_mix_f32 v196, v66, v164, v172 op_sel_hi:[1,0,0]\n\t"
    "v_fma_mix_f32 v197, v66, v165, v173 op_sel:[1,0,0] op_sel_hi:[1,0,0]\n\t"
    "v_mfma_f32_32x32x16_bf16 a[16:31], v[144:147], v[60:63], a[16:31]\n\t"
    "ds_read_b64_tr_b16 v[130:131], v245 offset:33280\n\t"
    "v_fma_mix_f32 v198, v67, v166, v174 op_sel_hi:[1,0,0]\n\t"
    "v_fma_mix_f32 v199, v67, v167, v175 op_sel:[1,0,0] op_sel_hi:[1,0,0]\n\t"
    "v_cvt_pk_bf16_f32 v64, v192, v193\n\t"
    "v_cvt_pk_bf16_f32 v65, v194, v195\n\t"
    "s_or_b32 m0, s56, 0x10000\n\t"
    "s_nop 0\n\t"
    "buffer_load_dwordx4 v224, s[40:43], s56 offen offset:0 lds\n\t"
    "s_waitcnt lgkmcnt(10)\n\t"
    "v_mfma_f32_32x32x16_bf16 a[32:47], v[148:151], v[56:59], a[32:47]\n\t"
    "ds_read_b64_tr_b16 v[132:133], v245 offset:33024\n\t"
    "v_cvt_pk_bf16_f32 v66, v196, v197\n\t"
    "v_cvt_pk_bf16_f32 v67, v198, v199\n\t"
    "v_fma_mix_f32 v200, v68, v160, v168 op_sel_hi:[1,0,0]\n\t"
    "v_fma_mix_f32 v201, v68, v161, v169 op_sel:[1,0,0] op_sel_hi:[1,0,0]\n\t"
    "v_mfma_f32_32x32x16_bf16 a[48:63], v[148:151], v[60:63], a[48:63]\n\t"
    "ds_read_b64_tr_b16 v[134:135], v245 offset:33536\n\t"
    "v_fma_mix_f32 v202, v69, v162, v170 op_sel_hi:[1,0,0]\n\t"
    "v_fma_mix_f32 v203, v69, v163, v171 op_sel:[1,0,0] op_sel_hi:[1,0,0]\n\t"
    "v_fma_mix_f32 v204, v70, v164, v172 op_sel_hi:[1,0,0]\n\t"
    "v_fma_mix_f32 v205, v70, v165, v173 op_sel:[1,0,0] op_sel_hi:[1,0,0]\n\t"
    "s_or_b32 m0, s56, 0x10000\n\t"
    "s_nop 0\n\t"
    "buffer_load_dwordx4 v224, s[40:43], s56 offen offset:1024 lds\n\t"
    "s_waitcnt lgkmcnt(10)\n\t"
    "v_mfma_f32_32x32x16_bf16 a[64:79], v[152:155], v[56:59], a[64:79]\n\t"
    "ds_read_b64_tr_b16 v[136:137], v245 offset:40960\n\t"
    "v_fma_mix_f32 v206, v71, v166, v174 op_sel_hi:[1,0,0]\n\t"
    "v_fma_mix_f32 v207, v71, v167, v175 op_sel:[1,0,0] op_sel_hi:[1,0,0]\n\t"
    "v_cvt_pk_bf16_f32 v68, v200, v201\n\t"
    "v_cvt_pk_bf16_f32 v69, v202, v203\n\t"
    "v_mfma_f32_32x32x16_bf16 a[80:95], v[152:155], v[60:63], a[80:95]\n\t"
    "ds_read_b64_tr_b16 v[138:139], v245 offset:41472\n\t"
    "v_cvt_pk_bf16_f32 v70, v204, v205\n\t"
    "v_cvt_pk_bf16_f32 v71, v206, v207\n\t"
    "ds_read_b128 v[160:163], v243 offset:3328\n\t"
    "ds_read_b128 v[164:167], v243 offset:3344\n\t"
    "s_or_b32 m0, s56, 0x10000\n\t"
    "s_nop 0\n\t"
    "buffer_load_dwordx4 v224, s[40:43], s56 offen offset:2048 lds\n\t"
    "s_waitcnt lgkmcnt(9)\n\t"
    "v_mfma_f32_32x32x16_bf16 a[96:111], v[156:159], v[56:59], a[96:111]\n\t"
    "ds_read_b64_tr_b16 v[140:141], v245 offset:41216\n\t"
    "ds_read_b128 v[168:171], v243 offset:3360\n\t"
    "ds_read_b128 v[172:175], v243 offset:3376\n\t"
    "s_waitcnt lgkmcnt(11)\n\t"
    "v_fma_mix_f32 v192, v72, v176, v184 op_sel_hi:[1,0,0]\n\t"
    "v_mfma_f32_32x32x16_bf16 a[112:127], v[156:159], v[60:63], a[112:127]\n\t"
    "ds_read_b64_tr_b16 v[142:143], v245 offset:41728\n\t"
    "v_fma_mix_f32 v193, v72, v177, v185 op_sel:[1,0,0] op_sel_hi:[1,0,0]\n\t"
    "v_fma_mix_f32 v194, v73, v178, v186 op_sel_hi:[1,0,0]\n\t"
    "v_fma_mix_f32 v195, v73, v179, v187 op_sel:[1,0,0] op_sel_hi:[1,0,0]\n\t"
    "v_fma_mix_f32 v196, v74, v180, v188 op_sel_hi:[1,0,0]\n\t"
    "s_or_b32 m0, s56, 0x10000\n\t"
    "s_nop 0\n\t"
    "buffer_load_dwordx4 v224, s[40:43], s56 offen offset:3072 lds\n\t"
    "s_waitcnt lgkmcnt(10)\n\t"
    "v_mfma_f32_32x32x16_bf16 a[0:15], v[128:131], v[64:67], a[0:15]\n\t"
    "ds_read_b64_tr_b16 v[144:145], v245 offset:33792\n\t"
    "v_fma_mix_f32 v197, v74, v181, v189 op_sel:[1,0,0] op_sel_hi:[1,0,0]\n\t"
    "v_fma_mix_f32 v198, v75, v182, v190 op_sel_hi:[1,0,0]\n\t"
    "v_fma_mix_f32 v199, v75, v183, v191 op_sel:[1,0,0] op_sel_hi:[1,0,0]\n\t"
    "v_cvt_pk_bf16_f32 v72, v192, v193\n\t"
    "v_mfma_f32_32x32x16_bf16 a[16:31], v[128:131], v[68:71], a[16:31]\n\t"
    "ds_read_b64_tr_b16 v[146:147], v245 offset:34304\n\t"
    "v_cvt_pk_bf16_f32 v73, v194, v195\n\t"
    "v_cvt_pk_bf16_f32 v74, v196, v197\n\t"
    "v_cvt_pk_bf16_f32 v75, v198, v199\n\t"
    "v_fma_mix_f32 v200, v76, v176, v184 op_sel_hi:[1,0,0]\n\t"
    "global_load_dword v253, v255, %12\n\t"
    "v_add_u32 v255, 0x2000, v224\n\t"
    "v_add_u32 v255, v255, v224\n\t"
    "s_waitcnt lgkmcnt(10)\n\t"
    "v_mfma_f32_32x32x16_bf16 a[32:47], v[132:135], v[64:67], a[32:47]\n\t"
    "ds_read_b64_tr_b16 v[148:149], v245 offset:34048\n\t"
    "v_fma_mix_f32 v201, v76, v177, v185 op_sel:[1,0,0] op_sel_hi:[1,0,0]\n\t"
    "v_fma_mix_f32 v202, v77, v178, v186 op_sel_hi:[1,0,0]\n\t"
    "v_fma_mix_f32 v203, v77, v179, v187 op_sel:[1,0,0] op_sel_hi:[1,0,0]\n\t"
    "v_fma_mix_f32 v204, v78, v180, v188 op_sel_hi:[1,0,0]\n\t"
    "v_mfma_f32_32x32x16_bf16 a[48:63], v[132:135], v[68:71], a[48:63]\n\t"
    "ds_read_b64_tr_b16 v[150:151], v245 offset:34560\n\t"
    "v_fma_mix_f32 v205, v78, v181, v189 op_sel:[1,0,0] op_sel_hi:[1,0,0]\n\t"
    "v_fma_mix_f32 v206, v79, v182, v190 op_sel_hi:[1,0,0]\n\t"
    "v_fma_mix_f32 v207, v79, v183, v191 op_sel:[1,0,0] op_sel_hi:[1,0,0]\n\t"
    "v_cvt_pk_bf16_f32 v76, v200, v201\n\t"
    "s_waitcnt lgkmcnt(10)\n\t"
    "v_mfma_f32_32x32x16_bf16 a[64:79], v[136:139], v[64:67], a[64:79]\n\t"
    "ds_read_b64_tr_b16 v[152:153], v245 offset:41984\n\t"
    "v_cvt_pk_bf16_f32 v77, v202, v203\n\t"
    "v_cvt_pk_bf16_f32 v78, v204, v205\n\t"
    "v_cvt_pk_bf16_f32 v79, v206, v207\n\t"
    "ds_read_b128 v[176:179], v243 offset:3456\n\t"
    "v_mfma_f32_32x32x16_bf16 a[80:95], v[136:139], v[68:71], a[80:95]\n\t"
    "ds_read_b64_tr_b16 v[154:155], v245 offset:42496\n\t"
    "ds_read_b128 v[180:183], v243 offset:3472\n\t"
    "ds_read_b128 v[184:187], v243 offset:3488\n\t"
    "ds_read_b128 v[188:191], v243 offset:3504\n\t"
    "s_waitcnt lgkmcnt(11)\n\t"
    "s_waitcnt lgkmcnt(10)\n\t"
    "v_mfma_f32_32x32x16_bf16 a[96:111], v[140:143], v[64:67], a[96:111]\n\t"
    "ds_read_b64_tr_b16 v[156:157], v245 offset:42240\n\t"
    "v_fma_mix_f32 v192, v80, v160, v168 op_sel_hi:[1,0,0]\n\t"
    "v_fma_mix_f32 v193, v80, v161, v169 op_sel:[1,0,0] op_sel_hi:[1,0,0]\n\t"
    "v_fma_mix_f32 v194, v81, v162, v170 op_sel_hi:[1,0,0]\n\t"
    "v_fma_mix_f32 v195, v81, v163, v171 op_sel:[1,0,0] op_sel_hi:[1,0,0]\n\t"
    "v_mfma_f32_32x32x16_bf16 a[112:127], v[140:143], v[68:71], a[112:127]\n\t"
    "ds_read_b64_tr_b16 v[158:159], v245 offset:42752\n\t"
    "v_fma_mix_f32 v196, v82, v164, v172 op_sel_hi:[1,0,0]\n\t"
    "v_fma_mix_f32 v197, v82, v165, v173 op_sel:[1,0,0] op_sel_hi:[1,0,0]\n\t"
    "v_fma_mix_f32 v198, v83, v166, v174 op_sel_hi:[1,0,0]\n\t"
    "v_fma_mix_f32 v199, v83, v167, v175 op_sel:[1,0,0] op_sel_hi:[1,0,0]\n\t"
    "s_waitcnt lgkmcnt(10)\n\t"
    "v_mfma_f32_32x32x16_bf16 a[0:15], v[144:147], v[72:75], a[0:15]\n\t"
    "ds_read_b64_tr_b16 v[128:129], v245 offset:34816\n\t"
    "v_cvt_pk_bf16_f32 v80, v192, v193\n\t"
    "v_cvt_pk_bf16_f32 v81, v194, v195\n\t"
    "v_cvt_pk_bf16_f32 v82, v196, v197\n\t"
    "v_cvt_pk_bf16_f32 v83, v198, v199\n\t"
    "v_mfma_f32_32x32x16_bf16 a[16:31], v[144:147], v[76:79], a[16:31]\n\t"
    "ds_read_b64_tr_b16 v[130:131], v245 offset:35328\n\t"
    "v_fma_mix_f32 v200, v84, v160, v168 op_sel_hi:[1,0,0]\n\t"
    "v_fma_mix_f32 v201, v84, v161, v169 op_sel:[1,0,0] op_sel_hi:[1,0,0]\n\t"
    "v_fma_mix_f32 v202, v85, v162, v170 op_sel_hi:[1,0,0]\n\t"
    "v_fma_mix_f32 v203, v85, v163, v171 op_sel:[1,0,0] op_sel_hi:[1,0,0]\n\t"
    "s_waitcnt lgkmcnt(10)\n\t"
    "v_mfma_f32_32x32x16_bf16 a[32:47], v[148:151], v[72:75], a[32:47]\n\t"
    "ds_read_b64_tr_b16 v[132:133], v245 offset:35072\n\t"
    "v_fma_mix_f32 v204, v86, v164, v172 op_sel_hi:[1,0,0]\n\t"
    "v_fma_mix_f32 v205, v86, v165, v173 op_sel:[1,0,0] op_sel_hi:[1,0,0]\n\t"
    "v_fma_mix_f32 v206, v87, v166, v174 op_sel_hi:[1,0,0]\n\t"
    "v_fma_mix_f32 v207, v87, v167, v175 op_sel:[1,0,0] op_sel_hi:[1,0,0]\n\t"
    "v_mfma_f32_32x32x16_bf16 a[48:63], v[148:151], v[76:79], a[48:63]\n\t"
    "ds_read_b64_tr_b16 v[134:135], v245 offset:35584\n\t"
    "v_cvt_pk_bf16_f32 v84, v200, v201\n\t"
    "v_cvt_pk_bf16_f32 v85, v202, v203\n\t"
    "v_cvt_pk_bf16_f32 v86, v204, v205\n\t"
    "v_cvt_pk_bf16_f32 v87, v206, v207\n\t"
    "s_waitcnt lgkmcnt(9)\n\t"
    "v_mfma_f32_32x32x16_bf16 a[64:79], v[152:155], v[72:75], a[64:79]\n\t"
    "ds_read_b64_tr_b16 v[136:137], v245 offset:43008\n\t"
    "ds_read_b128 v[160:163], v243 offset:3584\n\t"
    "ds_read_b128 v[164:167], v243 offset:3600\n\t"
    "ds_read_b128 v[168:171], v243 offset:3616\n\t"
    "ds_read_b128 v[172:175], v243 offset:3632\n\t"
    "v_mfma_f32_32x32x16_bf16 a[80:95], v[152:155], v[76:79], a[80:95]\n\t"
    "ds_read_b64_tr_b16 v[138:139], v245 offset:43520\n\t"
    "s_waitcnt lgkmcnt(12)\n\t"
    "v_fma_mix_f32 v192, v88, v176, v184 op_sel_hi:[1,0,0]\n\t"
    "v_fma_mix_f32 v193, v88, v177, v185 op_sel:[1,0,0] op_sel_hi:[1,0,0]\n\t"
    "v_fma_mix_f32 v194, v89, v178, v186 op_sel_hi:[1,0,0]\n\t"
    "s_waitcnt lgkmcnt(10)\n\t"
    "v_mfma_f32_32x32x16_bf16 a[96:111], v[156:159], v[72:75], a[96:111]\n\t"
    "ds_read_b64_tr_b16 v[140:141], v245 offset:43264\n\t"
    "v_fma_mix_f32 v195, v89, v179, v187 op_sel:[1,0,0] op_sel_hi:[1,0,0]\n\t"
    "v_fma_mix_f32 v196, v90, v180, v188 op_sel_hi:[1,0,0]\n\t"
    "v_fma_mix_f32 v197, v90, v181, v189 op_sel:[1,0,0] op_sel_hi:[1,0,0]\n\t"
    "v_fma_mix_f32 v198, v91, v182, v190 op_sel_hi:[1,0,0]\n\t"
    "v_mfma_f32_32x32x16_bf16 a[112:127], v[156:159], v[76:79], a[112:127]\n\t"
    "ds_read_b64_tr_b16 v[142:143], v245 offset:43776\n\t"
    "v_fma_mix_f32 v199, v91, v183, v191 op_sel:[1,0,0] op_sel_hi:[1,0,0]\n\t"
    "v_cvt_pk_bf16_f32 v88, v192, v193\n\t"
    "v_cvt_pk_bf16_f32 v89, v194, v195\n\t"
    "v_cvt_pk_bf16_f32 v90, v196, v197\n\t"
    "s_waitcnt lgkmcnt(10)\n\t"
    "v_mfma_f32_32x32x16_bf16 a[0:15], v[128:131], v[80:83], a[0:15]\n\t"
    "ds_read_b64_tr_b16 v[144:145], v245 offset:35840\n\t"
    "v_cvt_pk_bf16_f32 v91, v198, v199\n\t"
    "v_fma_mix_f32 v200, v92, v176, v184 op_sel_hi:[1,0,0]\n\t"
    "v_fma_mix_f32 v201, v92, v177, v185 op_sel:[1,0,0] op_sel_hi:[1,0,0]\n\t"
    "v_fma_mix_f32 v202, v93, v178, v186 op_sel_hi:[1,0,0]\n\t"
    "v_mfma_f32_32x32x16_bf16 a[16:31], v[128:131], v[84:87], a[16:31]\n\t"
    "ds_read_b64_tr_b16 v[146:147], v245 offset:36352\n\t"
    "v_fma_mix_f32 v203, v93, v179, v187 op_sel:[1,0,0] op_sel_hi:[1,0,0]\n\t"
    "v_fma_mix_f32 v204, v94, v180, v188 op_sel_hi:[1,0,0]\n\t"
    "v_fma_mix_f32 v205, v94, v181, v189 op_sel:[1,0,0] op_sel_hi:[1,0,0]\n\t"
    "v_fma_mix_f32 v206, v95, v182, v190 op_sel_hi:[1,0,0]\n\t"
    "s_waitcnt lgkmcnt(10)\n\t"
    "v_mfma_f32_32x32x16_bf16 a[32:47], v[132:135], v[80:83], a[32:47]\n\t"
    "ds_read_b64_tr_b16 v[148:149], v245 offset:36096\n\t"
    "v_fma_mix_f32 v207, v95, v183, v191 op_sel:[1,0,0] op_sel_hi:[1,0,0]\n\t"
    "v_cvt_pk_bf16_f32 v92, v200, v201\n\t"
    "v_cvt_pk_bf16_f32 v93, v202, v203\n\t"
    "v_cvt_pk_bf16_f32 v94, v204, v205\n\t"
    "v_mfma_f32_32x32x16_bf16 a[48:63], v[132:135], v[84:87], a[48:63]\n\t"
    "ds_read_b64_tr_b16 v[150:151], v245 offset:36608\n\t"
    "v_cvt_pk_bf16_f32 v95, v206, v207\n\t"
    "ds_read_b128 v[176:179], v243 offset:3712\n\t"
    "ds_read_b128 v[180:183], v243 offset:3728\n\t"
    "ds_read_b128 v[184:187], v243 offset:3744\n\t"
    "s_waitcnt lgkmcnt(9)\n\t"
    "v_mfma_f32_32x32x16_bf16 a[64:79], v[136:139], v[80:83], a[64:79]\n\t"
    "ds_read_b64_tr_b16 v[152:153], v245 offset:44032\n\t"
    "ds_read_b128 v[188:191], v243 offset:3760\n\t"
    "s_waitcnt vmcnt(13)\n\t"
    "v_fma_mix_f32 v192, v96, v160, v168 op_sel_hi:[1,0,0]\n\t"
    "v_fma_mix_f32 v193, v96, v161, v169 op_sel:[1,0,0] op_sel_hi:[1,0,0]\n\t"
    "v_mfma_f32_32x32x16_bf16 a[80:95], v[136:139], v[84:87], a[80:95]\n\t"
    "ds_read_b64_tr_b16 v[154:155], v245 offset:44544\n\t"
    "v_fma_mix_f32 v194, v97, v162, v170 op_sel_hi:[1,0,0]\n\t"
    "v_fma_mix_f32 v195, v97, v163, v171 op_sel:[1,0,0] op_sel_hi:[1,0,0]\n\t"
    "v_fma_mix_f32 v196, v98, v164, v172 op_sel_hi:[1,0,0]\n\t"
    "v_fma_mix_f32 v197, v98, v165, v173 op_sel:[1,0,0] op_sel_hi:[1,0,0]\n\t"
    "s_waitcnt lgkmcnt(10)\n\t"
    "v_mfma_f32_32x32x16_bf16 a[96:111], v[140:143], v[80:83], a[96:111]\n\t"
    "ds_read_b64_tr_b16 v[156:157], v245 offset:44288\n\t"
    "v_fma_mix_f32 v198, v99, v166, v174 op_sel_hi:[1,0,0]\n\t"
    "v_fma_mix_f32 v199, v99, v167, v175 op_sel:[1,0,0] op_sel_hi:[1,0,0]\n\t"
    "v_cvt_pk_bf16_f32 v96, v192, v193\n\t"
    "v_cvt_pk_bf16_f32 v97, v194, v195\n\t"
    "v_mfma_f32_32x32x16_bf16 a[112:127], v[140:143], v[84:87], a[112:127]\n\t"
    "ds_read_b64_tr_b16 v[158:159], v245 offset:44800\n\t"
    "v_cvt_pk_bf16_f32 v98, v196, v197\n\t"
    "v_cvt_pk_bf16_f32 v99, v198, v199\n\t"
    "v_fma_mix_f32 v200, v100, v160, v168 op_sel_hi:[1,0,0]\n\t"
    "v_fma_mix_f32 v201, v100, v161, v169 op_sel:[1,0,0] op_sel_hi:[1,0,0]\n\t"
    "s_barrier\n\t"
    "s_waitcnt lgkmcnt(10)\n\t"
    "v_mfma_f32_32x32x16_bf16 a[0:15], v[144:147], v[88:91], a[0:15]\n\t"
    "ds_read_b64_tr_b16 v[128:129], v245 offset:49152\n\t"
    "v_fma_mix_f32 v202, v101, v162, v170 op_sel_hi:[1,0,0]\n\t"
    "v_fma_mix_f32 v203, v101, v163, v171 op_sel:[1,0,0] op_sel_hi:[1,0,0]\n\t"
    "v_fma_mix_f32 v204, v102, v164, v172 op_sel_hi:[1,0,0]\n\t"
    "v_fma_mix_f32 v205, v102, v165, v173 op_sel:[1,0,0] op_sel_hi:[1,0,0]\n\t"
    "v_mfma_f32_32x32x16_bf16 a[16:31], v[144:147], v[92:95], a[16:31]\n\t"
    "ds_read_b64_tr_b16 v[130:131], v245 offset:49664\n\t"
    "v_fma_mix_f32 v206, v103, v166, v174 op_sel_hi:[1,0,0]\n\t"
    "v_fma_mix_f32 v207, v103, v167, v175 op_sel:[1,0,0] op_sel_hi:[1,0,0]\n\t"
    "v_cvt_pk_bf16_f32 v100, v200, v201\n\t"
    "v_cvt_pk_bf16_f32 v101, v202, v203\n\t"
    "s_waitcnt lgkmcnt(10)\n\t"
    "v_mfma_f32_32x32x16_bf16 a[32:47], v[148:151], v[88:91], a[32:47]\n\t"
    "ds_read_b64_tr_b16 v[132:133], v245 offset:50176\n\t"
    "v_cvt_pk_bf16_f32 v102, v204, v205\n\t"
    "v_cvt_pk_bf16_f32 v103, v206, v207\n\t"
    "ds_read_b128 v[160:163], v243 offset:3840\n\t"
    "ds_read_b128 v[164:167], v243 offset:3856\n\t"
    "v_mfma_f32_32x32x16_bf16 a[48:63], v[148:151], v[92:95], a[48:63]\n\t"
    "ds_read_b64_tr_b16 v[134:135], v245 offset:50688\n\t"
    "ds_read_b128 v[168:171], v243 offset:3872\n\t"
    "ds_read_b128 v[172:175], v243 offset:3888\n\t"
    "s_waitcnt lgkmcnt(11)\n\t"
    "v_fma_mix_f32 v192, v104, v176, v184 op_sel_hi:[1,0,0]\n\t"
    "s_waitcnt lgkmcnt(10)\n\t"
    "v_mfma_f32_32x32x16_bf16 a[64:79], v[152:155], v[88:91], a[64:79]\n\t"
    "ds_read_b64_tr_b16 v[136:137], v245 offset:51200\n\t"
    "v_fma_mix_f32 v193, v104, v177, v185 op_sel:[1,0,0] op_sel_hi:[1,0,0]\n\t"
    "v_fma_mix_f32 v194, v105, v178, v186 op_sel_hi:[1,0,0]\n\t"
    "v_fma_mix_f32 v195, v105, v179, v187 op_sel:[1,0,0] op_sel_hi:[1,0,0]\n\t"
    "v_fma_mix_f32 v196, v106, v180, v188 op_sel_hi:[1,0,0]\n\t"
    "v_mfma_f32_32x32x16_bf16 a[80:95], v[152:155], v[92:95], a[80:95]\n\t"
    "ds_read_b64_tr_b16 v[138:139], v245 offset:51712\n\t"
    "v_fma_mix_f32 v197, v106, v181, v189 op_sel:[1,0,0] op_sel_hi:[1,0,0]\n\t"
    "v_fma_mix_f32 v198, v107, v182, v190 op_sel_hi:[1,0,0]\n\t"
    "v_fma_mix_f32 v199, v107, v183, v191 op_sel:[1,0,0] op_sel_hi:[1,0,0]\n\t"
    "v_cvt_pk_bf16_f32 v104, v192, v193\n\t"
    "s_waitcnt lgkmcnt(10)\n\t"
    "v_mfma_f32_32x32x16_bf16 a[96:111], v[156:159], v[88:91], a[96:111]\n\t"
    "ds_read_b64_tr_b16 v[140:141], v245 offset:52224\n\t"
    "v_cvt_pk_bf16_f32 v105, v194, v195\n\t"
    "v_cvt_pk_bf16_f32 v106, v196, v197\n\t"
    "v_cvt_pk_bf16_f32 v107, v198, v199\n\t"
    "v_fma_mix_f32 v200, v108, v176, v184 op_sel_hi:[1,0,0]\n\t"
    "v_mfma_f32_32x32x16_bf16 a[112:127], v[156:159], v[92:95], a[112:127]\n\t"
    "ds_read_b64_tr_b16 v[142:143], v245 offset:52736\n\t"
    "v_fma_mix_f32 v201, v108, v177, v185 op_sel:[1,0,0] op_sel_hi:[1,0,0]\n\t"
    "v_fma_mix_f32 v202, v109, v178, v186 op_sel_hi:[1,0,0]\n\t"
    "v_fma_mix_f32 v203, v109, v179, v187 op_sel:[1,0,0] op_sel_hi:[1,0,0]\n\t"
    "v_fma_mix_f32 v204, v110, v180, v188 op_sel_hi:[1,0,0]\n\t"
    "s_waitcnt lgkmcnt(10)\n\t"
    "v_mfma_f32_32x32x16_bf16 a[0:15], v[128:131], v[96:99], a[0:15]\n\t"
    "ds_read_b64_tr_b16 v[144:145], v245 offset:49408\n\t"
    "v_fma_mix_f32 v205, v110, v181, v189 op_sel:[1,0,0] op_sel_hi:[1,0,0]\n\t"
    "v_fma_mix_f32 v206, v111, v182, v190 op_sel_hi:[1,0,0]\n\t"
    "v_fma_mix_f32 v207, v111, v183, v191 op_sel:[1,0,0] op_sel_hi:[1,0,0]\n\t"
    "v_cvt_pk_bf16_f32 v108, v200, v201\n\t"
    "v_mfma_f32_32x32x16_bf16 a[16:31], v[128:131], v[100:103], a[16:31]\n\t"
    "ds_read_b64_tr_b16 v[146:147], v245 offset:49920\n\t"
    "v_cvt_pk_bf16_f32 v109, v202, v203\n\t"
    "v_cvt_pk_bf16_f32 v110, v204, v205\n\t"
    "v_cvt_pk_bf16_f32 v111, v206, v207\n\t"
    "ds_read_b128 v[176:179], v243 offset:3968\n\t"
    "s_waitcnt lgkmcnt(9)\n\t"
    "v_mfma_f32_32x32x16_bf16 a[0:15], v[132:135], v[104:107], a[0:15]\n\t"
    "ds_read_b64_tr_b16 v[148:149], v245 offset:50432\n\t"
    "ds_read_b128 v[180:183], v243 offset:3984\n\t"
    "ds_read_b128 v[184:187], v243 offset:4000\n\t"
    "ds_read_b128 v[188:191], v243 offset:4016\n\t"
    "s_waitcnt lgkmcnt(11)\n\t"
    "v_mfma_f32_32x32x16_bf16 a[16:31], v[132:135], v[108:111], a[16:31]\n\t"
    "ds_read_b64_tr_b16 v[150:151], v245 offset:50944\n\t"
    "v_fma_mix_f32 v192, v112, v160, v168 op_sel_hi:[1,0,0]\n\t"
    "v_fma_mix_f32 v193, v112, v161, v169 op_sel:[1,0,0] op_sel_hi:[1,0,0]\n\t"
    "v_fma_mix_f32 v194, v113, v162, v170 op_sel_hi:[1,0,0]\n\t"
    "v_fma_mix_f32 v195, v113, v163, v171 op_sel:[1,0,0] op_sel_hi:[1,0,0]\n\t"
    "v_fma_mix_f32 v196, v114, v164, v172 op_sel_hi:[1,0,0]\n\t"
    "v_fma_mix_f32 v197, v114, v165, v173 op_sel:[1,0,0] op_sel_hi:[1,0,0]\n\t"
    "v_fma_mix_f32 v198, v115, v166, v174 op_sel_hi:[1,0,0]\n\t"
    "v_fma_mix_f32 v199, v115, v167, v175 op_sel:[1,0,0] op_sel_hi:[1,0,0]\n\t"
    "v_cvt_pk_bf16_f32 v112, v192, v193\n\t"
    "v_cvt_pk_bf16_f32 v113, v194, v195\n\t"
    "v_cvt_pk_bf16_f32 v114, v196, v197\n\t"
    "v_cvt_pk_bf16_f32 v115, v198, v199\n\t"
    "v_fma_mix_f32 v200, v116, v160, v168 op_sel_hi:[1,0,0]\n\t"
    "v_fma_mix_f32 v201, v116, v161, v169 op_sel:[1,0,0] op_sel_hi:[1,0,0]\n\t"
    "v_fma_mix_f32 v202, v117, v162, v170 op_sel_hi:[1,0,0]\n\t"
    "v_fma_mix_f32 v203, v117, v163, v171 op_sel:[1,0,0] op_sel_hi:[1,0,0]\n\t"
    "v_fma_mix_f32 v204, v118, v164, v172 op_sel_hi:[1,0,0]\n\t"
    "v_fma_mix_f32 v205, v118, v165, v173 op_sel:[1,0,0] op_sel_hi:[1,0,0]\n\t"
    "v_fma_mix_f32 v206, v119, v166, v174 op_sel_hi:[1,0,0]\n\t"
    "v_fma_mix_f32 v207, v119, v167, v175 op_sel:[1,0,0] op_sel_hi:[1,0,0]\n\t"
    "v_cvt_pk_bf16_f32 v116, v200, v201\n\t"
    "v_cvt_pk_bf16_f32 v117, v202, v203\n\t"
    "v_cvt_pk_bf16_f32 v118, v204, v205\n\t"
    "v_cvt_pk_bf16_f32 v119, v206, v207\n\t"
    "s_nop 1\n\t"
    "s_waitcnt lgkmcnt(10)\n\t"
    "v_mfma_f32_32x32x16_bf16 a[0:15], v[136:139], v[112:115], a[0:15]\n\t"
    "ds_read_b64_tr_b16 v[152:153], v245 offset:51456\n\t"
    "s_waitcnt lgkmcnt(2)\n\t"
    "v_fma_mix_f32 v192, v120, v176, v184 op_sel_hi:[1,0,0]\n\t"
    "v_fma_mix_f32 v193, v120, v177, v185 op_sel:[1,0,0] op_sel_hi:[1,0,0]\n\t"
    "v_fma_mix_f32 v194, v121, v178, v186 op_sel_hi:[1,0,0]\n\t"
    "v_mfma_f32_32x32x16_bf16 a[16:31], v[136:139], v[116:119], a[16:31]\n\t"
    "ds_read_b64_tr_b16 v[154:155], v245 offset:51968\n\t"
    "v_fma_mix_f32 v195, v121, v179, v187 op_sel:[1,0,0] op_sel_hi:[1,0,0]\n\t"
    "v_fma_mix_f32 v196, v122, v180, v188 op_sel_hi:[1,0,0]\n\t"
    "v_fma_mix_f32 v197, v122, v181, v189 op_sel:[1,0,0] op_sel_hi:[1,0,0]\n\t"
    "v_fma_mix_f32 v198, v123, v182, v190 op_sel_hi:[1,0,0]\n\t"
    "v_fma_mix_f32 v199, v123, v183, v191 op_sel:[1,0,0] op_sel_hi:[1,0,0]\n\t"
    "v_cvt_pk_bf16_f32 v120, v192, v193\n\t"
    "v_cvt_pk_bf16_f32 v121, v194, v195\n\t"
    "v_cvt_pk_bf16_f32 v122, v196, v197\n\t"
    "v_cvt_pk_bf16_f32 v123, v198, v199\n\t"
    "v_fma_mix_f32 v200, v124, v176, v184 op_sel_hi:[1,0,0]\n\t"
    "v_fma_mix_f32 v201, v124, v177, v185 op_sel:[1,0,0] op_sel_hi:[1,0,0]\n\t"
    "v_fma_mix_f32 v202, v125, v178, v186 op_sel_hi:[1,0,0]\n\t"
    "v_fma_mix_f32 v203, v125, v179, v187 op_sel:[1,0,0] op_sel_hi:[1,0,0]\n\t"
    "v_fma_mix_f32 v204, v126, v180, v188 op_sel_hi:[1,0,0]\n\t"
    "v_fma_mix_f32 v205, v126, v181, v189 op_sel:[1,0,0] op_sel_hi:[1,0,0]\n\t"
    "v_fma_mix_f32 v206, v127, v182, v190 op_sel_hi:[1,0,0]\n\t"
    "v_fma_mix_f32 v207, v127, v183, v191 op_sel:[1,0,0] op_sel_hi:[1,0,0]\n\t"
    "v_cvt_pk_bf16_f32 v124, v200, v201\n\t"
    "v_cvt_pk_bf16_f32 v125, v202, v203\n\t"
    "v_cvt_pk_bf16_f32 v126, v204, v205\n\t"
    "v_cvt_pk_bf16_f32 v127, v206, v207\n\t"
    "s_nop 1\n\t"
    "v_mfma_f32_32x32x16_bf16 a[0:15], v[140:143], v[120:123], a[0:15]\n\t"
    "ds_read_b64_tr_b16 v[156:157], v245 offset:52480\n\t"
    "v_mfma_f32_32x32x16_bf16 a[16:31], v[140:143], v[124:127], a[16:31]\n\t"
    "ds_read_b64_tr_b16 v[158:159], v245 offset:52992\n\t"
    "v_mfma_f32_32x32x16_bf16 a[32:47], v[144:147], v[96:99], a[32:47]\n\t"
    "ds_read_b64_tr_b16 v[128:129], v245 offset:57344\n\t"
    "v_mfma_f32_32x32x16_bf16 a[48:63], v[144:147], v[100:103], a[48:63]\n\t"
    "ds_read_b64_tr_b16 v[130:131], v245 offset:57856\n\t"
    "s_waitcnt lgkmcnt(6)\n\t"
    "v_mfma_f32_32x32x16_bf16 a[32:47], v[148:151], v[104:107], a[32:47]\n\t"
    "ds_read_b64_tr_b16 v[132:133], v245 offset:58368\n\t"
    "v_mfma_f32_32x32x16_bf16 a[48:63], v[148:151], v[108:111], a[48:63]\n\t"
    "ds_read_b64_tr_b16 v[134:135], v245 offset:58880\n\t"
    "v_accvgpr_read_b32 v0, a0\n\t"
    "v_accvgpr_read_b32 v1, a1\n\t"
    "v_accvgpr_read_b32 v2, a2\n\t"
    "v_accvgpr_read_b32 v3, a3\n\t"
    "v_accvgpr_read_b32 v4, a4\n\t"
    "v_accvgpr_read_b32 v5, a5\n\t"
    "v_accvgpr_read_b32 v6, a6\n\t"
    "s_waitcnt lgkmcnt(6)\n\t"
    "v_mfma_f32_32x32x16_bf16 a[32:47], v[152:155], v[112:115], a[32:47]\n\t"
    "ds_read_b64_tr_b16 v[136:137], v245 offset:59392\n\t"
    "v_accvgpr_read_b32 v7, a7\n\t"
    "v_accvgpr_read_b32 v8, a8\n\t"
    "v_accvgpr_read_b32 v9, a9\n\t"
    "v_accvgpr_read_b32 v10, a10\n\t"
    "v_accvgpr_read_b32 v11, a11\n\t"
    "v_accvgpr_read_b32 v12, a12\n\t"
    "v_accvgpr_read_b32 v13, a13\n\t"
    "v_mfma_f32_32x32x16_bf16 a[48:63], v[152:155], v[116:119], a[48:63]\n\t"
    "ds_read_b64_tr_b16 v[138:139], v245 offset:59904\n\t"
    "v_accvgpr_read_b32 v14, a14\n\t"
    "v_accvgpr_read_b32 v15, a15\n\t"
    "v_cvt_pk_bf16_f32 v0, v0, v1\n\t"
    "v_cvt_pk_bf16_f32 v1, v2, v3\n\t"
    "v_cvt_pk_bf16_f32 v2, v4, v5\n\t"
    "v_cvt_pk_bf16_f32 v3, v6, v7\n\t"
    "v_cvt_pk_bf16_f32 v8, v8, v9\n\t"
    "s_waitcnt lgkmcnt(6)\n\t"
    "v_mfma_f32_32x32x16_bf16 a[32:47], v[156:159], v[120:123], a[32:47]\n\t"
    "ds_read_b64_tr_b16 v[140:141], v245 offset:60416\n\t"
    "v_cvt_pk_bf16_f32 v9, v10, v11\n\t"
    "v_cvt_pk_bf16_f32 v10, v12, v13\n\t"
    "v_cvt_pk_bf16_f32 v11, v14, v15\n\t"
    "v_accvgpr_write_b32 a128, v0\n\t"
    "v_accvgpr_write_b32 a129, v1\n\t"
    "v_accvgpr_write_b32 a130, v2\n\t"
    "v_accvgpr_write_b32 a131, v3\n\t"
    "v_mfma_f32_32x32x16_bf16 a[48:63], v[156:159], v[124:127], a[48:63]\n\t"
    "ds_read_b64_tr_b16 v[142:143], v245 offset:60928\n\t"
    "v_accvgpr_write_b32 a132, v8\n\t"
    "v_accvgpr_write_b32 a133, v9\n\t"
    "v_accvgpr_write_b32 a134, v10\n\t"
    "v_accvgpr_write_b32 a135, v11\n\t"
    "v_accvgpr_read_b32 v16, a16\n\t"
    "v_accvgpr_read_b32 v17, a17\n\t"
    "v_accvgpr_read_b32 v18, a18\n\t"
    "s_waitcnt lgkmcnt(6)\n\t"
    "v_mfma_f32_32x32x16_bf16 a[64:79], v[128:131], v[96:99], a[64:79]\n\t"
    "ds_read_b64_tr_b16 v[144:145], v245 offset:57600\n\t"
    "v_accvgpr_read_b32 v19, a19\n\t"
    "v_accvgpr_read_b32 v20, a20\n\t"
    "v_accvgpr_read_b32 v21, a21\n\t"
    "v_accvgpr_read_b32 v22, a22\n\t"
    "v_accvgpr_read_b32 v23, a23\n\t"
    "v_accvgpr_read_b32 v24, a24\n\t"
    "v_accvgpr_read_b32 v25, a25\n\t"
    "v_mfma_f32_32x32x16_bf16 a[80:95], v[128:131], v[100:103], a[80:95]\n\t"
    "ds_read_b64_tr_b16 v[146:147], v245 offset:58112\n\t"
    "v_accvgpr_read_b32 v26, a26\n\t"
    "v_accvgpr_read_b32 v27, a27\n\t"
    "v_accvgpr_read_b32 v28, a28\n\t"
    "v_accvgpr_read_b32 v29, a29\n\t"
    "v_accvgpr_read_b32 v30, a30\n\t"
    "v_accvgpr_read_b32 v31, a31\n\t"
    "v_cvt_pk_bf16_f32 v16, v16, v17\n\t"
    "s_waitcnt lgkmcnt(6)\n\t"
    "v_mfma_f32_32x32x16_bf16 a[64:79], v[132:135], v[104:107], a[64:79]\n\t"
    "ds_read_b64_tr_b16 v[148:149], v245 offset:58624\n\t"
    "v_cvt_pk_bf16_f32 v17, v18, v19\n\t"
    "v_cvt_pk_bf16_f32 v18, v20, v21\n\t"
    "v_cvt_pk_bf16_f32 v19, v22, v23\n\t"
    "v_cvt_pk_bf16_f32 v24, v24, v25\n\t"
    "v_cvt_pk_bf16_f32 v25, v26, v27\n\t"
    "v_cvt_pk_bf16_f32 v26, v28, v29\n\t"
    "v_cvt_pk_bf16_f32 v27, v30, v31\n\t"
    "v_mfma_f32_32x32x16_bf16 a[80:95], v[132:135], v[108:111], a[80:95]\n\t"
    "ds_read_b64_tr_b16 v[150:151], v245 offset:59136\n\t"
    "v_accvgpr_write_b32 a160, v16\n\t"
    "v_accvgpr_write_b32 a161, v17\n\t"
    "v_accvgpr_write_b32 a162, v18\n\t"
    "v_accvgpr_write_b32 a163, v19\n\t"
    "v_accvgpr_write_b32 a164, v24\n\t"
    "v_accvgpr_write_b32 a165, v25\n\t"
    "v_accvgpr_write_b32 a166, v26\n\t"
    "s_waitcnt lgkmcnt(6)\n\t"
    "v_mfma_f32_32x32x16_bf16 a[64:79], v[136:139], v[112:115], a[64:79]\n\t"
    "ds_read_b64_tr_b16 v[152:153], v245 offset:59648\n\t"
    "v_accvgpr_write_b32 a167, v27\n\t"
    "v_accvgpr_read_b32 v32, a32\n\t"
    "v_accvgpr_read_b32 v33, a33\n\t"
    "v_accvgpr_read_b32 v34, a34\n\t"
    "v_accvgpr_read_b32 v35, a35\n\t"
    "v_accvgpr_read_b32 v36, a36\n\t"
    "v_accvgpr_read_b32 v37, a37\n\t"
    "v_mfma_f32_32x32x16_bf16 a[80:95], v[136:139], v[116:119], a[80:95]\n\t"
    "ds_read_b64_tr_b16 v[154:155], v245 offset:60160\n\t"
    "v_accvgpr_read_b32 v38, a38\n\t"
    "v_accvgpr_read_b32 v39, a39\n\t"
    "v_accvgpr_read_b32 v40, a40\n\t"
    "v_accvgpr_read_b32 v41, a41\n\t"
    "v_accvgpr_read_b32 v42, a42\n\t"
    "v_accvgpr_read_b32 v43, a43\n\t"
    "v_accvgpr_read_b32 v44, a44\n\t"
    "s_waitcnt lgkmcnt(6)\n\t"
    "v_mfma_f32_32x32x16_bf16 a[64:79], v[140:143], v[120:123], a[64:79]\n\t"
    "ds_read_b64_tr_b16 v[156:157], v245 offset:60672\n\t"
    "v_accvgpr_read_b32 v45, a45\n\t"
    "v_accvgpr_read_b32 v46, a46\n\t"
    "v_accvgpr_read_b32 v47, a47\n\t"
    "v_cvt_pk_bf16_f32 v32, v32, v33\n\t"
    "v_cvt_pk_bf16_f32 v33, v34, v35\n\t"
    "v_cvt_pk_bf16_f32 v34, v36, v37\n\t"
    "v_cvt_pk_bf16_f32 v35, v38, v39\n\t"
    "v_mfma_f32_32x32x16_bf16 a[80:95], v[140:143], v[124:127], a[80:95]\n\t"
    "ds_read_b64_tr_b16 v[158:159], v245 offset:61184\n\t"
    "v_cvt_pk_bf16_f32 v40, v40, v41\n\t"
    "v_cvt_pk_bf16_f32 v41, v42, v43\n\t"
    "v_cvt_pk_bf16_f32 v42, v44, v45\n\t"
    "v_cvt_pk_bf16_f32 v43, v46, v47\n\t"
    "v_accvgpr_write_b32 a136, v32\n\t"
    "v_accvgpr_write_b32 a137, v33\n\t"
    "v_accvgpr_write_b32 a138, v34\n\t"
    "s_barrier\n\t"
    "s_waitcnt lgkmcnt(6)\n\t"
    "v_mfma_f32_32x32x16_bf16 a[96:111], v[144:147], v[96:99], a[96:111]\n\t"
    "v_accvgpr_write_b32 a139, v35\n\t"
    "v_accvgpr_write_b32 a140, v40\n\t"
    "v_accvgpr_write_b32 a141, v41\n\t"
    "v_accvgpr_write_b32 a142, v42\n\t"
    "v_accvgpr_write_b32 a143, v43\n\t"
    "v_accvgpr_read_b32 v48, a48\n\t"
    "v_accvgpr_read_b32 v49, a49\n\t"
    "v_mfma_f32_32x32x16_bf16 a[112:127], v[144:147], v[100:103], a[112:127]\n\t"
    "v_accvgpr_read_b32 v50, a50\n\t"
    "v_accvgpr_read_b32 v51, a51\n\t"
    "v_accvgpr_read_b32 v52, a52\n\t"
    "v_accvgpr_read_b32 v53, a53\n\t"
    "v_accvgpr_read_b32 v54, a54\n\t"
    "v_accvgpr_read_b32 v55, a55\n\t"
    "v_accvgpr_read_b32 v56, a56\n\t"
    "s_waitcnt lgkmcnt(4)\n\t"
    "v_mfma_f32_32x32x16_bf16 a[96:111], v[148:151], v[104:107], a[96:111]\n\t"
    "v_accvgpr_read_b32 v57, a57\n\t"
    "v_accvgpr_read_b32 v58, a58\n\t"
    "v_accvgpr_read_b32 v59, a59\n\t"
    "v_accvgpr_read_b32 v60, a60\n\t"
    "v_accvgpr_read_b32 v61, a61\n\t"
    "v_accvgpr_read_b32 v62, a62\n\t"
    "v_accvgpr_read_b32 v63, a63\n\t"
    "v_mfma_f32_32x32x16_bf16 a[112:127], v[148:151], v[108:111], a[112:127]\n\t"
    "v_cvt_pk_bf16_f32 v48, v48, v49\n\t"
    "v_cvt_pk_bf16_f32 v49, v50, v51\n\t"
    "v_cvt_pk_bf16_f32 v50, v52, v53\n\t"
    "v_cvt_pk_bf16_f32 v51, v54, v55\n\t"
    "v_cvt_pk_bf16_f32 v56, v56, v57\n\t"
    "v_cvt_pk_bf16_f32 v57, v58, v59\n\t"
    "v_cvt_pk_bf16_f32 v58, v60, v61\n\t"
    "s_waitcnt lgkmcnt(2)\n\t"
    "v_mfma_f32_32x32x16_bf16 a[96:111], v[152:155], v[112:115], a[96:111]\n\t"
    "v_cvt_pk_bf16_f32 v59, v62, v63\n\t"
    "v_accvgpr_write_b32 a168, v48\n\t"
    "v_accvgpr_write_b32 a169, v49\n\t"
    "v_accvgpr_write_b32 a170, v50\n\t"
    "v_accvgpr_write_b32 a171, v51\n\t"
    "v_accvgpr_write_b32 a172, v56\n\t"
    "v_accvgpr_write_b32 a173, v57\n\t"
    "v_mfma_f32_32x32x16_bf16 a[112:127], v[152:155], v[116:119], a[112:127]\n\t"
    "v_accvgpr_write_b32 a174, v58\n\t"
    "v_accvgpr_write_b32 a175, v59\n\t"
    "v_accvgpr_read_b32 v64, a64\n\t"
    "v_accvgpr_read_b32 v65, a65\n\t"
    "v_accvgpr_read_b32 v66, a66\n\t"
    "v_accvgpr_read_b32 v67, a67\n\t"
    "v_accvgpr_read_b32 v68, a68\n\t"
    "s_waitcnt lgkmcnt(0)\n\t"
    "v_mfma_f32_32x32x16_bf16 a[96:111], v[156:159], v[120:123], a[96:111]\n\t"
    "v_accvgpr_read_b32 v69, a69\n\t"
    "v_accvgpr_read_b32 v70, a70\n\t"
    "v_accvgpr_read_b32 v71, a71\n\t"
    "v_accvgpr_read_b32 v72, a72\n\t"
    "v_accvgpr_read_b32 v73, a73\n\t"
    "v_accvgpr_read_b32 v74, a74\n\t"
    "v_accvgpr_read_b32 v75, a75\n\t"
    "v_mfma_f32_32x32x16_bf16 a[112:127], v[156:159], v[124:127], a[112:127]\n\t"
    "v_accvgpr_read_b32 v76, a76\n\t"
    "v_accvgpr_read_b32 v77, a77\n\t"
    "v_accvgpr_read_b32 v78, a78\n\t"
    "v_accvgpr_read_b32 v79, a79\n\t"
    "v_cvt_pk_bf16_f32 v64, v64, v65\n\t"
    "v_cvt_pk_bf16_f32 v65, v66, v67\n\t"
    "v_cvt_pk_bf16_f32 v66, v68, v69\n\t"
    "s_waitcnt vmcnt(9)\n\t"
    "s_barrier\n\t"
    "ds_read_b128 a[192:195], v225 offset:0\n\t"
    "ds_read_b128 a[196:199], v225 offset:1024\n\t"
    "ds_read_b128 a[200:203], v225 offset:2048\n\t"
    "ds_read_b128 a[204:207], v225 offset:3072\n\t"
    "ds_read_b128 a[208:211], v225 offset:4096\n\t"
    "ds_read_b128 a[212:215], v225 offset:5120\n\t"
    "ds_read_b128 a[216:219], v225 offset:6144\n\t"
    "ds_read_b128 a[220:223], v225 offset:7168\n\t"
    "ds_read_b128 a[224:227], v225 offset:8192\n\t"
    "ds_read_b128 a[228:231], v225 offset:9216\n\t"
    "ds_read_b128 a[232:235], v225 offset:10240\n\t"
    "ds_read_b128 a[236:239], v225 offset:11264\n\t"
    "ds_read_b128 a[240:243], v225 offset:12288\n\t"
    "ds_read_b128 a[244:247], v225 offset:13312\n\t"
    "ds_read_b128 a[248:251], v225 offset:14336\n\t"
    "ds_read_b128 a[252:255], v225 offset:15360\n\t"
    "v_cvt_pk_bf16_f32 v67, v70, v71\n\t"
    "v_cvt_pk_bf16_f32 v72, v72, v73\n\t"
    "v_cvt_pk_bf16_f32 v73, v74, v75\n\t"
    "v_cvt_pk_bf16_f32 v74, v76, v77\n\t"
    "v_cvt_pk_bf16_f32 v75, v78, v79\n\t"
    "v_accvgpr_write_b32 a144, v64\n\t"
    "v_accvgpr_write_b32 a145, v65\n\t"
    "v_accvgpr_write_b32 a146, v66\n\t"
    "v_accvgpr_write_b32 a147, v67\n\t"
    "v_accvgpr_write_b32 a148, v72\n\t"
    "v_accvgpr_write_b32 a149, v73\n\t"
    "v_accvgpr_write_b32 a150, v74\n\t"
    "v_accvgpr_write_b32 a151, v75\n\t"
    "v_accvgpr_read_b32 v80, a80\n\t"
    "v_accvgpr_read_b32 v81, a81\n\t"
    "v_accvgpr_read_b32 v82, a82\n\t"
    "v_accvgpr_read_b32 v83, a83\n\t"
    "v_accvgpr_read_b32 v84, a84\n\t"
    "v_accvgpr_read_b32 v85, a85\n\t"
    "v_accvgpr_read_b32 v86, a86\n\t"
    "v_accvgpr_read_b32 v87, a87\n\t"
    "v_accvgpr_read_b32 v88, a88\n\t"
    "v_accvgpr_read_b32 v89, a89\n\t"
    "v_accvgpr_read_b32 v90, a90\n\t"
    "v_accvgpr_read_b32 v91, a91\n\t"
    "v_accvgpr_read_b32 v92, a92\n\t"
    "v_accvgpr_read_b32 v93, a93\n\t"
    "v_accvgpr_read_b32 v94, a94\n\t"
    "v_accvgpr_read_b32 v95, a95\n\t"
    "v_cvt_pk_bf16_f32 v80, v80, v81\n\t"
    "v_cvt_pk_bf16_f32 v81, v82, v83\n\t"
    "v_cvt_pk_bf16_f32 v82, v84, v85\n\t"
    "v_cvt_pk_bf16_f32 v83, v86, v87\n\t"
    "v_cvt_pk_bf16_f32 v88, v88, v89\n\t"
    "v_cvt_pk_bf16_f32 v89, v90, v91\n\t"
    "v_cvt_pk_bf16_f32 v90, v92, v93\n\t"
    "v_cvt_pk_bf16_f32 v91, v94, v95\n\t"
    "v_accvgpr_write_b32 a176, v80\n\t"
    "v_accvgpr_write_b32 a177, v81\n\t"
    "v_accvgpr_write_b32 a178, v82\n\t"
    "v_accvgpr_write_b32 a179, v83\n\t"
    "v_accvgpr_write_b32 a180, v88\n\t"
    "v_accvgpr_write_b32 a181, v89\n\t"
    "v_accvgpr_write_b32 a182, v90\n\t"
    "v_accvgpr_write_b32 a183, v91\n\t"
    "v_accvgpr_read_b32 v96, a96\n\t"
    "v_accvgpr_read_b32 v97, a97\n\t"
    "v_accvgpr_read_b32 v98, a98\n\t"
    "v_accvgpr_read_b32 v99, a99\n\t"
    "v_accvgpr_read_b32 v100, a100\n\t"
    "v_accvgpr_read_b32 v101, a101\n\t"
    "v_accvgpr_read_b32 v102, a102\n\t"
    "v_accvgpr_read_b32 v103, a103\n\t"
    "v_accvgpr_read_b32 v104, a104\n\t"
    "v_accvgpr_read_b32 v105, a105\n\t"
    "v_accvgpr_read_b32 v106, a106\n\t"
    "v_accvgpr_read_b32 v107, a107\n\t"
    "v_accvgpr_read_b32 v108, a108\n\t"
    "v_accvgpr_read_b32 v109, a109\n\t"
    "v_accvgpr_read_b32 v110, a110\n\t"
    "v_accvgpr_read_b32 v111, a111\n\t"
    "v_cvt_pk_bf16_f32 v96, v96, v97\n\t"
    "v_cvt_pk_bf16_f32 v97, v98, v99\n\t"
    "v_cvt_pk_bf16_f32 v98, v100, v101\n\t"
    "v_cvt_pk_bf16_f32 v99, v102, v103\n\t"
    "v_cvt_pk_bf16_f32 v104, v104, v105\n\t"
    "v_cvt_pk_bf16_f32 v105, v106, v107\n\t"
    "v_cvt_pk_bf16_f32 v106, v108, v109\n\t"
    "v_cvt_pk_bf16_f32 v107, v110, v111\n\t"
    "v_accvgpr_write_b32 a152, v96\n\t"
    "v_accvgpr_write_b32 a153, v97\n\t"
    "v_accvgpr_write_b32 a154, v98\n\t"
    "v_accvgpr_write_b32 a155, v99\n\t"
    "v_accvgpr_write_b32 a156, v104\n\t"
    "v_accvgpr_write_b32 a157, v105\n\t"
    "v_accvgpr_write_b32 a158, v106\n\t"
    "v_accvgpr_write_b32 a159, v107\n\t"
    "v_accvgpr_read_b32 v112, a112\n\t"
    "v_accvgpr_read_b32 v113, a113\n\t"
    "v_accvgpr_read_b32 v114, a114\n\t"
    "v_accvgpr_read_b32 v115, a115\n\t"
    "v_accvgpr_read_b32 v116, a116\n\t"
    "v_accvgpr_read_b32 v117, a117\n\t"
    "v_accvgpr_read_b32 v118, a118\n\t"
    "v_accvgpr_read_b32 v119, a119\n\t"
    "v_accvgpr_read_b32 v120, a120\n\t"
    "v_accvgpr_read_b32 v121, a121\n\t"
    "v_accvgpr_read_b32 v122, a122\n\t"
    "v_accvgpr_read_b32 v123, a123\n\t"
    "v_accvgpr_read_b32 v124, a124\n\t"
    "v_accvgpr_read_b32 v125, a125\n\t"
    "v_accvgpr_read_b32 v126, a126\n\t"
    "v_accvgpr_read_b32 v127, a127\n\t"
    "v_cvt_pk_bf16_f32 v112, v112, v113\n\t"
    "v_cvt_pk_bf16_f32 v113, v114, v115\n\t"
    "v_cvt_pk_bf16_f32 v114, v116, v117\n\t"
    "v_cvt_pk_bf16_f32 v115, v118, v119\n\t"
    "v_cvt_pk_bf16_f32 v120, v120, v121\n\t"
    "v_cvt_pk_bf16_f32 v121, v122, v123\n\t"
    "v_cvt_pk_bf16_f32 v122, v124, v125\n\t"
    "v_cvt_pk_bf16_f32 v123, v126, v127\n\t"
    "v_accvgpr_write_b32 a184, v112\n\t"
    "v_accvgpr_write_b32 a185, v113\n\t"
    "v_accvgpr_write_b32 a186, v114\n\t"
    "v_accvgpr_write_b32 a187, v115\n\t"
    "v_accvgpr_write_b32 a188, v120\n\t"
    "v_accvgpr_write_b32 a189, v121\n\t"
    "v_accvgpr_write_b32 a190, v122\n\t"
    "v_accvgpr_write_b32 a191, v123\n\t"
    "v_add_u32 v240, s86, v224\n\t"
    "v_mov_b32 v244, -1\n\t"
    "v_mov_b32 v245, -1\n\t"
    "v_mov_b32 v246, -1\n\t"
    "v_mov_b32 v247, -1\n\t"
    "v_mov_b32 v160, 0\n\t"
    "v_mov_b32 v161, 0\n\t"
    "v_mov_b32 v162, 0\n\t"
    "v_mov_b32 v163, 0\n\t"
    "s_waitcnt lgkmcnt(0)\n\t"
    "v_mfma_f32_32x32x16_bf16 v[0:15], a[192:195], a[128:131], 0\n\t"
    "v_mfma_f32_32x32x16_bf16 v[16:31], a[192:195], a[160:163], 0\n\t"
    "s_and_b32 m0, s58, 0xffff\n\t"
    "v_mfma_f32_32x32x16_bf16 v[32:47], a[224:227], a[128:131], 0\n\t"
    "buffer_load_dwordx4 v224, s[36:39], s58 offen offset:0 lds\n\t"
    "v_mfma_f32_32x32x16_bf16 v[48:63], a[224:227], a[160:163], 0\n\t"
    "v_mfma_f32_32x32x16_bf16 v[0:15], a[196:199], a[132:135], v[0:15]\n\t"
    "buffer_load_dwordx4 v224, s[36:39], s58 offen offset:1024 lds\n\t"
    "v_mfma_f32_32x32x16_bf16 v[16:31], a[196:199], a[164:167], v[16:31]\n\t"
    "v_mfma_f32_32x32x16_bf16 v[32:47], a[228:231], a[132:135], v[32:47]\n\t"
    "buffer_load_dwordx4 v224, s[36:39], s58 offen offset:2048 lds\n\t"
    "v_mfma_f32_32x32x16_bf16 v[48:63], a[228:231], a[164:167], v[48:63]\n\t"
    "v_mfma_f32_32x32x16_bf16 v[0:15], a[200:203], a[136:139], v[0:15]\n\t"
    "buffer_load_dwordx4 v224, s[36:39], s58 offen offset:3072 lds\n\t"
    "v_mfma_f32_32x32x16_bf16 v[16:31], a[200:203], a[168:171], v[16:31]\n\t"
    "v_mfma_f32_32x32x16_bf16 v[32:47], a[232:235], a[136:139], v[32:47]\n\t"
    "s_cmp_lg_u32 s53, 0\n\t"
    "s_cbranch_scc1 Lsk_1_%=\n\t"
    "s_add_u32 m0, m0, 0x1000\n\t"
    "s_add_u32 s68, s58, 0x1000\n\t"
    "buffer_load_dwordx4 v224, s[36:39], s68 offen offset:0 lds\n\t"
    "buffer_load_dwordx4 v224, s[36:39], s68 offen offset:1024 lds\n\t"
    "buffer_load_dwordx4 v224, s[36:39], s68 offen offset:2048 lds\n\t"
    "buffer_load_dwordx4 v224, s[36:39], s68 offen offset:3072 lds\n\t"
    "Lsk_1_%=:\n\t"
    "v_mfma_f32_32x32x16_bf16 v[48:63], a[232:235], a[168:171], v[48:63]\n\t"
    "s_cmp_lt_u32 s89, 2\n\t"
    "s_cselect_b64 exec, -1, 0\n\t"
    "v_mfma_f32_32x32x16_bf16 v[0:15], a[204:207], a[140:143], v[0:15]\n\t"
    "ds_write_b128 v240, a[128:131] offset:0\n\t"
    "v_mfma_f32_32x32x16_bf16 v[16:31], a[204:207], a[172:175], v[16:31]\n\t"
    "ds_write_b128 v240, a[132:135] offset:1024\n\t"
    "v_mfma_f32_32x32x16_bf16 v[32:47], a[236:239], a[140:143], v[32:47]\n\t"
    "ds_write_b128 v240, a[136:139] offset:2048\n\t"
    "v_mfma_f32_32x32x16_bf16 v[48:63], a[236:239], a[172:175], v[48:63]\n\t"
    "ds_write_b128 v240, a[140:143] offset:3072\n\t"
    "v_mfma_f32_32x32x16_bf16 v[0:15], a[208:211], a[144:147], v[0:15]\n\t"
    "ds_write_b128 v240, a[144:147] offset:4096\n\t"
    "v_mfma_f32_32x32x16_bf16 v[16:31], a[208:211], a[176:179], v[16:31]\n\t"
    "ds_write_b128 v240, a[148:151] offset:5120\n\t"
    "v_mfma_f32_32x32x16_bf16 v[32:47], a[240:243], a[144:147], v[32:47]\n\t"
    "ds_write_b128 v240, a[152:155] offset:6144\n\t"
    "v_mfma_f32_32x32x16_bf16 v[48:63], a[240:243], a[176:179], v[48:63]\n\t"
    "ds_write_b128 v240, a[156:159] offset:7168\n\t"
    "v_mfma_f32_32x32x16_bf16 v[0:15], a[212:215], a[148:151], v[0:15]\n\t"
    "s_and_b32 s66, s57, 0xc000\n\t"
    "ds_write_b128 v240, a[160:163] offset:8192\n\t"
    "v_mfma_f32_32x32x16_bf16 v[16:31], a[212:215], a[180:183], v[16:31]\n\t"
    "ds_write_b128 v240, a[164:167] offset:9216\n\t"
    "v_mfma_f32_32x32x16_bf16 v[32:47], a[244:247], a[148:151], v[32:47]\n\t"
    "ds_write_b128 v240, a[168:171] offset:10240\n\t"
    "v_mfma_f32_32x32x16_bf16 v[48:63], a[244:247], a[180:183], v[48:63]\n\t"
    "ds_write_b128 v240, a[172:175] offset:11264\n\t"
    "v_mfma_f32_32x32x16_bf16 v[0:15], a[216:219], a[152:155], v[0:15]\n\t"
    "ds_write_b128 v240, a[176:179] offset:12288\n\t"
    "v_mfma_f32_32x32x16_bf16 v[16:31], a[216:219], a[184:187], v[16:31]\n\t"
    "ds_write_b128 v240, a[180:183] offset:13312\n\t"
    "v_mfma_f32_32x32x16_bf16 v[32:47], a[248:251], a[152:155], v[32:47]\n\t"
    "ds_write_b128 v240, a[184:187] offset:14336\n\t"
    "v_mfma_f32_32x32x16_bf16 v[48:63], a[248:251], a[184:187], v[48:63]\n\t"
    "ds_write_b128 v240, a[188:191] offset:15360\n\t"
    "v_mfma_f32_32x32x16_bf16 v[0:15], a[220:223], a[156:159], v[0:15]\n\t"
    "s_mov_b64 exec, -1\n\t"
    "v_mfma_f32_32x32x16_bf16 v[16:31], a[220:223], a[188:191], v[16:31]\n\t"
    "v_mfma_f32_32x32x16_bf16 v[32:47], a[252:255], a[156:159], v[32:47]\n\t"
    "v_add_u32 v225, s66, v224\n\t"
    "v_mfma_f32_32x32x16_bf16 v[48:63], a[252:255], a[188:191], v[48:63]\n\t"
    "s_cmp_lg_u32 s53, 0\n\t"
    "s_cbranch_scc1 Lwa_2_%=\n\t"
    "s_waitcnt vmcnt(13)\n\t"
    "s_branch Lwb_3_%=\n\t"
    "Lwa_2_%=:\n\t"
    "s_waitcnt vmcnt(9)\n\t"
    "Lwb_3_%=:\n\t"
    "s_waitcnt lgkmcnt(0)\n\t"
    "s_barrier\n\t"
    "s_cmp_ge_u32 s56, s63\n\t"
    "s_cbranch_scc1 Lst_60_%=\n\t"
    "Lbk_61_%=:\n\t"
    "ds_read_b128 a[192:195], v225 offset:0\n\t"
    "ds_read_b128 a[196:199], v225 offset:1024\n\t"
    "ds_read_b128 a[200:203], v225 offset:2048\n\t"
    "ds_read_b128 a[204:207], v225 offset:3072\n\t"
    "s_and_b32 s69, s56, 0xc000\n\t"
    "v_add_u32 v226, s69, v227\n\t"
    "s_nop 1\n\t"
    "v_max3_f32 v234, v0, v1, v2\n\t"
    "v_max3_f32 v235, v16, v17, v18\n\t"
    "v_max3_f32 v236, v32, v33, v34\n\t"
    "v_max3_f32 v237, v48, v49, v50\n\t"
    "ds_read_b128 a[208:211], v225 offset:4096\n\t"
    "v_max3_f32 v234, v234, v3, v4\n\t"
    "v_max3_f32 v235, v235, v19, v20\n\t"
    "v_max3_f32 v236, v236, v35, v36\n\t"
    "v_max3_f32 v237, v237, v51, v52\n\t"
    "ds_read_b128 a[212:215], v225 offset:5120\n\t"
    "v_max3_f32 v234, v234, v5, v6\n\t"
    "v_max3_f32 v235, v235, v21, v22\n\t"
    "v_max3_f32 v236, v236, v37, v38\n\t"
    "v_max3_f32 v237, v237, v53, v54\n\t"
    "ds_read_b128 a[216:219], v225 offset:6144\n\t"
    "v_max3_f32 v234, v234, v7, v8\n\t"
    "v_max3_f32 v235, v235, v23, v24\n\t"
    "v_max3_f32 v236, v236, v39, v40\n\t"
    "v_max3_f32 v237, v237, v55, v56\n\t"
    "ds_read_b128 a[220:223], v225 offset:7168\n\t"
    "v_max3_f32 v234, v234, v9, v10\n\t"
    "v_max3_f32 v235, v235, v25, v26\n\t"
    "v_max3_f32 v236, v236, v41, v42\n\t"
    "v_max3_f32 v237, v237, v57, v58\n\t"
    "ds_read_b128 a[224:227], v225 offset:8192\n\t"
    "v_max3_f32 v234, v234, v11, v12\n\t"
    "v_max3_f32 v235, v235, v27, v28\n\t"
    "v_max3_f32 v236, v236, v43, v44\n\t"
    "v_max3_f32 v237, v237, v59, v60\n\t"
    "ds_read_b128 a[228:231], v225 offset:9216\n\t"
    "v_max3_f32 v234, v234, v13, v14\n\t"
    "v_max3_f32 v235, v235, v29, v30\n\t"
    "v_max3_f32 v236, v236, v45, v46\n\t"
    "v_max3_f32 v237, v237, v61, v62\n\t"
    "ds_read_b128 a[232:235], v225 offset:10240\n\t"
    "v_max_f32 v234, v234, v15\n\t"
    "v_max_f32 v235, v235, v31\n\t"
    "v_max_f32 v236, v236, v47\n\t"
    "v_max_f32 v237, v237, v63\n\t"
    "ds_read_b128 a[236:239], v225 offset:11264\n\t"
    "v_max_f32 v240, v234, v236\n\t"
    "v_max_f32 v241, v235, v237\n\t"
    "v_mov_b32 v242, v240\n\t"
    "v_mov_b32 v243, v241\n\t"
    "s_nop 1\n\t"
    "v_permlane32_swap_b32 v240, v242\n\t"
    "v_permlane32_swap_b32 v241, v243\n\t"
    "s_nop 0\n\t"
    "v_max_f32 v240, v240, v242\n\t"
    "v_max_f32 v241, v241, v243\n\t"
    "v_max_f32 v240, s61, v240\n\t"
    "v_max_f32 v241, s61, v241\n\t"
    "v_add_f32 v232, v232, v240\n\t"
    "v_add_f32 v233, v233, v241\n\t"
    "v_pk_add_f32 v[0:1], v[0:1], v[240:241] op_sel:[0,0] op_sel_hi:[1,0] neg_lo:[0,1] neg_hi:[0,1]\n\t"
    "v_pk_add_f32 v[2:3], v[2:3], v[240:241] op_sel:[0,0] op_sel_hi:[1,0] neg_lo:[0,1] neg_hi:[0,1]\n\t"
    "v_pk_add_f32 v[4:5], v[4:5], v[240:241] op_sel:[0,0] op_sel_hi:[1,0] neg_lo:[0,1] neg_hi:[0,1]\n\t"
    "v_pk_add_f32 v[6:7], v[6:7], v[240:241] op_sel:[0,0] op_sel_hi:[1,0] neg_lo:[0,1] neg_hi:[0,1]\n\t"
    "v_pk_add_f32 v[8:9], v[8:9], v[240:241] op_sel:[0,0] op_sel_hi:[1,0] neg_lo:[0,1] neg_hi:[0,1]\n\t"
    "v_pk_add_f32 v[10:11], v[10:11], v[240:241] op_sel:[0,0] op_sel_hi:[1,0] neg_lo:[0,1] neg_hi:[0,1]\n\t"
    "v_pk_add_f32 v[12:13], v[12:13], v[240:241] op_sel:[0,0] op_sel_hi:[1,0] neg_lo:[0,1] neg_hi:[0,1]\n\t"
    "v_pk_add_f32 v[14:15], v[14:15], v[240:241] op_sel:[0,0] op_sel_hi:[1,0] neg_lo:[0,1] neg_hi:[0,1]\n\t"
    "v_pk_add_f32 v[16:17], v[16:17], v[240:241] op_sel:[0,1] op_sel_hi:[1,1] neg_lo:[0,1] neg_hi:[0,1]\n\t"
    "v_pk_add_f32 v[18:19], v[18:19], v[240:241] op_sel:[0,1] op_sel_hi:[1,1] neg_lo:[0,1] neg_hi:[0,1]\n\t"
    "v_pk_add_f32 v[20:21], v[20:21], v[240:241] op_sel:[0,1] op_sel_hi:[1,1] neg_lo:[0,1] neg_hi:[0,1]\n\t"
    "v_pk_add_f32 v[22:23], v[22:23], v[240:241] op_sel:[0,1] op_sel_hi:[1,1] neg_lo:[0,1] neg_hi:[0,1]\n\t"
    "v_pk_add_f32 v[24:25], v[24:25], v[240:241] op_sel:[0,1] op_sel_hi:[1,1] neg_lo:[0,1] neg_hi:[0,1]\n\t"
    "v_pk_add_f32 v[26:27], v[26:27], v[240:241] op_sel:[0,1] op_sel_hi:[1,1] neg_lo:[0,1] neg_hi:[0,1]\n\t"
    "v_pk_add_f32 v[28:29], v[28:29], v[240:241] op_sel:[0,1] op_sel_hi:[1,1] neg_lo:[0,1] neg_hi:[0,1]\n\t"
    "v_pk_add_f32 v[30:31], v[30:31], v[240:241] op_sel:[0,1] op_sel_hi:[1,1] neg_lo:[0,1] neg_hi:[0,1]\n\t"
    "v_pk_add_f32 v[32:33], v[32:33], v[240:241] op_sel:[0,0] op_sel_hi:[1,0] neg_lo:[0,1] neg_hi:[0,1]\n\t"
    "v_pk_add_f32 v[34:35], v[34:35], v[240:241] op_sel:[0,0] op_sel_hi:[1,0] neg_lo:[0,1] neg_hi:[0,1]\n\t"
    "v_pk_add_f32 v[36:37], v[36:37], v[240:241] op_sel:[0,0] op_sel_hi:[1,0] neg_lo:[0,1] neg_hi:[0,1]\n\t"
    "v_pk_add_f32 v[38:39], v[38:39], v[240:241] op_sel:[0,0] op_sel_hi:[1,0] neg_lo:[0,1] neg_hi:[0,1]\n\t"
    "v_pk_add_f32 v[40:41], v[40:41], v[240:241] op_sel:[0,0] op_sel_hi:[1,0] neg_lo:[0,1] neg_hi:[0,1]\n\t"
    "v_pk_add_f32 v[42:43], v[42:43], v[240:241] op_sel:[0,0] op_sel_hi:[1,0] neg_lo:[0,1] neg_hi:[0,1]\n\t"
    "v_pk_add_f32 v[44:45], v[44:45], v[240:241] op_sel:[0,0] op_sel_hi:[1,0] neg_lo:[0,1] neg_hi:[0,1]\n\t"
    "v_pk_add_f32 v[46:47], v[46:47], v[240:241] op_sel:[0,0] op_sel_hi:[1,0] neg_lo:[0,1] neg_hi:[0,1]\n\t"
    "v_pk_add_f32 v[48:49], v[48:49], v[240:241] op_sel:[0,1] op_sel_hi:[1,1] neg_lo:[0,1] neg_hi:[0,1]\n\t"
    "v_pk_add_f32 v[50:51], v[50:51], v[240:241] op_sel:[0,1] op_sel_hi:[1,1] neg_lo:[0,1] neg_hi:[0,1]\n\t"
    "v_pk_add_f32 v[52:53], v[52:53], v[240:241] op_sel:[0,1] op_sel_hi:[1,1] neg_lo:[0,1] neg_hi:[0,1]\n\t"
    "v_pk_add_f32 v[54:55], v[54:55], v[240:241] op_sel:[0,1] op_sel_hi:[1,1] neg_lo:[0,1] neg_hi:[0,1]\n\t"
    "v_pk_add_f32 v[56:57], v[56:57], v[240:241] op_sel:[0,1] op_sel_hi:[1,1] neg_lo:[0,1] neg_hi:[0,1]\n\t"
    "v_pk_add_f32 v[58:59], v[58:59], v[240:241] op_sel:[0,1] op_sel_hi:[1,1] neg_lo:[0,1] neg_hi:[0,1]\n\t"
    "v_pk_add_f32 v[60:61], v[60:61], v[240:241] op_sel:[0,1] op_sel_hi:[1,1] neg_lo:[0,1] neg_hi:[0,1]\n\t"
    "v_pk_add_f32 v[62:63], v[62:63], v[240:241] op_sel:[0,1] op_sel_hi:[1,1] neg_lo:[0,1] neg_hi:[0,1]\n\t"
    "v_sub_f32 v242, 0, v232\n\t"
    "v_sub_f32 v243, 0, v233\n\t"
    "v_pk_mov_b32 v[128:129], v[242:243], v[242:243] op_sel:[0,0]\n\t"
    "v_pk_mov_b32 v[130:131], v[242:243], v[242:243] op_sel:[0,0]\n\t"
    "v_pk_mov_b32 v[132:133], v[242:243], v[242:243] op_sel:[0,0]\n\t"
    "v_pk_mov_b32 v[134:135], v[242:243], v[242:243] op_sel:[0,0]\n\t"
    "v_pk_mov_b32 v[136:137], v[242:243], v[242:243] op_sel:[0,0]\n\t"
    "v_pk_mov_b32 v[138:139], v[242:243], v[242:243] op_sel:[0,0]\n\t"
    "v_pk_mov_b32 v[140:141], v[242:243], v[242:243] op_sel:[0,0]\n\t"
    "v_pk_mov_b32 v[142:143], v[242:243], v[242:243] op_sel:[0,0]\n\t"
    "v_pk_mov_b32 v[144:145], v[242:243], v[242:243] op_sel:[1,1]\n\t"
    "v_pk_mov_b32 v[146:147], v[242:243], v[242:243] op_sel:[1,1]\n\t"
    "v_pk_mov_b32 v[148:149], v[242:243], v[242:243] op_sel:[1,1]\n\t"
    "v_pk_mov_b32 v[150:151], v[242:243], v[242:243] op_sel:[1,1]\n\t"
    "v_pk_mov_b32 v[152:153], v[242:243], v[242:243] op_sel:[1,1]\n\t"
    "v_pk_mov_b32 v[154:155], v[242:243], v[242:243] op_sel:[1,1]\n\t"
    "v_pk_mov_b32 v[156:157], v[242:243], v[242:243] op_sel:[1,1]\n\t"
    "v_pk_mov_b32 v[158:159], v[242:243], v[242:243] op_sel:[1,1]\n\t"
    "s_mov_b32 s93, 0\n\t"
    "s_mov_b32 s60, 0x41000000\n\t"
    "s_mov_b32 s61, 0\n\t"
    "s_and_b32 s67, s57, 0xffff\n\t"
    "s_or_b32 m0, s67, 0x10000\n\t"
    "ds_read_b128 a[240:243], v225 offset:12288\n\t"
    "buffer_load_dwordx4 v224, s[40:43], s57 offen offset:0 lds\n\t"
    "v_mfma_f32_32x32x16_bf16 a[0:15], v[160:163], v[160:163], 0\n\t"
    "v_exp_f32 v0, v0\n\t"
    "ds_read_b128 a[244:247], v225 offset:13312\n\t"
    "v_exp_f32 v1, v1\n\t"
    "v_exp_f32 v2, v2\n\t"
    "v_exp_f32 v3, v3\n\t"
    "ds_read_b128 a[248:251], v225 offset:14336\n\t"
    "v_mfma_f32_32x32x16_bf16 a[16:31], v[160:163], v[160:163], 0\n\t"
    "v_exp_f32 v4, v4\n\t"
    "v_exp_f32 v5, v5\n\t"
    "ds_read_b128 a[252:255], v225 offset:15360\n\t"
    "v_exp_f32 v6, v6\n\t"
    "v_exp_f32 v7, v7\n\t"
    "v_exp_f32 v16, v16\n\t"
    "v_mfma_f32_32x32x16_bf16 a[32:47], v[160:163], v[160:163], 0\n\t"
    "v_exp_f32 v17, v17\n\t"
    "v_exp_f32 v18, v18\n\t"
    "v_exp_f32 v19, v19\n\t"
    "v_exp_f32 v20, v20\n\t"
    "v_exp_f32 v21, v21\n\t"
    "buffer_load_dwordx4 v224, s[40:43], s57 offen offset:1024 lds\n\t"
    "v_mfma_f32_32x32x16_bf16 a[48:63], v[160:163], v[160:163], 0\n\t"
    "v_exp_f32 v22, v22\n\t"
    "v_exp_f32 v23, v23\n\t"
    "v_exp_f32 v8, v8\n\t"
    "v_exp_f32 v9, v9\n\t"
    "v_mfma_f32_32x32x16_bf16 a[64:79], v[160:163], v[160:163], 0\n\t"
    "v_exp_f32 v10, v10\n\t"
    "v_exp_f32 v11, v11\n\t"
    "v_exp_f32 v12, v12\n\t"
    "v_exp_f32 v13, v13\n\t"
    "v_exp_f32 v14, v14\n\t"
    "v_mfma_f32_32x32x16_bf16 a[80:95], v[160:163], v[160:163], 0\n\t"
    "v_exp_f32 v15, v15\n\t"
    "v_exp_f32 v24, v24\n\t"
    "v_exp_f32 v25, v25\n\t"
    "v_exp_f32 v26, v26\n\t"
    "v_exp_f32 v27, v27\n\t"
    "buffer_load_dwordx4 v224, s[40:43], s57 offen offset:2048 lds\n\t"
    "v_mfma_f32_32x32x16_bf16 a[96:111], v[160:163], v[160:163], 0\n\t"
    "v_add_f32 v228, v228, v0\n\t"
    "v_add_f32 v229, v229, v1\n\t"
    "v_add_f32 v228, v228, v2\n\t"
    "v_add_f32 v229, v229, v3\n\t"
    "v_add_f32 v228, v228, v4\n\t"
    "v_add_f32 v229, v229, v5\n\t"
    "v_add_f32 v228, v228, v6\n\t"
    "v_add_f32 v229, v229, v7\n\t"
    "v_mfma_f32_32x32x16_bf16 a[112:127], v[160:163], v[160:163], 0\n\t"
    "v_add_f32 v230, v230, v16\n\t"
    "v_add_f32 v231, v231, v17\n\t"
    "v_add_f32 v230, v230, v18\n\t"
    "v_add_f32 v231, v231, v19\n\t"
    "v_add_f32 v230, v230, v20\n\t"
    "v_add_f32 v231, v231, v21\n\t"
    "v_add_f32 v230, v230, v22\n\t"
    "v_add_f32 v231, v231, v23\n\t"
    "v_add_f32 v228, v228, v8\n\t"
    "v_add_f32 v229, v229, v9\n\t"
    "v_add_f32 v228, v228, v10\n\t"
    "v_add_f32 v229, v229, v11\n\t"
    "v_add_f32 v228, v228, v12\n\t"
    "v_add_f32 v229, v229, v13\n\t"
    "v_add_f32 v228, v228, v14\n\t"
    "v_add_f32 v229, v229, v15\n\t"
    "v_add_f32 v230, v230, v24\n\t"
    "v_add_f32 v231, v231, v25\n\t"
    "v_add_f32 v230, v230, v26\n\t"
    "v_add_f32 v231, v231, v27\n\t"
    "buffer_load_dwordx4 v224, s[40:43], s57 offen offset:3072 lds\n\t"
    "s_xor_b32 s66, s58, 0x4000\n\t"
    "s_and_b32 s66, s66, 0xffff\n\t"
    "v_add_u32 v240, s66, v227\n\t"
    "ds_write_b64 v240, v[232:233] offset:0\n\t"
    "s_mov_b32 s56, s57\n\t"
    "s_mov_b32 s57, s58\n\t"
    "s_cmp_eq_u32 s53, 0\n\t"
    "s_cselect_b32 s66, 0x8000, s59\n\t"
    "s_add_u32 s58, s58, s66\n\t"
    "s_cmp_eq_u32 s53, 0\n\t"
    "s_cbranch_scc1 Lwa_66_%=\n\t"
    "s_waitcnt vmcnt(9)\n\t"
    "Lwa_66_%=:\n\t"
    "s_waitcnt vmcnt(13) lgkmcnt(0)\n\t"
    "s_barrier\n\t"
    "s_add_u32 s52, s52, 1\n\t"
    "s_cmp_eq_u32 s53, 0\n\t"
    "s_cbranch_scc1 L2P_%=\n\t"
    "L1_%=:\n\t"
    "v_mfma_f32_32x32x16_bf16 v[64:79], a[192:195], a[128:131], v[128:143]\n\t"
    "ds_read_b128 v[160:163], v226 offset:0\n\t"
    "s_and_b32 m0, s58, 0xffff\n\t"
    "v_exp_f32 v28, v28\n\t"
    "v_exp_f32 v29, v29\n\t"
    "v_mfma_f32_32x32x16_bf16 v[80:95], a[192:195], a[160:163], v[144:159]\n\t"
    "ds_read_b128 v[164:167], v226 offset:2048\n\t"
    "buffer_load_dwordx4 v224, s[36:39], s58 offen offset:0 lds\n\t"
    "v_exp_f32 v30, v30\n\t"
    "v_add_f32 v230, v230, v28\n\t"
    "v_mfma_f32_32x32x16_bf16 v[96:111], a[224:227], a[128:131], v[128:143]\n\t"
    "ds_read_b128 v[168:171], v226 offset:4096\n\t"
    "v_exp_f32 v31, v31\n\t"
    "v_exp_f32 v32, v32\n\t"
    "v_add_f32 v231, v231, v29\n\t"
    "v_mfma_f32_32x32x16_bf16 v[112:127], a[224:227], a[160:163], v[144:159]\n\t"
    "ds_read_b128 v[172:175], v226 offset:6144\n\t"
    "v_exp_f32 v33, v33\n\t"
    "v_exp_f32 v34, v34\n\t"
    "v_add_f32 v230, v230, v30\n\t"
    "v_mfma_f32_32x32x16_bf16 v[64:79], a[196:199], a[132:135], v[64:79]\n\t"
    "ds_read_b128 v[176:179], v226 offset:1024\n\t"
    "v_exp_f32 v35, v35\n\t"
    "v_exp_f32 v36, v36\n\t"
    "v_add_f32 v231, v231, v31\n\t"
    "v_mfma_f32_32x32x16_bf16 v[80:95], a[196:199], a[164:167], v[80:95]\n\t"
    "ds_read_b128 v[180:183], v226 offset:3072\n\t"
    "v_exp_f32 v37, v37\n\t"
    "v_exp_f32 v38, v38\n\t"
    "v_add_f32 v228, v228, v32\n\t"
    "v_mfma_f32_32x32x16_bf16 v[96:111], a[228:231], a[132:135], v[96:111]\n\t"
    "ds_read_b128 v[184:187], v226 offset:5120\n\t"
    "v_exp_f32 v39, v39\n\t"
    "v_exp_f32 v48, v48\n\t"
    "v_add_f32 v229, v229, v33\n\t"
    "v_mfma_f32_32x32x16_bf16 v[112:127], a[228:231], a[164:167], v[112:127]\n\t"
    "ds_read_b128 v[188:191], v226 offset:7168\n\t"
    "v_exp_f32 v49, v49\n\t"
    "v_exp_f32 v50, v50\n\t"
    "v_add_f32 v228, v228, v34\n\t"
    "v_mfma_f32_32x32x16_bf16 v[64:79], a[200:203], a[136:139], v[64:79]\n\t"
    "ds_read_b128 v[192:195], v226 offset:8192\n\t"
    "v_exp_f32 v51, v51\n\t"
    "v_exp_f32 v52, v52\n\t"
    "v_add_f32 v229, v229, v35\n\t"
    "v_mfma_f32_32x32x16_bf16 v[80:95], a[200:203], a[168:171], v[80:95]\n\t"
    "ds_read_b128 v[196:199], v226 offset:10240\n\t"
    "buffer_load_dwordx4 v224, s[36:39], s58 offen offset:1024 lds\n\t"
    "v_exp_f32 v53, v53\n\t"
    "v_add_f32 v228, v228, v36\n\t"
    "v_mfma_f32_32x32x16_bf16 v[96:111], a[232:235], a[136:139], v[96:111]\n\t"
    "ds_read_b128 v[200:203], v226 offset:12288\n\t"
    "v_exp_f32 v54, v54\n\t"
    "v_exp_f32 v55, v55\n\t"
    "v_add_f32 v229, v229, v37\n\t"
    "v_mfma_f32_32x32x16_bf16 v[112:127], a[232:235], a[168:171], v[112:127]\n\t"
    "ds_read_b128 v[204:207], v226 offset:14336\n\t"
    "v_exp_f32 v40, v40\n\t"
    "v_exp_f32 v41, v41\n\t"
    "v_add_f32 v228, v228, v38\n\t"
    "v_mfma_f32_32x32x16_bf16 v[64:79], a[204:207], a[140:143], v[64:79]\n\t"
    "ds_read_b128 v[208:211], v226 offset:9216\n\t"
    "v_exp_f32 v42, v42\n\t"
    "v_exp_f32 v43, v43\n\t"
    "v_add_f32 v229, v229, v39\n\t"
    "v_mfma_f32_32x32x16_bf16 v[80:95], a[204:207], a[172:175], v[80:95]\n\t"
    "ds_read_b128 v[212:215], v226 offset:11264\n\t"
    "v_exp_f32 v44, v44\n\t"
    "v_exp_f32 v45, v45\n\t"
    "v_add_f32 v230, v230, v48\n\t"
    "v_mfma_f32_32x32x16_bf16 v[96:111], a[236:239], a[140:143], v[96:111]\n\t"
    "ds_read_b128 v[216:219], v226 offset:13312\n\t"
    "v_exp_f32 v46, v46\n\t"
    "v_exp_f32 v47, v47\n\t"
    "v_add_f32 v231, v231, v49\n\t"
    "v_mfma_f32_32x32x16_bf16 v[112:127], a[236:239], a[172:175], v[112:127]\n\t"
    "ds_read_b128 v[220:223], v226 offset:15360\n\t"
    "v_exp_f32 v56, v56\n\t"
    "v_exp_f32 v57, v57\n\t"
    "v_add_f32 v230, v230, v50\n\t"
    "v_mfma_f32_32x32x16_bf16 v[64:79], a[208:211], a[144:147], v[64:79]\n\t"
    "v_exp_f32 v58, v58\n\t"
    "v_exp_f32 v59, v59\n\t"
    "v_add_f32 v231, v231, v51\n\t"
    "v_add_f32 v230, v230, v52\n\t"
    "v_mfma_f32_32x32x16_bf16 v[80:95], a[208:211], a[176:179], v[80:95]\n\t"
    "buffer_load_dwordx4 v224, s[36:39], s58 offen offset:2048 lds\n\t"
    "v_exp_f32 v60, v60\n\t"
    "v_exp_f32 v61, v61\n\t"
    "v_mfma_f32_32x32x16_bf16 v[96:111], a[240:243], a[144:147], v[96:111]\n\t"
    "v_exp_f32 v62, v62\n\t"
    "v_exp_f32 v63, v63\n\t"
    "v_add_f32 v231, v231, v53\n\t"
    "v_add_f32 v230, v230, v54\n\t"
    "v_mfma_f32_32x32x16_bf16 v[112:127], a[240:243], a[176:179], v[112:127]\n\t"
    "v_add_f32 v231, v231, v55\n\t"
    "v_add_f32 v228, v228, v40\n\t"
    "v_add_f32 v229, v229, v41\n\t"
    "v_add_f32 v228, v228, v42\n\t"
    "v_add_f32 v229, v229, v43\n\t"
    "v_add_f32 v228, v228, v44\n\t"
    "v_mfma_f32_32x32x16_bf16 v[64:79], a[212:215], a[148:151], v[64:79]\n\t"
    "s_and_b32 s66, s57, 0xc000\n\t"
    "v_add_f32 v229, v229, v45\n\t"
    "v_add_f32 v228, v228, v46\n\t"
    "v_add_f32 v229, v229, v47\n\t"
    "v_add_f32 v230, v230, v56\n\t"
    "v_add_f32 v231, v231, v57\n\t"
    "v_mfma_f32_32x32x16_bf16 v[80:95], a[212:215], a[180:183], v[80:95]\n\t"
    "v_add_u32 v225, s66, v224\n\t"
    "v_add_f32 v230, v230, v58\n\t"
    "v_add_f32 v231, v231, v59\n\t"
    "v_add_f32 v230, v230, v60\n\t"
    "v_add_f32 v231, v231, v61\n\t"
    "v_add_f32 v230, v230, v62\n\t"
    "v_mfma_f32_32x32x16_bf16 v[96:111], a[244:247], a[148:151], v[96:111]\n\t"
    "v_add_f32 v231, v231, v63\n\t"
    "v_cvt_pk_bf16_f32 v0, v0, v1\n\t"
    "v_cvt_pk_bf16_f32 v1, v2, v3\n\t"
    "v_cvt_pk_bf16_f32 v2, v4, v5\n\t"
    "v_cvt_pk_bf16_f32 v3, v6, v7\n\t"
    "v_cvt_pk_bf16_f32 v16, v16, v17\n\t"
    "v_mfma_f32_32x32x16_bf16 v[112:127], a[244:247], a[180:183], v[112:127]\n\t"
    "v_cvt_pk_bf16_f32 v17, v18, v19\n\t"
    "v_cvt_pk_bf16_f32 v18, v20, v21\n\t"
    "v_cvt_pk_bf16_f32 v19, v22, v23\n\t"
    "v_cvt_pk_bf16_f32 v8, v8, v9\n\t"
    "v_cvt_pk_bf16_f32 v9, v10, v11\n\t"
    "v_cvt_pk_bf16_f32 v10, v12, v13\n\t"
    "v_mfma_f32_32x32x16_bf16 v[64:79], a[216:219], a[152:155], v[64:79]\n\t"
    "v_cvt_pk_bf16_f32 v11, v14, v15\n\t"
    "v_cvt_pk_bf16_f32 v24, v24, v25\n\t"
    "v_cvt_pk_bf16_f32 v25, v26, v27\n\t"
    "v_cvt_pk_bf16_f32 v26, v28, v29\n\t"
    "v_cvt_pk_bf16_f32 v27, v30, v31\n\t"
    "v_cvt_pk_bf16_f32 v32, v32, v33\n\t"
    "v_mfma_f32_32x32x16_bf16 v[80:95], a[216:219], a[184:187], v[80:95]\n\t"
    "buffer_load_dwordx4 v224, s[36:39], s58 offen offset:3072 lds\n\t"
    "v_cvt_pk_bf16_f32 v33, v34, v35\n\t"
    "v_cvt_pk_bf16_f32 v34, v36, v37\n\t"
    "v_cvt_pk_bf16_f32 v35, v38, v39\n\t"
    "v_cvt_pk_bf16_f32 v48, v48, v49\n\t"
    "v_mfma_f32_32x32x16_bf16 v[96:111], a[248:251], a[152:155], v[96:111]\n\t"
    "v_cvt_pk_bf16_f32 v49, v50, v51\n\t"
    "v_cvt_pk_bf16_f32 v50, v52, v53\n\t"
    "v_cvt_pk_bf16_f32 v51, v54, v55\n\t"
    "v_cvt_pk_bf16_f32 v40, v40, v41\n\t"
    "v_cvt_pk_bf16_f32 v41, v42, v43\n\t"
    "v_cvt_pk_bf16_f32 v42, v44, v45\n\t"
    "v_mfma_f32_32x32x16_bf16 v[112:127], a[248:251], a[184:187], v[112:127]\n\t"
    "v_cvt_pk_bf16_f32 v43, v46, v47\n\t"
    "v_cvt_pk_bf16_f32 v56, v56, v57\n\t"
    "v_cvt_pk_bf16_f32 v57, v58, v59\n\t"
    "v_cvt_pk_bf16_f32 v58, v60, v61\n\t"
    "v_cvt_pk_bf16_f32 v59, v62, v63\n\t"
    "v_mfma_f32_32x32x16_bf16 v[64:79], a[220:223], a[156:159], v[64:79]\n\t"
    "v_mfma_f32_32x32x16_bf16 v[80:95], a[220:223], a[188:191], v[80:95]\n\t"
    "v_mfma_f32_32x32x16_bf16 v[96:111], a[252:255], a[156:159], v[96:111]\n\t"
    "v_mfma_f32_32x32x16_bf16 v[112:127], a[252:255], a[188:191], v[112:127]\n\t"
    "s_waitcnt vmcnt(8) lgkmcnt(0)\n\t"
    "s_barrier\n\t"
    "v_mfma_f32_32x32x16_bf16 a[0:15], v[160:163], v[0:3], a[0:15]\n\t"
    "ds_read_b128 a[192:195], v225 offset:0\n\t"
    "ds_read_b128 a[196:199], v225 offset:1024\n\t"
    "ds_read_b128 a[200:203], v225 offset:2048\n\t"
    "ds_read_b128 a[204:207], v225 offset:3072\n\t"
    "s_and_b32 s69, s56, 0xc000\n\t"
    "v_add_u32 v226, s69, v227\n\t"
    "v_mfma_f32_32x32x16_bf16 a[16:31], v[160:163], v[16:19], a[16:31]\n\t"
    "v_max3_f32 v234, v64, v65, v66\n\t"
    "v_max3_f32 v235, v80, v81, v82\n\t"
    "v_max3_f32 v236, v96, v97, v98\n\t"
    "v_max3_f32 v237, v112, v113, v114\n\t"
    "ds_read_b128 a[208:211], v225 offset:4096\n\t"
    "v_mfma_f32_32x32x16_bf16 a[32:47], v[164:167], v[0:3], a[32:47]\n\t"
    "v_max3_f32 v234, v234, v67, v68\n\t"
    "v_max3_f32 v235, v235, v83, v84\n\t"
    "v_max3_f32 v236, v236, v99, v100\n\t"
    "v_max3_f32 v237, v237, v115, v116\n\t"
    "ds_read_b128 a[212:215], v225 offset:5120\n\t"
    "v_mfma_f32_32x32x16_bf16 a[48:63], v[164:167], v[16:19], a[48:63]\n\t"
    "v_max3_f32 v234, v234, v69, v70\n\t"
    "v_max3_f32 v235, v235, v85, v86\n\t"
    "v_max3_f32 v236, v236, v101, v102\n\t"
    "v_max3_f32 v237, v237, v117, v118\n\t"
    "ds_read_b128 a[216:219], v225 offset:6144\n\t"
    "v_mfma_f32_32x32x16_bf16 a[64:79], v[168:171], v[0:3], a[64:79]\n\t"
    "v_max3_f32 v234, v234, v71, v72\n\t"
    "v_max3_f32 v235, v235, v87, v88\n\t"
    "v_max3_f32 v236, v236, v103, v104\n\t"
    "v_max3_f32 v237, v237, v119, v120\n\t"
    "ds_read_b128 a[220:223], v225 offset:7168\n\t"
    "v_mfma_f32_32x32x16_bf16 a[80:95], v[168:171], v[16:19], a[80:95]\n\t"
    "v_max3_f32 v234, v234, v73, v74\n\t"
    "v_max3_f32 v235, v235, v89, v90\n\t"
    "v_max3_f32 v236, v236, v105, v106\n\t"
    "v_max3_f32 v237, v237, v121, v122\n\t"
    "ds_read_b128 a[224:227], v225 offset:8192\n\t"
    "v_mfma_f32_32x32x16_bf16 a[96:111], v[172:175], v[0:3], a[96:111]\n\t"
    "v_max3_f32 v234, v234, v75, v76\n\t"
    "v_max3_f32 v235, v235, v91, v92\n\t"
    "v_max3_f32 v236, v236, v107, v108\n\t"
    "v_max3_f32 v237, v237, v123, v124\n\t"
    "ds_read_b128 a[228:231], v225 offset:9216\n\t"
    "v_mfma_f32_32x32x16_bf16 a[112:127], v[172:175], v[16:19], a[112:127]\n\t"
    "v_max3_f32 v234, v234, v77, v78\n\t"
    "v_max3_f32 v235, v235, v93, v94\n\t"
    "v_max3_f32 v236, v236, v109, v110\n\t"
    "v_max3_f32 v237, v237, v125, v126\n\t"
    "ds_read_b128 a[232:235], v225 offset:10240\n\t"
    "v_mfma_f32_32x32x16_bf16 a[0:15], v[176:179], v[8:11], a[0:15]\n\t"
    "v_max_f32 v234, v234, v79\n\t"
    "v_max_f32 v235, v235, v95\n\t"
    "v_max_f32 v236, v236, v111\n\t"
    "v_max_f32 v237, v237, v127\n\t"
    "ds_read_b128 a[236:239], v225 offset:11264\n\t"
    "v_mfma_f32_32x32x16_bf16 a[16:31], v[176:179], v[24:27], a[16:31]\n\t"
    "v_max3_f32 v238, v234, v235, v236\n\t"
    "v_max_f32 v238, v238, v237\n\t"
    "v_cmp_lt_f32 vcc, s60, v238\n\t"
    "s_cbranch_vccnz Lst_67_%=\n\t"
    "Lbk_68_%=:\n\t"
    "s_and_b32 s67, s57, 0xffff\n\t"
    "s_or_b32 m0, s67, 0x10000\n\t"
    "v_mfma_f32_32x32x16_bf16 a[32:47], v[180:183], v[8:11], a[32:47]\n\t"
    "ds_read_b128 a[240:243], v225 offset:12288\n\t"
    "buffer_load_dwordx4 v224, s[40:43], s57 offen offset:0 lds\n\t"
    "v_exp_f32 v64, v64\n\t"
    "v_mfma_f32_32x32x16_bf16 a[48:63], v[180:183], v[24:27], a[48:63]\n\t"
    "ds_read_b128 a[244:247], v225 offset:13312\n\t"
    "global_store_dwordx4 v224, v[244:247], s[98:99] offset:0 sc1\n\t"
    "v_exp_f32 v65, v65\n\t"
    "v_add_f32 v228, v228, v64\n\t"
    "v_mfma_f32_32x32x16_bf16 a[64:79], v[184:187], v[8:11], a[64:79]\n\t"
    "ds_read_b128 a[248:251], v225 offset:14336\n\t"
    "v_exp_f32 v66, v66\n\t"
    "v_exp_f32 v67, v67\n\t"
    "v_add_f32 v229, v229, v65\n\t"
    "v_mfma_f32_32x32x16_bf16 a[80:95], v[184:187], v[24:27], a[80:95]\n\t"
    "ds_read_b128 a[252:255], v225 offset:15360\n\t"
    "v_exp_f32 v68, v68\n\t"
    "v_exp_f32 v69, v69\n\t"
    "v_add_f32 v228, v228, v66\n\t"
    "v_mfma_f32_32x32x16_bf16 a[96:111], v[188:191], v[8:11], a[96:111]\n\t"
    "v_exp_f32 v70, v70\n\t"
    "v_exp_f32 v71, v71\n\t"
    "v_add_f32 v229, v229, v67\n\t"
    "v_add_f32 v228, v228, v68\n\t"
    "v_mfma_f32_32x32x16_bf16 a[112:127], v[188:191], v[24:27], a[112:127]\n\t"
    "v_exp_f32 v80, v80\n\t"
    "v_exp_f32 v81, v81\n\t"
    "v_add_f32 v229, v229, v69\n\t"
    "v_add_f32 v228, v228, v70\n\t"
    "v_mfma_f32_32x32x16_bf16 a[0:15], v[192:195], v[32:35], a[0:15]\n\t"
    "buffer_load_dwordx4 v224, s[40:43], s57 offen offset:1024 lds\n\t"
    "v_exp_f32 v82, v82\n\t"
    "v_exp_f32 v83, v83\n\t"
    "v_mfma_f32_32x32x16_bf16 a[16:31], v[192:195], v[48:51], a[16:31]\n\t"
    "global_store_dwordx4 v224, v[244:247], s[98:99] offset:1024 sc1\n\t"
    "v_exp_f32 v84, v84\n\t"
    "v_exp_f32 v85, v85\n\t"
    "v_mfma_f32_32x32x16_bf16 a[32:47], v[196:199], v[32:35], a[32:47]\n\t"
    "v_exp_f32 v86, v86\n\t"
    "v_exp_f32 v87, v87\n\t"
    "v_add_f32 v229, v229, v71\n\t"
    "v_add_f32 v230, v230, v80\n\t"
    "v_mfma_f32_32x32x16_bf16 a[48:63], v[196:199], v[48:51], a[48:63]\n\t"
    "v_exp_f32 v72, v72\n\t"
    "v_exp_f32 v73, v73\n\t"
    "v_add_f32 v231, v231, v81\n\t"
    "v_add_f32 v230, v230, v82\n\t"
    "v_mfma_f32_32x32x16_bf16 a[64:79], v[200:203], v[32:35], a[64:79]\n\t"
    "v_exp_f32 v74, v74\n\t"
    "v_exp_f32 v75, v75\n\t"
    "v_add_f32 v231, v231, v83\n\t"
    "v_add_f32 v230, v230, v84\n\t"
    "v_mfma_f32_32x32x16_bf16 a[80:95], v[200:203], v[48:51], a[80:95]\n\t"
    "v_exp_f32 v76, v76\n\t"
    "v_exp_f32 v77, v77\n\t"
    "v_add_f32 v231, v231, v85\n\t"
    "v_add_f32 v230, v230, v86\n\t"
    "v_mfma_f32_32x32x16_bf16 a[96:111], v[204:207], v[32:35], a[96:111]\n\t"
    "buffer_load_dwordx4 v224, s[40:43], s57 offen offset:2048 lds\n\t"
    "v_exp_f32 v78, v78\n\t"
    "v_exp_f32 v79, v79\n\t"
    "v_mfma_f32_32x32x16_bf16 a[112:127], v[204:207], v[48:51], a[112:127]\n\t"
    "global_store_dwordx4 v224, v[244:247], s[98:99] offset:2048 sc1\n\t"
    "v_exp_f32 v88, v88\n\t"
    "v_exp_f32 v89, v89\n\t"
    "v_mfma_f32_32x32x16_bf16 a[0:15], v[208:211], v[40:43], a[0:15]\n\t"
    "v_exp_f32 v90, v90\n\t"
    "v_exp_f32 v91, v91\n\t"
    "v_add_f32 v231, v231, v87\n\t"
    "v_add_f32 v228, v228, v72\n\t"
    "v_mfma_f32_32x32x16_bf16 a[16:31], v[208:211], v[56:59], a[16:31]\n\t"
    "v_add_f32 v229, v229, v73\n\t"
    "v_add_f32 v228, v228, v74\n\t"
    "v_add_f32 v229, v229, v75\n\t"
    "v_add_f32 v228, v228, v76\n\t"
    "v_add_f32 v229, v229, v77\n\t"
    "v_add_f32 v228, v228, v78\n\t"
    "v_mfma_f32_32x32x16_bf16 a[32:47], v[212:215], v[40:43], a[32:47]\n\t"
    "v_add_f32 v229, v229, v79\n\t"
    "v_add_f32 v230, v230, v88\n\t"
    "v_add_f32 v231, v231, v89\n\t"
    "v_add_f32 v230, v230, v90\n\t"
    "v_add_f32 v231, v231, v91\n\t"
    "v_mfma_f32_32x32x16_bf16 a[48:63], v[212:215], v[56:59], a[48:63]\n\t"
    "v_mfma_f32_32x32x16_bf16 a[64:79], v[216:219], v[40:43], a[64:79]\n\t"
    "buffer_load_dwordx4 v224, s[40:43], s57 offen offset:3072 lds\n\t"
    "v_mfma_f32_32x32x16_bf16 a[80:95], v[216:219], v[56:59], a[80:95]\n\t"
    "global_store_dwordx4 v224, v[244:247], s[98:99] offset:3072 sc1\n\t"
    "v_mfma_f32_32x32x16_bf16 a[96:111], v[220:223], v[40:43], a[96:111]\n\t"
    "s_add_u32 s98, s98, 0x1000\n\t"
    "s_addc_u32 s99, s99, 0\n\t"
    "v_mfma_f32_32x32x16_bf16 a[112:127], v[220:223], v[56:59], a[112:127]\n\t"
    "global_load_dword v253, v255, %12\n\t"
    "v_add_u32 v255, 0x800, v255\n\t"
    "s_mov_b32 s56, s57\n\t"
    "s_mov_b32 s57, s58\n\t"
    "s_cmp_eq_u32 s52, s54\n\t"
    "s_cselect_b32 s66, s55, 0x4000\n\t"
    "s_add_u32 s58, s58, s66\n\t"
    "s_cmp_lg_u32 s62, 0\n\t"
    "s_cbranch_scc1 Lst_69_%=\n\t"
    "Lbk_70_%=:\n\t"
    "s_waitcnt vmcnt(13) lgkmcnt(0)\n\t"
    "s_barrier\n\t"
    "s_add_u32 s52, s52, 1\n\t"
    "s_cmp_eq_u32 s52, s53\n\t"
    "s_cbranch_scc1 LB21_%=\n\t"
    "v_mfma_f32_32x32x16_bf16 v[0:15], a[192:195], a[128:131], v[128:143]\n\t"
    "ds_read_b128 v[160:163], v226 offset:0\n\t"
    "s_and_b32 m0, s58, 0xffff\n\t"
    "v_exp_f32 v92, v92\n\t"
    "v_exp_f32 v93, v93\n\t"
    "v_mfma_f32_32x32x16_bf16 v[16:31], a[192:195], a[160:163], v[144:159]\n\t"
    "ds_read_b128 v[164:167], v226 offset:2048\n\t"
    "buffer_load_dwordx4 v224, s[36:39], s58 offen offset:0 lds\n\t"
    "v_exp_f32 v94, v94\n\t"
    "v_add_f32 v230, v230, v92\n\t"
    "v_mfma_f32_32x32x16_bf16 v[32:47], a[224:227], a[128:131], v[128:143]\n\t"
    "ds_read_b128 v[168:171], v226 offset:4096\n\t"
    "v_exp_f32 v95, v95\n\t"
    "v_exp_f32 v96, v96\n\t"
    "v_add_f32 v231, v231, v93\n\t"
    "v_mfma_f32_32x32x16_bf16 v[48:63], a[224:227], a[160:163], v[144:159]\n\t"
    "ds_read_b128 v[172:175], v226 offset:6144\n\t"
    "v_exp_f32 v97, v97\n\t"
    "v_exp_f32 v98, v98\n\t"
    "v_add_f32 v230, v230, v94\n\t"
    "v_mfma_f32_32x32x16_bf16 v[0:15], a[196:199], a[132:135], v[0:15]\n\t"
    "ds_read_b128 v[176:179], v226 offset:1024\n\t"
    "v_exp_f32 v99, v99\n\t"
    "v_exp_f32 v100, v100\n\t"
    "v_add_f32 v231, v231, v95\n\t"
    "v_mfma_f32_32x32x16_bf16 v[16:31], a[196:199], a[164:167], v[16:31]\n\t"
    "ds_read_b128 v[180:183], v226 offset:3072\n\t"
    "v_exp_f32 v101, v101\n\t"
    "v_exp_f32 v102, v102\n\t"
    "v_add_f32 v228, v228, v96\n\t"
    "v_mfma_f32_32x32x16_bf16 v[32:47], a[228:231], a[132:135], v[32:47]\n\t"
    "ds_read_b128 v[184:187], v226 offset:5120\n\t"
    "v_exp_f32 v103, v103\n\t"
    "v_exp_f32 v112, v112\n\t"
    "v_add_f32 v229, v229, v97\n\t"
    "v_mfma_f32_32x32x16_bf16 v[48:63], a[228:231], a[164:167], v[48:63]\n\t"
    "ds_read_b128 v[188:191], v226 offset:7168\n\t"
    "v_exp_f32 v113, v113\n\t"
    "v_exp_f32 v114, v114\n\t"
    "v_add_f32 v228, v228, v98\n\t"
    "v_mfma_f32_32x32x16_bf16 v[0:15], a[200:203], a[136:139], v[0:15]\n\t"
    "ds_read_b128 v[192:195], v226 offset:8192\n\t"
    "v_exp_f32 v115, v115\n\t"
    "v_exp_f32 v116, v116\n\t"
    "v_add_f32 v229, v229, v99\n\t"
    "v_mfma_f32_32x32x16_bf16 v[16:31], a[200:203], a[168:171], v[16:31]\n\t"
    "ds_read_b128 v[196:199], v226 offset:10240\n\t"
    "buffer_load_dwordx4 v224, s[36:39], s58 offen offset:1024 lds\n\t"
    "v_exp_f32 v117, v117\n\t"
    "v_add_f32 v228, v228, v100\n\t"
    "v_mfma_f32_32x32x16_bf16 v[32:47], a[232:235], a[136:139], v[32:47]\n\t"
    "ds_read_b128 v[200:203], v226 offset:12288\n\t"
    "v_exp_f32 v118, v118\n\t"
    "v_exp_f32 v119, v119\n\t"
    "v_add_f32 v229, v229, v101\n\t"
    "v_mfma_f32_32x32x16_bf16 v[48:63], a[232:235], a[168:171], v[48:63]\n\t"
    "ds_read_b128 v[204:207], v226 offset:14336\n\t"
    "v_exp_f32 v104, v104\n\t"
    "v_exp_f32 v105, v105\n\t"
    "v_add_f32 v228, v228, v102\n\t"
    "v_mfma_f32_32x32x16_bf16 v[0:15], a[204:207], a[140:143], v[0:15]\n\t"
    "ds_read_b128 v[208:211], v226 offset:9216\n\t"
    "v_exp_f32 v106, v106\n\t"
    "v_exp_f32 v107, v107\n\t"
    "v_add_f32 v229, v229, v103\n\t"
    "v_mfma_f32_32x32x16_bf16 v[16:31], a[204:207], a[172:175], v[16:31]\n\t"
    "ds_read_b128 v[212:215], v226 offset:11264\n\t"
    "v_exp_f32 v108, v108\n\t"
    "v_exp_f32 v109, v109\n\t"
    "v_add_f32 v230, v230, v112\n\t"
    "v_mfma_f32_32x32x16_bf16 v[32:47], a[236:239], a[140:143], v[32:47]\n\t"
    "ds_read_b128 v[216:219], v226 offset:13312\n\t"
    "v_exp_f32 v110, v110\n\t"
    "v_exp_f32 v111, v111\n\t"
    "v_add_f32 v231, v231, v113\n\t"
    "v_mfma_f32_32x32x16_bf16 v[48:63], a[236:239], a[172:175], v[48:63]\n\t"
    "ds_read_b128 v[220:223], v226 offset:15360\n\t"
    "v_exp_f32 v120, v120\n\t"
    "v_exp_f32 v121, v121\n\t"
    "v_add_f32 v230, v230, v114\n\t"
    "v_mfma_f32_32x32x16_bf16 v[0:15], a[208:211], a[144:147], v[0:15]\n\t"
    "v_exp_f32 v122, v122\n\t"
    "v_exp_f32 v123, v123\n\t"
    "v_add_f32 v231, v231, v115\n\t"
    "v_add_f32 v230, v230, v116\n\t"
    "v_mfma_f32_32x32x16_bf16 v[16:31], a[208:211], a[176:179], v[16:31]\n\t"
    "buffer_load_dwordx4 v224, s[36:39], s58 offen offset:2048 lds\n\t"
    "v_exp_f32 v124, v124\n\t"
    "v_exp_f32 v125, v125\n\t"
    "v_mfma_f32_32x32x16_bf16 v[32:47], a[240:243], a[144:147], v[32:47]\n\t"
    "v_exp_f32 v126, v126\n\t"
    "v_exp_f32 v127, v127\n\t"
    "v_add_f32 v231, v231, v117\n\t"
    "v_add_f32 v230, v230, v118\n\t"
    "v_mfma_f32_32x32x16_bf16 v[48:63], a[240:243], a[176:179], v[48:63]\n\t"
    "v_add_f32 v231, v231, v119\n\t"
    "v_add_f32 v228, v228, v104\n\t"
    "v_add_f32 v229, v229, v105\n\t"
    "v_add_f32 v228, v228, v106\n\t"
    "v_add_f32 v229, v229, v107\n\t"
    "v_add_f32 v228, v228, v108\n\t"
    "v_mfma_f32_32x32x16_bf16 v[0:15], a[212:215], a[148:151], v[0:15]\n\t"
    "s_and_b32 s66, s57, 0xc000\n\t"
    "v_add_f32 v229, v229, v109\n\t"
    "v_add_f32 v228, v228, v110\n\t"
    "v_add_f32 v229, v229, v111\n\t"
    "v_add_f32 v230, v230, v120\n\t"
    "v_add_f32 v231, v231, v121\n\t"
    "v_mfma_f32_32x32x16_bf16 v[16:31], a[212:215], a[180:183], v[16:31]\n\t"
    "v_add_u32 v225, s66, v224\n\t"
    "v_add_f32 v230, v230, v122\n\t"
    "v_add_f32 v231, v231, v123\n\t"
    "v_add_f32 v230, v230, v124\n\t"
    "v_add_f32 v231, v231, v125\n\t"
    "v_add_f32 v230, v230, v126\n\t"
    "v_mfma_f32_32x32x16_bf16 v[32:47], a[244:247], a[148:151], v[32:47]\n\t"
    "v_add_f32 v231, v231, v127\n\t"
    "v_cvt_pk_bf16_f32 v64, v64, v65\n\t"
    "v_cvt_pk_bf16_f32 v65, v66, v67\n\t"
    "v_cvt_pk_bf16_f32 v66, v68, v69\n\t"
    "v_cvt_pk_bf16_f32 v67, v70, v71\n\t"
    "v_cvt_pk_bf16_f32 v80, v80, v81\n\t"
    "v_mfma_f32_32x32x16_bf16 v[48:63], a[244:247], a[180:183], v[48:63]\n\t"
    "v_cvt_pk_bf16_f32 v81, v82, v83\n\t"
    "v_cvt_pk_bf16_f32 v82, v84, v85\n\t"
    "v_cvt_pk_bf16_f32 v83, v86, v87\n\t"
    "v_cvt_pk_bf16_f32 v72, v72, v73\n\t"
    "v_cvt_pk_bf16_f32 v73, v74, v75\n\t"
    "v_cvt_pk_bf16_f32 v74, v76, v77\n\t"
    "v_mfma_f32_32x32x16_bf16 v[0:15], a[216:219], a[152:155], v[0:15]\n\t"
    "v_cvt_pk_bf16_f32 v75, v78, v79\n\t"
    "v_cvt_pk_bf16_f32 v88, v88, v89\n\t"
    "v_cvt_pk_bf16_f32 v89, v90, v91\n\t"
    "v_cvt_pk_bf16_f32 v90, v92, v93\n\t"
    "v_cvt_pk_bf16_f32 v91, v94, v95\n\t"
    "v_cvt_pk_bf16_f32 v96, v96, v97\n\t"
    "v_mfma_f32_32x32x16_bf16 v[16:31], a[216:219], a[184:187], v[16:31]\n\t"
    "buffer_load_dwordx4 v224, s[36:39], s58 offen offset:3072 lds\n\t"
    "v_cvt_pk_bf16_f32 v97, v98, v99\n\t"
    "v_cvt_pk_bf16_f32 v98, v100, v101\n\t"
    "v_cvt_pk_bf16_f32 v99, v102, v103\n\t"
    "v_cvt_pk_bf16_f32 v112, v112, v113\n\t"
    "v_mfma_f32_32x32x16_bf16 v[32:47], a[248:251], a[152:155], v[32:47]\n\t"
    "v_cvt_pk_bf16_f32 v113, v114, v115\n\t"
    "v_cvt_pk_bf16_f32 v114, v116, v117\n\t"
    "v_cvt_pk_bf16_f32 v115, v118, v119\n\t"
    "v_cvt_pk_bf16_f32 v104, v104, v105\n\t"
    "v_cvt_pk_bf16_f32 v105, v106, v107\n\t"
    "v_cvt_pk_bf16_f32 v106, v108, v109\n\t"
    "v_mfma_f32_32x32x16_bf16 v[48:63], a[248:251], a[184:187], v[48:63]\n\t"
    "v_cvt_pk_bf16_f32 v107, v110, v111\n\t"
    "v_cvt_pk_bf16_f32 v120, v120, v121\n\t"
    "v_cvt_pk_bf16_f32 v121, v122, v123\n\t"
    "v_cvt_pk_bf16_f32 v122, v124, v125\n\t"
    "v_cvt_pk_bf16_f32 v123, v126, v127\n\t"
    "v_mfma_f32_32x32x16_bf16 v[0:15], a[220:223], a[156:159], v[0:15]\n\t"
    "v_mfma_f32_32x32x16_bf16 v[16:31], a[220:223], a[188:191], v[16:31]\n\t"
    "v_mfma_f32_32x32x16_bf16 v[32:47], a[252:255], a[156:159], v[32:47]\n\t"
    "v_mfma_f32_32x32x16_bf16 v[48:63], a[252:255], a[188:191], v[48:63]\n\t"
    "s_waitcnt vmcnt(13) lgkmcnt(0)\n\t"
    "s_barrier\n\t"
    "v_mfma_f32_32x32x16_bf16 a[0:15], v[160:163], v[64:67], a[0:15]\n\t"
    "ds_read_b128 a[192:195], v225 offset:0\n\t"
    "ds_read_b128 a[196:199], v225 offset:1024\n\t"
    "ds_read_b128 a[200:203], v225 offset:2048\n\t"
    "ds_read_b128 a[204:207], v225 offset:3072\n\t"
    "s_and_b32 s69, s56, 0xc000\n\t"
    "v_add_u32 v226, s69, v227\n\t"
    "v_mfma_f32_32x32x16_bf16 a[16:31], v[160:163], v[80:83], a[16:31]\n\t"
    "v_max3_f32 v234, v0, v1, v2\n\t"
    "v_max3_f32 v235, v16, v17, v18\n\t"
    "v_max3_f32 v236, v32, v33, v34\n\t"
    "v_max3_f32 v237, v48, v49, v50\n\t"
    "ds_read_b128 a[208:211], v225 offset:4096\n\t"
    "v_mfma_f32_32x32x16_bf16 a[32:47], v[164:167], v[64:67], a[32:47]\n\t"
    "v_max3_f32 v234, v234, v3, v4\n\t"
    "v_max3_f32 v235, v235, v19, v20\n\t"
    "v_max3_f32 v236, v236, v35, v36\n\t"
    "v_max3_f32 v237, v237, v51, v52\n\t"
    "ds_read_b128 a[212:215], v225 offset:5120\n\t"
    "v_mfma_f32_32x32x16_bf16 a[48:63], v[164:167], v[80:83], a[48:63]\n\t"
    "v_max3_f32 v234, v234, v5, v6\n\t"
    "v_max3_f32 v235, v235, v21, v22\n\t"
    "v_max3_f32 v236, v236, v37, v38\n\t"
    "v_max3_f32 v237, v237, v53, v54\n\t"
    "ds_read_b128 a[216:219], v225 offset:6144\n\t"
    "v_mfma_f32_32x32x16_bf16 a[64:79], v[168:171], v[64:67], a[64:79]\n\t"
    "v_max3_f32 v234, v234, v7, v8\n\t"
    "v_max3_f32 v235, v235, v23, v24\n\t"
    "v_max3_f32 v236, v236, v39, v40\n\t"
    "v_max3_f32 v237, v237, v55, v56\n\t"
    "ds_read_b128 a[220:223], v225 offset:7168\n\t"
    "v_mfma_f32_32x32x16_bf16 a[80:95], v[168:171], v[80:83], a[80:95]\n\t"
    "v_max3_f32 v234, v234, v9, v10\n\t"
    "v_max3_f32 v235, v235, v25, v26\n\t"
    "v_max3_f32 v236, v236, v41, v42\n\t"
    "v_max3_f32 v237, v237, v57, v58\n\t"
    "ds_read_b128 a[224:227], v225 offset:8192\n\t"
    "v_mfma_f32_32x32x16_bf16 a[96:111], v[172:175], v[64:67], a[96:111]\n\t"
    "v_max3_f32 v234, v234, v11, v12\n\t"
    "v_max3_f32 v235, v235, v27, v28\n\t"
    "v_max3_f32 v236, v236, v43, v44\n\t"
    "v_max3_f32 v237, v237, v59, v60\n\t"
    "ds_read_b128 a[228:231], v225 offset:9216\n\t"
    "v_mfma_f32_32x32x16_bf16 a[112:127], v[172:175], v[80:83], a[112:127]\n\t"
    "v_max3_f32 v234, v234, v13, v14\n\t"
    "v_max3_f32 v235, v235, v29, v30\n\t"
    "v_max3_f32 v236, v236, v45, v46\n\t"
    "v_max3_f32 v237, v237, v61, v62\n\t"
    "ds_read_b128 a[232:235], v225 offset:10240\n\t"
    "v_mfma_f32_32x32x16_bf16 a[0:15], v[176:179], v[72:75], a[0:15]\n\t"
    "v_max_f32 v234, v234, v15\n\t"
    "v_max_f32 v235, v235, v31\n\t"
    "v_max_f32 v236, v236, v47\n\t"
    "v_max_f32 v237, v237, v63\n\t"
    "ds_read_b128 a[236:239], v225 offset:11264\n\t"
    "v_mfma_f32_32x32x16_bf16 a[16:31], v[176:179], v[88:91], a[16:31]\n\t"
    "v_max3_f32 v238, v234, v235, v236\n\t"
    "v_max_f32 v238, v238, v237\n\t"
    "v_cmp_lt_f32 vcc, s60, v238\n\t"
    "s_cbranch_vccnz Lst_71_%=\n\t"
    "Lbk_72_%=:\n\t"
    "s_and_b32 s67, s57, 0xffff\n\t"
    "s_or_b32 m0, s67, 0x10000\n\t"
    "v_mfma_f32_32x32x16_bf16 a[32:47], v[180:183], v[72:75], a[32:47]\n\t"
    "ds_read_b128 a[240:243], v225 offset:12288\n\t"
    "buffer_load_dwordx4 v224, s[40:43], s57 offen offset:0 lds\n\t"
    "v_exp_f32 v0, v0\n\t"
    "v_mfma_f32_32x32x16_bf16 a[48:63], v[180:183], v[88:91], a[48:63]\n\t"
    "ds_read_b128 a[244:247], v225 offset:13312\n\t"
    "v_exp_f32 v1, v1\n\t"
    "v_exp_f32 v2, v2\n\t"
    "v_add_f32 v228, v228, v0\n\t"
    "v_mfma_f32_32x32x16_bf16 a[64:79], v[184:187], v[72:75], a[64:79]\n\t"
    "ds_read_b128 a[248:251], v225 offset:14336\n\t"
    "v_exp_f32 v3, v3\n\t"
    "v_exp_f32 v4, v4\n\t"
    "v_add_f32 v229, v229, v1\n\t"
    "v_mfma_f32_32x32x16_bf16 a[80:95], v[184:187], v[88:91], a[80:95]\n\t"
    "ds_read_b128 a[252:255], v225 offset:15360\n\t"
    "v_exp_f32 v5, v5\n\t"
    "v_exp_f32 v6, v6\n\t"
    "v_add_f32 v228, v228, v2\n\t"
    "v_mfma_f32_32x32x16_bf16 a[96:111], v[188:191], v[72:75], a[96:111]\n\t"
    "v_exp_f32 v7, v7\n\t"
    "v_exp_f32 v16, v16\n\t"
    "v_add_f32 v229, v229, v3\n\t"
    "v_add_f32 v228, v228, v4\n\t"
    "v_mfma_f32_32x32x16_bf16 a[112:127], v[188:191], v[88:91], a[112:127]\n\t"
    "v_exp_f32 v17, v17\n\t"
    "v_exp_f32 v18, v18\n\t"
    "v_add_f32 v229, v229, v5\n\t"
    "v_add_f32 v228, v228, v6\n\t"
    "v_mfma_f32_32x32x16_bf16 a[0:15], v[192:195], v[96:99], a[0:15]\n\t"
    "buffer_load_dwordx4 v224, s[40:43], s57 offen offset:1024 lds\n\t"
    "v_exp_f32 v19, v19\n\t"
    "v_exp_f32 v20, v20\n\t"
    "v_mfma_f32_32x32x16_bf16 a[16:31], v[192:195], v[112:115], a[16:31]\n\t"
    "v_exp_f32 v21, v21\n\t"
    "v_exp_f32 v22, v22\n\t"
    "v_add_f32 v229, v229, v7\n\t"
    "v_add_f32 v230, v230, v16\n\t"
    "v_mfma_f32_32x32x16_bf16 a[32:47], v[196:199], v[96:99], a[32:47]\n\t"
    "v_exp_f32 v23, v23\n\t"
    "v_exp_f32 v8, v8\n\t"
    "v_add_f32 v231, v231, v17\n\t"
    "v_add_f32 v230, v230, v18\n\t"
    "v_mfma_f32_32x32x16_bf16 a[48:63], v[196:199], v[112:115], a[48:63]\n\t"
    "v_exp_f32 v9, v9\n\t"
    "v_exp_f32 v10, v10\n\t"
    "v_add_f32 v231, v231, v19\n\t"
    "v_add_f32 v230, v230, v20\n\t"
    "v_mfma_f32_32x32x16_bf16 a[64:79], v[200:203], v[96:99], a[64:79]\n\t"
    "v_exp_f32 v11, v11\n\t"
    "v_exp_f32 v12, v12\n\t"
    "v_add_f32 v231, v231, v21\n\t"
    "v_add_f32 v230, v230, v22\n\t"
    "v_mfma_f32_32x32x16_bf16 a[80:95], v[200:203], v[112:115], a[80:95]\n\t"
    "v_exp_f32 v13, v13\n\t"
    "v_exp_f32 v14, v14\n\t"
    "v_add_f32 v231, v231, v23\n\t"
    "v_add_f32 v228, v228, v8\n\t"
    "v_mfma_f32_32x32x16_bf16 a[96:111], v[204:207], v[96:99], a[96:111]\n\t"
    "buffer_load_dwordx4 v224, s[40:43], s57 offen offset:2048 lds\n\t"
    "v_exp_f32 v15, v15\n\t"
    "v_exp_f32 v24, v24\n\t"
    "v_mfma_f32_32x32x16_bf16 a[112:127], v[204:207], v[112:115], a[112:127]\n\t"
    "v_exp_f32 v25, v25\n\t"
    "v_exp_f32 v26, v26\n\t"
    "v_add_f32 v229, v229, v9\n\t"
    "v_add_f32 v228, v228, v10\n\t"
    "v_mfma_f32_32x32x16_bf16 a[0:15], v[208:211], v[104:107], a[0:15]\n\t"
    "v_exp_f32 v27, v27\n\t"
    "v_add_f32 v229, v229, v11\n\t"
    "v_add_f32 v228, v228, v12\n\t"
    "v_add_f32 v229, v229, v13\n\t"
    "v_add_f32 v228, v228, v14\n\t"
    "v_mfma_f32_32x32x16_bf16 a[16:31], v[208:211], v[120:123], a[16:31]\n\t"
    "v_add_f32 v229, v229, v15\n\t"
    "v_add_f32 v230, v230, v24\n\t"
    "v_add_f32 v231, v231, v25\n\t"
    "v_add_f32 v230, v230, v26\n\t"
    "v_add_f32 v231, v231, v27\n\t"
    "v_mfma_f32_32x32x16_bf16 a[32:47], v[212:215], v[104:107], a[32:47]\n\t"
    "v_mfma_f32_32x32x16_bf16 a[48:63], v[212:215], v[120:123], a[48:63]\n\t"
    "v_mfma_f32_32x32x16_bf16 a[64:79], v[216:219], v[104:107], a[64:79]\n\t"
    "buffer_load_dwordx4 v224, s[40:43], s57 offen offset:3072 lds\n\t"
    "v_mfma_f32_32x32x16_bf16 a[80:95], v[216:219], v[120:123], a[80:95]\n\t"
    "v_mfma_f32_32x32x16_bf16 a[96:111], v[220:223], v[104:107], a[96:111]\n\t"
    "v_mfma_f32_32x32x16_bf16 a[112:127], v[220:223], v[120:123], a[112:127]\n\t"
    "s_mov_b32 s56, s57\n\t"
    "s_mov_b32 s57, s58\n\t"
    "s_add_u32 s58, s58, 0x4000\n\t"
    "s_cmp_lg_u32 s62, 0\n\t"
    "s_cbranch_scc1 Lst_73_%=\n\t"
    "Lbk_74_%=:\n\t"
    "s_waitcnt vmcnt(9) lgkmcnt(0)\n\t"
    "s_barrier\n\t"
    "s_add_u32 s52, s52, 1\n\t"
    "s_branch L1_%=\n\t"
    "LB21_%=:\n\t"
    "v_mfma_f32_32x32x16_bf16 v[0:15], a[192:195], a[128:131], v[128:143]\n\t"
    "ds_read_b128 v[160:163], v226 offset:0\n\t"
    "s_and_b32 m0, s58, 0xffff\n\t"
    "v_exp_f32 v92, v92\n\t"
    "v_exp_f32 v93, v93\n\t"
    "v_mfma_f32_32x32x16_bf16 v[16:31], a[192:195], a[160:163], v[144:159]\n\t"
    "ds_read_b128 v[164:167], v226 offset:2048\n\t"
    "buffer_load_dwordx4 v224, s[36:39], s58 offen offset:0 lds\n\t"
    "v_exp_f32 v94, v94\n\t"
    "v_add_f32 v230, v230, v92\n\t"
    "v_mfma_f32_32x32x16_bf16 v[32:47], a[224:227], a[128:131], v[128:143]\n\t"
    "ds_read_b128 v[168:171], v226 offset:4096\n\t"
    "v_exp_f32 v95, v95\n\t"
    "v_exp_f32 v96, v96\n\t"
    "v_add_f32 v231, v231, v93\n\t"
    "v_mfma_f32_32x32x16_bf16 v[48:63], a[224:227], a[160:163], v[144:159]\n\t"
    "ds_read_b128 v[172:175], v226 offset:6144\n\t"
    "v_exp_f32 v97, v97\n\t"
    "v_exp_f32 v98, v98\n\t"
    "v_add_f32 v230, v230, v94\n\t"
    "v_mfma_f32_32x32x16_bf16 v[0:15], a[196:199], a[132:135], v[0:15]\n\t"
    "ds_read_b128 v[176:179], v226 offset:1024\n\t"
    "v_exp_f32 v99, v99\n\t"
    "v_exp_f32 v100, v100\n\t"
    "v_add_f32 v231, v231, v95\n\t"
    "v_mfma_f32_32x32x16_bf16 v[16:31], a[196:199], a[164:167], v[16:31]\n\t"
    "ds_read_b128 v[180:183], v226 offset:3072\n\t"
    "buffer_load_dwordx4 v224, s[36:39], s58 offen offset:1024 lds\n\t"
    "v_exp_f32 v101, v101\n\t"
    "v_add_f32 v228, v228, v96\n\t"
    "v_mfma_f32_32x32x16_bf16 v[32:47], a[228:231], a[132:135], v[32:47]\n\t"
    "ds_read_b128 v[184:187], v226 offset:5120\n\t"
    "v_exp_f32 v102, v102\n\t"
    "v_exp_f32 v103, v103\n\t"
    "v_add_f32 v229, v229, v97\n\t"
    "v_mfma_f32_32x32x16_bf16 v[48:63], a[228:231], a[164:167], v[48:63]\n\t"
    "ds_read_b128 v[188:191], v226 offset:7168\n\t"
    "v_exp_f32 v112, v112\n\t"
    "v_exp_f32 v113, v113\n\t"
    "v_add_f32 v228, v228, v98\n\t"
    "v_mfma_f32_32x32x16_bf16 v[0:15], a[200:203], a[136:139], v[0:15]\n\t"
    "ds_read_b128 v[192:195], v226 offset:8192\n\t"
    "v_exp_f32 v114, v114\n\t"
    "v_exp_f32 v115, v115\n\t"
    "v_add_f32 v229, v229, v99\n\t"
    "v_mfma_f32_32x32x16_bf16 v[16:31], a[200:203], a[168:171], v[16:31]\n\t"
    "ds_read_b128 v[196:199], v226 offset:10240\n\t"
    "buffer_load_dwordx4 v224, s[36:39], s58 offen offset:2048 lds\n\t"
    "v_exp_f32 v116, v116\n\t"
    "v_add_f32 v228, v228, v100\n\t"
    "v_mfma_f32_32x32x16_bf16 v[32:47], a[232:235], a[136:139], v[32:47]\n\t"
    "ds_read_b128 v[200:203], v226 offset:12288\n\t"
    "v_exp_f32 v117, v117\n\t"
    "v_exp_f32 v118, v118\n\t"
    "v_add_f32 v229, v229, v101\n\t"
    "v_mfma_f32_32x32x16_bf16 v[48:63], a[232:235], a[168:171], v[48:63]\n\t"
    "ds_read_b128 v[204:207], v226 offset:14336\n\t"
    "v_exp_f32 v119, v119\n\t"
    "v_exp_f32 v104, v104\n\t"
    "v_add_f32 v228, v228, v102\n\t"
    "v_mfma_f32_32x32x16_bf16 v[0:15], a[204:207], a[140:143], v[0:15]\n\t"
    "ds_read_b128 v[208:211], v226 offset:9216\n\t"
    "v_exp_f32 v105, v105\n\t"
    "v_exp_f32 v106, v106\n\t"
    "v_add_f32 v229, v229, v103\n\t"
    "v_mfma_f32_32x32x16_bf16 v[16:31], a[204:207], a[172:175], v[16:31]\n\t"
    "ds_read_b128 v[212:215], v226 offset:11264\n\t"
    "buffer_load_dwordx4 v224, s[36:39], s58 offen offset:3072 lds\n\t"
    "v_exp_f32 v107, v107\n\t"
    "v_add_f32 v230, v230, v112\n\t"
    "v_mfma_f32_32x32x16_bf16 v[32:47], a[236:239], a[140:143], v[32:47]\n\t"
    "ds_read_b128 v[216:219], v226 offset:13312\n\t"
    "v_exp_f32 v108, v108\n\t"
    "v_exp_f32 v109, v109\n\t"
    "v_add_f32 v231, v231, v113\n\t"
    "v_mfma_f32_32x32x16_bf16 v[48:63], a[236:239], a[172:175], v[48:63]\n\t"
    "ds_read_b128 v[220:223], v226 offset:15360\n\t"
    "v_exp_f32 v110, v110\n\t"
    "v_exp_f32 v111, v111\n\t"
    "v_add_f32 v230, v230, v114\n\t"
    "v_mfma_f32_32x32x16_bf16 v[0:15], a[208:211], a[144:147], v[0:15]\n\t"
    "s_add_u32 m0, m0, 0x1000\n\t"
    "s_add_u32 s68, s58, 0x1000\n\t"
    "v_exp_f32 v120, v120\n\t"
    "v_exp_f32 v121, v121\n\t"
    "v_mfma_f32_32x32x16_bf16 v[16:31], a[208:211], a[176:179], v[16:31]\n\t"
    "buffer_load_dwordx4 v224, s[36:39], s68 offen offset:0 lds\n\t"
    "v_exp_f32 v122, v122\n\t"
    "v_exp_f32 v123, v123\n\t"
    "v_mfma_f32_32x32x16_bf16 v[32:47], a[240:243], a[144:147], v[32:47]\n\t"
    "v_exp_f32 v124, v124\n\t"
    "v_exp_f32 v125, v125\n\t"
    "v_add_f32 v231, v231, v115\n\t"
    "v_add_f32 v230, v230, v116\n\t"
    "v_mfma_f32_32x32x16_bf16 v[48:63], a[240:243], a[176:179], v[48:63]\n\t"
    "v_exp_f32 v126, v126\n\t"
    "v_exp_f32 v127, v127\n\t"
    "v_add_f32 v231, v231, v117\n\t"
    "v_add_f32 v230, v230, v118\n\t"
    "v_mfma_f32_32x32x16_bf16 v[0:15], a[212:215], a[148:151], v[0:15]\n\t"
    "s_and_b32 s66, s57, 0xc000\n\t"
    "v_add_f32 v231, v231, v119\n\t"
    "v_add_f32 v228, v228, v104\n\t"
    "v_add_f32 v229, v229, v105\n\t"
    "v_add_f32 v228, v228, v106\n\t"
    "v_add_f32 v229, v229, v107\n\t"
    "v_mfma_f32_32x32x16_bf16 v[16:31], a[212:215], a[180:183], v[16:31]\n\t"
    "v_add_u32 v225, s66, v224\n\t"
    "buffer_load_dwordx4 v224, s[36:39], s68 offen offset:1024 lds\n\t"
    "v_add_f32 v228, v228, v108\n\t"
    "v_add_f32 v229, v229, v109\n\t"
    "v_add_f32 v228, v228, v110\n\t"
    "v_mfma_f32_32x32x16_bf16 v[32:47], a[244:247], a[148:151], v[32:47]\n\t"
    "v_add_f32 v229, v229, v111\n\t"
    "v_add_f32 v230, v230, v120\n\t"
    "v_add_f32 v231, v231, v121\n\t"
    "v_add_f32 v230, v230, v122\n\t"
    "v_add_f32 v231, v231, v123\n\t"
    "v_add_f32 v230, v230, v124\n\t"
    "v_mfma_f32_32x32x16_bf16 v[48:63], a[244:247], a[180:183], v[48:63]\n\t"
    "v_add_f32 v231, v231, v125\n\t"
    "v_add_f32 v230, v230, v126\n\t"
    "v_add_f32 v231, v231, v127\n\t"
    "v_cvt_pk_bf16_f32 v64, v64, v65\n\t"
    "v_cvt_pk_bf16_f32 v65, v66, v67\n\t"
    "v_cvt_pk_bf16_f32 v66, v68, v69\n\t"
    "v_mfma_f32_32x32x16_bf16 v[0:15], a[216:219], a[152:155], v[0:15]\n\t"
    "v_cvt_pk_bf16_f32 v67, v70, v71\n\t"
    "v_cvt_pk_bf16_f32 v80, v80, v81\n\t"
    "v_cvt_pk_bf16_f32 v81, v82, v83\n\t"
    "v_cvt_pk_bf16_f32 v82, v84, v85\n\t"
    "v_cvt_pk_bf16_f32 v83, v86, v87\n\t"
    "v_cvt_pk_bf16_f32 v72, v72, v73\n\t"
    "v_mfma_f32_32x32x16_bf16 v[16:31], a[216:219], a[184:187], v[16:31]\n\t"
    "buffer_load_dwordx4 v224, s[36:39], s68 offen offset:2048 lds\n\t"
    "v_cvt_pk_bf16_f32 v73, v74, v75\n\t"
    "v_cvt_pk_bf16_f32 v74, v76, v77\n\t"
    "v_cvt_pk_bf16_f32 v75, v78, v79\n\t"
    "v_cvt_pk_bf16_f32 v88, v88, v89\n\t"
    "v_mfma_f32_32x32x16_bf16 v[32:47], a[248:251], a[152:155], v[32:47]\n\t"
    "v_cvt_pk_bf16_f32 v89, v90, v91\n\t"
    "v_cvt_pk_bf16_f32 v90, v92, v93\n\t"
    "v_cvt_pk_bf16_f32 v91, v94, v95\n\t"
    "v_cvt_pk_bf16_f32 v96, v96, v97\n\t"
    "v_cvt_pk_bf16_f32 v97, v98, v99\n\t"
    "v_cvt_pk_bf16_f32 v98, v100, v101\n\t"
    "v_mfma_f32_32x32x16_bf16 v[48:63], a[248:251], a[184:187], v[48:63]\n\t"
    "v_cvt_pk_bf16_f32 v99, v102, v103\n\t"
    "v_cvt_pk_bf16_f32 v112, v112, v113\n\t"
    "v_cvt_pk_bf16_f32 v113, v114, v115\n\t"
    "v_cvt_pk_bf16_f32 v114, v116, v117\n\t"
    "v_cvt_pk_bf16_f32 v115, v118, v119\n\t"
    "v_cvt_pk_bf16_f32 v104, v104, v105\n\t"
    "v_mfma_f32_32x32x16_bf16 v[0:15], a[220:223], a[156:159], v[0:15]\n\t"
    "v_cvt_pk_bf16_f32 v105, v106, v107\n\t"
    "v_cvt_pk_bf16_f32 v106, v108, v109\n\t"
    "v_cvt_pk_bf16_f32 v107, v110, v111\n\t"
    "v_cvt_pk_bf16_f32 v120, v120, v121\n\t"
    "v_cvt_pk_bf16_f32 v121, v122, v123\n\t"
    "v_cvt_pk_bf16_f32 v122, v124, v125\n\t"
    "v_mfma_f32_32x32x16_bf16 v[16:31], a[220:223], a[188:191], v[16:31]\n\t"
    "buffer_load_dwordx4 v224, s[36:39], s68 offen offset:3072 lds\n\t"
    "v_cvt_pk_bf16_f32 v123, v126, v127\n\t"
    "v_mfma_f32_32x32x16_bf16 v[32:47], a[252:255], a[156:159], v[32:47]\n\t"
    "v_mfma_f32_32x32x16_bf16 v[48:63], a[252:255], a[188:191], v[48:63]\n\t"
    "s_waitcnt vmcnt(17) lgkmcnt(0)\n\t"
    "s_barrier\n\t"
    "s_cmp_ge_u32 s56, s63\n\t"
    "s_cbranch_scc0 Lin_76_%=\n\t"
    "s_sub_u32 s66, s56, s63\n\t"
    "s_cmp_ge_u32 s66, 0x4000\n\t"
    "s_cselect_b32 s66, -1, 0\n\t"
    "v_or_b32 v242, s66, v239\n\t"
    "v_cmp_le_i32_e64 s[70:71], 0, v242\n\t"
    "v_cmp_le_i32_e64 s[72:73], 1, v242\n\t"
    "v_cmp_le_i32_e64 s[74:75], 2, v242\n\t"
    "v_cmp_le_i32_e64 s[76:77], 3, v242\n\t"
    "v_cmp_le_i32_e64 s[78:79], 8, v242\n\t"
    "v_cmp_le_i32_e64 s[80:81], 9, v242\n\t"
    "v_cmp_le_i32_e64 s[82:83], 10, v242\n\t"
    "v_cmp_le_i32_e64 s[84:85], 11, v242\n\t"
    "v_pk_mov_b32 v[32:33], v[252:253], v[252:253] op_sel:[0,0]\n\t"
    "v_pk_mov_b32 v[34:35], v[252:253], v[252:253] op_sel:[0,0]\n\t"
    "v_pk_mov_b32 v[36:37], v[252:253], v[252:253] op_sel:[0,0]\n\t"
    "v_pk_mov_b32 v[38:39], v[252:253], v[252:253] op_sel:[0,0]\n\t"
    "v_cndmask_b32_e64 v0, v252, v0, s[70:71]\n\t"
    "v_cndmask_b32_e64 v48, v252, v48, s[70:71]\n\t"
    "v_cndmask_b32_e64 v1, v252, v1, s[72:73]\n\t"
    "v_cndmask_b32_e64 v49, v252, v49, s[72:73]\n\t"
    "v_cndmask_b32_e64 v2, v252, v2, s[74:75]\n\t"
    "v_cndmask_b32_e64 v50, v252, v50, s[74:75]\n\t"
    "v_cndmask_b32_e64 v3, v252, v3, s[76:77]\n\t"
    "v_cndmask_b32_e64 v51, v252, v51, s[76:77]\n\t"
    "v_cndmask_b32_e64 v4, v252, v4, s[78:79]\n\t"
    "v_cndmask_b32_e64 v52, v252, v52, s[78:79]\n\t"
    "v_cndmask_b32_e64 v5, v252, v5, s[80:81]\n\t"
    "v_cndmask_b32_e64 v53, v252, v53, s[80:81]\n\t"
    "v_cndmask_b32_e64 v6, v252, v6, s[82:83]\n\t"
    "v_cndmask_b32_e64 v54, v252, v54, s[82:83]\n\t"
    "v_cndmask_b32_e64 v7, v252, v7, s[84:85]\n\t"
    "v_cndmask_b32_e64 v55, v252, v55, s[84:85]\n\t"
    "v_cmp_le_i32_e64 s[70:71], 16, v242\n\t"
    "v_cmp_le_i32_e64 s[72:73], 17, v242\n\t"
    "v_cmp_le_i32_e64 s[74:75], 18, v242\n\t"
    "v_cmp_le_i32_e64 s[76:77], 19, v242\n\t"
    "v_cmp_le_i32_e64 s[78:79], 24, v242\n\t"
    "v_cmp_le_i32_e64 s[80:81], 25, v242\n\t"
    "v_cmp_le_i32_e64 s[82:83], 26, v242\n\t"
    "v_cmp_le_i32_e64 s[84:85], 27, v242\n\t"
    "v_pk_mov_b32 v[40:41], v[252:253], v[252:253] op_sel:[0,0]\n\t"
    "v_pk_mov_b32 v[42:43], v[252:253], v[252:253] op_sel:[0,0]\n\t"
    "v_pk_mov_b32 v[44:45], v[252:253], v[252:253] op_sel:[0,0]\n\t"
    "v_pk_mov_b32 v[46:47], v[252:253], v[252:253] op_sel:[0,0]\n\t"
    "v_cndmask_b32_e64 v8, v252, v8, s[70:71]\n\t"
    "v_cndmask_b32_e64 v56, v252, v56, s[70:71]\n\t"
    "v_cndmask_b32_e64 v9, v252, v9, s[72:73]\n\t"
    "v_cndmask_b32_e64 v57, v252, v57, s[72:73]\n\t"
    "v_cndmask_b32_e64 v10, v252, v10, s[74:75]\n\t"
    "v_cndmask_b32_e64 v58, v252, v58, s[74:75]\n\t"
    "v_cndmask_b32_e64 v11, v252, v11, s[76:77]\n\t"
    "v_cndmask_b32_e64 v59, v252, v59, s[76:77]\n\t"
    "v_cndmask_b32_e64 v12, v252, v12, s[78:79]\n\t"
    "v_cndmask_b32_e64 v60, v252, v60, s[78:79]\n\t"
    "v_cndmask_b32_e64 v13, v252, v13, s[80:81]\n\t"
    "v_cndmask_b32_e64 v61, v252, v61, s[80:81]\n\t"
    "v_cndmask_b32_e64 v14, v252, v14, s[82:83]\n\t"
    "v_cndmask_b32_e64 v62, v252, v62, s[82:83]\n\t"
    "v_cndmask_b32_e64 v15, v252, v15, s[84:85]\n\t"
    "v_cndmask_b32_e64 v63, v252, v63, s[84:85]\n\t"
    "s_cbranch_scc0 Lfull_75_%=\n\t"
    "v_pk_mov_b32 v[16:17], v[252:253], v[252:253] op_sel:[0,0]\n\t"
    "v_pk_mov_b32 v[18:19], v[252:253], v[252:253] op_sel:[0,0]\n\t"
    "v_pk_mov_b32 v[20:21], v[252:253], v[252:253] op_sel:[0,0]\n\t"
    "v_pk_mov_b32 v[22:23], v[252:253], v[252:253] op_sel:[0,0]\n\t"
    "v_pk_mov_b32 v[24:25], v[252:253], v[252:253] op_sel:[0,0]\n\t"
    "v_pk_mov_b32 v[26:27], v[252:253], v[252:253] op_sel:[0,0]\n\t"
    "v_pk_mov_b32 v[28:29], v[252:253], v[252:253] op_sel:[0,0]\n\t"
    "v_pk_mov_b32 v[30:31], v[252:253], v[252:253] op_sel:[0,0]\n\t"
    "Lfull_75_%=:\n\t"
    "Lin_76_%=:\n\t"
    "v_mfma_f32_32x32x16_bf16 a[0:15], v[160:163], v[64:67], a[0:15]\n\t"
    "ds_read_b128 a[192:195], v225 offset:0\n\t"
    "ds_read_b128 a[196:199], v225 offset:1024\n\t"
    "ds_read_b128 a[200:203], v225 offset:2048\n\t"
    "ds_read_b128 a[204:207], v225 offset:3072\n\t"
    "s_and_b32 s69, s56, 0xc000\n\t"
    "v_add_u32 v226, s69, v227\n\t"
    "v_mfma_f32_32x32x16_bf16 a[16:31], v[160:163], v[80:83], a[16:31]\n\t"
    "v_max3_f32 v234, v0, v1, v2\n\t"
    "v_max3_f32 v235, v16, v17, v18\n\t"
    "v_max3_f32 v236, v32, v33, v34\n\t"
    "v_max3_f32 v237, v48, v49, v50\n\t"
    "ds_read_b128 a[208:211], v225 offset:4096\n\t"
    "v_mfma_f32_32x32x16_bf16 a[32:47], v[164:167], v[64:67], a[32:47]\n\t"
    "v_max3_f32 v234, v234, v3, v4\n\t"
    "v_max3_f32 v235, v235, v19, v20\n\t"
    "v_max3_f32 v236, v236, v35, v36\n\t"
    "v_max3_f32 v237, v237, v51, v52\n\t"
    "ds_read_b128 a[212:215], v225 offset:5120\n\t"
    "v_mfma_f32_32x32x16_bf16 a[48:63], v[164:167], v[80:83], a[48:63]\n\t"
    "v_max3_f32 v234, v234, v5, v6\n\t"
    "v_max3_f32 v235, v235, v21, v22\n\t"
    "v_max3_f32 v236, v236, v37, v38\n\t"
    "v_max3_f32 v237, v237, v53, v54\n\t"
    "ds_read_b128 a[216:219], v225 offset:6144\n\t"
    "v_mfma_f32_32x32x16_bf16 a[64:79], v[168:171], v[64:67], a[64:79]\n\t"
    "v_max3_f32 v234, v234, v7, v8\n\t"
    "v_max3_f32 v235, v235, v23, v24\n\t"
    "v_max3_f32 v236, v236, v39, v40\n\t"
    "v_max3_f32 v237, v237, v55, v56\n\t"
    "ds_read_b128 a[220:223], v225 offset:7168\n\t"
    "v_mfma_f32_32x32x16_bf16 a[80:95], v[168:171], v[80:83], a[80:95]\n\t"
    "v_max3_f32 v234, v234, v9, v10\n\t"
    "v_max3_f32 v235, v235, v25, v26\n\t"
    "v_max3_f32 v236, v236, v41, v42\n\t"
    "v_max3_f32 v237, v237, v57, v58\n\t"
    "ds_read_b128 a[224:227], v225 offset:8192\n\t"
    "v_mfma_f32_32x32x16_bf16 a[96:111], v[172:175], v[64:67], a[96:111]\n\t"
    "v_max3_f32 v234, v234, v11, v12\n\t"
    "v_max3_f32 v235, v235, v27, v28\n\t"
    "v_max3_f32 v236, v236, v43, v44\n\t"
    "v_max3_f32 v237, v237, v59, v60\n\t"
    "ds_read_b128 a[228:231], v225 offset:9216\n\t"
    "v_mfma_f32_32x32x16_bf16 a[112:127], v[172:175], v[80:83], a[112:127]\n\t"
    "v_max3_f32 v234, v234, v13, v14\n\t"
    "v_max3_f32 v235, v235, v29, v30\n\t"
    "v_max3_f32 v236, v236, v45, v46\n\t"
    "v_max3_f32 v237, v237, v61, v62\n\t"
    "ds_read_b128 a[232:235], v225 offset:10240\n\t"
    "v_mfma_f32_32x32x16_bf16 a[0:15], v[176:179], v[72:75], a[0:15]\n\t"
    "v_max_f32 v234, v234, v15\n\t"
    "v_max_f32 v235, v235, v31\n\t"
    "v_max_f32 v236, v236, v47\n\t"
    "v_max_f32 v237, v237, v63\n\t"
    "ds_read_b128 a[236:239], v225 offset:11264\n\t"
    "v_mfma_f32_32x32x16_bf16 a[16:31], v[176:179], v[88:91], a[16:31]\n\t"
    "v_max3_f32 v238, v234, v235, v236\n\t"
    "v_max_f32 v238, v238, v237\n\t"
    "v_cmp_lt_f32 vcc, s60, v238\n\t"
    "s_cbranch_vccnz Lst_77_%=\n\t"
    "Lbk_78_%=:\n\t"
    "s_and_b32 s67, s57, 0xffff\n\t"
    "s_or_b32 m0, s67, 0x10000\n\t"
    "v_mfma_f32_32x32x16_bf16 a[32:47], v[180:183], v[72:75], a[32:47]\n\t"
    "ds_read_b128 a[240:243], v225 offset:12288\n\t"
    "buffer_load_dwordx4 v224, s[40:43], s57 offen offset:0 lds\n\t"
    "v_exp_f32 v0, v0\n\t"
    "v_mfma_f32_32x32x16_bf16 a[48:63], v[180:183], v[88:91], a[48:63]\n\t"
    "ds_read_b128 a[244:247], v225 offset:13312\n\t"
    "v_exp_f32 v1, v1\n\t"
    "v_exp_f32 v2, v2\n\t"
    "v_add_f32 v228, v228, v0\n\t"
    "v_mfma_f32_32x32x16_bf16 a[64:79], v[184:187], v[72:75], a[64:79]\n\t"
    "ds_read_b128 a[248:251], v225 offset:14336\n\t"
    "v_exp_f32 v3, v3\n\t"
    "v_exp_f32 v4, v4\n\t"
    "v_add_f32 v229, v229, v1\n\t"
    "v_mfma_f32_32x32x16_bf16 a[80:95], v[184:187], v[88:91], a[80:95]\n\t"
    "ds_read_b128 a[252:255], v225 offset:15360\n\t"
    "v_exp_f32 v5, v5\n\t"
    "v_exp_f32 v6, v6\n\t"
    "v_add_f32 v228, v228, v2\n\t"
    "v_mfma_f32_32x32x16_bf16 a[96:111], v[188:191], v[72:75], a[96:111]\n\t"
    "v_exp_f32 v7, v7\n\t"
    "v_exp_f32 v16, v16\n\t"
    "v_add_f32 v229, v229, v3\n\t"
    "v_add_f32 v228, v228, v4\n\t"
    "v_mfma_f32_32x32x16_bf16 a[112:127], v[188:191], v[88:91], a[112:127]\n\t"
    "v_exp_f32 v17, v17\n\t"
    "v_exp_f32 v18, v18\n\t"
    "v_add_f32 v229, v229, v5\n\t"
    "v_add_f32 v228, v228, v6\n\t"
    "v_mfma_f32_32x32x16_bf16 a[0:15], v[192:195], v[96:99], a[0:15]\n\t"
    "buffer_load_dwordx4 v224, s[40:43], s57 offen offset:1024 lds\n\t"
    "v_exp_f32 v19, v19\n\t"
    "v_exp_f32 v20, v20\n\t"
    "v_mfma_f32_32x32x16_bf16 a[16:31], v[192:195], v[112:115], a[16:31]\n\t"
    "v_exp_f32 v21, v21\n\t"
    "v_exp_f32 v22, v22\n\t"
    "v_add_f32 v229, v229, v7\n\t"
    "v_add_f32 v230, v230, v16\n\t"
    "v_mfma_f32_32x32x16_bf16 a[32:47], v[196:199], v[96:99], a[32:47]\n\t"
    "v_exp_f32 v23, v23\n\t"
    "v_exp_f32 v8, v8\n\t"
    "v_add_f32 v231, v231, v17\n\t"
    "v_add_f32 v230, v230, v18\n\t"
    "v_mfma_f32_32x32x16_bf16 a[48:63], v[196:199], v[112:115], a[48:63]\n\t"
    "v_exp_f32 v9, v9\n\t"
    "v_exp_f32 v10, v10\n\t"
    "v_add_f32 v231, v231, v19\n\t"
    "v_add_f32 v230, v230, v20\n\t"
    "v_mfma_f32_32x32x16_bf16 a[64:79], v[200:203], v[96:99], a[64:79]\n\t"
    "v_exp_f32 v11, v11\n\t"
    "v_exp_f32 v12, v12\n\t"
    "v_add_f32 v231, v231, v21\n\t"
    "v_add_f32 v230, v230, v22\n\t"
    "v_mfma_f32_32x32x16_bf16 a[80:95], v[200:203], v[112:115], a[80:95]\n\t"
    "v_exp_f32 v13, v13\n\t"
    "v_exp_f32 v14, v14\n\t"
    "v_add_f32 v231, v231, v23\n\t"
    "v_add_f32 v228, v228, v8\n\t"
    "v_mfma_f32_32x32x16_bf16 a[96:111], v[204:207], v[96:99], a[96:111]\n\t"
    "buffer_load_dwordx4 v224, s[40:43], s57 offen offset:2048 lds\n\t"
    "v_exp_f32 v15, v15\n\t"
    "v_exp_f32 v24, v24\n\t"
    "v_mfma_f32_32x32x16_bf16 a[112:127], v[204:207], v[112:115], a[112:127]\n\t"
    "v_exp_f32 v25, v25\n\t"
    "v_exp_f32 v26, v26\n\t"
    "v_add_f32 v229, v229, v9\n\t"
    "v_add_f32 v228, v228, v10\n\t"
    "v_mfma_f32_32x32x16_bf16 a[0:15], v[208:211], v[104:107], a[0:15]\n\t"
    "v_exp_f32 v27, v27\n\t"
    "v_add_f32 v229, v229, v11\n\t"
    "v_add_f32 v228, v228, v12\n\t"
    "v_add_f32 v229, v229, v13\n\t"
    "v_add_f32 v228, v228, v14\n\t"
    "v_mfma_f32_32x32x16_bf16 a[16:31], v[208:211], v[120:123], a[16:31]\n\t"
    "v_add_f32 v229, v229, v15\n\t"
    "v_add_f32 v230, v230, v24\n\t"
    "v_add_f32 v231, v231, v25\n\t"
    "v_add_f32 v230, v230, v26\n\t"
    "v_add_f32 v231, v231, v27\n\t"
    "v_mfma_f32_32x32x16_bf16 a[32:47], v[212:215], v[104:107], a[32:47]\n\t"
    "v_mfma_f32_32x32x16_bf16 a[48:63], v[212:215], v[120:123], a[48:63]\n\t"
    "v_mfma_f32_32x32x16_bf16 a[64:79], v[216:219], v[104:107], a[64:79]\n\t"
    "buffer_load_dwordx4 v224, s[40:43], s57 offen offset:3072 lds\n\t"
    "v_mfma_f32_32x32x16_bf16 a[80:95], v[216:219], v[120:123], a[80:95]\n\t"
    "v_mfma_f32_32x32x16_bf16 a[96:111], v[220:223], v[104:107], a[96:111]\n\t"
    "s_xor_b32 s66, s58, 0x4000\n\t"
    "s_and_b32 s66, s66, 0xffff\n\t"
    "v_add_u32 v240, s66, v227\n\t"
    "ds_write_b64 v240, v[232:233] offset:0\n\t"
    "v_mfma_f32_32x32x16_bf16 a[112:127], v[220:223], v[120:123], a[112:127]\n\t"
    "s_mov_b32 s56, s57\n\t"
    "s_mov_b32 s57, s58\n\t"
    "s_add_u32 s58, s58, 0x8000\n\t"
    "s_cmp_lg_u32 s62, 0\n\t"
    "s_cbranch_scc1 Lst_79_%=\n\t"
    "Lbk_80_%=:\n\t"
    "s_waitcnt vmcnt(13) lgkmcnt(0)\n\t"
    "s_barrier\n\t"
    "s_add_u32 s52, s52, 1\n\t"
    "s_cmp_eq_u32 s52, 15\n\t"
    "s_cbranch_scc1 L7_%=\n\t"
    "L2P_%=:\n\t"
    "v_mfma_f32_32x32x16_bf16 v[64:79], a[192:195], a[128:131], v[128:143]\n\t"
    "ds_read_b128 v[160:163], v226 offset:0\n\t"
    "s_and_b32 m0, s58, 0xffff\n\t"
    "v_exp_f32 v28, v28\n\t"
    "v_exp_f32 v29, v29\n\t"
    "v_mfma_f32_32x32x16_bf16 v[80:95], a[192:195], a[160:163], v[144:159]\n\t"
    "ds_read_b128 v[164:167], v226 offset:2048\n\t"
    "buffer_load_dwordx4 v224, s[36:39], s58 offen offset:0 lds\n\t"
    "v_exp_f32 v30, v30\n\t"
    "v_add_f32 v230, v230, v28\n\t"
    "v_mfma_f32_32x32x16_bf16 v[96:111], a[224:227], a[128:131], v[128:143]\n\t"
    "ds_read_b128 v[168:171], v226 offset:4096\n\t"
    "v_exp_f32 v31, v31\n\t"
    "v_exp_f32 v32, v32\n\t"
    "v_add_f32 v231, v231, v29\n\t"
    "v_mfma_f32_32x32x16_bf16 v[112:127], a[224:227], a[160:163], v[144:159]\n\t"
    "ds_read_b128 v[172:175], v226 offset:6144\n\t"
    "v_exp_f32 v33, v33\n\t"
    "v_exp_f32 v34, v34\n\t"
    "v_add_f32 v230, v230, v30\n\t"
    "v_mfma_f32_32x32x16_bf16 v[64:79], a[196:199], a[132:135], v[64:79]\n\t"
    "ds_read_b128 v[176:179], v226 offset:1024\n\t"
    "v_exp_f32 v35, v35\n\t"
    "v_exp_f32 v36, v36\n\t"
    "v_add_f32 v231, v231, v31\n\t"
    "v_mfma_f32_32x32x16_bf16 v[80:95], a[196:199], a[164:167], v[80:95]\n\t"
    "ds_read_b128 v[180:183], v226 offset:3072\n\t"
    "buffer_load_dwordx4 v224, s[36:39], s58 offen offset:1024 lds\n\t"
    "v_exp_f32 v37, v37\n\t"
    "v_add_f32 v228, v228, v32\n\t"
    "v_mfma_f32_32x32x16_bf16 v[96:111], a[228:231], a[132:135], v[96:111]\n\t"
    "ds_read_b128 v[184:187], v226 offset:5120\n\t"
    "v_exp_f32 v38, v38\n\t"
    "v_exp_f32 v39, v39\n\t"
    "v_add_f32 v229, v229, v33\n\t"
    "v_mfma_f32_32x32x16_bf16 v[112:127], a[228:231], a[164:167], v[112:127]\n\t"
    "ds_read_b128 v[188:191], v226 offset:7168\n\t"
    "v_exp_f32 v48, v48\n\t"
    "v_exp_f32 v49, v49\n\t"
    "v_add_f32 v228, v228, v34\n\t"
    "v_mfma_f32_32x32x16_bf16 v[64:79], a[200:203], a[136:139], v[64:79]\n\t"
    "ds_read_b128 v[192:195], v226 offset:8192\n\t"
    "v_exp_f32 v50, v50\n\t"
    "v_exp_f32 v51, v51\n\t"
    "v_add_f32 v229, v229, v35\n\t"
    "v_mfma_f32_32x32x16_bf16 v[80:95], a[200:203], a[168:171], v[80:95]\n\t"
    "ds_read_b128 v[196:199], v226 offset:10240\n\t"
    "buffer_load_dwordx4 v224, s[36:39], s58 offen offset:2048 lds\n\t"
    "v_exp_f32 v52, v52\n\t"
    "v_add_f32 v228, v228, v36\n\t"
    "v_mfma_f32_32x32x16_bf16 v[96:111], a[232:235], a[136:139], v[96:111]\n\t"
    "ds_read_b128 v[200:203], v226 offset:12288\n\t"
    "v_exp_f32 v53, v53\n\t"
    "v_exp_f32 v54, v54\n\t"
    "v_add_f32 v229, v229, v37\n\t"
    "v_mfma_f32_32x32x16_bf16 v[112:127], a[232:235], a[168:171], v[112:127]\n\t"
    "ds_read_b128 v[204:207], v226 offset:14336\n\t"
    "v_exp_f32 v55, v55\n\t"
    "v_exp_f32 v40, v40\n\t"
    "v_add_f32 v228, v228, v38\n\t"
    "v_mfma_f32_32x32x16_bf16 v[64:79], a[204:207], a[140:143], v[64:79]\n\t"
    "ds_read_b128 v[208:211], v226 offset:9216\n\t"
    "v_exp_f32 v41, v41\n\t"
    "v_exp_f32 v42, v42\n\t"
    "v_add_f32 v229, v229, v39\n\t"
    "v_mfma_f32_32x32x16_bf16 v[80:95], a[204:207], a[172:175], v[80:95]\n\t"
    "ds_read_b128 v[212:215], v226 offset:11264\n\t"
    "buffer_load_dwordx4 v224, s[36:39], s58 offen offset:3072 lds\n\t"
    "v_exp_f32 v43, v43\n\t"
    "v_add_f32 v230, v230, v48\n\t"
    "v_mfma_f32_32x32x16_bf16 v[96:111], a[236:239], a[140:143], v[96:111]\n\t"
    "ds_read_b128 v[216:219], v226 offset:13312\n\t"
    "v_exp_f32 v44, v44\n\t"
    "v_exp_f32 v45, v45\n\t"
    "v_add_f32 v231, v231, v49\n\t"
    "v_mfma_f32_32x32x16_bf16 v[112:127], a[236:239], a[172:175], v[112:127]\n\t"
    "ds_read_b128 v[220:223], v226 offset:15360\n\t"
    "v_exp_f32 v46, v46\n\t"
    "v_exp_f32 v47, v47\n\t"
    "v_add_f32 v230, v230, v50\n\t"
    "v_mfma_f32_32x32x16_bf16 v[64:79], a[208:211], a[144:147], v[64:79]\n\t"
    "s_add_u32 m0, m0, 0x1000\n\t"
    "s_add_u32 s68, s58, 0x1000\n\t"
    "v_exp_f32 v56, v56\n\t"
    "v_exp_f32 v57, v57\n\t"
    "v_mfma_f32_32x32x16_bf16 v[80:95], a[208:211], a[176:179], v[80:95]\n\t"
    "buffer_load_dwordx4 v224, s[36:39], s68 offen offset:0 lds\n\t"
    "v_exp_f32 v58, v58\n\t"
    "v_exp_f32 v59, v59\n\t"
    "v_mfma_f32_32x32x16_bf16 v[96:111], a[240:243], a[144:147], v[96:111]\n\t"
    "v_exp_f32 v60, v60\n\t"
    "v_exp_f32 v61, v61\n\t"
    "v_add_f32 v231, v231, v51\n\t"
    "v_add_f32 v230, v230, v52\n\t"
    "v_mfma_f32_32x32x16_bf16 v[112:127], a[240:243], a[176:179], v[112:127]\n\t"
    "v_exp_f32 v62, v62\n\t"
    "v_exp_f32 v63, v63\n\t"
    "v_add_f32 v231, v231, v53\n\t"
    "v_add_f32 v230, v230, v54\n\t"
    "v_mfma_f32_32x32x16_bf16 v[64:79], a[212:215], a[148:151], v[64:79]\n\t"
    "s_and_b32 s66, s57, 0xc000\n\t"
    "v_add_f32 v231, v231, v55\n\t"
    "v_add_f32 v228, v228, v40\n\t"
    "v_add_f32 v229, v229, v41\n\t"
    "v_add_f32 v228, v228, v42\n\t"
    "v_add_f32 v229, v229, v43\n\t"
    "v_mfma_f32_32x32x16_bf16 v[80:95], a[212:215], a[180:183], v[80:95]\n\t"
    "v_add_u32 v225, s66, v224\n\t"
    "buffer_load_dwordx4 v224, s[36:39], s68 offen offset:1024 lds\n\t"
    "v_add_f32 v228, v228, v44\n\t"
    "v_add_f32 v229, v229, v45\n\t"
    "v_add_f32 v228, v228, v46\n\t"
    "v_mfma_f32_32x32x16_bf16 v[96:111], a[244:247], a[148:151], v[96:111]\n\t"
    "v_add_f32 v229, v229, v47\n\t"
    "v_add_f32 v230, v230, v56\n\t"
    "v_add_f32 v231, v231, v57\n\t"
    "v_add_f32 v230, v230, v58\n\t"
    "v_add_f32 v231, v231, v59\n\t"
    "v_add_f32 v230, v230, v60\n\t"
    "v_mfma_f32_32x32x16_bf16 v[112:127], a[244:247], a[180:183], v[112:127]\n\t"
    "v_add_f32 v231, v231, v61\n\t"
    "v_add_f32 v230, v230, v62\n\t"
    "v_add_f32 v231, v231, v63\n\t"
    "v_cvt_pk_bf16_f32 v0, v0, v1\n\t"
    "v_cvt_pk_bf16_f32 v1, v2, v3\n\t"
    "v_cvt_pk_bf16_f32 v2, v4, v5\n\t"
    "v_mfma_f32_32x32x16_bf16 v[64:79], a[216:219], a[152:155], v[64:79]\n\t"
    "v_cvt_pk_bf16_f32 v3, v6, v7\n\t"
    "v_cvt_pk_bf16_f32 v16, v16, v17\n\t"
    "v_cvt_pk_bf16_f32 v17, v18, v19\n\t"
    "v_cvt_pk_bf16_f32 v18, v20, v21\n\t"
    "v_cvt_pk_bf16_f32 v19, v22, v23\n\t"
    "v_cvt_pk_bf16_f32 v8, v8, v9\n\t"
    "v_mfma_f32_32x32x16_bf16 v[80:95], a[216:219], a[184:187], v[80:95]\n\t"
    "buffer_load_dwordx4 v224, s[36:39], s68 offen offset:2048 lds\n\t"
    "v_cvt_pk_bf16_f32 v9, v10, v11\n\t"
    "v_cvt_pk_bf16_f32 v10, v12, v13\n\t"
    "v_cvt_pk_bf16_f32 v11, v14, v15\n\t"
    "v_cvt_pk_bf16_f32 v24, v24, v25\n\t"
    "v_mfma_f32_32x32x16_bf16 v[96:111], a[248:251], a[152:155], v[96:111]\n\t"
    "v_cvt_pk_bf16_f32 v25, v26, v27\n\t"
    "v_cvt_pk_bf16_f32 v26, v28, v29\n\t"
    "v_cvt_pk_bf16_f32 v27, v30, v31\n\t"
    "v_cvt_pk_bf16_f32 v32, v32, v33\n\t"
    "v_cvt_pk_bf16_f32 v33, v34, v35\n\t"
    "v_cvt_pk_bf16_f32 v34, v36, v37\n\t"
    "v_mfma_f32_32x32x16_bf16 v[112:127], a[248:251], a[184:187], v[112:127]\n\t"
    "v_cvt_pk_bf16_f32 v35, v38, v39\n\t"
    "v_cvt_pk_bf16_f32 v48, v48, v49\n\t"
    "v_cvt_pk_bf16_f32 v49, v50, v51\n\t"
    "v_cvt_pk_bf16_f32 v50, v52, v53\n\t"
    "v_cvt_pk_bf16_f32 v51, v54, v55\n\t"
    "v_cvt_pk_bf16_f32 v40, v40, v41\n\t"
    "v_mfma_f32_32x32x16_bf16 v[64:79], a[220:223], a[156:159], v[64:79]\n\t"
    "v_cvt_pk_bf16_f32 v41, v42, v43\n\t"
    "v_cvt_pk_bf16_f32 v42, v44, v45\n\t"
    "v_cvt_pk_bf16_f32 v43, v46, v47\n\t"
    "v_cvt_pk_bf16_f32 v56, v56, v57\n\t"
    "v_cvt_pk_bf16_f32 v57, v58, v59\n\t"
    "v_cvt_pk_bf16_f32 v58, v60, v61\n\t"
    "v_mfma_f32_32x32x16_bf16 v[80:95], a[220:223], a[188:191], v[80:95]\n\t"
    "buffer_load_dwordx4 v224, s[36:39], s68 offen offset:3072 lds\n\t"
    "v_cvt_pk_bf16_f32 v59, v62, v63\n\t"
    "v_mfma_f32_32x32x16_bf16 v[96:111], a[252:255], a[156:159], v[96:111]\n\t"
    "v_mfma_f32_32x32x16_bf16 v[112:127], a[252:255], a[188:191], v[112:127]\n\t"
    "s_waitcnt vmcnt(12) lgkmcnt(0)\n\t"
    "s_barrier\n\t"
    "s_cmp_eq_u32 s52, s64\n\t"
    "s_cbranch_scc0 Lrx_81_%=\n\t"
    "v_add_u32 v240, s86, v224\n\t"
    "s_and_b32 s66, s57, 0xffff\n\t"
    "v_add_u32 v241, s66, v227\n\t"
    "ds_read_b64 v[250:251], v241 offset:0\n\t"
    "ds_read_b128 a[128:131], v240 offset:0\n\t"
    "ds_read_b128 a[132:135], v240 offset:1024\n\t"
    "ds_read_b128 a[136:139], v240 offset:2048\n\t"
    "ds_read_b128 a[140:143], v240 offset:3072\n\t"
    "ds_read_b128 a[144:147], v240 offset:4096\n\t"
    "ds_read_b128 a[148:151], v240 offset:5120\n\t"
    "ds_read_b128 a[152:155], v240 offset:6144\n\t"
    "ds_read_b128 a[156:159], v240 offset:7168\n\t"
    "ds_read_b128 a[160:163], v240 offset:8192\n\t"
    "ds_read_b128 a[164:167], v240 offset:9216\n\t"
    "ds_read_b128 a[168:171], v240 offset:10240\n\t"
    "ds_read_b128 a[172:175], v240 offset:11264\n\t"
    "ds_read_b128 a[176:179], v240 offset:12288\n\t"
    "ds_read_b128 a[180:183], v240 offset:13312\n\t"
    "ds_read_b128 a[184:187], v240 offset:14336\n\t"
    "ds_read_b128 a[188:191], v240 offset:15360\n\t"
    "s_waitcnt lgkmcnt(15)\n\t"
    "s_sub_u32 s66, s56, s63\n\t"
    "s_cmp_ge_u32 s66, 0x4000\n\t"
    "s_cselect_b32 s66, -1, 0\n\t"
    "v_or_b32 v242, s66, v239\n\t"
    "v_cmp_le_i32_e64 s[70:71], 0, v242\n\t"
    "v_cmp_le_i32_e64 s[72:73], 1, v242\n\t"
    "v_cmp_le_i32_e64 s[74:75], 2, v242\n\t"
    "v_cmp_le_i32_e64 s[76:77], 3, v242\n\t"
    "v_cmp_le_i32_e64 s[78:79], 8, v242\n\t"
    "v_cmp_le_i32_e64 s[80:81], 9, v242\n\t"
    "v_cmp_le_i32_e64 s[82:83], 10, v242\n\t"
    "v_cmp_le_i32_e64 s[84:85], 11, v242\n\t"
    "v_pk_mov_b32 v[96:97], v[252:253], v[252:253] op_sel:[0,0]\n\t"
    "v_pk_mov_b32 v[98:99], v[252:253], v[252:253] op_sel:[0,0]\n\t"
    "v_pk_mov_b32 v[100:101], v[252:253], v[252:253] op_sel:[0,0]\n\t"
    "v_pk_mov_b32 v[102:103], v[252:253], v[252:253] op_sel:[0,0]\n\t"
    "v_cndmask_b32_e64 v64, v252, v64, s[70:71]\n\t"
    "v_cndmask_b32_e64 v112, v252, v112, s[70:71]\n\t"
    "v_cndmask_b32_e64 v65, v252, v65, s[72:73]\n\t"
    "v_cndmask_b32_e64 v113, v252, v113, s[72:73]\n\t"
    "v_cndmask_b32_e64 v66, v252, v66, s[74:75]\n\t"
    "v_cndmask_b32_e64 v114, v252, v114, s[74:75]\n\t"
    "v_cndmask_b32_e64 v67, v252, v67, s[76:77]\n\t"
    "v_cndmask_b32_e64 v115, v252, v115, s[76:77]\n\t"
    "v_cndmask_b32_e64 v68, v252, v68, s[78:79]\n\t"
    "v_cndmask_b32_e64 v116, v252, v116, s[78:79]\n\t"
    "v_cndmask_b32_e64 v69, v252, v69, s[80:81]\n\t"
    "v_cndmask_b32_e64 v117, v252, v117, s[80:81]\n\t"
    "v_cndmask_b32_e64 v70, v252, v70, s[82:83]\n\t"
    "v_cndmask_b32_e64 v118, v252, v118, s[82:83]\n\t"
    "v_cndmask_b32_e64 v71, v252, v71, s[84:85]\n\t"
    "v_cndmask_b32_e64 v119, v252, v119, s[84:85]\n\t"
    "v_cmp_le_i32_e64 s[70:71], 16, v242\n\t"
    "v_cmp_le_i32_e64 s[72:73], 17, v242\n\t"
    "v_cmp_le_i32_e64 s[74:75], 18, v242\n\t"
    "v_cmp_le_i32_e64 s[76:77], 19, v242\n\t"
    "v_cmp_le_i32_e64 s[78:79], 24, v242\n\t"
    "v_cmp_le_i32_e64 s[80:81], 25, v242\n\t"
    "v_cmp_le_i32_e64 s[82:83], 26, v242\n\t"
    "v_cmp_le_i32_e64 s[84:85], 27, v242\n\t"
    "v_pk_mov_b32 v[104:105], v[252:253], v[252:253] op_sel:[0,0]\n\t"
    "v_pk_mov_b32 v[106:107], v[252:253], v[252:253] op_sel:[0,0]\n\t"
    "v_pk_mov_b32 v[108:109], v[252:253], v[252:253] op_sel:[0,0]\n\t"
    "v_pk_mov_b32 v[110:111], v[252:253], v[252:253] op_sel:[0,0]\n\t"
    "v_cndmask_b32_e64 v72, v252, v72, s[70:71]\n\t"
    "v_cndmask_b32_e64 v120, v252, v120, s[70:71]\n\t"
    "v_cndmask_b32_e64 v73, v252, v73, s[72:73]\n\t"
    "v_cndmask_b32_e64 v121, v252, v121, s[72:73]\n\t"
    "v_cndmask_b32_e64 v74, v252, v74, s[74:75]\n\t"
    "v_cndmask_b32_e64 v122, v252, v122, s[74:75]\n\t"
    "v_cndmask_b32_e64 v75, v252, v75, s[76:77]\n\t"
    "v_cndmask_b32_e64 v123, v252, v123, s[76:77]\n\t"
    "v_cndmask_b32_e64 v76, v252, v76, s[78:79]\n\t"
    "v_cndmask_b32_e64 v124, v252, v124, s[78:79]\n\t"
    "v_cndmask_b32_e64 v77, v252, v77, s[80:81]\n\t"
    "v_cndmask_b32_e64 v125, v252, v125, s[80:81]\n\t"
    "v_cndmask_b32_e64 v78, v252, v78, s[82:83]\n\t"
    "v_cndmask_b32_e64 v126, v252, v126, s[82:83]\n\t"
    "v_cndmask_b32_e64 v79, v252, v79, s[84:85]\n\t"
    "v_cndmask_b32_e64 v127, v252, v127, s[84:85]\n\t"
    "s_cbranch_scc0 Lfull_83_%=\n\t"
    "v_pk_mov_b32 v[80:81], v[252:253], v[252:253] op_sel:[0,0]\n\t"
    "v_pk_mov_b32 v[82:83], v[252:253], v[252:253] op_sel:[0,0]\n\t"
    "v_pk_mov_b32 v[84:85], v[252:253], v[252:253] op_sel:[0,0]\n\t"
    "v_pk_mov_b32 v[86:87], v[252:253], v[252:253] op_sel:[0,0]\n\t"
    "v_pk_mov_b32 v[88:89], v[252:253], v[252:253] op_sel:[0,0]\n\t"
    "v_pk_mov_b32 v[90:91], v[252:253], v[252:253] op_sel:[0,0]\n\t"
    "v_pk_mov_b32 v[92:93], v[252:253], v[252:253] op_sel:[0,0]\n\t"
    "v_pk_mov_b32 v[94:95], v[252:253], v[252:253] op_sel:[0,0]\n\t"
    "Lfull_83_%=:\n\t"
    "s_branch Lin_82_%=\n\t"
    "Lrx_81_%=:\n\t"
    "s_add_u32 s70, s88, 0\n\t"
    "buffer_store_dwordx4 v[244:247], v224, s[48:51], s70 offen offset:0 sc1\n\t"
    "buffer_store_dwordx4 v[244:247], v224, s[48:51], s70 offen offset:1024 sc1\n\t"
    "buffer_store_dwordx4 v[244:247], v224, s[48:51], s70 offen offset:2048 sc1\n\t"
    "buffer_store_dwordx4 v[244:247], v224, s[48:51], s70 offen offset:3072 sc1\n\t"
    "s_add_u32 s70, s88, 4096\n\t"
    "buffer_store_dwordx4 v[244:247], v224, s[48:51], s70 offen offset:0 sc1\n\t"
    "buffer_store_dwordx4 v[244:247], v224, s[48:51], s70 offen offset:1024 sc1\n\t"
    "buffer_store_dwordx4 v[244:247], v224, s[48:51], s70 offen offset:2048 sc1\n\t"
    "buffer_store_dwordx4 v[244:247], v224, s[48:51], s70 offen offset:3072 sc1\n\t"
    "s_add_u32 s70, s88, 262144\n\t"
    "buffer_store_dwordx4 v[244:247], v224, s[48:51], s70 offen offset:0 sc1\n\t"
    "buffer_store_dwordx4 v[244:247], v224, s[48:51], s70 offen offset:1024 sc1\n\t"
    "buffer_store_dwordx4 v[244:247], v224, s[48:51], s70 offen offset:2048 sc1\n\t"
    "buffer_store_dwordx4 v[244:247], v224, s[48:51], s70 offen offset:3072 sc1\n\t"
    "s_add_u32 s70, s88, 266240\n\t"
    "buffer_store_dwordx4 v[244:247], v224, s[48:51], s70 offen offset:0 sc1\n\t"
    "buffer_store_dwordx4 v[244:247], v224, s[48:51], s70 offen offset:1024 sc1\n\t"
    "buffer_store_dwordx4 v[244:247], v224, s[48:51], s70 offen offset:2048 sc1\n\t"
    "buffer_store_dwordx4 v[244:247], v224, s[48:51], s70 offen offset:3072 sc1\n\t"
    "Lin_82_%=:\n\t"
    "v_mfma_f32_32x32x16_bf16 a[0:15], v[160:163], v[0:3], a[0:15]\n\t"
    "ds_read_b128 a[192:195], v225 offset:0\n\t"
    "ds_read_b128 a[196:199], v225 offset:1024\n\t"
    "ds_read_b128 a[200:203], v225 offset:2048\n\t"
    "ds_read_b128 a[204:207], v225 offset:3072\n\t"
    "s_and_b32 s69, s56, 0xc000\n\t"
    "v_add_u32 v226, s69, v227\n\t"
    "v_mfma_f32_32x32x16_bf16 a[16:31], v[160:163], v[16:19], a[16:31]\n\t"
    "v_max3_f32 v234, v64, v65, v66\n\t"
    "v_max3_f32 v235, v80, v81, v82\n\t"
    "v_max3_f32 v236, v96, v97, v98\n\t"
    "v_max3_f32 v237, v112, v113, v114\n\t"
    "ds_read_b128 a[208:211], v225 offset:4096\n\t"
    "v_mfma_f32_32x32x16_bf16 a[32:47], v[164:167], v[0:3], a[32:47]\n\t"
    "v_max3_f32 v234, v234, v67, v68\n\t"
    "v_max3_f32 v235, v235, v83, v84\n\t"
    "v_max3_f32 v236, v236, v99, v100\n\t"
    "v_max3_f32 v237, v237, v115, v116\n\t"
    "ds_read_b128 a[212:215], v225 offset:5120\n\t"
    "v_mfma_f32_32x32x16_bf16 a[48:63], v[164:167], v[16:19], a[48:63]\n\t"
    "v_max3_f32 v234, v234, v69, v70\n\t"
    "v_max3_f32 v235, v235, v85, v86\n\t"
    "v_max3_f32 v236, v236, v101, v102\n\t"
    "v_max3_f32 v237, v237, v117, v118\n\t"
    "ds_read_b128 a[216:219], v225 offset:6144\n\t"
    "v_mfma_f32_32x32x16_bf16 a[64:79], v[168:171], v[0:3], a[64:79]\n\t"
    "v_max3_f32 v234, v234, v71, v72\n\t"
    "v_max3_f32 v235, v235, v87, v88\n\t"
    "v_max3_f32 v236, v236, v103, v104\n\t"
    "v_max3_f32 v237, v237, v119, v120\n\t"
    "ds_read_b128 a[220:223], v225 offset:7168\n\t"
    "v_mfma_f32_32x32x16_bf16 a[80:95], v[168:171], v[16:19], a[80:95]\n\t"
    "v_max3_f32 v234, v234, v73, v74\n\t"
    "v_max3_f32 v235, v235, v89, v90\n\t"
    "v_max3_f32 v236, v236, v105, v106\n\t"
    "v_max3_f32 v237, v237, v121, v122\n\t"
    "ds_read_b128 a[224:227], v225 offset:8192\n\t"
    "v_mfma_f32_32x32x16_bf16 a[96:111], v[172:175], v[0:3], a[96:111]\n\t"
    "v_max3_f32 v234, v234, v75, v76\n\t"
    "v_max3_f32 v235, v235, v91, v92\n\t"
    "v_max3_f32 v236, v236, v107, v108\n\t"
    "v_max3_f32 v237, v237, v123, v124\n\t"
    "ds_read_b128 a[228:231], v225 offset:9216\n\t"
    "v_mfma_f32_32x32x16_bf16 a[112:127], v[172:175], v[16:19], a[112:127]\n\t"
    "v_max3_f32 v234, v234, v77, v78\n\t"
    "v_max3_f32 v235, v235, v93, v94\n\t"
    "v_max3_f32 v236, v236, v109, v110\n\t"
    "v_max3_f32 v237, v237, v125, v126\n\t"
    "ds_read_b128 a[232:235], v225 offset:10240\n\t"
    "v_mfma_f32_32x32x16_bf16 a[0:15], v[176:179], v[8:11], a[0:15]\n\t"
    "v_max_f32 v234, v234, v79\n\t"
    "v_max_f32 v235, v235, v95\n\t"
    "v_max_f32 v236, v236, v111\n\t"
    "v_max_f32 v237, v237, v127\n\t"
    "ds_read_b128 a[236:239], v225 offset:11264\n\t"
    "v_mfma_f32_32x32x16_bf16 a[16:31], v[176:179], v[24:27], a[16:31]\n\t"
    "v_max3_f32 v238, v234, v235, v236\n\t"
    "v_max_f32 v238, v238, v237\n\t"
    "v_cmp_lt_f32 vcc, s60, v238\n\t"
    "s_cbranch_vccnz Lst_84_%=\n\t"
    "Lbk_85_%=:\n\t"
    "s_and_b32 s67, s57, 0xffff\n\t"
    "s_or_b32 m0, s67, 0x10000\n\t"
    "v_mfma_f32_32x32x16_bf16 a[32:47], v[180:183], v[8:11], a[32:47]\n\t"
    "ds_read_b128 a[240:243], v225 offset:12288\n\t"
    "buffer_load_dwordx4 v224, s[40:43], s57 offen offset:0 lds\n\t"
    "v_exp_f32 v64, v64\n\t"
    "v_mfma_f32_32x32x16_bf16 a[48:63], v[180:183], v[24:27], a[48:63]\n\t"
    "ds_read_b128 a[244:247], v225 offset:13312\n\t"
    "global_store_dwordx4 v224, v[244:247], s[98:99] offset:0 sc1\n\t"
    "v_exp_f32 v65, v65\n\t"
    "v_add_f32 v228, v228, v64\n\t"
    "v_mfma_f32_32x32x16_bf16 a[64:79], v[184:187], v[8:11], a[64:79]\n\t"
    "ds_read_b128 a[248:251], v225 offset:14336\n\t"
    "v_exp_f32 v66, v66\n\t"
    "v_exp_f32 v67, v67\n\t"
    "v_add_f32 v229, v229, v65\n\t"
    "v_mfma_f32_32x32x16_bf16 a[80:95], v[184:187], v[24:27], a[80:95]\n\t"
    "ds_read_b128 a[252:255], v225 offset:15360\n\t"
    "buffer_load_dwordx4 v224, s[40:43], s57 offen offset:1024 lds\n\t"
    "v_exp_f32 v68, v68\n\t"
    "v_add_f32 v228, v228, v66\n\t"
    "v_mfma_f32_32x32x16_bf16 a[96:111], v[188:191], v[8:11], a[96:111]\n\t"
    "v_exp_f32 v69, v69\n\t"
    "v_exp_f32 v70, v70\n\t"
    "v_add_f32 v229, v229, v67\n\t"
    "v_add_f32 v228, v228, v68\n\t"
    "v_mfma_f32_32x32x16_bf16 a[112:127], v[188:191], v[24:27], a[112:127]\n\t"
    "v_exp_f32 v71, v71\n\t"
    "v_exp_f32 v80, v80\n\t"
    "v_add_f32 v229, v229, v69\n\t"
    "v_add_f32 v228, v228, v70\n\t"
    "v_mfma_f32_32x32x16_bf16 a[0:15], v[192:195], v[32:35], a[0:15]\n\t"
    "buffer_load_dwordx4 v224, s[40:43], s57 offen offset:2048 lds\n\t"
    "v_exp_f32 v81, v81\n\t"
    "v_exp_f32 v82, v82\n\t"
    "v_mfma_f32_32x32x16_bf16 a[16:31], v[192:195], v[48:51], a[16:31]\n\t"
    "global_store_dwordx4 v224, v[244:247], s[98:99] offset:1024 sc1\n\t"
    "v_exp_f32 v83, v83\n\t"
    "v_exp_f32 v84, v84\n\t"
    "v_mfma_f32_32x32x16_bf16 a[32:47], v[196:199], v[32:35], a[32:47]\n\t"
    "v_exp_f32 v85, v85\n\t"
    "v_exp_f32 v86, v86\n\t"
    "v_add_f32 v229, v229, v71\n\t"
    "v_add_f32 v230, v230, v80\n\t"
    "v_mfma_f32_32x32x16_bf16 a[48:63], v[196:199], v[48:51], a[48:63]\n\t"
    "buffer_load_dwordx4 v224, s[40:43], s57 offen offset:3072 lds\n\t"
    "v_exp_f32 v87, v87\n\t"
    "v_exp_f32 v72, v72\n\t"
    "v_mfma_f32_32x32x16_bf16 a[64:79], v[200:203], v[32:35], a[64:79]\n\t"
    "v_exp_f32 v73, v73\n\t"
    "v_exp_f32 v74, v74\n\t"
    "v_add_f32 v231, v231, v81\n\t"
    "v_add_f32 v230, v230, v82\n\t"
    "v_mfma_f32_32x32x16_bf16 a[80:95], v[200:203], v[48:51], a[80:95]\n\t"
    "s_add_u32 m0, m0, 0x1000\n\t"
    "s_add_u32 s68, s57, 0x1000\n\t"
    "v_exp_f32 v75, v75\n\t"
    "v_exp_f32 v76, v76\n\t"
    "v_mfma_f32_32x32x16_bf16 a[96:111], v[204:207], v[32:35], a[96:111]\n\t"
    "buffer_load_dwordx4 v224, s[40:43], s68 offen offset:0 lds\n\t"
    "v_exp_f32 v77, v77\n\t"
    "v_exp_f32 v78, v78\n\t"
    "v_mfma_f32_32x32x16_bf16 a[112:127], v[204:207], v[48:51], a[112:127]\n\t"
    "global_store_dwordx4 v224, v[244:247], s[98:99] offset:2048 sc1\n\t"
    "v_exp_f32 v79, v79\n\t"
    "v_exp_f32 v88, v88\n\t"
    "v_mfma_f32_32x32x16_bf16 a[0:15], v[208:211], v[40:43], a[0:15]\n\t"
    "v_exp_f32 v89, v89\n\t"
    "v_exp_f32 v90, v90\n\t"
    "v_add_f32 v231, v231, v83\n\t"
    "v_add_f32 v230, v230, v84\n\t"
    "v_mfma_f32_32x32x16_bf16 a[16:31], v[208:211], v[56:59], a[16:31]\n\t"
    "buffer_load_dwordx4 v224, s[40:43], s68 offen offset:1024 lds\n\t"
    "v_exp_f32 v91, v91\n\t"
    "v_add_f32 v231, v231, v85\n\t"
    "v_add_f32 v230, v230, v86\n\t"
    "v_mfma_f32_32x32x16_bf16 a[32:47], v[212:215], v[40:43], a[32:47]\n\t"
    "v_add_f32 v231, v231, v87\n\t"
    "v_add_f32 v228, v228, v72\n\t"
    "v_add_f32 v229, v229, v73\n\t"
    "v_add_f32 v228, v228, v74\n\t"
    "v_add_f32 v229, v229, v75\n\t"
    "v_add_f32 v228, v228, v76\n\t"
    "v_mfma_f32_32x32x16_bf16 a[48:63], v[212:215], v[56:59], a[48:63]\n\t"
    "v_add_f32 v229, v229, v77\n\t"
    "v_add_f32 v228, v228, v78\n\t"
    "v_add_f32 v229, v229, v79\n\t"
    "v_add_f32 v230, v230, v88\n\t"
    "v_add_f32 v231, v231, v89\n\t"
    "v_add_f32 v230, v230, v90\n\t"
    "v_mfma_f32_32x32x16_bf16 a[64:79], v[216:219], v[40:43], a[64:79]\n\t"
    "buffer_load_dwordx4 v224, s[40:43], s68 offen offset:2048 lds\n\t"
    "v_add_f32 v231, v231, v91\n\t"
    "v_mfma_f32_32x32x16_bf16 a[80:95], v[216:219], v[56:59], a[80:95]\n\t"
    "global_store_dwordx4 v224, v[244:247], s[98:99] offset:3072 sc1\n\t"
    "v_mfma_f32_32x32x16_bf16 a[96:111], v[220:223], v[40:43], a[96:111]\n\t"
    "s_add_u32 s98, s98, 0x1000\n\t"
    "s_addc_u32 s99, s99, 0\n\t"
    "v_mfma_f32_32x32x16_bf16 a[112:127], v[220:223], v[56:59], a[112:127]\n\t"
    "buffer_load_dwordx4 v224, s[40:43], s68 offen offset:3072 lds\n\t"
    "s_mov_b32 s56, s57\n\t"
    "s_mov_b32 s57, s58\n\t"
    "s_add_u32 s58, s58, 0x8000\n\t"
    "s_cmp_lg_u32 s62, 0\n\t"
    "s_cbranch_scc1 Lst_86_%=\n\t"
    "Lbk_87_%=:\n\t"
    "s_cmp_eq_u32 s52, s64\n\t"
    "s_cbranch_scc0 Lin_88_%=\n\t"
    "v_sub_f32 v242, 0, v250\n\t"
    "v_sub_f32 v243, 0, v251\n\t"
    "v_mov_b32 v232, v250\n\t"
    "v_mov_b32 v233, v251\n\t"
    "v_pk_mov_b32 v[128:129], v[242:243], v[242:243] op_sel:[0,0]\n\t"
    "v_pk_mov_b32 v[130:131], v[242:243], v[242:243] op_sel:[0,0]\n\t"
    "v_pk_mov_b32 v[132:133], v[242:243], v[242:243] op_sel:[0,0]\n\t"
    "v_pk_mov_b32 v[134:135], v[242:243], v[242:243] op_sel:[0,0]\n\t"
    "v_pk_mov_b32 v[136:137], v[242:243], v[242:243] op_sel:[0,0]\n\t"
    "v_pk_mov_b32 v[138:139], v[242:243], v[242:243] op_sel:[0,0]\n\t"
    "v_pk_mov_b32 v[140:141], v[242:243], v[242:243] op_sel:[0,0]\n\t"
    "v_pk_mov_b32 v[142:143], v[242:243], v[242:243] op_sel:[0,0]\n\t"
    "v_pk_mov_b32 v[144:145], v[242:243], v[242:243] op_sel:[1,1]\n\t"
    "v_pk_mov_b32 v[146:147], v[242:243], v[242:243] op_sel:[1,1]\n\t"
    "v_pk_mov_b32 v[148:149], v[242:243], v[242:243] op_sel:[1,1]\n\t"
    "v_pk_mov_b32 v[150:151], v[242:243], v[242:243] op_sel:[1,1]\n\t"
    "v_pk_mov_b32 v[152:153], v[242:243], v[242:243] op_sel:[1,1]\n\t"
    "v_pk_mov_b32 v[154:155], v[242:243], v[242:243] op_sel:[1,1]\n\t"
    "v_pk_mov_b32 v[156:157], v[242:243], v[242:243] op_sel:[1,1]\n\t"
    "v_pk_mov_b32 v[158:159], v[242:243], v[242:243] op_sel:[1,1]\n\t"
    "s_mov_b32 s93, 2\n\t"
    "s_mov_b32 s63, s92\n\t"
    "Lin_88_%=:\n\t"
    "s_waitcnt vmcnt(20) lgkmcnt(0)\n\t"
    "s_barrier\n\t"
    "s_add_u32 s52, s52, 1\n\t"
    "v_mfma_f32_32x32x16_bf16 v[0:15], a[192:195], a[128:131], v[128:143]\n\t"
    "ds_read_b128 v[160:163], v226 offset:0\n\t"
    "s_and_b32 m0, s58, 0xffff\n\t"
    "v_exp_f32 v92, v92\n\t"
    "v_exp_f32 v93, v93\n\t"
    "v_mfma_f32_32x32x16_bf16 v[16:31], a[192:195], a[160:163], v[144:159]\n\t"
    "ds_read_b128 v[164:167], v226 offset:2048\n\t"
    "buffer_load_dwordx4 v224, s[36:39], s58 offen offset:0 lds\n\t"
    "v_exp_f32 v94, v94\n\t"
    "v_add_f32 v230, v230, v92\n\t"
    "v_mfma_f32_32x32x16_bf16 v[32:47], a[224:227], a[128:131], v[128:143]\n\t"
    "ds_read_b128 v[168:171], v226 offset:4096\n\t"
    "v_exp_f32 v95, v95\n\t"
    "v_exp_f32 v96, v96\n\t"
    "v_add_f32 v231, v231, v93\n\t"
    "v_mfma_f32_32x32x16_bf16 v[48:63], a[224:227], a[160:163], v[144:159]\n\t"
    "ds_read_b128 v[172:175], v226 offset:6144\n\t"
    "v_exp_f32 v97, v97\n\t"
    "v_exp_f32 v98, v98\n\t"
    "v_add_f32 v230, v230, v94\n\t"
    "v_mfma_f32_32x32x16_bf16 v[0:15], a[196:199], a[132:135], v[0:15]\n\t"
    "ds_read_b128 v[176:179], v226 offset:1024\n\t"
    "v_exp_f32 v99, v99\n\t"
    "v_exp_f32 v100, v100\n\t"
    "v_add_f32 v231, v231, v95\n\t"
    "v_mfma_f32_32x32x16_bf16 v[16:31], a[196:199], a[164:167], v[16:31]\n\t"
    "ds_read_b128 v[180:183], v226 offset:3072\n\t"
    "buffer_load_dwordx4 v224, s[36:39], s58 offen offset:1024 lds\n\t"
    "v_exp_f32 v101, v101\n\t"
    "v_add_f32 v228, v228, v96\n\t"
    "v_mfma_f32_32x32x16_bf16 v[32:47], a[228:231], a[132:135], v[32:47]\n\t"
    "ds_read_b128 v[184:187], v226 offset:5120\n\t"
    "v_exp_f32 v102, v102\n\t"
    "v_exp_f32 v103, v103\n\t"
    "v_add_f32 v229, v229, v97\n\t"
    "v_mfma_f32_32x32x16_bf16 v[48:63], a[228:231], a[164:167], v[48:63]\n\t"
    "ds_read_b128 v[188:191], v226 offset:7168\n\t"
    "v_exp_f32 v112, v112\n\t"
    "v_exp_f32 v113, v113\n\t"
    "v_add_f32 v228, v228, v98\n\t"
    "v_mfma_f32_32x32x16_bf16 v[0:15], a[200:203], a[136:139], v[0:15]\n\t"
    "ds_read_b128 v[192:195], v226 offset:8192\n\t"
    "v_exp_f32 v114, v114\n\t"
    "v_exp_f32 v115, v115\n\t"
    "v_add_f32 v229, v229, v99\n\t"
    "v_mfma_f32_32x32x16_bf16 v[16:31], a[200:203], a[168:171], v[16:31]\n\t"
    "ds_read_b128 v[196:199], v226 offset:10240\n\t"
    "buffer_load_dwordx4 v224, s[36:39], s58 offen offset:2048 lds\n\t"
    "v_exp_f32 v116, v116\n\t"
    "v_add_f32 v228, v228, v100\n\t"
    "v_mfma_f32_32x32x16_bf16 v[32:47], a[232:235], a[136:139], v[32:47]\n\t"
    "ds_read_b128 v[200:203], v226 offset:12288\n\t"
    "v_exp_f32 v117, v117\n\t"
    "v_exp_f32 v118, v118\n\t"
    "v_add_f32 v229, v229, v101\n\t"
    "v_mfma_f32_32x32x16_bf16 v[48:63], a[232:235], a[168:171], v[48:63]\n\t"
    "ds_read_b128 v[204:207], v226 offset:14336\n\t"
    "v_exp_f32 v119, v119\n\t"
    "v_exp_f32 v104, v104\n\t"
    "v_add_f32 v228, v228, v102\n\t"
    "v_mfma_f32_32x32x16_bf16 v[0:15], a[204:207], a[140:143], v[0:15]\n\t"
    "ds_read_b128 v[208:211], v226 offset:9216\n\t"
    "v_exp_f32 v105, v105\n\t"
    "v_exp_f32 v106, v106\n\t"
    "v_add_f32 v229, v229, v103\n\t"
    "v_mfma_f32_32x32x16_bf16 v[16:31], a[204:207], a[172:175], v[16:31]\n\t"
    "ds_read_b128 v[212:215], v226 offset:11264\n\t"
    "buffer_load_dwordx4 v224, s[36:39], s58 offen offset:3072 lds\n\t"
    "v_exp_f32 v107, v107\n\t"
    "v_add_f32 v230, v230, v112\n\t"
    "v_mfma_f32_32x32x16_bf16 v[32:47], a[236:239], a[140:143], v[32:47]\n\t"
    "ds_read_b128 v[216:219], v226 offset:13312\n\t"
    "v_exp_f32 v108, v108\n\t"
    "v_exp_f32 v109, v109\n\t"
    "v_add_f32 v231, v231, v113\n\t"
    "v_mfma_f32_32x32x16_bf16 v[48:63], a[236:239], a[172:175], v[48:63]\n\t"
    "ds_read_b128 v[220:223], v226 offset:15360\n\t"
    "v_exp_f32 v110, v110\n\t"
    "v_exp_f32 v111, v111\n\t"
    "v_add_f32 v230, v230, v114\n\t"
    "v_mfma_f32_32x32x16_bf16 v[0:15], a[208:211], a[144:147], v[0:15]\n\t"
    "s_add_u32 m0, m0, 0x1000\n\t"
    "s_add_u32 s68, s58, 0x1000\n\t"
    "v_exp_f32 v120, v120\n\t"
    "v_exp_f32 v121, v121\n\t"
    "v_mfma_f32_32x32x16_bf16 v[16:31], a[208:211], a[176:179], v[16:31]\n\t"
    "buffer_load_dwordx4 v224, s[36:39], s68 offen offset:0 lds\n\t"
    "v_exp_f32 v122, v122\n\t"
    "v_exp_f32 v123, v123\n\t"
    "v_mfma_f32_32x32x16_bf16 v[32:47], a[240:243], a[144:147], v[32:47]\n\t"
    "v_exp_f32 v124, v124\n\t"
    "v_exp_f32 v125, v125\n\t"
    "v_add_f32 v231, v231, v115\n\t"
    "v_add_f32 v230, v230, v116\n\t"
    "v_mfma_f32_32x32x16_bf16 v[48:63], a[240:243], a[176:179], v[48:63]\n\t"
    "v_exp_f32 v126, v126\n\t"
    "v_exp_f32 v127, v127\n\t"
    "v_add_f32 v231, v231, v117\n\t"
    "v_add_f32 v230, v230, v118\n\t"
    "v_mfma_f32_32x32x16_bf16 v[0:15], a[212:215], a[148:151], v[0:15]\n\t"
    "s_and_b32 s66, s57, 0xc000\n\t"
    "v_add_f32 v231, v231, v119\n\t"
    "v_add_f32 v228, v228, v104\n\t"
    "v_add_f32 v229, v229, v105\n\t"
    "v_add_f32 v228, v228, v106\n\t"
    "v_add_f32 v229, v229, v107\n\t"
    "v_mfma_f32_32x32x16_bf16 v[16:31], a[212:215], a[180:183], v[16:31]\n\t"
    "v_add_u32 v225, s66, v224\n\t"
    "buffer_load_dwordx4 v224, s[36:39], s68 offen offset:1024 lds\n\t"
    "v_add_f32 v228, v228, v108\n\t"
    "v_add_f32 v229, v229, v109\n\t"
    "v_add_f32 v228, v228, v110\n\t"
    "v_mfma_f32_32x32x16_bf16 v[32:47], a[244:247], a[148:151], v[32:47]\n\t"
    "v_add_f32 v229, v229, v111\n\t"
    "v_add_f32 v230, v230, v120\n\t"
    "v_add_f32 v231, v231, v121\n\t"
    "v_add_f32 v230, v230, v122\n\t"
    "v_add_f32 v231, v231, v123\n\t"
    "v_add_f32 v230, v230, v124\n\t"
    "v_mfma_f32_32x32x16_bf16 v[48:63], a[244:247], a[180:183], v[48:63]\n\t"
    "v_add_f32 v231, v231, v125\n\t"
    "v_add_f32 v230, v230, v126\n\t"
    "v_add_f32 v231, v231, v127\n\t"
    "v_cvt_pk_bf16_f32 v64, v64, v65\n\t"
    "v_cvt_pk_bf16_f32 v65, v66, v67\n\t"
    "v_cvt_pk_bf16_f32 v66, v68, v69\n\t"
    "v_mfma_f32_32x32x16_bf16 v[0:15], a[216:219], a[152:155], v[0:15]\n\t"
    "v_cvt_pk_bf16_f32 v67, v70, v71\n\t"
    "v_cvt_pk_bf16_f32 v80, v80, v81\n\t"
    "v_cvt_pk_bf16_f32 v81, v82, v83\n\t"
    "v_cvt_pk_bf16_f32 v82, v84, v85\n\t"
    "v_cvt_pk_bf16_f32 v83, v86, v87\n\t"
    "v_cvt_pk_bf16_f32 v72, v72, v73\n\t"
    "v_mfma_f32_32x32x16_bf16 v[16:31], a[216:219], a[184:187], v[16:31]\n\t"
    "buffer_load_dwordx4 v224, s[36:39], s68 offen offset:2048 lds\n\t"
    "v_cvt_pk_bf16_f32 v73, v74, v75\n\t"
    "v_cvt_pk_bf16_f32 v74, v76, v77\n\t"
    "v_cvt_pk_bf16_f32 v75, v78, v79\n\t"
    "v_cvt_pk_bf16_f32 v88, v88, v89\n\t"
    "v_mfma_f32_32x32x16_bf16 v[32:47], a[248:251], a[152:155], v[32:47]\n\t"
    "v_cvt_pk_bf16_f32 v89, v90, v91\n\t"
    "v_cvt_pk_bf16_f32 v90, v92, v93\n\t"
    "v_cvt_pk_bf16_f32 v91, v94, v95\n\t"
    "v_cvt_pk_bf16_f32 v96, v96, v97\n\t"
    "v_cvt_pk_bf16_f32 v97, v98, v99\n\t"
    "v_cvt_pk_bf16_f32 v98, v100, v101\n\t"
    "v_mfma_f32_32x32x16_bf16 v[48:63], a[248:251], a[184:187], v[48:63]\n\t"
    "v_cvt_pk_bf16_f32 v99, v102, v103\n\t"
    "v_cvt_pk_bf16_f32 v112, v112, v113\n\t"
    "v_cvt_pk_bf16_f32 v113, v114, v115\n\t"
    "v_cvt_pk_bf16_f32 v114, v116, v117\n\t"
    "v_cvt_pk_bf16_f32 v115, v118, v119\n\t"
    "v_cvt_pk_bf16_f32 v104, v104, v105\n\t"
    "v_mfma_f32_32x32x16_bf16 v[0:15], a[220:223], a[156:159], v[0:15]\n\t"
    "v_cvt_pk_bf16_f32 v105, v106, v107\n\t"
    "v_cvt_pk_bf16_f32 v106, v108, v109\n\t"
    "v_cvt_pk_bf16_f32 v107, v110, v111\n\t"
    "v_cvt_pk_bf16_f32 v120, v120, v121\n\t"
    "v_cvt_pk_bf16_f32 v121, v122, v123\n\t"
    "v_cvt_pk_bf16_f32 v122, v124, v125\n\t"
    "v_mfma_f32_32x32x16_bf16 v[16:31], a[220:223], a[188:191], v[16:31]\n\t"
    "buffer_load_dwordx4 v224, s[36:39], s68 offen offset:3072 lds\n\t"
    "v_cvt_pk_bf16_f32 v123, v126, v127\n\t"
    "v_mfma_f32_32x32x16_bf16 v[32:47], a[252:255], a[156:159], v[32:47]\n\t"
    "v_mfma_f32_32x32x16_bf16 v[48:63], a[252:255], a[188:191], v[48:63]\n\t"
    "s_waitcnt vmcnt(20) lgkmcnt(0)\n\t"
    "s_barrier\n\t"
    "v_mfma_f32_32x32x16_bf16 a[0:15], v[160:163], v[64:67], a[0:15]\n\t"
    "ds_read_b128 a[192:195], v225 offset:0\n\t"
    "ds_read_b128 a[196:199], v225 offset:1024\n\t"
    "ds_read_b128 a[200:203], v225 offset:2048\n\t"
    "ds_read_b128 a[204:207], v225 offset:3072\n\t"
    "s_and_b32 s69, s56, 0xc000\n\t"
    "v_add_u32 v226, s69, v227\n\t"
    "v_mfma_f32_32x32x16_bf16 a[16:31], v[160:163], v[80:83], a[16:31]\n\t"
    "v_max3_f32 v234, v0, v1, v2\n\t"
    "v_max3_f32 v235, v16, v17, v18\n\t"
    "v_max3_f32 v236, v32, v33, v34\n\t"
    "v_max3_f32 v237, v48, v49, v50\n\t"
    "ds_read_b128 a[208:211], v225 offset:4096\n\t"
    "v_mfma_f32_32x32x16_bf16 a[32:47], v[164:167], v[64:67], a[32:47]\n\t"
    "v_max3_f32 v234, v234, v3, v4\n\t"
    "v_max3_f32 v235, v235, v19, v20\n\t"
    "v_max3_f32 v236, v236, v35, v36\n\t"
    "v_max3_f32 v237, v237, v51, v52\n\t"
    "ds_read_b128 a[212:215], v225 offset:5120\n\t"
    "v_mfma_f32_32x32x16_bf16 a[48:63], v[164:167], v[80:83], a[48:63]\n\t"
    "v_max3_f32 v234, v234, v5, v6\n\t"
    "v_max3_f32 v235, v235, v21, v22\n\t"
    "v_max3_f32 v236, v236, v37, v38\n\t"
    "v_max3_f32 v237, v237, v53, v54\n\t"
    "ds_read_b128 a[216:219], v225 offset:6144\n\t"
    "v_mfma_f32_32x32x16_bf16 a[64:79], v[168:171], v[64:67], a[64:79]\n\t"
    "v_max3_f32 v234, v234, v7, v8\n\t"
    "v_max3_f32 v235, v235, v23, v24\n\t"
    "v_max3_f32 v236, v236, v39, v40\n\t"
    "v_max3_f32 v237, v237, v55, v56\n\t"
    "ds_read_b128 a[220:223], v225 offset:7168\n\t"
    "v_mfma_f32_32x32x16_bf16 a[80:95], v[168:171], v[80:83], a[80:95]\n\t"
    "v_max3_f32 v234, v234, v9, v10\n\t"
    "v_max3_f32 v235, v235, v25, v26\n\t"
    "v_max3_f32 v236, v236, v41, v42\n\t"
    "v_max3_f32 v237, v237, v57, v58\n\t"
    "ds_read_b128 a[224:227], v225 offset:8192\n\t"
    "v_mfma_f32_32x32x16_bf16 a[96:111], v[172:175], v[64:67], a[96:111]\n\t"
    "v_max3_f32 v234, v234, v11, v12\n\t"
    "v_max3_f32 v235, v235, v27, v28\n\t"
    "v_max3_f32 v236, v236, v43, v44\n\t"
    "v_max3_f32 v237, v237, v59, v60\n\t"
    "ds_read_b128 a[228:231], v225 offset:9216\n\t"
    "v_mfma_f32_32x32x16_bf16 a[112:127], v[172:175], v[80:83], a[112:127]\n\t"
    "v_max3_f32 v234, v234, v13, v14\n\t"
    "v_max3_f32 v235, v235, v29, v30\n\t"
    "v_max3_f32 v236, v236, v45, v46\n\t"
    "v_max3_f32 v237, v237, v61, v62\n\t"
    "ds_read_b128 a[232:235], v225 offset:10240\n\t"
    "v_mfma_f32_32x32x16_bf16 a[0:15], v[176:179], v[72:75], a[0:15]\n\t"
    "v_max_f32 v234, v234, v15\n\t"
    "v_max_f32 v235, v235, v31\n\t"
    "v_max_f32 v236, v236, v47\n\t"
    "v_max_f32 v237, v237, v63\n\t"
    "ds_read_b128 a[236:239], v225 offset:11264\n\t"
    "v_mfma_f32_32x32x16_bf16 a[16:31], v[176:179], v[88:91], a[16:31]\n\t"
    "v_max3_f32 v238, v234, v235, v236\n\t"
    "v_max_f32 v238, v238, v237\n\t"
    "v_cmp_lt_f32 vcc, s60, v238\n\t"
    "s_cbranch_vccnz Lst_89_%=\n\t"
    "Lbk_90_%=:\n\t"
    "s_cmp_eq_u32 s93, 0\n\t"
    "s_cbranch_scc1 Lin_91_%=\n\t"
    "v_add_f32 v250, v228, v229\n\t"
    "v_mov_b32 v228, 0\n\t"
    "v_mov_b32 v229, 0\n\t"
    "v_add_f32 v251, v230, v231\n\t"
    "v_mov_b32 v230, 0\n\t"
    "v_mov_b32 v231, 0\n\t"
    "s_mov_b32 s93, 0\n\t"
    "Lin_91_%=:\n\t"
    "s_and_b32 s67, s57, 0xffff\n\t"
    "s_or_b32 m0, s67, 0x10000\n\t"
    "v_mfma_f32_32x32x16_bf16 a[32:47], v[180:183], v[72:75], a[32:47]\n\t"
    "ds_read_b128 a[240:243], v225 offset:12288\n\t"
    "buffer_load_dwordx4 v224, s[40:43], s57 offen offset:0 lds\n\t"
    "v_exp_f32 v0, v0\n\t"
    "v_mfma_f32_32x32x16_bf16 a[48:63], v[180:183], v[88:91], a[48:63]\n\t"
    "ds_read_b128 a[244:247], v225 offset:13312\n\t"
    "v_exp_f32 v1, v1\n\t"
    "v_exp_f32 v2, v2\n\t"
    "v_add_f32 v228, v228, v0\n\t"
    "v_mfma_f32_32x32x16_bf16 a[64:79], v[184:187], v[72:75], a[64:79]\n\t"
    "ds_read_b128 a[248:251], v225 offset:14336\n\t"
    "v_exp_f32 v3, v3\n\t"
    "v_exp_f32 v4, v4\n\t"
    "v_add_f32 v229, v229, v1\n\t"
    "v_mfma_f32_32x32x16_bf16 a[80:95], v[184:187], v[88:91], a[80:95]\n\t"
    "ds_read_b128 a[252:255], v225 offset:15360\n\t"
    "buffer_load_dwordx4 v224, s[40:43], s57 offen offset:1024 lds\n\t"
    "v_exp_f32 v5, v5\n\t"
    "v_add_f32 v228, v228, v2\n\t"
    "v_mfma_f32_32x32x16_bf16 a[96:111], v[188:191], v[72:75], a[96:111]\n\t"
    "v_exp_f32 v6, v6\n\t"
    "v_exp_f32 v7, v7\n\t"
    "v_add_f32 v229, v229, v3\n\t"
    "v_add_f32 v228, v228, v4\n\t"
    "v_mfma_f32_32x32x16_bf16 a[112:127], v[188:191], v[88:91], a[112:127]\n\t"
    "v_exp_f32 v16, v16\n\t"
    "v_exp_f32 v17, v17\n\t"
    "v_add_f32 v229, v229, v5\n\t"
    "v_add_f32 v228, v228, v6\n\t"
    "v_mfma_f32_32x32x16_bf16 a[0:15], v[192:195], v[96:99], a[0:15]\n\t"
    "buffer_load_dwordx4 v224, s[40:43], s57 offen offset:2048 lds\n\t"
    "v_exp_f32 v18, v18\n\t"
    "v_exp_f32 v19, v19\n\t"
    "v_mfma_f32_32x32x16_bf16 a[16:31], v[192:195], v[112:115], a[16:31]\n\t"
    "v_exp_f32 v20, v20\n\t"
    "v_exp_f32 v21, v21\n\t"
    "v_add_f32 v229, v229, v7\n\t"
    "v_add_f32 v230, v230, v16\n\t"
    "v_mfma_f32_32x32x16_bf16 a[32:47], v[196:199], v[96:99], a[32:47]\n\t"
    "v_exp_f32 v22, v22\n\t"
    "v_exp_f32 v23, v23\n\t"
    "v_add_f32 v231, v231, v17\n\t"
    "v_add_f32 v230, v230, v18\n\t"
    "v_mfma_f32_32x32x16_bf16 a[48:63], v[196:199], v[112:115], a[48:63]\n\t"
    "buffer_load_dwordx4 v224, s[40:43], s57 offen offset:3072 lds\n\t"
    "v_exp_f32 v8, v8\n\t"
    "v_exp_f32 v9, v9\n\t"
    "v_mfma_f32_32x32x16_bf16 a[64:79], v[200:203], v[96:99], a[64:79]\n\t"
    "v_exp_f32 v10, v10\n\t"
    "v_exp_f32 v11, v11\n\t"
    "v_add_f32 v231, v231, v19\n\t"
    "v_add_f32 v230, v230, v20\n\t"
    "v_mfma_f32_32x32x16_bf16 a[80:95], v[200:203], v[112:115], a[80:95]\n\t"
    "s_add_u32 m0, m0, 0x1000\n\t"
    "s_add_u32 s68, s57, 0x1000\n\t"
    "v_exp_f32 v12, v12\n\t"
    "v_exp_f32 v13, v13\n\t"
    "v_mfma_f32_32x32x16_bf16 a[96:111], v[204:207], v[96:99], a[96:111]\n\t"
    "buffer_load_dwordx4 v224, s[40:43], s68 offen offset:0 lds\n\t"
    "v_exp_f32 v14, v14\n\t"
    "v_exp_f32 v15, v15\n\t"
    "v_mfma_f32_32x32x16_bf16 a[112:127], v[204:207], v[112:115], a[112:127]\n\t"
    "v_exp_f32 v24, v24\n\t"
    "v_exp_f32 v25, v25\n\t"
    "v_add_f32 v231, v231, v21\n\t"
    "v_add_f32 v230, v230, v22\n\t"
    "v_mfma_f32_32x32x16_bf16 a[0:15], v[208:211], v[104:107], a[0:15]\n\t"
    "v_exp_f32 v26, v26\n\t"
    "v_exp_f32 v27, v27\n\t"
    "v_add_f32 v231, v231, v23\n\t"
    "v_add_f32 v228, v228, v8\n\t"
    "v_mfma_f32_32x32x16_bf16 a[16:31], v[208:211], v[120:123], a[16:31]\n\t"
    "buffer_load_dwordx4 v224, s[40:43], s68 offen offset:1024 lds\n\t"
    "v_add_f32 v229, v229, v9\n\t"
    "v_add_f32 v228, v228, v10\n\t"
    "v_add_f32 v229, v229, v11\n\t"
    "v_add_f32 v228, v228, v12\n\t"
    "v_mfma_f32_32x32x16_bf16 a[32:47], v[212:215], v[104:107], a[32:47]\n\t"
    "v_add_f32 v229, v229, v13\n\t"
    "v_add_f32 v228, v228, v14\n\t"
    "v_add_f32 v229, v229, v15\n\t"
    "v_add_f32 v230, v230, v24\n\t"
    "v_add_f32 v231, v231, v25\n\t"
    "v_add_f32 v230, v230, v26\n\t"
    "v_mfma_f32_32x32x16_bf16 a[48:63], v[212:215], v[120:123], a[48:63]\n\t"
    "v_add_f32 v231, v231, v27\n\t"
    "v_mfma_f32_32x32x16_bf16 a[64:79], v[216:219], v[104:107], a[64:79]\n\t"
    "buffer_load_dwordx4 v224, s[40:43], s68 offen offset:2048 lds\n\t"
    "v_mfma_f32_32x32x16_bf16 a[80:95], v[216:219], v[120:123], a[80:95]\n\t"
    "v_mfma_f32_32x32x16_bf16 a[96:111], v[220:223], v[104:107], a[96:111]\n\t"
    "v_mfma_f32_32x32x16_bf16 a[112:127], v[220:223], v[120:123], a[112:127]\n\t"
    "buffer_load_dwordx4 v224, s[40:43], s68 offen offset:3072 lds\n\t"
    "s_mov_b32 s56, s57\n\t"
    "s_mov_b32 s57, s58\n\t"
    "s_add_u32 s58, s58, 0x8000\n\t"
    "s_cmp_lg_u32 s62, 0\n\t"
    "s_cbranch_scc1 Lst_92_%=\n\t"
    "Lbk_93_%=:\n\t"
    "s_nop 3\n\t"
    "s_sub_u32 s67, s86, 0\n\t"
    "v_add_u32 v164, s67, v224\n\t"
    "s_cmp_lt_u32 s89, 2\n\t"
    "s_cbranch_scc1 Lrx_94_%=\n\t"
    "ds_write_b128 v164, a[16:19] offset:0\n\t"
    "v_mov_b32 v241, v251\n\t"
    "s_nop 1\n\t"
    "v_permlane32_swap_b32 v251, v241\n\t"
    "s_nop 0\n\t"
    "ds_write_b128 v164, a[20:23] offset:1024\n\t"
    "v_add_f32 v251, v251, v241\n\t"
    "v_rcp_f32 v162, v251\n\t"
    "v_mov_b32 v240, v250\n\t"
    "s_nop 1\n\t"
    "ds_write_b128 v164, a[24:27] offset:2048\n\t"
    "v_permlane32_swap_b32 v250, v240\n\t"
    "s_nop 0\n\t"
    "v_add_f32 v250, v250, v240\n\t"
    "v_rcp_f32 v160, v250\n\t"
    "ds_write_b32 v164, v162 offset:15360\n\t"
    "v_mov_b32 v240, 0\n\t"
    "v_mov_b32 v241, 0\n\t"
    "v_mov_b32 v242, 0\n\t"
    "v_mov_b32 v243, 0\n\t"
    "ds_write_b128 v164, a[28:31] offset:3072\n\t"
    "s_add_u32 s66, s87, 0\n\t"
    "v_accvgpr_read_b32 v64, a0\n\t"
    "v_accvgpr_read_b32 v65, a1\n\t"
    "v_accvgpr_read_b32 v66, a2\n\t"
    "ds_write_b128 v164, a[48:51] offset:4096\n\t"
    "v_accvgpr_read_b32 v67, a3\n\t"
    "v_accvgpr_read_b32 v68, a4\n\t"
    "v_accvgpr_read_b32 v69, a5\n\t"
    "v_accvgpr_read_b32 v70, a6\n\t"
    "ds_write_b128 v164, a[52:55] offset:5120\n\t"
    "v_accvgpr_read_b32 v71, a7\n\t"
    "v_accvgpr_read_b32 v72, a8\n\t"
    "v_accvgpr_read_b32 v73, a9\n\t"
    "v_accvgpr_read_b32 v74, a10\n\t"
    "ds_write_b128 v164, a[56:59] offset:6144\n\t"
    "v_accvgpr_read_b32 v75, a11\n\t"
    "v_accvgpr_read_b32 v76, a12\n\t"
    "v_accvgpr_read_b32 v77, a13\n\t"
    "v_accvgpr_read_b32 v78, a14\n\t"
    "ds_write_b128 v164, a[60:63] offset:7168\n\t"
    "v_accvgpr_read_b32 v79, a15\n\t"
    "v_pk_mul_f32 v[64:65], v[64:65], v[160:161] op_sel_hi:[1,0]\n\t"
    "v_pk_mul_f32 v[66:67], v[66:67], v[160:161] op_sel_hi:[1,0]\n\t"
    "v_pk_mul_f32 v[68:69], v[68:69], v[160:161] op_sel_hi:[1,0]\n\t"
    "ds_write_b128 v164, a[80:83] offset:8192\n\t"
    "v_pk_mul_f32 v[70:71], v[70:71], v[160:161] op_sel_hi:[1,0]\n\t"
    "v_pk_mul_f32 v[72:73], v[72:73], v[160:161] op_sel_hi:[1,0]\n\t"
    "v_pk_mul_f32 v[74:75], v[74:75], v[160:161] op_sel_hi:[1,0]\n\t"
    "v_pk_mul_f32 v[76:77], v[76:77], v[160:161] op_sel_hi:[1,0]\n\t"
    "ds_write_b128 v164, a[84:87] offset:9216\n\t"
    "v_pk_mul_f32 v[78:79], v[78:79], v[160:161] op_sel_hi:[1,0]\n\t"
    "v_mfma_f32_32x32x16_bf16 a[0:15], v[240:243], v[240:243], 0\n\t"
    "v_cvt_pk_bf16_f32 v64, v64, v65\n\t"
    "v_cvt_pk_bf16_f32 v65, v66, v67\n\t"
    "ds_write_b128 v164, a[88:91] offset:10240\n\t"
    "v_cvt_pk_bf16_f32 v66, v68, v69\n\t"
    "v_cvt_pk_bf16_f32 v67, v70, v71\n\t"
    "v_cvt_pk_bf16_f32 v72, v72, v73\n\t"
    "v_cvt_pk_bf16_f32 v73, v74, v75\n\t"
    "ds_write_b128 v164, a[92:95] offset:11264\n\t"
    "v_cvt_pk_bf16_f32 v74, v76, v77\n\t"
    "v_cvt_pk_bf16_f32 v75, v78, v79\n\t"
    "buffer_store_dwordx4 v[64:67], v224, s[48:51], s66 offen offset:0 sc1\n\t"
    "buffer_store_dwordx4 v[72:75], v224, s[48:51], s66 offen offset:1024 sc1\n\t"
    "ds_write_b128 v164, a[112:115] offset:12288\n\t"
    "s_add_u32 s66, s87, 0\n\t"
    "v_accvgpr_read_b32 v80, a32\n\t"
    "v_accvgpr_read_b32 v81, a33\n\t"
    "v_accvgpr_read_b32 v82, a34\n\t"
    "ds_write_b128 v164, a[116:119] offset:13312\n\t"
    "v_accvgpr_read_b32 v83, a35\n\t"
    "v_accvgpr_read_b32 v84, a36\n\t"
    "v_accvgpr_read_b32 v85, a37\n\t"
    "v_accvgpr_read_b32 v86, a38\n\t"
    "v_accvgpr_read_b32 v87, a39\n\t"
    "v_accvgpr_read_b32 v88, a40\n\t"
    "v_accvgpr_read_b32 v89, a41\n\t"
    "v_accvgpr_read_b32 v90, a42\n\t"
    "v_accvgpr_read_b32 v91, a43\n\t"
    "s_waitcnt lgkmcnt(0)\n\t"
    "s_barrier\n\t"
    "v_accvgpr_read_b32 v92, a44\n\t"
    "v_accvgpr_read_b32 v93, a45\n\t"
    "v_accvgpr_read_b32 v94, a46\n\t"
    "v_accvgpr_read_b32 v95, a47\n\t"
    "v_pk_mul_f32 v[80:81], v[80:81], v[160:161] op_sel_hi:[1,0]\n\t"
    "v_pk_mul_f32 v[82:83], v[82:83], v[160:161] op_sel_hi:[1,0]\n\t"
    "v_pk_mul_f32 v[84:85], v[84:85], v[160:161] op_sel_hi:[1,0]\n\t"
    "v_pk_mul_f32 v[86:87], v[86:87], v[160:161] op_sel_hi:[1,0]\n\t"
    "v_pk_mul_f32 v[88:89], v[88:89], v[160:161] op_sel_hi:[1,0]\n\t"
    "v_pk_mul_f32 v[90:91], v[90:91], v[160:161] op_sel_hi:[1,0]\n\t"
    "v_pk_mul_f32 v[92:93], v[92:93], v[160:161] op_sel_hi:[1,0]\n\t"
    "v_pk_mul_f32 v[94:95], v[94:95], v[160:161] op_sel_hi:[1,0]\n\t"
    "v_mfma_f32_32x32x16_bf16 a[32:47], v[240:243], v[240:243], 0\n\t"
    "v_cvt_pk_bf16_f32 v80, v80, v81\n\t"
    "v_cvt_pk_bf16_f32 v81, v82, v83\n\t"
    "v_cvt_pk_bf16_f32 v82, v84, v85\n\t"
    "v_cvt_pk_bf16_f32 v83, v86, v87\n\t"
    "v_cvt_pk_bf16_f32 v88, v88, v89\n\t"
    "v_cvt_pk_bf16_f32 v89, v90, v91\n\t"
    "v_cvt_pk_bf16_f32 v90, v92, v93\n\t"
    "v_cvt_pk_bf16_f32 v91, v94, v95\n\t"
    "buffer_store_dwordx4 v[80:83], v224, s[48:51], s66 offen offset:2048 sc1\n\t"
    "buffer_store_dwordx4 v[88:91], v224, s[48:51], s66 offen offset:3072 sc1\n\t"
    "s_add_u32 s66, s87, 4096\n\t"
    "v_accvgpr_read_b32 v96, a64\n\t"
    "v_accvgpr_read_b32 v97, a65\n\t"
    "v_accvgpr_read_b32 v98, a66\n\t"
    "v_accvgpr_read_b32 v99, a67\n\t"
    "v_accvgpr_read_b32 v100, a68\n\t"
    "v_accvgpr_read_b32 v101, a69\n\t"
    "v_accvgpr_read_b32 v102, a70\n\t"
    "v_accvgpr_read_b32 v103, a71\n\t"
    "v_accvgpr_read_b32 v104, a72\n\t"
    "v_accvgpr_read_b32 v105, a73\n\t"
    "v_accvgpr_read_b32 v106, a74\n\t"
    "v_accvgpr_read_b32 v107, a75\n\t"
    "v_accvgpr_read_b32 v108, a76\n\t"
    "v_accvgpr_read_b32 v109, a77\n\t"
    "v_accvgpr_read_b32 v110, a78\n\t"
    "v_accvgpr_read_b32 v111, a79\n\t"
    "v_pk_mul_f32 v[96:97], v[96:97], v[160:161] op_sel_hi:[1,0]\n\t"
    "v_pk_mul_f32 v[98:99], v[98:99], v[160:161] op_sel_hi:[1,0]\n\t"
    "v_pk_mul_f32 v[100:101], v[100:101], v[160:161] op_sel_hi:[1,0]\n\t"
    "v_pk_mul_f32 v[102:103], v[102:103], v[160:161] op_sel_hi:[1,0]\n\t"
    "v_pk_mul_f32 v[104:105], v[104:105], v[160:161] op_sel_hi:[1,0]\n\t"
    "v_pk_mul_f32 v[106:107], v[106:107], v[160:161] op_sel_hi:[1,0]\n\t"
    "v_pk_mul_f32 v[108:109], v[108:109], v[160:161] op_sel_hi:[1,0]\n\t"
    "v_pk_mul_f32 v[110:111], v[110:111], v[160:161] op_sel_hi:[1,0]\n\t"
    "v_mfma_f32_32x32x16_bf16 a[64:79], v[240:243], v[240:243], 0\n\t"
    "v_cvt_pk_bf16_f32 v96, v96, v97\n\t"
    "v_cvt_pk_bf16_f32 v97, v98, v99\n\t"
    "v_cvt_pk_bf16_f32 v98, v100, v101\n\t"
    "v_cvt_pk_bf16_f32 v99, v102, v103\n\t"
    "v_cvt_pk_bf16_f32 v104, v104, v105\n\t"
    "v_cvt_pk_bf16_f32 v105, v106, v107\n\t"
    "v_cvt_pk_bf16_f32 v106, v108, v109\n\t"
    "v_cvt_pk_bf16_f32 v107, v110, v111\n\t"
    "v_mfma_f32_32x32x16_bf16 a[16:31], v[240:243], v[240:243], 0\n\t"
    "buffer_store_dwordx4 v[96:99], v224, s[48:51], s66 offen offset:0 sc1\n\t"
    "buffer_store_dwordx4 v[104:107], v224, s[48:51], s66 offen offset:1024 sc1\n\t"
    "s_add_u32 s66, s87, 4096\n\t"
    "v_accvgpr_read_b32 v112, a96\n\t"
    "v_accvgpr_read_b32 v113, a97\n\t"
    "v_accvgpr_read_b32 v114, a98\n\t"
    "v_accvgpr_read_b32 v115, a99\n\t"
    "v_accvgpr_read_b32 v116, a100\n\t"
    "v_accvgpr_read_b32 v117, a101\n\t"
    "v_accvgpr_read_b32 v118, a102\n\t"
    "v_accvgpr_read_b32 v119, a103\n\t"
    "v_accvgpr_read_b32 v120, a104\n\t"
    "v_accvgpr_read_b32 v121, a105\n\t"
    "v_accvgpr_read_b32 v122, a106\n\t"
    "v_accvgpr_read_b32 v123, a107\n\t"
    "v_accvgpr_read_b32 v124, a108\n\t"
    "v_accvgpr_read_b32 v125, a109\n\t"
    "v_accvgpr_read_b32 v126, a110\n\t"
    "v_accvgpr_read_b32 v127, a111\n\t"
    "v_pk_mul_f32 v[112:113], v[112:113], v[160:161] op_sel_hi:[1,0]\n\t"
    "v_pk_mul_f32 v[114:115], v[114:115], v[160:161] op_sel_hi:[1,0]\n\t"
    "v_pk_mul_f32 v[116:117], v[116:117], v[160:161] op_sel_hi:[1,0]\n\t"
    "v_pk_mul_f32 v[118:119], v[118:119], v[160:161] op_sel_hi:[1,0]\n\t"
    "v_pk_mul_f32 v[120:121], v[120:121], v[160:161] op_sel_hi:[1,0]\n\t"
    "v_pk_mul_f32 v[122:123], v[122:123], v[160:161] op_sel_hi:[1,0]\n\t"
    "v_pk_mul_f32 v[124:125], v[124:125], v[160:161] op_sel_hi:[1,0]\n\t"
    "v_pk_mul_f32 v[126:127], v[126:127], v[160:161] op_sel_hi:[1,0]\n\t"
    "v_mfma_f32_32x32x16_bf16 a[96:111], v[240:243], v[240:243], 0\n\t"
    "v_cvt_pk_bf16_f32 v112, v112, v113\n\t"
    "v_cvt_pk_bf16_f32 v113, v114, v115\n\t"
    "v_cvt_pk_bf16_f32 v114, v116, v117\n\t"
    "v_cvt_pk_bf16_f32 v115, v118, v119\n\t"
    "v_cvt_pk_bf16_f32 v120, v120, v121\n\t"
    "v_cvt_pk_bf16_f32 v121, v122, v123\n\t"
    "v_cvt_pk_bf16_f32 v122, v124, v125\n\t"
    "v_cvt_pk_bf16_f32 v123, v126, v127\n\t"
    "v_mfma_f32_32x32x16_bf16 a[48:63], v[240:243], v[240:243], 0\n\t"
    "buffer_store_dwordx4 v[112:115], v224, s[48:51], s66 offen offset:2048 sc1\n\t"
    "buffer_store_dwordx4 v[120:123], v224, s[48:51], s66 offen offset:3072 sc1\n\t"
    "s_add_u32 s66, s87, 266240\n\t"
    "v_accvgpr_read_b32 v72, a120\n\t"
    "v_accvgpr_read_b32 v73, a121\n\t"
    "v_accvgpr_read_b32 v74, a122\n\t"
    "v_accvgpr_read_b32 v75, a123\n\t"
    "v_accvgpr_read_b32 v76, a124\n\t"
    "v_accvgpr_read_b32 v77, a125\n\t"
    "v_accvgpr_read_b32 v78, a126\n\t"
    "v_accvgpr_read_b32 v79, a127\n\t"
    "v_pk_mul_f32 v[72:73], v[72:73], v[162:163] op_sel_hi:[1,0]\n\t"
    "v_pk_mul_f32 v[74:75], v[74:75], v[162:163] op_sel_hi:[1,0]\n\t"
    "v_pk_mul_f32 v[76:77], v[76:77], v[162:163] op_sel_hi:[1,0]\n\t"
    "v_pk_mul_f32 v[78:79], v[78:79], v[162:163] op_sel_hi:[1,0]\n\t"
    "v_mfma_f32_32x32x16_bf16 a[112:127], v[240:243], v[240:243], 0\n\t"
    "v_cvt_pk_bf16_f32 v72, v72, v73\n\t"
    "v_cvt_pk_bf16_f32 v73, v74, v75\n\t"
    "v_cvt_pk_bf16_f32 v74, v76, v77\n\t"
    "v_cvt_pk_bf16_f32 v75, v78, v79\n\t"
    "v_mfma_f32_32x32x16_bf16 a[80:95], v[240:243], v[240:243], 0\n\t"
    "buffer_store_dwordx4 v[72:75], v224, s[48:51], s66 offen offset:3072 sc1\n\t"
    "s_nop 1\n\t"
    "s_branch Lin_95_%=\n\t"
    "Lrx_94_%=:\n\t"
    "s_barrier\n\t"
    "ds_read_b32 v162, v164 offset:15360\n\t"
    "ds_read_b128 v[64:67], v164 offset:0\n\t"
    "ds_read_b128 v[68:71], v164 offset:1024\n\t"
    "ds_read_b128 v[72:75], v164 offset:2048\n\t"
    "ds_read_b128 v[76:79], v164 offset:3072\n\t"
    "ds_read_b128 v[80:83], v164 offset:4096\n\t"
    "ds_read_b128 v[84:87], v164 offset:5120\n\t"
    "ds_read_b128 v[88:91], v164 offset:6144\n\t"
    "ds_read_b128 v[92:95], v164 offset:7168\n\t"
    "ds_read_b128 v[96:99], v164 offset:8192\n\t"
    "ds_read_b128 v[100:103], v164 offset:9216\n\t"
    "ds_read_b128 v[104:107], v164 offset:10240\n\t"
    "ds_read_b128 v[108:111], v164 offset:11264\n\t"
    "ds_read_b128 v[112:115], v164 offset:12288\n\t"
    "ds_read_b128 v[116:119], v164 offset:13312\n\t"
    "s_add_u32 s66, s87, 262144\n\t"
    "s_waitcnt lgkmcnt(10)\n\t"
    "v_pk_mul_f32 v[64:65], v[64:65], v[162:163] op_sel_hi:[1,0]\n\t"
    "v_pk_mul_f32 v[66:67], v[66:67], v[162:163] op_sel_hi:[1,0]\n\t"
    "v_pk_mul_f32 v[68:69], v[68:69], v[162:163] op_sel_hi:[1,0]\n\t"
    "v_pk_mul_f32 v[70:71], v[70:71], v[162:163] op_sel_hi:[1,0]\n\t"
    "v_pk_mul_f32 v[72:73], v[72:73], v[162:163] op_sel_hi:[1,0]\n\t"
    "v_pk_mul_f32 v[74:75], v[74:75], v[162:163] op_sel_hi:[1,0]\n\t"
    "v_pk_mul_f32 v[76:77], v[76:77], v[162:163] op_sel_hi:[1,0]\n\t"
    "v_pk_mul_f32 v[78:79], v[78:79], v[162:163] op_sel_hi:[1,0]\n\t"
    "v_cvt_pk_bf16_f32 v64, v64, v65\n\t"
    "v_cvt_pk_bf16_f32 v65, v66, v67\n\t"
    "v_cvt_pk_bf16_f32 v66, v68, v69\n\t"
    "v_cvt_pk_bf16_f32 v67, v70, v71\n\t"
    "v_cvt_pk_bf16_f32 v72, v72, v73\n\t"
    "v_cvt_pk_bf16_f32 v73, v74, v75\n\t"
    "v_cvt_pk_bf16_f32 v74, v76, v77\n\t"
    "v_cvt_pk_bf16_f32 v75, v78, v79\n\t"
    "buffer_store_dwordx4 v[64:67], v224, s[48:51], s66 offen offset:0 sc1\n\t"
    "buffer_store_dwordx4 v[72:75], v224, s[48:51], s66 offen offset:1024 sc1\n\t"
    "s_add_u32 s66, s87, 262144\n\t"
    "s_waitcnt lgkmcnt(6)\n\t"
    "v_pk_mul_f32 v[80:81], v[80:81], v[162:163] op_sel_hi:[1,0]\n\t"
    "v_pk_mul_f32 v[82:83], v[82:83], v[162:163] op_sel_hi:[1,0]\n\t"
    "v_pk_mul_f32 v[84:85], v[84:85], v[162:163] op_sel_hi:[1,0]\n\t"
    "v_pk_mul_f32 v[86:87], v[86:87], v[162:163] op_sel_hi:[1,0]\n\t"
    "v_pk_mul_f32 v[88:89], v[88:89], v[162:163] op_sel_hi:[1,0]\n\t"
    "v_pk_mul_f32 v[90:91], v[90:91], v[162:163] op_sel_hi:[1,0]\n\t"
    "v_pk_mul_f32 v[92:93], v[92:93], v[162:163] op_sel_hi:[1,0]\n\t"
    "v_pk_mul_f32 v[94:95], v[94:95], v[162:163] op_sel_hi:[1,0]\n\t"
    "v_cvt_pk_bf16_f32 v80, v80, v81\n\t"
    "v_cvt_pk_bf16_f32 v81, v82, v83\n\t"
    "v_cvt_pk_bf16_f32 v82, v84, v85\n\t"
    "v_cvt_pk_bf16_f32 v83, v86, v87\n\t"
    "v_cvt_pk_bf16_f32 v88, v88, v89\n\t"
    "v_cvt_pk_bf16_f32 v89, v90, v91\n\t"
    "v_cvt_pk_bf16_f32 v90, v92, v93\n\t"
    "v_cvt_pk_bf16_f32 v91, v94, v95\n\t"
    "buffer_store_dwordx4 v[80:83], v224, s[48:51], s66 offen offset:2048 sc1\n\t"
    "buffer_store_dwordx4 v[88:91], v224, s[48:51], s66 offen offset:3072 sc1\n\t"
    "s_add_u32 s66, s87, 266240\n\t"
    "s_waitcnt lgkmcnt(2)\n\t"
    "v_pk_mul_f32 v[96:97], v[96:97], v[162:163] op_sel_hi:[1,0]\n\t"
    "v_pk_mul_f32 v[98:99], v[98:99], v[162:163] op_sel_hi:[1,0]\n\t"
    "v_pk_mul_f32 v[100:101], v[100:101], v[162:163] op_sel_hi:[1,0]\n\t"
    "v_pk_mul_f32 v[102:103], v[102:103], v[162:163] op_sel_hi:[1,0]\n\t"
    "v_pk_mul_f32 v[104:105], v[104:105], v[162:163] op_sel_hi:[1,0]\n\t"
    "v_pk_mul_f32 v[106:107], v[106:107], v[162:163] op_sel_hi:[1,0]\n\t"
    "v_pk_mul_f32 v[108:109], v[108:109], v[162:163] op_sel_hi:[1,0]\n\t"
    "v_pk_mul_f32 v[110:111], v[110:111], v[162:163] op_sel_hi:[1,0]\n\t"
    "v_cvt_pk_bf16_f32 v96, v96, v97\n\t"
    "v_cvt_pk_bf16_f32 v97, v98, v99\n\t"
    "v_cvt_pk_bf16_f32 v98, v100, v101\n\t"
    "v_cvt_pk_bf16_f32 v99, v102, v103\n\t"
    "v_cvt_pk_bf16_f32 v104, v104, v105\n\t"
    "v_cvt_pk_bf16_f32 v105, v106, v107\n\t"
    "v_cvt_pk_bf16_f32 v106, v108, v109\n\t"
    "v_cvt_pk_bf16_f32 v107, v110, v111\n\t"
    "buffer_store_dwordx4 v[96:99], v224, s[48:51], s66 offen offset:0 sc1\n\t"
    "buffer_store_dwordx4 v[104:107], v224, s[48:51], s66 offen offset:1024 sc1\n\t"
    "s_add_u32 s66, s87, 266240\n\t"
    "s_waitcnt lgkmcnt(0)\n\t"
    "v_pk_mul_f32 v[112:113], v[112:113], v[162:163] op_sel_hi:[1,0]\n\t"
    "v_pk_mul_f32 v[114:115], v[114:115], v[162:163] op_sel_hi:[1,0]\n\t"
    "v_pk_mul_f32 v[116:117], v[116:117], v[162:163] op_sel_hi:[1,0]\n\t"
    "v_pk_mul_f32 v[118:119], v[118:119], v[162:163] op_sel_hi:[1,0]\n\t"
    "v_cvt_pk_bf16_f32 v112, v112, v113\n\t"
    "v_cvt_pk_bf16_f32 v113, v114, v115\n\t"
    "v_cvt_pk_bf16_f32 v114, v116, v117\n\t"
    "v_cvt_pk_bf16_f32 v115, v118, v119\n\t"
    "buffer_store_dwordx4 v[112:115], v224, s[48:51], s66 offen offset:2048 sc1\n\t"
    "s_nop 1\n\t"
    "Lin_95_%=:\n\t"
    "s_waitcnt vmcnt(16) lgkmcnt(0)\n\t"
    "s_barrier\n\t"
    "s_add_u32 s52, s52, 1\n\t"
    "s_cmp_eq_u32 s52, 15\n\t"
    "s_cbranch_scc1 L3_%=\n\t"
    "L2_%=:\n\t"
    "v_mfma_f32_32x32x16_bf16 v[64:79], a[192:195], a[128:131], v[128:143]\n\t"
    "ds_read_b128 v[160:163], v226 offset:0\n\t"
    "s_and_b32 m0, s58, 0xffff\n\t"
    "v_exp_f32 v28, v28\n\t"
    "v_exp_f32 v29, v29\n\t"
    "v_mfma_f32_32x32x16_bf16 v[80:95], a[192:195], a[160:163], v[144:159]\n\t"
    "ds_read_b128 v[164:167], v226 offset:2048\n\t"
    "buffer_load_dwordx4 v224, s[36:39], s58 offen offset:0 lds\n\t"
    "v_exp_f32 v30, v30\n\t"
    "v_add_f32 v230, v230, v28\n\t"
    "v_mfma_f32_32x32x16_bf16 v[96:111], a[224:227], a[128:131], v[128:143]\n\t"
    "ds_read_b128 v[168:171], v226 offset:4096\n\t"
    "v_exp_f32 v31, v31\n\t"
    "v_exp_f32 v32, v32\n\t"
    "v_add_f32 v231, v231, v29\n\t"
    "v_mfma_f32_32x32x16_bf16 v[112:127], a[224:227], a[160:163], v[144:159]\n\t"
    "ds_read_b128 v[172:175], v226 offset:6144\n\t"
    "v_exp_f32 v33, v33\n\t"
    "v_exp_f32 v34, v34\n\t"
    "v_add_f32 v230, v230, v30\n\t"
    "v_mfma_f32_32x32x16_bf16 v[64:79], a[196:199], a[132:135], v[64:79]\n\t"
    "ds_read_b128 v[176:179], v226 offset:1024\n\t"
    "v_exp_f32 v35, v35\n\t"
    "v_exp_f32 v36, v36\n\t"
    "v_add_f32 v231, v231, v31\n\t"
    "v_mfma_f32_32x32x16_bf16 v[80:95], a[196:199], a[164:167], v[80:95]\n\t"
    "ds_read_b128 v[180:183], v226 offset:3072\n\t"
    "buffer_load_dwordx4 v224, s[36:39], s58 offen offset:1024 lds\n\t"
    "v_exp_f32 v37, v37\n\t"
    "v_add_f32 v228, v228, v32\n\t"
    "v_mfma_f32_32x32x16_bf16 v[96:111], a[228:231], a[132:135], v[96:111]\n\t"
    "ds_read_b128 v[184:187], v226 offset:5120\n\t"
    "v_exp_f32 v38, v38\n\t"
    "v_exp_f32 v39, v39\n\t"
    "v_add_f32 v229, v229, v33\n\t"
    "v_mfma_f32_32x32x16_bf16 v[112:127], a[228:231], a[164:167], v[112:127]\n\t"
    "ds_read_b128 v[188:191], v226 offset:7168\n\t"
    "v_exp_f32 v48, v48\n\t"
    "v_exp_f32 v49, v49\n\t"
    "v_add_f32 v228, v228, v34\n\t"
    "v_mfma_f32_32x32x16_bf16 v[64:79], a[200:203], a[136:139], v[64:79]\n\t"
    "ds_read_b128 v[192:195], v226 offset:8192\n\t"
    "v_exp_f32 v50, v50\n\t"
    "v_exp_f32 v51, v51\n\t"
    "v_add_f32 v229, v229, v35\n\t"
    "v_mfma_f32_32x32x16_bf16 v[80:95], a[200:203], a[168:171], v[80:95]\n\t"
    "ds_read_b128 v[196:199], v226 offset:10240\n\t"
    "buffer_load_dwordx4 v224, s[36:39], s58 offen offset:2048 lds\n\t"
    "v_exp_f32 v52, v52\n\t"
    "v_add_f32 v228, v228, v36\n\t"
    "v_mfma_f32_32x32x16_bf16 v[96:111], a[232:235], a[136:139], v[96:111]\n\t"
    "ds_read_b128 v[200:203], v226 offset:12288\n\t"
    "v_exp_f32 v53, v53\n\t"
    "v_exp_f32 v54, v54\n\t"
    "v_add_f32 v229, v229, v37\n\t"
    "v_mfma_f32_32x32x16_bf16 v[112:127], a[232:235], a[168:171], v[112:127]\n\t"
    "ds_read_b128 v[204:207], v226 offset:14336\n\t"
    "v_exp_f32 v55, v55\n\t"
    "v_exp_f32 v40, v40\n\t"
    "v_add_f32 v228, v228, v38\n\t"
    "v_mfma_f32_32x32x16_bf16 v[64:79], a[204:207], a[140:143], v[64:79]\n\t"
    "ds_read_b128 v[208:211], v226 offset:9216\n\t"
    "v_exp_f32 v41, v41\n\t"
    "v_exp_f32 v42, v42\n\t"
    "v_add_f32 v229, v229, v39\n\t"
    "v_mfma_f32_32x32x16_bf16 v[80:95], a[204:207], a[172:175], v[80:95]\n\t"
    "ds_read_b128 v[212:215], v226 offset:11264\n\t"
    "buffer_load_dwordx4 v224, s[36:39], s58 offen offset:3072 lds\n\t"
    "v_exp_f32 v43, v43\n\t"
    "v_add_f32 v230, v230, v48\n\t"
    "v_mfma_f32_32x32x16_bf16 v[96:111], a[236:239], a[140:143], v[96:111]\n\t"
    "ds_read_b128 v[216:219], v226 offset:13312\n\t"
    "v_exp_f32 v44, v44\n\t"
    "v_exp_f32 v45, v45\n\t"
    "v_add_f32 v231, v231, v49\n\t"
    "v_mfma_f32_32x32x16_bf16 v[112:127], a[236:239], a[172:175], v[112:127]\n\t"
    "ds_read_b128 v[220:223], v226 offset:15360\n\t"
    "v_exp_f32 v46, v46\n\t"
    "v_exp_f32 v47, v47\n\t"
    "v_add_f32 v230, v230, v50\n\t"
    "v_mfma_f32_32x32x16_bf16 v[64:79], a[208:211], a[144:147], v[64:79]\n\t"
    "s_add_u32 m0, m0, 0x1000\n\t"
    "s_add_u32 s68, s58, 0x1000\n\t"
    "v_exp_f32 v56, v56\n\t"
    "v_exp_f32 v57, v57\n\t"
    "v_mfma_f32_32x32x16_bf16 v[80:95], a[208:211], a[176:179], v[80:95]\n\t"
    "buffer_load_dwordx4 v224, s[36:39], s68 offen offset:0 lds\n\t"
    "v_exp_f32 v58, v58\n\t"
    "v_exp_f32 v59, v59\n\t"
    "v_mfma_f32_32x32x16_bf16 v[96:111], a[240:243], a[144:147], v[96:111]\n\t"
    "v_exp_f32 v60, v60\n\t"
    "v_exp_f32 v61, v61\n\t"
    "v_add_f32 v231, v231, v51\n\t"
    "v_add_f32 v230, v230, v52\n\t"
    "v_mfma_f32_32x32x16_bf16 v[112:127], a[240:243], a[176:179], v[112:127]\n\t"
    "v_exp_f32 v62, v62\n\t"
    "v_exp_f32 v63, v63\n\t"
    "v_add_f32 v231, v231, v53\n\t"
    "v_add_f32 v230, v230, v54\n\t"
    "v_mfma_f32_32x32x16_bf16 v[64:79], a[212:215], a[148:151], v[64:79]\n\t"
    "s_and_b32 s66, s57, 0xc000\n\t"
    "v_add_f32 v231, v231, v55\n\t"
    "v_add_f32 v228, v228, v40\n\t"
    "v_add_f32 v229, v229, v41\n\t"
    "v_add_f32 v228, v228, v42\n\t"
    "v_add_f32 v229, v229, v43\n\t"
    "v_mfma_f32_32x32x16_bf16 v[80:95], a[212:215], a[180:183], v[80:95]\n\t"
    "v_add_u32 v225, s66, v224\n\t"
    "buffer_load_dwordx4 v224, s[36:39], s68 offen offset:1024 lds\n\t"
    "v_add_f32 v228, v228, v44\n\t"
    "v_add_f32 v229, v229, v45\n\t"
    "v_add_f32 v228, v228, v46\n\t"
    "v_mfma_f32_32x32x16_bf16 v[96:111], a[244:247], a[148:151], v[96:111]\n\t"
    "v_add_f32 v229, v229, v47\n\t"
    "v_add_f32 v230, v230, v56\n\t"
    "v_add_f32 v231, v231, v57\n\t"
    "v_add_f32 v230, v230, v58\n\t"
    "v_add_f32 v231, v231, v59\n\t"
    "v_add_f32 v230, v230, v60\n\t"
    "v_mfma_f32_32x32x16_bf16 v[112:127], a[244:247], a[180:183], v[112:127]\n\t"
    "v_add_f32 v231, v231, v61\n\t"
    "v_add_f32 v230, v230, v62\n\t"
    "v_add_f32 v231, v231, v63\n\t"
    "v_cvt_pk_bf16_f32 v0, v0, v1\n\t"
    "v_cvt_pk_bf16_f32 v1, v2, v3\n\t"
    "v_cvt_pk_bf16_f32 v2, v4, v5\n\t"
    "v_mfma_f32_32x32x16_bf16 v[64:79], a[216:219], a[152:155], v[64:79]\n\t"
    "v_cvt_pk_bf16_f32 v3, v6, v7\n\t"
    "v_cvt_pk_bf16_f32 v16, v16, v17\n\t"
    "v_cvt_pk_bf16_f32 v17, v18, v19\n\t"
    "v_cvt_pk_bf16_f32 v18, v20, v21\n\t"
    "v_cvt_pk_bf16_f32 v19, v22, v23\n\t"
    "v_cvt_pk_bf16_f32 v8, v8, v9\n\t"
    "v_mfma_f32_32x32x16_bf16 v[80:95], a[216:219], a[184:187], v[80:95]\n\t"
    "buffer_load_dwordx4 v224, s[36:39], s68 offen offset:2048 lds\n\t"
    "v_cvt_pk_bf16_f32 v9, v10, v11\n\t"
    "v_cvt_pk_bf16_f32 v10, v12, v13\n\t"
    "v_cvt_pk_bf16_f32 v11, v14, v15\n\t"
    "v_cvt_pk_bf16_f32 v24, v24, v25\n\t"
    "v_mfma_f32_32x32x16_bf16 v[96:111], a[248:251], a[152:155], v[96:111]\n\t"
    "v_cvt_pk_bf16_f32 v25, v26, v27\n\t"
    "v_cvt_pk_bf16_f32 v26, v28, v29\n\t"
    "v_cvt_pk_bf16_f32 v27, v30, v31\n\t"
    "v_cvt_pk_bf16_f32 v32, v32, v33\n\t"
    "v_cvt_pk_bf16_f32 v33, v34, v35\n\t"
    "v_cvt_pk_bf16_f32 v34, v36, v37\n\t"
    "v_mfma_f32_32x32x16_bf16 v[112:127], a[248:251], a[184:187], v[112:127]\n\t"
    "v_cvt_pk_bf16_f32 v35, v38, v39\n\t"
    "v_cvt_pk_bf16_f32 v48, v48, v49\n\t"
    "v_cvt_pk_bf16_f32 v49, v50, v51\n\t"
    "v_cvt_pk_bf16_f32 v50, v52, v53\n\t"
    "v_cvt_pk_bf16_f32 v51, v54, v55\n\t"
    "v_cvt_pk_bf16_f32 v40, v40, v41\n\t"
    "v_mfma_f32_32x32x16_bf16 v[64:79], a[220:223], a[156:159], v[64:79]\n\t"
    "v_cvt_pk_bf16_f32 v41, v42, v43\n\t"
    "v_cvt_pk_bf16_f32 v42, v44, v45\n\t"
    "v_cvt_pk_bf16_f32 v43, v46, v47\n\t"
    "v_cvt_pk_bf16_f32 v56, v56, v57\n\t"
    "v_cvt_pk_bf16_f32 v57, v58, v59\n\t"
    "v_cvt_pk_bf16_f32 v58, v60, v61\n\t"
    "v_mfma_f32_32x32x16_bf16 v[80:95], a[220:223], a[188:191], v[80:95]\n\t"
    "buffer_load_dwordx4 v224, s[36:39], s68 offen offset:3072 lds\n\t"
    "v_cvt_pk_bf16_f32 v59, v62, v63\n\t"
    "v_mfma_f32_32x32x16_bf16 v[96:111], a[252:255], a[156:159], v[96:111]\n\t"
    "v_mfma_f32_32x32x16_bf16 v[112:127], a[252:255], a[188:191], v[112:127]\n\t"
    "s_waitcnt vmcnt(12) lgkmcnt(0)\n\t"
    "s_barrier\n\t"
    "v_mfma_f32_32x32x16_bf16 a[0:15], v[160:163], v[0:3], a[0:15]\n\t"
    "ds_read_b128 a[192:195], v225 offset:0\n\t"
    "ds_read_b128 a[196:199], v225 offset:1024\n\t"
    "ds_read_b128 a[200:203], v225 offset:2048\n\t"
    "ds_read_b128 a[204:207], v225 offset:3072\n\t"
    "s_and_b32 s69, s56, 0xc000\n\t"
    "v_add_u32 v226, s69, v227\n\t"
    "v_mfma_f32_32x32x16_bf16 a[16:31], v[160:163], v[16:19], a[16:31]\n\t"
    "v_max3_f32 v234, v64, v65, v66\n\t"
    "v_max3_f32 v235, v80, v81, v82\n\t"
    "v_max3_f32 v236, v96, v97, v98\n\t"
    "v_max3_f32 v237, v112, v113, v114\n\t"
    "ds_read_b128 a[208:211], v225 offset:4096\n\t"
    "v_mfma_f32_32x32x16_bf16 a[32:47], v[164:167], v[0:3], a[32:47]\n\t"
    "v_max3_f32 v234, v234, v67, v68\n\t"
    "v_max3_f32 v235, v235, v83, v84\n\t"
    "v_max3_f32 v236, v236, v99, v100\n\t"
    "v_max3_f32 v237, v237, v115, v116\n\t"
    "ds_read_b128 a[212:215], v225 offset:5120\n\t"
    "v_mfma_f32_32x32x16_bf16 a[48:63], v[164:167], v[16:19], a[48:63]\n\t"
    "v_max3_f32 v234, v234, v69, v70\n\t"
    "v_max3_f32 v235, v235, v85, v86\n\t"
    "v_max3_f32 v236, v236, v101, v102\n\t"
    "v_max3_f32 v237, v237, v117, v118\n\t"
    "ds_read_b128 a[216:219], v225 offset:6144\n\t"
    "v_mfma_f32_32x32x16_bf16 a[64:79], v[168:171], v[0:3], a[64:79]\n\t"
    "v_max3_f32 v234, v234, v71, v72\n\t"
    "v_max3_f32 v235, v235, v87, v88\n\t"
    "v_max3_f32 v236, v236, v103, v104\n\t"
    "v_max3_f32 v237, v237, v119, v120\n\t"
    "ds_read_b128 a[220:223], v225 offset:7168\n\t"
    "v_mfma_f32_32x32x16_bf16 a[80:95], v[168:171], v[16:19], a[80:95]\n\t"
    "v_max3_f32 v234, v234, v73, v74\n\t"
    "v_max3_f32 v235, v235, v89, v90\n\t"
    "v_max3_f32 v236, v236, v105, v106\n\t"
    "v_max3_f32 v237, v237, v121, v122\n\t"
    "ds_read_b128 a[224:227], v225 offset:8192\n\t"
    "v_mfma_f32_32x32x16_bf16 a[96:111], v[172:175], v[0:3], a[96:111]\n\t"
    "v_max3_f32 v234, v234, v75, v76\n\t"
    "v_max3_f32 v235, v235, v91, v92\n\t"
    "v_max3_f32 v236, v236, v107, v108\n\t"
    "v_max3_f32 v237, v237, v123, v124\n\t"
    "ds_read_b128 a[228:231], v225 offset:9216\n\t"
    "v_mfma_f32_32x32x16_bf16 a[112:127], v[172:175], v[16:19], a[112:127]\n\t"
    "v_max3_f32 v234, v234, v77, v78\n\t"
    "v_max3_f32 v235, v235, v93, v94\n\t"
    "v_max3_f32 v236, v236, v109, v110\n\t"
    "v_max3_f32 v237, v237, v125, v126\n\t"
    "ds_read_b128 a[232:235], v225 offset:10240\n\t"
    "v_mfma_f32_32x32x16_bf16 a[0:15], v[176:179], v[8:11], a[0:15]\n\t"
    "v_max_f32 v234, v234, v79\n\t"
    "v_max_f32 v235, v235, v95\n\t"
    "v_max_f32 v236, v236, v111\n\t"
    "v_max_f32 v237, v237, v127\n\t"
    "ds_read_b128 a[236:239], v225 offset:11264\n\t"
    "v_mfma_f32_32x32x16_bf16 a[16:31], v[176:179], v[24:27], a[16:31]\n\t"
    "v_max3_f32 v238, v234, v235, v236\n\t"
    "v_max_f32 v238, v238, v237\n\t"
    "v_cmp_lt_f32 vcc, s60, v238\n\t"
    "s_cbranch_vccnz Lst_96_%=\n\t"
    "Lbk_97_%=:\n\t"
    "s_and_b32 s67, s57, 0xffff\n\t"
    "s_or_b32 m0, s67, 0x10000\n\t"
    "v_mfma_f32_32x32x16_bf16 a[32:47], v[180:183], v[8:11], a[32:47]\n\t"
    "ds_read_b128 a[240:243], v225 offset:12288\n\t"
    "buffer_load_dwordx4 v224, s[40:43], s57 offen offset:0 lds\n\t"
    "v_exp_f32 v64, v64\n\t"
    "v_mfma_f32_32x32x16_bf16 a[48:63], v[180:183], v[24:27], a[48:63]\n\t"
    "ds_read_b128 a[244:247], v225 offset:13312\n\t"
    "global_store_dwordx4 v224, v[244:247], s[98:99] offset:0 sc1\n\t"
    "v_exp_f32 v65, v65\n\t"
    "v_add_f32 v228, v228, v64\n\t"
    "v_mfma_f32_32x32x16_bf16 a[64:79], v[184:187], v[8:11], a[64:79]\n\t"
    "ds_read_b128 a[248:251], v225 offset:14336\n\t"
    "v_exp_f32 v66, v66\n\t"
    "v_exp_f32 v67, v67\n\t"
    "v_add_f32 v229, v229, v65\n\t"
    "v_mfma_f32_32x32x16_bf16 a[80:95], v[184:187], v[24:27], a[80:95]\n\t"
    "ds_read_b128 a[252:255], v225 offset:15360\n\t"
    "buffer_load_dwordx4 v224, s[40:43], s57 offen offset:1024 lds\n\t"
    "v_exp_f32 v68, v68\n\t"
    "v_add_f32 v228, v228, v66\n\t"
    "v_mfma_f32_32x32x16_bf16 a[96:111], v[188:191], v[8:11], a[96:111]\n\t"
    "v_exp_f32 v69, v69\n\t"
    "v_exp_f32 v70, v70\n\t"
    "v_add_f32 v229, v229, v67\n\t"
    "v_add_f32 v228, v228, v68\n\t"
    "v_mfma_f32_32x32x16_bf16 a[112:127], v[188:191], v[24:27], a[112:127]\n\t"
    "v_exp_f32 v71, v71\n\t"
    "v_exp_f32 v80, v80\n\t"
    "v_add_f32 v229, v229, v69\n\t"
    "v_add_f32 v228, v228, v70\n\t"
    "v_mfma_f32_32x32x16_bf16 a[0:15], v[192:195], v[32:35], a[0:15]\n\t"
    "buffer_load_dwordx4 v224, s[40:43], s57 offen offset:2048 lds\n\t"
    "v_exp_f32 v81, v81\n\t"
    "v_exp_f32 v82, v82\n\t"
    "v_mfma_f32_32x32x16_bf16 a[16:31], v[192:195], v[48:51], a[16:31]\n\t"
    "global_store_dwordx4 v224, v[244:247], s[98:99] offset:1024 sc1\n\t"
    "v_exp_f32 v83, v83\n\t"
    "v_exp_f32 v84, v84\n\t"
    "v_mfma_f32_32x32x16_bf16 a[32:47], v[196:199], v[32:35], a[32:47]\n\t"
    "v_exp_f32 v85, v85\n\t"
    "v_exp_f32 v86, v86\n\t"
    "v_add_f32 v229, v229, v71\n\t"
    "v_add_f32 v230, v230, v80\n\t"
    "v_mfma_f32_32x32x16_bf16 a[48:63], v[196:199], v[48:51], a[48:63]\n\t"
    "buffer_load_dwordx4 v224, s[40:43], s57 offen offset:3072 lds\n\t"
    "v_exp_f32 v87, v87\n\t"
    "v_exp_f32 v72, v72\n\t"
    "v_mfma_f32_32x32x16_bf16 a[64:79], v[200:203], v[32:35], a[64:79]\n\t"
    "v_exp_f32 v73, v73\n\t"
    "v_exp_f32 v74, v74\n\t"
    "v_add_f32 v231, v231, v81\n\t"
    "v_add_f32 v230, v230, v82\n\t"
    "v_mfma_f32_32x32x16_bf16 a[80:95], v[200:203], v[48:51], a[80:95]\n\t"
    "s_add_u32 m0, m0, 0x1000\n\t"
    "s_add_u32 s68, s57, 0x1000\n\t"
    "v_exp_f32 v75, v75\n\t"
    "v_exp_f32 v76, v76\n\t"
    "v_mfma_f32_32x32x16_bf16 a[96:111], v[204:207], v[32:35], a[96:111]\n\t"
    "buffer_load_dwordx4 v224, s[40:43], s68 offen offset:0 lds\n\t"
    "v_exp_f32 v77, v77\n\t"
    "v_exp_f32 v78, v78\n\t"
    "v_mfma_f32_32x32x16_bf16 a[112:127], v[204:207], v[48:51], a[112:127]\n\t"
    "global_store_dwordx4 v224, v[244:247], s[98:99] offset:2048 sc1\n\t"
    "v_exp_f32 v79, v79\n\t"
    "v_exp_f32 v88, v88\n\t"
    "v_mfma_f32_32x32x16_bf16 a[0:15], v[208:211], v[40:43], a[0:15]\n\t"
    "v_exp_f32 v89, v89\n\t"
    "v_exp_f32 v90, v90\n\t"
    "v_add_f32 v231, v231, v83\n\t"
    "v_add_f32 v230, v230, v84\n\t"
    "v_mfma_f32_32x32x16_bf16 a[16:31], v[208:211], v[56:59], a[16:31]\n\t"
    "buffer_load_dwordx4 v224, s[40:43], s68 offen offset:1024 lds\n\t"
    "v_exp_f32 v91, v91\n\t"
    "v_add_f32 v231, v231, v85\n\t"
    "v_add_f32 v230, v230, v86\n\t"
    "v_mfma_f32_32x32x16_bf16 a[32:47], v[212:215], v[40:43], a[32:47]\n\t"
    "v_add_f32 v231, v231, v87\n\t"
    "v_add_f32 v228, v228, v72\n\t"
    "v_add_f32 v229, v229, v73\n\t"
    "v_add_f32 v228, v228, v74\n\t"
    "v_add_f32 v229, v229, v75\n\t"
    "v_add_f32 v228, v228, v76\n\t"
    "v_mfma_f32_32x32x16_bf16 a[48:63], v[212:215], v[56:59], a[48:63]\n\t"
    "v_add_f32 v229, v229, v77\n\t"
    "v_add_f32 v228, v228, v78\n\t"
    "v_add_f32 v229, v229, v79\n\t"
    "v_add_f32 v230, v230, v88\n\t"
    "v_add_f32 v231, v231, v89\n\t"
    "v_add_f32 v230, v230, v90\n\t"
    "v_mfma_f32_32x32x16_bf16 a[64:79], v[216:219], v[40:43], a[64:79]\n\t"
    "buffer_load_dwordx4 v224, s[40:43], s68 offen offset:2048 lds\n\t"
    "v_add_f32 v231, v231, v91\n\t"
    "v_mfma_f32_32x32x16_bf16 a[80:95], v[216:219], v[56:59], a[80:95]\n\t"
    "global_store_dwordx4 v224, v[244:247], s[98:99] offset:3072 sc1\n\t"
    "v_mfma_f32_32x32x16_bf16 a[96:111], v[220:223], v[40:43], a[96:111]\n\t"
    "s_add_u32 s98, s98, 0x1000\n\t"
    "s_addc_u32 s99, s99, 0\n\t"
    "v_mfma_f32_32x32x16_bf16 a[112:127], v[220:223], v[56:59], a[112:127]\n\t"
    "buffer_load_dwordx4 v224, s[40:43], s68 offen offset:3072 lds\n\t"
    "s_mov_b32 s56, s57\n\t"
    "s_mov_b32 s57, s58\n\t"
    "s_add_u32 s58, s58, 0x8000\n\t"
    "s_cmp_lg_u32 s62, 0\n\t"
    "s_cbranch_scc1 Lst_98_%=\n\t"
    "Lbk_99_%=:\n\t"
    "s_waitcnt vmcnt(20) lgkmcnt(0)\n\t"
    "s_barrier\n\t"
    "s_add_u32 s52, s52, 1\n\t"
    "v_mfma_f32_32x32x16_bf16 v[0:15], a[192:195], a[128:131], v[128:143]\n\t"
    "ds_read_b128 v[160:163], v226 offset:0\n\t"
    "s_and_b32 m0, s58, 0xffff\n\t"
    "v_exp_f32 v92, v92\n\t"
    "v_exp_f32 v93, v93\n\t"
    "v_mfma_f32_32x32x16_bf16 v[16:31], a[192:195], a[160:163], v[144:159]\n\t"
    "ds_read_b128 v[164:167], v226 offset:2048\n\t"
    "buffer_load_dwordx4 v224, s[36:39], s58 offen offset:0 lds\n\t"
    "v_exp_f32 v94, v94\n\t"
    "v_add_f32 v230, v230, v92\n\t"
    "v_mfma_f32_32x32x16_bf16 v[32:47], a[224:227], a[128:131], v[128:143]\n\t"
    "ds_read_b128 v[168:171], v226 offset:4096\n\t"
    "v_exp_f32 v95, v95\n\t"
    "v_exp_f32 v96, v96\n\t"
    "v_add_f32 v231, v231, v93\n\t"
    "v_mfma_f32_32x32x16_bf16 v[48:63], a[224:227], a[160:163], v[144:159]\n\t"
    "ds_read_b128 v[172:175], v226 offset:6144\n\t"
    "v_exp_f32 v97, v97\n\t"
    "v_exp_f32 v98, v98\n\t"
    "v_add_f32 v230, v230, v94\n\t"
    "v_mfma_f32_32x32x16_bf16 v[0:15], a[196:199], a[132:135], v[0:15]\n\t"
    "ds_read_b128 v[176:179], v226 offset:1024\n\t"
    "v_exp_f32 v99, v99\n\t"
    "v_exp_f32 v100, v100\n\t"
    "v_add_f32 v231, v231, v95\n\t"
    "v_mfma_f32_32x32x16_bf16 v[16:31], a[196:199], a[164:167], v[16:31]\n\t"
    "ds_read_b128 v[180:183], v226 offset:3072\n\t"
    "buffer_load_dwordx4 v224, s[36:39], s58 offen offset:1024 lds\n\t"
    "v_exp_f32 v101, v101\n\t"
    "v_add_f32 v228, v228, v96\n\t"
    "v_mfma_f32_32x32x16_bf16 v[32:47], a[228:231], a[132:135], v[32:47]\n\t"
    "ds_read_b128 v[184:187], v226 offset:5120\n\t"
    "v_exp_f32 v102, v102\n\t"
    "v_exp_f32 v103, v103\n\t"
    "v_add_f32 v229, v229, v97\n\t"
    "v_mfma_f32_32x32x16_bf16 v[48:63], a[228:231], a[164:167], v[48:63]\n\t"
    "ds_read_b128 v[188:191], v226 offset:7168\n\t"
    "v_exp_f32 v112, v112\n\t"
    "v_exp_f32 v113, v113\n\t"
    "v_add_f32 v228, v228, v98\n\t"
    "v_mfma_f32_32x32x16_bf16 v[0:15], a[200:203], a[136:139], v[0:15]\n\t"
    "ds_read_b128 v[192:195], v226 offset:8192\n\t"
    "v_exp_f32 v114, v114\n\t"
    "v_exp_f32 v115, v115\n\t"
    "v_add_f32 v229, v229, v99\n\t"
    "v_mfma_f32_32x32x16_bf16 v[16:31], a[200:203], a[168:171], v[16:31]\n\t"
    "ds_read_b128 v[196:199], v226 offset:10240\n\t"
    "buffer_load_dwordx4 v224, s[36:39], s58 offen offset:2048 lds\n\t"
    "v_exp_f32 v116, v116\n\t"
    "v_add_f32 v228, v228, v100\n\t"
    "v_mfma_f32_32x32x16_bf16 v[32:47], a[232:235], a[136:139], v[32:47]\n\t"
    "ds_read_b128 v[200:203], v226 offset:12288\n\t"
    "v_exp_f32 v117, v117\n\t"
    "v_exp_f32 v118, v118\n\t"
    "v_add_f32 v229, v229, v101\n\t"
    "v_mfma_f32_32x32x16_bf16 v[48:63], a[232:235], a[168:171], v[48:63]\n\t"
    "ds_read_b128 v[204:207], v226 offset:14336\n\t"
    "v_exp_f32 v119, v119\n\t"
    "v_exp_f32 v104, v104\n\t"
    "v_add_f32 v228, v228, v102\n\t"
    "v_mfma_f32_32x32x16_bf16 v[0:15], a[204:207], a[140:143], v[0:15]\n\t"
    "ds_read_b128 v[208:211], v226 offset:9216\n\t"
    "v_exp_f32 v105, v105\n\t"
    "v_exp_f32 v106, v106\n\t"
    "v_add_f32 v229, v229, v103\n\t"
    "v_mfma_f32_32x32x16_bf16 v[16:31], a[204:207], a[172:175], v[16:31]\n\t"
    "ds_read_b128 v[212:215], v226 offset:11264\n\t"
    "buffer_load_dwordx4 v224, s[36:39], s58 offen offset:3072 lds\n\t"
    "v_exp_f32 v107, v107\n\t"
    "v_add_f32 v230, v230, v112\n\t"
    "v_mfma_f32_32x32x16_bf16 v[32:47], a[236:239], a[140:143], v[32:47]\n\t"
    "ds_read_b128 v[216:219], v226 offset:13312\n\t"
    "v_exp_f32 v108, v108\n\t"
    "v_exp_f32 v109, v109\n\t"
    "v_add_f32 v231, v231, v113\n\t"
    "v_mfma_f32_32x32x16_bf16 v[48:63], a[236:239], a[172:175], v[48:63]\n\t"
    "ds_read_b128 v[220:223], v226 offset:15360\n\t"
    "v_exp_f32 v110, v110\n\t"
    "v_exp_f32 v111, v111\n\t"
    "v_add_f32 v230, v230, v114\n\t"
    "v_mfma_f32_32x32x16_bf16 v[0:15], a[208:211], a[144:147], v[0:15]\n\t"
    "s_add_u32 m0, m0, 0x1000\n\t"
    "s_add_u32 s68, s58, 0x1000\n\t"
    "v_exp_f32 v120, v120\n\t"
    "v_exp_f32 v121, v121\n\t"
    "v_mfma_f32_32x32x16_bf16 v[16:31], a[208:211], a[176:179], v[16:31]\n\t"
    "buffer_load_dwordx4 v224, s[36:39], s68 offen offset:0 lds\n\t"
    "v_exp_f32 v122, v122\n\t"
    "v_exp_f32 v123, v123\n\t"
    "v_mfma_f32_32x32x16_bf16 v[32:47], a[240:243], a[144:147], v[32:47]\n\t"
    "v_exp_f32 v124, v124\n\t"
    "v_exp_f32 v125, v125\n\t"
    "v_add_f32 v231, v231, v115\n\t"
    "v_add_f32 v230, v230, v116\n\t"
    "v_mfma_f32_32x32x16_bf16 v[48:63], a[240:243], a[176:179], v[48:63]\n\t"
    "v_exp_f32 v126, v126\n\t"
    "v_exp_f32 v127, v127\n\t"
    "v_add_f32 v231, v231, v117\n\t"
    "v_add_f32 v230, v230, v118\n\t"
    "v_mfma_f32_32x32x16_bf16 v[0:15], a[212:215], a[148:151], v[0:15]\n\t"
    "s_and_b32 s66, s57, 0xc000\n\t"
    "v_add_f32 v231, v231, v119\n\t"
    "v_add_f32 v228, v228, v104\n\t"
    "v_add_f32 v229, v229, v105\n\t"
    "v_add_f32 v228, v228, v106\n\t"
    "v_add_f32 v229, v229, v107\n\t"
    "v_mfma_f32_32x32x16_bf16 v[16:31], a[212:215], a[180:183], v[16:31]\n\t"
    "v_add_u32 v225, s66, v224\n\t"
    "buffer_load_dwordx4 v224, s[36:39], s68 offen offset:1024 lds\n\t"
    "v_add_f32 v228, v228, v108\n\t"
    "v_add_f32 v229, v229, v109\n\t"
    "v_add_f32 v228, v228, v110\n\t"
    "v_mfma_f32_32x32x16_bf16 v[32:47], a[244:247], a[148:151], v[32:47]\n\t"
    "v_add_f32 v229, v229, v111\n\t"
    "v_add_f32 v230, v230, v120\n\t"
    "v_add_f32 v231, v231, v121\n\t"
    "v_add_f32 v230, v230, v122\n\t"
    "v_add_f32 v231, v231, v123\n\t"
    "v_add_f32 v230, v230, v124\n\t"
    "v_mfma_f32_32x32x16_bf16 v[48:63], a[244:247], a[180:183], v[48:63]\n\t"
    "v_add_f32 v231, v231, v125\n\t"
    "v_add_f32 v230, v230, v126\n\t"
    "v_add_f32 v231, v231, v127\n\t"
    "v_cvt_pk_bf16_f32 v64, v64, v65\n\t"
    "v_cvt_pk_bf16_f32 v65, v66, v67\n\t"
    "v_cvt_pk_bf16_f32 v66, v68, v69\n\t"
    "v_mfma_f32_32x32x16_bf16 v[0:15], a[216:219], a[152:155], v[0:15]\n\t"
    "v_cvt_pk_bf16_f32 v67, v70, v71\n\t"
    "v_cvt_pk_bf16_f32 v80, v80, v81\n\t"
    "v_cvt_pk_bf16_f32 v81, v82, v83\n\t"
    "v_cvt_pk_bf16_f32 v82, v84, v85\n\t"
    "v_cvt_pk_bf16_f32 v83, v86, v87\n\t"
    "v_cvt_pk_bf16_f32 v72, v72, v73\n\t"
    "v_mfma_f32_32x32x16_bf16 v[16:31], a[216:219], a[184:187], v[16:31]\n\t"
    "buffer_load_dwordx4 v224, s[36:39], s68 offen offset:2048 lds\n\t"
    "v_cvt_pk_bf16_f32 v73, v74, v75\n\t"
    "v_cvt_pk_bf16_f32 v74, v76, v77\n\t"
    "v_cvt_pk_bf16_f32 v75, v78, v79\n\t"
    "v_cvt_pk_bf16_f32 v88, v88, v89\n\t"
    "v_mfma_f32_32x32x16_bf16 v[32:47], a[248:251], a[152:155], v[32:47]\n\t"
    "v_cvt_pk_bf16_f32 v89, v90, v91\n\t"
    "v_cvt_pk_bf16_f32 v90, v92, v93\n\t"
    "v_cvt_pk_bf16_f32 v91, v94, v95\n\t"
    "v_cvt_pk_bf16_f32 v96, v96, v97\n\t"
    "v_cvt_pk_bf16_f32 v97, v98, v99\n\t"
    "v_cvt_pk_bf16_f32 v98, v100, v101\n\t"
    "v_mfma_f32_32x32x16_bf16 v[48:63], a[248:251], a[184:187], v[48:63]\n\t"
    "v_cvt_pk_bf16_f32 v99, v102, v103\n\t"
    "v_cvt_pk_bf16_f32 v112, v112, v113\n\t"
    "v_cvt_pk_bf16_f32 v113, v114, v115\n\t"
    "v_cvt_pk_bf16_f32 v114, v116, v117\n\t"
    "v_cvt_pk_bf16_f32 v115, v118, v119\n\t"
    "v_cvt_pk_bf16_f32 v104, v104, v105\n\t"
    "v_mfma_f32_32x32x16_bf16 v[0:15], a[220:223], a[156:159], v[0:15]\n\t"
    "v_cvt_pk_bf16_f32 v105, v106, v107\n\t"
    "v_cvt_pk_bf16_f32 v106, v108, v109\n\t"
    "v_cvt_pk_bf16_f32 v107, v110, v111\n\t"
    "v_cvt_pk_bf16_f32 v120, v120, v121\n\t"
    "v_cvt_pk_bf16_f32 v121, v122, v123\n\t"
    "v_cvt_pk_bf16_f32 v122, v124, v125\n\t"
    "v_mfma_f32_32x32x16_bf16 v[16:31], a[220:223], a[188:191], v[16:31]\n\t"
    "buffer_load_dwordx4 v224, s[36:39], s68 offen offset:3072 lds\n\t"
    "v_cvt_pk_bf16_f32 v123, v126, v127\n\t"
    "v_mfma_f32_32x32x16_bf16 v[32:47], a[252:255], a[156:159], v[32:47]\n\t"
    "v_mfma_f32_32x32x16_bf16 v[48:63], a[252:255], a[188:191], v[48:63]\n\t"
    "s_waitcnt vmcnt(20) lgkmcnt(0)\n\t"
    "s_barrier\n\t"
    "v_mfma_f32_32x32x16_bf16 a[0:15], v[160:163], v[64:67], a[0:15]\n\t"
    "ds_read_b128 a[192:195], v225 offset:0\n\t"
    "ds_read_b128 a[196:199], v225 offset:1024\n\t"
    "ds_read_b128 a[200:203], v225 offset:2048\n\t"
    "ds_read_b128 a[204:207], v225 offset:3072\n\t"
    "s_and_b32 s69, s56, 0xc000\n\t"
    "v_add_u32 v226, s69, v227\n\t"
    "v_mfma_f32_32x32x16_bf16 a[16:31], v[160:163], v[80:83], a[16:31]\n\t"
    "v_max3_f32 v234, v0, v1, v2\n\t"
    "v_max3_f32 v235, v16, v17, v18\n\t"
    "v_max3_f32 v236, v32, v33, v34\n\t"
    "v_max3_f32 v237, v48, v49, v50\n\t"
    "ds_read_b128 a[208:211], v225 offset:4096\n\t"
    "v_mfma_f32_32x32x16_bf16 a[32:47], v[164:167], v[64:67], a[32:47]\n\t"
    "v_max3_f32 v234, v234, v3, v4\n\t"
    "v_max3_f32 v235, v235, v19, v20\n\t"
    "v_max3_f32 v236, v236, v35, v36\n\t"
    "v_max3_f32 v237, v237, v51, v52\n\t"
    "ds_read_b128 a[212:215], v225 offset:5120\n\t"
    "v_mfma_f32_32x32x16_bf16 a[48:63], v[164:167], v[80:83], a[48:63]\n\t"
    "v_max3_f32 v234, v234, v5, v6\n\t"
    "v_max3_f32 v235, v235, v21, v22\n\t"
    "v_max3_f32 v236, v236, v37, v38\n\t"
    "v_max3_f32 v237, v237, v53, v54\n\t"
    "ds_read_b128 a[216:219], v225 offset:6144\n\t"
    "v_mfma_f32_32x32x16_bf16 a[64:79], v[168:171], v[64:67], a[64:79]\n\t"
    "v_max3_f32 v234, v234, v7, v8\n\t"
    "v_max3_f32 v235, v235, v23, v24\n\t"
    "v_max3_f32 v236, v236, v39, v40\n\t"
    "v_max3_f32 v237, v237, v55, v56\n\t"
    "ds_read_b128 a[220:223], v225 offset:7168\n\t"
    "v_mfma_f32_32x32x16_bf16 a[80:95], v[168:171], v[80:83], a[80:95]\n\t"
    "v_max3_f32 v234, v234, v9, v10\n\t"
    "v_max3_f32 v235, v235, v25, v26\n\t"
    "v_max3_f32 v236, v236, v41, v42\n\t"
    "v_max3_f32 v237, v237, v57, v58\n\t"
    "ds_read_b128 a[224:227], v225 offset:8192\n\t"
    "v_mfma_f32_32x32x16_bf16 a[96:111], v[172:175], v[64:67], a[96:111]\n\t"
    "v_max3_f32 v234, v234, v11, v12\n\t"
    "v_max3_f32 v235, v235, v27, v28\n\t"
    "v_max3_f32 v236, v236, v43, v44\n\t"
    "v_max3_f32 v237, v237, v59, v60\n\t"
    "ds_read_b128 a[228:231], v225 offset:9216\n\t"
    "v_mfma_f32_32x32x16_bf16 a[112:127], v[172:175], v[80:83], a[112:127]\n\t"
    "v_max3_f32 v234, v234, v13, v14\n\t"
    "v_max3_f32 v235, v235, v29, v30\n\t"
    "v_max3_f32 v236, v236, v45, v46\n\t"
    "v_max3_f32 v237, v237, v61, v62\n\t"
    "ds_read_b128 a[232:235], v225 offset:10240\n\t"
    "v_mfma_f32_32x32x16_bf16 a[0:15], v[176:179], v[72:75], a[0:15]\n\t"
    "v_max_f32 v234, v234, v15\n\t"
    "v_max_f32 v235, v235, v31\n\t"
    "v_max_f32 v236, v236, v47\n\t"
    "v_max_f32 v237, v237, v63\n\t"
    "ds_read_b128 a[236:239], v225 offset:11264\n\t"
    "v_mfma_f32_32x32x16_bf16 a[16:31], v[176:179], v[88:91], a[16:31]\n\t"
    "v_max3_f32 v238, v234, v235, v236\n\t"
    "v_max_f32 v238, v238, v237\n\t"
    "v_cmp_lt_f32 vcc, s60, v238\n\t"
    "s_cbranch_vccnz Lst_100_%=\n\t"
    "Lbk_101_%=:\n\t"
    "s_and_b32 s67, s57, 0xffff\n\t"
    "s_or_b32 m0, s67, 0x10000\n\t"
    "v_mfma_f32_32x32x16_bf16 a[32:47], v[180:183], v[72:75], a[32:47]\n\t"
    "ds_read_b128 a[240:243], v225 offset:12288\n\t"
    "buffer_load_dwordx4 v224, s[40:43], s57 offen offset:0 lds\n\t"
    "v_exp_f32 v0, v0\n\t"
    "v_mfma_f32_32x32x16_bf16 a[48:63], v[180:183], v[88:91], a[48:63]\n\t"
    "ds_read_b128 a[244:247], v225 offset:13312\n\t"
    "v_exp_f32 v1, v1\n\t"
    "v_exp_f32 v2, v2\n\t"
    "v_add_f32 v228, v228, v0\n\t"
    "v_mfma_f32_32x32x16_bf16 a[64:79], v[184:187], v[72:75], a[64:79]\n\t"
    "ds_read_b128 a[248:251], v225 offset:14336\n\t"
    "v_exp_f32 v3, v3\n\t"
    "v_exp_f32 v4, v4\n\t"
    "v_add_f32 v229, v229, v1\n\t"
    "v_mfma_f32_32x32x16_bf16 a[80:95], v[184:187], v[88:91], a[80:95]\n\t"
    "ds_read_b128 a[252:255], v225 offset:15360\n\t"
    "buffer_load_dwordx4 v224, s[40:43], s57 offen offset:1024 lds\n\t"
    "v_exp_f32 v5, v5\n\t"
    "v_add_f32 v228, v228, v2\n\t"
    "v_mfma_f32_32x32x16_bf16 a[96:111], v[188:191], v[72:75], a[96:111]\n\t"
    "v_exp_f32 v6, v6\n\t"
    "v_exp_f32 v7, v7\n\t"
    "v_add_f32 v229, v229, v3\n\t"
    "v_add_f32 v228, v228, v4\n\t"
    "v_mfma_f32_32x32x16_bf16 a[112:127], v[188:191], v[88:91], a[112:127]\n\t"
    "v_exp_f32 v16, v16\n\t"
    "v_exp_f32 v17, v17\n\t"
    "v_add_f32 v229, v229, v5\n\t"
    "v_add_f32 v228, v228, v6\n\t"
    "v_mfma_f32_32x32x16_bf16 a[0:15], v[192:195], v[96:99], a[0:15]\n\t"
    "buffer_load_dwordx4 v224, s[40:43], s57 offen offset:2048 lds\n\t"
    "v_exp_f32 v18, v18\n\t"
    "v_exp_f32 v19, v19\n\t"
    "v_mfma_f32_32x32x16_bf16 a[16:31], v[192:195], v[112:115], a[16:31]\n\t"
    "v_exp_f32 v20, v20\n\t"
    "v_exp_f32 v21, v21\n\t"
    "v_add_f32 v229, v229, v7\n\t"
    "v_add_f32 v230, v230, v16\n\t"
    "v_mfma_f32_32x32x16_bf16 a[32:47], v[196:199], v[96:99], a[32:47]\n\t"
    "v_exp_f32 v22, v22\n\t"
    "v_exp_f32 v23, v23\n\t"
    "v_add_f32 v231, v231, v17\n\t"
    "v_add_f32 v230, v230, v18\n\t"
    "v_mfma_f32_32x32x16_bf16 a[48:63], v[196:199], v[112:115], a[48:63]\n\t"
    "buffer_load_dwordx4 v224, s[40:43], s57 offen offset:3072 lds\n\t"
    "v_exp_f32 v8, v8\n\t"
    "v_exp_f32 v9, v9\n\t"
    "v_mfma_f32_32x32x16_bf16 a[64:79], v[200:203], v[96:99], a[64:79]\n\t"
    "v_exp_f32 v10, v10\n\t"
    "v_exp_f32 v11, v11\n\t"
    "v_add_f32 v231, v231, v19\n\t"
    "v_add_f32 v230, v230, v20\n\t"
    "v_mfma_f32_32x32x16_bf16 a[80:95], v[200:203], v[112:115], a[80:95]\n\t"
    "s_add_u32 m0, m0, 0x1000\n\t"
    "s_add_u32 s68, s57, 0x1000\n\t"
    "v_exp_f32 v12, v12\n\t"
    "v_exp_f32 v13, v13\n\t"
    "v_mfma_f32_32x32x16_bf16 a[96:111], v[204:207], v[96:99], a[96:111]\n\t"
    "buffer_load_dwordx4 v224, s[40:43], s68 offen offset:0 lds\n\t"
    "v_exp_f32 v14, v14\n\t"
    "v_exp_f32 v15, v15\n\t"
    "v_mfma_f32_32x32x16_bf16 a[112:127], v[204:207], v[112:115], a[112:127]\n\t"
    "v_exp_f32 v24, v24\n\t"
    "v_exp_f32 v25, v25\n\t"
    "v_add_f32 v231, v231, v21\n\t"
    "v_add_f32 v230, v230, v22\n\t"
    "v_mfma_f32_32x32x16_bf16 a[0:15], v[208:211], v[104:107], a[0:15]\n\t"
    "v_exp_f32 v26, v26\n\t"
    "v_exp_f32 v27, v27\n\t"
    "v_add_f32 v231, v231, v23\n\t"
    "v_add_f32 v228, v228, v8\n\t"
    "v_mfma_f32_32x32x16_bf16 a[16:31], v[208:211], v[120:123], a[16:31]\n\t"
    "buffer_load_dwordx4 v224, s[40:43], s68 offen offset:1024 lds\n\t"
    "v_add_f32 v229, v229, v9\n\t"
    "v_add_f32 v228, v228, v10\n\t"
    "v_add_f32 v229, v229, v11\n\t"
    "v_add_f32 v228, v228, v12\n\t"
    "v_mfma_f32_32x32x16_bf16 a[32:47], v[212:215], v[104:107], a[32:47]\n\t"
    "v_add_f32 v229, v229, v13\n\t"
    "v_add_f32 v228, v228, v14\n\t"
    "v_add_f32 v229, v229, v15\n\t"
    "v_add_f32 v230, v230, v24\n\t"
    "v_add_f32 v231, v231, v25\n\t"
    "v_add_f32 v230, v230, v26\n\t"
    "v_mfma_f32_32x32x16_bf16 a[48:63], v[212:215], v[120:123], a[48:63]\n\t"
    "v_add_f32 v231, v231, v27\n\t"
    "v_mfma_f32_32x32x16_bf16 a[64:79], v[216:219], v[104:107], a[64:79]\n\t"
    "buffer_load_dwordx4 v224, s[40:43], s68 offen offset:2048 lds\n\t"
    "v_mfma_f32_32x32x16_bf16 a[80:95], v[216:219], v[120:123], a[80:95]\n\t"
    "v_mfma_f32_32x32x16_bf16 a[96:111], v[220:223], v[104:107], a[96:111]\n\t"
    "v_mfma_f32_32x32x16_bf16 a[112:127], v[220:223], v[120:123], a[112:127]\n\t"
    "buffer_load_dwordx4 v224, s[40:43], s68 offen offset:3072 lds\n\t"
    "s_mov_b32 s56, s57\n\t"
    "s_mov_b32 s57, s58\n\t"
    "s_add_u32 s58, s58, 0x8000\n\t"
    "s_cmp_lg_u32 s62, 0\n\t"
    "s_cbranch_scc1 Lst_102_%=\n\t"
    "Lbk_103_%=:\n\t"
    "s_waitcnt vmcnt(16) lgkmcnt(0)\n\t"
    "s_barrier\n\t"
    "s_add_u32 s52, s52, 1\n\t"
    "s_cmp_lt_u32 s52, 15\n\t"
    "s_cbranch_scc1 L2_%=\n\t"
    "L3_%=:\n\t"
    "v_mfma_f32_32x32x16_bf16 v[64:79], a[192:195], a[128:131], v[128:143]\n\t"
    "ds_read_b128 v[160:163], v226 offset:0\n\t"
    "v_exp_f32 v28, v28\n\t"
    "v_exp_f32 v29, v29\n\t"
    "v_add_f32 v230, v230, v28\n\t"
    "v_mfma_f32_32x32x16_bf16 v[80:95], a[192:195], a[160:163], v[144:159]\n\t"
    "ds_read_b128 v[164:167], v226 offset:2048\n\t"
    "v_exp_f32 v30, v30\n\t"
    "v_exp_f32 v31, v31\n\t"
    "v_add_f32 v231, v231, v29\n\t"
    "v_mfma_f32_32x32x16_bf16 v[96:111], a[224:227], a[128:131], v[128:143]\n\t"
    "ds_read_b128 v[168:171], v226 offset:4096\n\t"
    "v_exp_f32 v32, v32\n\t"
    "v_exp_f32 v33, v33\n\t"
    "v_add_f32 v230, v230, v30\n\t"
    "v_mfma_f32_32x32x16_bf16 v[112:127], a[224:227], a[160:163], v[144:159]\n\t"
    "ds_read_b128 v[172:175], v226 offset:6144\n\t"
    "v_exp_f32 v34, v34\n\t"
    "v_exp_f32 v35, v35\n\t"
    "v_add_f32 v231, v231, v31\n\t"
    "v_mfma_f32_32x32x16_bf16 v[64:79], a[196:199], a[132:135], v[64:79]\n\t"
    "ds_read_b128 v[176:179], v226 offset:1024\n\t"
    "v_exp_f32 v36, v36\n\t"
    "v_exp_f32 v37, v37\n\t"
    "v_add_f32 v228, v228, v32\n\t"
    "v_mfma_f32_32x32x16_bf16 v[80:95], a[196:199], a[164:167], v[80:95]\n\t"
    "ds_read_b128 v[180:183], v226 offset:3072\n\t"
    "v_exp_f32 v38, v38\n\t"
    "v_exp_f32 v39, v39\n\t"
    "v_add_f32 v229, v229, v33\n\t"
    "v_mfma_f32_32x32x16_bf16 v[96:111], a[228:231], a[132:135], v[96:111]\n\t"
    "ds_read_b128 v[184:187], v226 offset:5120\n\t"
    "v_exp_f32 v48, v48\n\t"
    "v_exp_f32 v49, v49\n\t"
    "v_add_f32 v228, v228, v34\n\t"
    "v_mfma_f32_32x32x16_bf16 v[112:127], a[228:231], a[164:167], v[112:127]\n\t"
    "ds_read_b128 v[188:191], v226 offset:7168\n\t"
    "v_exp_f32 v50, v50\n\t"
    "v_exp_f32 v51, v51\n\t"
    "v_add_f32 v229, v229, v35\n\t"
    "v_mfma_f32_32x32x16_bf16 v[64:79], a[200:203], a[136:139], v[64:79]\n\t"
    "ds_read_b128 v[192:195], v226 offset:8192\n\t"
    "v_exp_f32 v52, v52\n\t"
    "v_exp_f32 v53, v53\n\t"
    "v_add_f32 v228, v228, v36\n\t"
    "v_mfma_f32_32x32x16_bf16 v[80:95], a[200:203], a[168:171], v[80:95]\n\t"
    "ds_read_b128 v[196:199], v226 offset:10240\n\t"
    "v_exp_f32 v54, v54\n\t"
    "v_exp_f32 v55, v55\n\t"
    "v_add_f32 v229, v229, v37\n\t"
    "v_mfma_f32_32x32x16_bf16 v[96:111], a[232:235], a[136:139], v[96:111]\n\t"
    "ds_read_b128 v[200:203], v226 offset:12288\n\t"
    "v_exp_f32 v40, v40\n\t"
    "v_exp_f32 v41, v41\n\t"
    "v_add_f32 v228, v228, v38\n\t"
    "v_mfma_f32_32x32x16_bf16 v[112:127], a[232:235], a[168:171], v[112:127]\n\t"
    "ds_read_b128 v[204:207], v226 offset:14336\n\t"
    "v_exp_f32 v42, v42\n\t"
    "v_exp_f32 v43, v43\n\t"
    "v_add_f32 v229, v229, v39\n\t"
    "v_mfma_f32_32x32x16_bf16 v[64:79], a[204:207], a[140:143], v[64:79]\n\t"
    "ds_read_b128 v[208:211], v226 offset:9216\n\t"
    "v_exp_f32 v44, v44\n\t"
    "v_exp_f32 v45, v45\n\t"
    "v_add_f32 v230, v230, v48\n\t"
    "v_mfma_f32_32x32x16_bf16 v[80:95], a[204:207], a[172:175], v[80:95]\n\t"
    "ds_read_b128 v[212:215], v226 offset:11264\n\t"
    "v_exp_f32 v46, v46\n\t"
    "v_exp_f32 v47, v47\n\t"
    "v_add_f32 v231, v231, v49\n\t"
    "v_mfma_f32_32x32x16_bf16 v[96:111], a[236:239], a[140:143], v[96:111]\n\t"
    "ds_read_b128 v[216:219], v226 offset:13312\n\t"
    "v_exp_f32 v56, v56\n\t"
    "v_exp_f32 v57, v57\n\t"
    "v_add_f32 v230, v230, v50\n\t"
    "v_mfma_f32_32x32x16_bf16 v[112:127], a[236:239], a[172:175], v[112:127]\n\t"
    "ds_read_b128 v[220:223], v226 offset:15360\n\t"
    "v_exp_f32 v58, v58\n\t"
    "v_exp_f32 v59, v59\n\t"
    "v_add_f32 v231, v231, v51\n\t"
    "v_mfma_f32_32x32x16_bf16 v[64:79], a[208:211], a[144:147], v[64:79]\n\t"
    "v_exp_f32 v60, v60\n\t"
    "v_exp_f32 v61, v61\n\t"
    "v_add_f32 v230, v230, v52\n\t"
    "v_add_f32 v231, v231, v53\n\t"
    "v_mfma_f32_32x32x16_bf16 v[80:95], a[208:211], a[176:179], v[80:95]\n\t"
    "v_exp_f32 v62, v62\n\t"
    "v_exp_f32 v63, v63\n\t"
    "v_add_f32 v230, v230, v54\n\t"
    "v_add_f32 v231, v231, v55\n\t"
    "v_mfma_f32_32x32x16_bf16 v[96:111], a[240:243], a[144:147], v[96:111]\n\t"
    "v_add_f32 v228, v228, v40\n\t"
    "v_add_f32 v229, v229, v41\n\t"
    "v_add_f32 v228, v228, v42\n\t"
    "v_add_f32 v229, v229, v43\n\t"
    "v_add_f32 v228, v228, v44\n\t"
    "v_add_f32 v229, v229, v45\n\t"
    "v_mfma_f32_32x32x16_bf16 v[112:127], a[240:243], a[176:179], v[112:127]\n\t"
    "v_add_f32 v228, v228, v46\n\t"
    "v_add_f32 v229, v229, v47\n\t"
    "v_add_f32 v230, v230, v56\n\t"
    "v_add_f32 v231, v231, v57\n\t"
    "v_add_f32 v230, v230, v58\n\t"
    "v_add_f32 v231, v231, v59\n\t"
    "v_mfma_f32_32x32x16_bf16 v[64:79], a[212:215], a[148:151], v[64:79]\n\t"
    "s_and_b32 s66, s57, 0xc000\n\t"
    "v_add_f32 v230, v230, v60\n\t"
    "v_add_f32 v231, v231, v61\n\t"
    "v_add_f32 v230, v230, v62\n\t"
    "v_add_f32 v231, v231, v63\n\t"
    "v_cvt_pk_bf16_f32 v0, v0, v1\n\t"
    "v_mfma_f32_32x32x16_bf16 v[80:95], a[212:215], a[180:183], v[80:95]\n\t"
    "v_add_u32 v225, s66, v224\n\t"
    "v_cvt_pk_bf16_f32 v1, v2, v3\n\t"
    "v_cvt_pk_bf16_f32 v2, v4, v5\n\t"
    "v_cvt_pk_bf16_f32 v3, v6, v7\n\t"
    "v_cvt_pk_bf16_f32 v16, v16, v17\n\t"
    "v_cvt_pk_bf16_f32 v17, v18, v19\n\t"
    "v_mfma_f32_32x32x16_bf16 v[96:111], a[244:247], a[148:151], v[96:111]\n\t"
    "v_cvt_pk_bf16_f32 v18, v20, v21\n\t"
    "v_cvt_pk_bf16_f32 v19, v22, v23\n\t"
    "v_cvt_pk_bf16_f32 v8, v8, v9\n\t"
    "v_cvt_pk_bf16_f32 v9, v10, v11\n\t"
    "v_cvt_pk_bf16_f32 v10, v12, v13\n\t"
    "v_cvt_pk_bf16_f32 v11, v14, v15\n\t"
    "v_mfma_f32_32x32x16_bf16 v[112:127], a[244:247], a[180:183], v[112:127]\n\t"
    "v_cvt_pk_bf16_f32 v24, v24, v25\n\t"
    "v_cvt_pk_bf16_f32 v25, v26, v27\n\t"
    "v_cvt_pk_bf16_f32 v26, v28, v29\n\t"
    "v_cvt_pk_bf16_f32 v27, v30, v31\n\t"
    "v_cvt_pk_bf16_f32 v32, v32, v33\n\t"
    "v_cvt_pk_bf16_f32 v33, v34, v35\n\t"
    "v_mfma_f32_32x32x16_bf16 v[64:79], a[216:219], a[152:155], v[64:79]\n\t"
    "v_cvt_pk_bf16_f32 v34, v36, v37\n\t"
    "v_cvt_pk_bf16_f32 v35, v38, v39\n\t"
    "v_cvt_pk_bf16_f32 v48, v48, v49\n\t"
    "v_cvt_pk_bf16_f32 v49, v50, v51\n\t"
    "v_cvt_pk_bf16_f32 v50, v52, v53\n\t"
    "v_cvt_pk_bf16_f32 v51, v54, v55\n\t"
    "v_mfma_f32_32x32x16_bf16 v[80:95], a[216:219], a[184:187], v[80:95]\n\t"
    "v_cvt_pk_bf16_f32 v40, v40, v41\n\t"
    "v_cvt_pk_bf16_f32 v41, v42, v43\n\t"
    "v_cvt_pk_bf16_f32 v42, v44, v45\n\t"
    "v_cvt_pk_bf16_f32 v43, v46, v47\n\t"
    "v_cvt_pk_bf16_f32 v56, v56, v57\n\t"
    "v_cvt_pk_bf16_f32 v57, v58, v59\n\t"
    "v_mfma_f32_32x32x16_bf16 v[96:111], a[248:251], a[152:155], v[96:111]\n\t"
    "v_cvt_pk_bf16_f32 v58, v60, v61\n\t"
    "v_cvt_pk_bf16_f32 v59, v62, v63\n\t"
    "v_mfma_f32_32x32x16_bf16 v[112:127], a[248:251], a[184:187], v[112:127]\n\t"
    "v_mfma_f32_32x32x16_bf16 v[64:79], a[220:223], a[156:159], v[64:79]\n\t"
    "v_mfma_f32_32x32x16_bf16 v[80:95], a[220:223], a[188:191], v[80:95]\n\t"
    "v_mfma_f32_32x32x16_bf16 v[96:111], a[252:255], a[156:159], v[96:111]\n\t"
    "v_mfma_f32_32x32x16_bf16 v[112:127], a[252:255], a[188:191], v[112:127]\n\t"
    "s_waitcnt vmcnt(4) lgkmcnt(0)\n\t"
    "s_barrier\n\t"
    "v_mfma_f32_32x32x16_bf16 a[0:15], v[160:163], v[0:3], a[0:15]\n\t"
    "ds_read_b128 a[192:195], v225 offset:0\n\t"
    "ds_read_b128 a[196:199], v225 offset:1024\n\t"
    "ds_read_b128 a[200:203], v225 offset:2048\n\t"
    "ds_read_b128 a[204:207], v225 offset:3072\n\t"
    "s_and_b32 s69, s56, 0xc000\n\t"
    "v_add_u32 v226, s69, v227\n\t"
    "v_mfma_f32_32x32x16_bf16 a[16:31], v[160:163], v[16:19], a[16:31]\n\t"
    "v_max3_f32 v234, v64, v65, v66\n\t"
    "v_max3_f32 v235, v80, v81, v82\n\t"
    "v_max3_f32 v236, v96, v97, v98\n\t"
    "v_max3_f32 v237, v112, v113, v114\n\t"
    "ds_read_b128 a[208:211], v225 offset:4096\n\t"
    "v_mfma_f32_32x32x16_bf16 a[32:47], v[164:167], v[0:3], a[32:47]\n\t"
    "v_max3_f32 v234, v234, v67, v68\n\t"
    "v_max3_f32 v235, v235, v83, v84\n\t"
    "v_max3_f32 v236, v236, v99, v100\n\t"
    "v_max3_f32 v237, v237, v115, v116\n\t"
    "ds_read_b128 a[212:215], v225 offset:5120\n\t"
    "v_mfma_f32_32x32x16_bf16 a[48:63], v[164:167], v[16:19], a[48:63]\n\t"
    "v_max3_f32 v234, v234, v69, v70\n\t"
    "v_max3_f32 v235, v235, v85, v86\n\t"
    "v_max3_f32 v236, v236, v101, v102\n\t"
    "v_max3_f32 v237, v237, v117, v118\n\t"
    "ds_read_b128 a[216:219], v225 offset:6144\n\t"
    "v_mfma_f32_32x32x16_bf16 a[64:79], v[168:171], v[0:3], a[64:79]\n\t"
    "v_max3_f32 v234, v234, v71, v72\n\t"
    "v_max3_f32 v235, v235, v87, v88\n\t"
    "v_max3_f32 v236, v236, v103, v104\n\t"
    "v_max3_f32 v237, v237, v119, v120\n\t"
    "ds_read_b128 a[220:223], v225 offset:7168\n\t"
    "v_mfma_f32_32x32x16_bf16 a[80:95], v[168:171], v[16:19], a[80:95]\n\t"
    "v_max3_f32 v234, v234, v73, v74\n\t"
    "v_max3_f32 v235, v235, v89, v90\n\t"
    "v_max3_f32 v236, v236, v105, v106\n\t"
    "v_max3_f32 v237, v237, v121, v122\n\t"
    "ds_read_b128 a[224:227], v225 offset:8192\n\t"
    "v_mfma_f32_32x32x16_bf16 a[96:111], v[172:175], v[0:3], a[96:111]\n\t"
    "v_max3_f32 v234, v234, v75, v76\n\t"
    "v_max3_f32 v235, v235, v91, v92\n\t"
    "v_max3_f32 v236, v236, v107, v108\n\t"
    "v_max3_f32 v237, v237, v123, v124\n\t"
    "ds_read_b128 a[228:231], v225 offset:9216\n\t"
    "v_mfma_f32_32x32x16_bf16 a[112:127], v[172:175], v[16:19], a[112:127]\n\t"
    "v_max3_f32 v234, v234, v77, v78\n\t"
    "v_max3_f32 v235, v235, v93, v94\n\t"
    "v_max3_f32 v236, v236, v109, v110\n\t"
    "v_max3_f32 v237, v237, v125, v126\n\t"
    "ds_read_b128 a[232:235], v225 offset:10240\n\t"
    "v_mfma_f32_32x32x16_bf16 a[0:15], v[176:179], v[8:11], a[0:15]\n\t"
    "v_max_f32 v234, v234, v79\n\t"
    "v_max_f32 v235, v235, v95\n\t"
    "v_max_f32 v236, v236, v111\n\t"
    "v_max_f32 v237, v237, v127\n\t"
    "ds_read_b128 a[236:239], v225 offset:11264\n\t"
    "v_mfma_f32_32x32x16_bf16 a[16:31], v[176:179], v[24:27], a[16:31]\n\t"
    "v_max3_f32 v238, v234, v235, v236\n\t"
    "v_max_f32 v238, v238, v237\n\t"
    "v_cmp_lt_f32 vcc, s60, v238\n\t"
    "s_cbranch_vccnz Lst_104_%=\n\t"
    "Lbk_105_%=:\n\t"
    "s_and_b32 s67, s57, 0xffff\n\t"
    "s_or_b32 m0, s67, 0x10000\n\t"
    "v_mfma_f32_32x32x16_bf16 a[32:47], v[180:183], v[8:11], a[32:47]\n\t"
    "ds_read_b128 a[240:243], v225 offset:12288\n\t"
    "buffer_load_dwordx4 v224, s[40:43], s57 offen offset:0 lds\n\t"
    "v_exp_f32 v64, v64\n\t"
    "v_mfma_f32_32x32x16_bf16 a[48:63], v[180:183], v[24:27], a[48:63]\n\t"
    "ds_read_b128 a[244:247], v225 offset:13312\n\t"
    "v_exp_f32 v65, v65\n\t"
    "v_exp_f32 v66, v66\n\t"
    "v_add_f32 v228, v228, v64\n\t"
    "v_mfma_f32_32x32x16_bf16 a[64:79], v[184:187], v[8:11], a[64:79]\n\t"
    "ds_read_b128 a[248:251], v225 offset:14336\n\t"
    "v_exp_f32 v67, v67\n\t"
    "v_exp_f32 v68, v68\n\t"
    "v_add_f32 v229, v229, v65\n\t"
    "v_mfma_f32_32x32x16_bf16 a[80:95], v[184:187], v[24:27], a[80:95]\n\t"
    "ds_read_b128 a[252:255], v225 offset:15360\n\t"
    "buffer_load_dwordx4 v224, s[40:43], s57 offen offset:1024 lds\n\t"
    "v_exp_f32 v69, v69\n\t"
    "v_add_f32 v228, v228, v66\n\t"
    "v_mfma_f32_32x32x16_bf16 a[96:111], v[188:191], v[8:11], a[96:111]\n\t"
    "v_exp_f32 v70, v70\n\t"
    "v_exp_f32 v71, v71\n\t"
    "v_add_f32 v229, v229, v67\n\t"
    "v_add_f32 v228, v228, v68\n\t"
    "v_mfma_f32_32x32x16_bf16 a[112:127], v[188:191], v[24:27], a[112:127]\n\t"
    "v_exp_f32 v80, v80\n\t"
    "v_exp_f32 v81, v81\n\t"
    "v_add_f32 v229, v229, v69\n\t"
    "v_add_f32 v228, v228, v70\n\t"
    "v_mfma_f32_32x32x16_bf16 a[0:15], v[192:195], v[32:35], a[0:15]\n\t"
    "buffer_load_dwordx4 v224, s[40:43], s57 offen offset:2048 lds\n\t"
    "v_exp_f32 v82, v82\n\t"
    "v_exp_f32 v83, v83\n\t"
    "v_mfma_f32_32x32x16_bf16 a[16:31], v[192:195], v[48:51], a[16:31]\n\t"
    "v_exp_f32 v84, v84\n\t"
    "v_exp_f32 v85, v85\n\t"
    "v_add_f32 v229, v229, v71\n\t"
    "v_add_f32 v230, v230, v80\n\t"
    "v_mfma_f32_32x32x16_bf16 a[32:47], v[196:199], v[32:35], a[32:47]\n\t"
    "v_exp_f32 v86, v86\n\t"
    "v_exp_f32 v87, v87\n\t"
    "v_add_f32 v231, v231, v81\n\t"
    "v_add_f32 v230, v230, v82\n\t"
    "v_mfma_f32_32x32x16_bf16 a[48:63], v[196:199], v[48:51], a[48:63]\n\t"
    "buffer_load_dwordx4 v224, s[40:43], s57 offen offset:3072 lds\n\t"
    "v_exp_f32 v72, v72\n\t"
    "v_exp_f32 v73, v73\n\t"
    "v_mfma_f32_32x32x16_bf16 a[64:79], v[200:203], v[32:35], a[64:79]\n\t"
    "v_exp_f32 v74, v74\n\t"
    "v_exp_f32 v75, v75\n\t"
    "v_add_f32 v231, v231, v83\n\t"
    "v_add_f32 v230, v230, v84\n\t"
    "v_mfma_f32_32x32x16_bf16 a[80:95], v[200:203], v[48:51], a[80:95]\n\t"
    "s_add_u32 m0, m0, 0x1000\n\t"
    "s_add_u32 s68, s57, 0x1000\n\t"
    "v_exp_f32 v76, v76\n\t"
    "v_exp_f32 v77, v77\n\t"
    "v_mfma_f32_32x32x16_bf16 a[96:111], v[204:207], v[32:35], a[96:111]\n\t"
    "buffer_load_dwordx4 v224, s[40:43], s68 offen offset:0 lds\n\t"
    "v_exp_f32 v78, v78\n\t"
    "v_exp_f32 v79, v79\n\t"
    "v_mfma_f32_32x32x16_bf16 a[112:127], v[204:207], v[48:51], a[112:127]\n\t"
    "v_exp_f32 v88, v88\n\t"
    "v_exp_f32 v89, v89\n\t"
    "v_add_f32 v231, v231, v85\n\t"
    "v_add_f32 v230, v230, v86\n\t"
    "v_mfma_f32_32x32x16_bf16 a[0:15], v[208:211], v[40:43], a[0:15]\n\t"
    "v_exp_f32 v90, v90\n\t"
    "v_exp_f32 v91, v91\n\t"
    "v_add_f32 v231, v231, v87\n\t"
    "v_add_f32 v228, v228, v72\n\t"
    "v_mfma_f32_32x32x16_bf16 a[16:31], v[208:211], v[56:59], a[16:31]\n\t"
    "buffer_load_dwordx4 v224, s[40:43], s68 offen offset:1024 lds\n\t"
    "v_add_f32 v229, v229, v73\n\t"
    "v_add_f32 v228, v228, v74\n\t"
    "v_add_f32 v229, v229, v75\n\t"
    "v_add_f32 v228, v228, v76\n\t"
    "v_mfma_f32_32x32x16_bf16 a[32:47], v[212:215], v[40:43], a[32:47]\n\t"
    "v_add_f32 v229, v229, v77\n\t"
    "v_add_f32 v228, v228, v78\n\t"
    "v_add_f32 v229, v229, v79\n\t"
    "v_add_f32 v230, v230, v88\n\t"
    "v_add_f32 v231, v231, v89\n\t"
    "v_add_f32 v230, v230, v90\n\t"
    "v_mfma_f32_32x32x16_bf16 a[48:63], v[212:215], v[56:59], a[48:63]\n\t"
    "v_add_f32 v231, v231, v91\n\t"
    "v_mfma_f32_32x32x16_bf16 a[64:79], v[216:219], v[40:43], a[64:79]\n\t"
    "buffer_load_dwordx4 v224, s[40:43], s68 offen offset:2048 lds\n\t"
    "v_mfma_f32_32x32x16_bf16 a[80:95], v[216:219], v[56:59], a[80:95]\n\t"
    "v_mfma_f32_32x32x16_bf16 a[96:111], v[220:223], v[40:43], a[96:111]\n\t"
    "v_mfma_f32_32x32x16_bf16 a[112:127], v[220:223], v[56:59], a[112:127]\n\t"
    "buffer_load_dwordx4 v224, s[40:43], s68 offen offset:3072 lds\n\t"
    "s_mov_b32 s56, s57\n\t"
    "s_mov_b32 s57, s58\n\t"
    "s_add_u32 s58, s58, 0x8000\n\t"
    "s_cmp_lg_u32 s62, 0\n\t"
    "s_cbranch_scc1 Lst_106_%=\n\t"
    "Lbk_107_%=:\n\t"
    "s_waitcnt vmcnt(8) lgkmcnt(0)\n\t"
    "s_barrier\n\t"
    "s_add_u32 s52, s52, 1\n\t"
    "v_mfma_f32_32x32x16_bf16 v[0:15], a[192:195], a[128:131], v[128:143]\n\t"
    "s_cmp_lt_u32 s94, 16\n\t"
    "s_cbranch_scc0 Lpb_50_%=\n\t"
    "v_mov_b32 v241, %14\n\t"
    "s_mov_b64 exec, 1\n\t"
    "global_store_dword v241, v241, %18 sc1\n\t"
    "s_mov_b64 exec, -1\n\t"
    "s_nop 1\n\t"
    "Lpb_50_%=:\n\t"
    "ds_read_b128 v[160:163], v226 offset:0\n\t"
    "v_mfma_f32_32x32x16_bf16 v[16:31], a[192:195], a[160:163], v[144:159]\n\t"
    "s_getpc_b64 s[66:67]\n\t"
    "LPT_49_%=:\n\t"
    "s_add_u32 s66, s66, LTAIL_BEG-LPT_49_%=\n\t"
    "s_addc_u32 s67, s67, 0\n\t"
    "s_and_b32 s69, %10, 7\n\t"
    "s_lshl_b32 s69, s69, 2\n\t"
    "s_add_u32 s69, s69, s89\n\t"
    "s_lshl_b32 s69, s69, 7\n\t"
    "v_lshl_add_u32 v241, v224, 8, s69\n\t"
    "v_min_u32 v241, LTAIL_END-LTAIL_BEG-4+14080, v241\n\t"
    "global_load_dword v253, v241, s[66:67]\n\t"
    "ds_read_b128 v[164:167], v226 offset:2048\n\t"
    "v_mfma_f32_32x32x16_bf16 v[32:47], a[224:227], a[128:131], v[128:143]\n\t"
    "v_and_b32 v255, 48, v224\n\t"
    "v_bfe_u32 v243, v224, 6, 2\n\t"
    "v_lshl_add_u32 v255, v243, 10, v255\n\t"
    "v_lshrrev_b32 v243, 8, v224\n\t"
    "v_lshl_add_u32 v255, v243, 6, v255\n\t"
    "ds_read_b128 v[168:171], v226 offset:4096\n\t"
    "v_mfma_f32_32x32x16_bf16 v[48:63], a[224:227], a[160:163], v[144:159]\n\t"
    "s_mov_b64 s[44:45], %13\n\t"
    "s_mov_b32 s46, 0x314000\n\t"
    "s_lshl_b32 s68, s89, 12\n\t"
    "ds_read_b128 v[172:175], v226 offset:6144\n\t"
    "v_exp_f32 v92, v92\n\t"
    "v_mfma_f32_32x32x16_bf16 v[0:15], a[196:199], a[132:135], v[0:15]\n\t"
    "s_lshl_b32 s69, %14, 18\n\t"
    "s_add_u32 s69, s69, 2097152\n\t"
    "s_and_b32 s69, s69, 3145728\n\t"
    "s_add_u32 m0, s68, 0\n\t"
    "s_add_u32 s67, s69, 0\n\t"
    "buffer_load_dwordx4 v255, s[44:47], s67 offen offset:0 lds\n\t"
    "ds_read_b128 v[176:179], v226 offset:1024\n\t"
    "v_mfma_f32_32x32x16_bf16 v[16:31], a[196:199], a[164:167], v[16:31]\n\t"
    "ds_read_b128 v[180:183], v226 offset:3072\n\t"
    "v_exp_f32 v93, v93\n\t"
    "v_exp_f32 v94, v94\n\t"
    "v_add_f32 v230, v230, v92\n\t"
    "v_mfma_f32_32x32x16_bf16 v[32:47], a[228:231], a[132:135], v[32:47]\n\t"
    "ds_read_b128 v[184:187], v226 offset:5120\n\t"
    "v_exp_f32 v95, v95\n\t"
    "v_exp_f32 v96, v96\n\t"
    "v_add_f32 v231, v231, v93\n\t"
    "v_mfma_f32_32x32x16_bf16 v[48:63], a[228:231], a[164:167], v[48:63]\n\t"
    "s_add_u32 m0, s68, 1024\n\t"
    "s_add_u32 s67, s69, 4096\n\t"
    "buffer_load_dwordx4 v255, s[44:47], s67 offen offset:0 lds\n\t"
    "ds_read_b128 v[188:191], v226 offset:7168\n\t"
    "v_add_f32 v230, v230, v94\n\t"
    "v_mfma_f32_32x32x16_bf16 v[0:15], a[200:203], a[136:139], v[0:15]\n\t"
    "ds_read_b128 v[192:195], v226 offset:8192\n\t"
    "v_exp_f32 v97, v97\n\t"
    "v_exp_f32 v98, v98\n\t"
    "v_add_f32 v231, v231, v95\n\t"
    "v_mfma_f32_32x32x16_bf16 v[16:31], a[200:203], a[168:171], v[16:31]\n\t"
    "ds_read_b128 v[196:199], v226 offset:10240\n\t"
    "v_exp_f32 v99, v99\n\t"
    "v_exp_f32 v100, v100\n\t"
    "v_add_f32 v228, v228, v96\n\t"
    "v_mfma_f32_32x32x16_bf16 v[32:47], a[232:235], a[136:139], v[32:47]\n\t"
    "s_add_u32 m0, s68, 2048\n\t"
    "s_add_u32 s67, s69, 8192\n\t"
    "buffer_load_dwordx4 v255, s[44:47], s67 offen offset:0 lds\n\t"
    "ds_read_b128 v[200:203], v226 offset:12288\n\t"
    "v_add_f32 v229, v229, v97\n\t"
    "v_mfma_f32_32x32x16_bf16 v[48:63], a[232:235], a[168:171], v[48:63]\n\t"
    "ds_read_b128 v[204:207], v226 offset:14336\n\t"
    "v_exp_f32 v101, v101\n\t"
    "v_exp_f32 v102, v102\n\t"
    "v_add_f32 v228, v228, v98\n\t"
    "v_mfma_f32_32x32x16_bf16 v[0:15], a[204:207], a[140:143], v[0:15]\n\t"
    "ds_read_b128 v[208:211], v226 offset:9216\n\t"
    "v_exp_f32 v103, v103\n\t"
    "v_exp_f32 v112, v112\n\t"
    "v_add_f32 v229, v229, v99\n\t"
    "v_mfma_f32_32x32x16_bf16 v[16:31], a[204:207], a[172:175], v[16:31]\n\t"
    "s_add_u32 m0, s68, 3072\n\t"
    "s_add_u32 s67, s69, 12288\n\t"
    "buffer_load_dwordx4 v255, s[44:47], s67 offen offset:0 lds\n\t"
    "ds_read_b128 v[212:215], v226 offset:11264\n\t"
    "v_add_f32 v228, v228, v100\n\t"
    "v_mfma_f32_32x32x16_bf16 v[32:47], a[236:239], a[140:143], v[32:47]\n\t"
    "ds_read_b128 v[216:219], v226 offset:13312\n\t"
    "v_exp_f32 v113, v113\n\t"
    "v_exp_f32 v114, v114\n\t"
    "v_add_f32 v229, v229, v101\n\t"
    "v_mfma_f32_32x32x16_bf16 v[48:63], a[236:239], a[172:175], v[48:63]\n\t"
    "ds_read_b128 v[220:223], v226 offset:15360\n\t"
    "v_exp_f32 v115, v115\n\t"
    "v_exp_f32 v116, v116\n\t"
    "v_add_f32 v228, v228, v102\n\t"
    "v_mfma_f32_32x32x16_bf16 v[0:15], a[208:211], a[144:147], v[0:15]\n\t"
    "s_add_u32 m0, s68, 16384\n\t"
    "s_add_u32 s67, s69, 65536\n\t"
    "buffer_load_dwordx4 v255, s[44:47], s67 offen offset:0 lds\n\t"
    "v_exp_f32 v117, v117\n\t"
    "v_mfma_f32_32x32x16_bf16 v[16:31], a[208:211], a[176:179], v[16:31]\n\t"
    "v_exp_f32 v118, v118\n\t"
    "v_exp_f32 v119, v119\n\t"
    "v_add_f32 v229, v229, v103\n\t"
    "v_add_f32 v230, v230, v112\n\t"
    "v_mfma_f32_32x32x16_bf16 v[32:47], a[240:243], a[144:147], v[32:47]\n\t"
    "v_exp_f32 v104, v104\n\t"
    "v_exp_f32 v105, v105\n\t"
    "v_add_f32 v231, v231, v113\n\t"
    "v_add_f32 v230, v230, v114\n\t"
    "v_mfma_f32_32x32x16_bf16 v[48:63], a[240:243], a[176:179], v[48:63]\n\t"
    "s_add_u32 m0, s68, 17408\n\t"
    "s_add_u32 s67, s69, 69632\n\t"
    "buffer_load_dwordx4 v255, s[44:47], s67 offen offset:0 lds\n\t"
    "v_exp_f32 v106, v106\n\t"
    "v_mfma_f32_32x32x16_bf16 v[0:15], a[212:215], a[148:151], v[0:15]\n\t"
    "s_and_b32 s66, s57, 0xc000\n\t"
    "v_exp_f32 v107, v107\n\t"
    "v_exp_f32 v108, v108\n\t"
    "v_add_f32 v231, v231, v115\n\t"
    "v_mfma_f32_32x32x16_bf16 v[16:31], a[212:215], a[180:183], v[16:31]\n\t"
    "v_add_u32 v225, s66, v224\n\t"
    "v_exp_f32 v109, v109\n\t"
    "v_exp_f32 v110, v110\n\t"
    "v_add_f32 v230, v230, v116\n\t"
    "v_mfma_f32_32x32x16_bf16 v[32:47], a[244:247], a[148:151], v[32:47]\n\t"
    "s_add_u32 m0, s68, 18432\n\t"
    "s_add_u32 s67, s69, 73728\n\t"
    "buffer_load_dwordx4 v255, s[44:47], s67 offen offset:0 lds\n\t"
    "v_exp_f32 v111, v111\n\t"
    "v_mfma_f32_32x32x16_bf16 v[48:63], a[244:247], a[180:183], v[48:63]\n\t"
    "v_exp_f32 v120, v120\n\t"
    "v_exp_f32 v121, v121\n\t"
    "v_add_f32 v231, v231, v117\n\t"
    "v_add_f32 v230, v230, v118\n\t"
    "v_mfma_f32_32x32x16_bf16 v[0:15], a[216:219], a[152:155], v[0:15]\n\t"
    "v_exp_f32 v122, v122\n\t"
    "v_exp_f32 v123, v123\n\t"
    "v_add_f32 v231, v231, v119\n\t"
    "v_add_f32 v228, v228, v104\n\t"
    "v_mfma_f32_32x32x16_bf16 v[16:31], a[216:219], a[184:187], v[16:31]\n\t"
    "s_add_u32 m0, s68, 19456\n\t"
    "s_add_u32 s67, s69, 77824\n\t"
    "buffer_load_dwordx4 v255, s[44:47], s67 offen offset:0 lds\n\t"
    "v_exp_f32 v124, v124\n\t"
    "v_mfma_f32_32x32x16_bf16 v[32:47], a[248:251], a[152:155], v[32:47]\n\t"
    "v_exp_f32 v125, v125\n\t"
    "v_exp_f32 v126, v126\n\t"
    "v_add_f32 v229, v229, v105\n\t"
    "v_add_f32 v228, v228, v106\n\t"
    "v_mfma_f32_32x32x16_bf16 v[48:63], a[248:251], a[184:187], v[48:63]\n\t"
    "v_exp_f32 v127, v127\n\t"
    "v_add_f32 v229, v229, v107\n\t"
    "v_add_f32 v228, v228, v108\n\t"
    "v_add_f32 v229, v229, v109\n\t"
    "v_add_f32 v228, v228, v110\n\t"
    "v_mfma_f32_32x32x16_bf16 v[0:15], a[220:223], a[156:159], v[0:15]\n\t"
    "v_add_f32 v229, v229, v111\n\t"
    "v_add_f32 v230, v230, v120\n\t"
    "v_add_f32 v231, v231, v121\n\t"
    "v_add_f32 v230, v230, v122\n\t"
    "v_add_f32 v231, v231, v123\n\t"
    "v_add_f32 v230, v230, v124\n\t"
    "v_mfma_f32_32x32x16_bf16 v[16:31], a[220:223], a[188:191], v[16:31]\n\t"
    "v_add_f32 v231, v231, v125\n\t"
    "v_add_f32 v230, v230, v126\n\t"
    "v_add_f32 v231, v231, v127\n\t"
    "v_cvt_pk_bf16_f32 v64, v64, v65\n\t"
    "v_cvt_pk_bf16_f32 v65, v66, v67\n\t"
    "v_cvt_pk_bf16_f32 v66, v68, v69\n\t"
    "v_mfma_f32_32x32x16_bf16 v[32:47], a[252:255], a[156:159], v[32:47]\n\t"
    "v_cvt_pk_bf16_f32 v67, v70, v71\n\t"
    "v_cvt_pk_bf16_f32 v80, v80, v81\n\t"
    "v_cvt_pk_bf16_f32 v81, v82, v83\n\t"
    "v_cvt_pk_bf16_f32 v82, v84, v85\n\t"
    "v_cvt_pk_bf16_f32 v83, v86, v87\n\t"
    "v_cvt_pk_bf16_f32 v72, v72, v73\n\t"
    "v_mfma_f32_32x32x16_bf16 v[48:63], a[252:255], a[188:191], v[48:63]\n\t"
    "v_cvt_pk_bf16_f32 v73, v74, v75\n\t"
    "v_cvt_pk_bf16_f32 v74, v76, v77\n\t"
    "v_cvt_pk_bf16_f32 v75, v78, v79\n\t"
    "v_cvt_pk_bf16_f32 v88, v88, v89\n\t"
    "v_cvt_pk_bf16_f32 v89, v90, v91\n\t"
    "v_cvt_pk_bf16_f32 v90, v92, v93\n\t"
    "v_cvt_pk_bf16_f32 v91, v94, v95\n\t"
    "v_cvt_pk_bf16_f32 v96, v96, v97\n\t"
    "v_cvt_pk_bf16_f32 v97, v98, v99\n\t"
    "v_cvt_pk_bf16_f32 v98, v100, v101\n\t"
    "v_cvt_pk_bf16_f32 v99, v102, v103\n\t"
    "v_cvt_pk_bf16_f32 v112, v112, v113\n\t"
    "v_cvt_pk_bf16_f32 v113, v114, v115\n\t"
    "v_cvt_pk_bf16_f32 v114, v116, v117\n\t"
    "v_cvt_pk_bf16_f32 v115, v118, v119\n\t"
    "v_cvt_pk_bf16_f32 v104, v104, v105\n\t"
    "v_cvt_pk_bf16_f32 v105, v106, v107\n\t"
    "v_cvt_pk_bf16_f32 v106, v108, v109\n\t"
    "v_cvt_pk_bf16_f32 v107, v110, v111\n\t"
    "v_cvt_pk_bf16_f32 v120, v120, v121\n\t"
    "v_cvt_pk_bf16_f32 v121, v122, v123\n\t"
    "v_cvt_pk_bf16_f32 v122, v124, v125\n\t"
    "v_cvt_pk_bf16_f32 v123, v126, v127\n\t"
    "s_waitcnt vmcnt(17) lgkmcnt(0)\n\t"
    "s_barrier\n\t"
    "s_cmp_ge_u32 s56, s63\n\t"
    "s_cbranch_scc0 Lin_109_%=\n\t"
    "s_sub_u32 s66, s56, s63\n\t"
    "s_cmp_ge_u32 s66, 0x4000\n\t"
    "s_cselect_b32 s66, -1, 0\n\t"
    "v_or_b32 v242, s66, v239\n\t"
    "v_cmp_le_i32_e64 s[70:71], 0, v242\n\t"
    "v_cmp_le_i32_e64 s[72:73], 1, v242\n\t"
    "v_cmp_le_i32_e64 s[74:75], 2, v242\n\t"
    "v_cmp_le_i32_e64 s[76:77], 3, v242\n\t"
    "v_cmp_le_i32_e64 s[78:79], 8, v242\n\t"
    "v_cmp_le_i32_e64 s[80:81], 9, v242\n\t"
    "v_cmp_le_i32_e64 s[82:83], 10, v242\n\t"
    "v_cmp_le_i32_e64 s[84:85], 11, v242\n\t"
    "v_pk_mov_b32 v[32:33], v[252:253], v[252:253] op_sel:[0,0]\n\t"
    "v_pk_mov_b32 v[34:35], v[252:253], v[252:253] op_sel:[0,0]\n\t"
    "v_pk_mov_b32 v[36:37], v[252:253], v[252:253] op_sel:[0,0]\n\t"
    "v_pk_mov_b32 v[38:39], v[252:253], v[252:253] op_sel:[0,0]\n\t"
    "v_cndmask_b32_e64 v0, v252, v0, s[70:71]\n\t"
    "v_cndmask_b32_e64 v48, v252, v48, s[70:71]\n\t"
    "v_cndmask_b32_e64 v1, v252, v1, s[72:73]\n\t"
    "v_cndmask_b32_e64 v49, v252, v49, s[72:73]\n\t"
    "v_cndmask_b32_e64 v2, v252, v2, s[74:75]\n\t"
    "v_cndmask_b32_e64 v50, v252, v50, s[74:75]\n\t"
    "v_cndmask_b32_e64 v3, v252, v3, s[76:77]\n\t"
    "v_cndmask_b32_e64 v51, v252, v51, s[76:77]\n\t"
    "v_cndmask_b32_e64 v4, v252, v4, s[78:79]\n\t"
    "v_cndmask_b32_e64 v52, v252, v52, s[78:79]\n\t"
    "v_cndmask_b32_e64 v5, v252, v5, s[80:81]\n\t"
    "v_cndmask_b32_e64 v53, v252, v53, s[80:81]\n\t"
    "v_cndmask_b32_e64 v6, v252, v6, s[82:83]\n\t"
    "v_cndmask_b32_e64 v54, v252, v54, s[82:83]\n\t"
    "v_cndmask_b32_e64 v7, v252, v7, s[84:85]\n\t"
    "v_cndmask_b32_e64 v55, v252, v55, s[84:85]\n\t"
    "v_cmp_le_i32_e64 s[70:71], 16, v242\n\t"
    "v_cmp_le_i32_e64 s[72:73], 17, v242\n\t"
    "v_cmp_le_i32_e64 s[74:75], 18, v242\n\t"
    "v_cmp_le_i32_e64 s[76:77], 19, v242\n\t"
    "v_cmp_le_i32_e64 s[78:79], 24, v242\n\t"
    "v_cmp_le_i32_e64 s[80:81], 25, v242\n\t"
    "v_cmp_le_i32_e64 s[82:83], 26, v242\n\t"
    "v_cmp_le_i32_e64 s[84:85], 27, v242\n\t"
    "v_pk_mov_b32 v[40:41], v[252:253], v[252:253] op_sel:[0,0]\n\t"
    "v_pk_mov_b32 v[42:43], v[252:253], v[252:253] op_sel:[0,0]\n\t"
    "v_pk_mov_b32 v[44:45], v[252:253], v[252:253] op_sel:[0,0]\n\t"
    "v_pk_mov_b32 v[46:47], v[252:253], v[252:253] op_sel:[0,0]\n\t"
    "v_cndmask_b32_e64 v8, v252, v8, s[70:71]\n\t"
    "v_cndmask_b32_e64 v56, v252, v56, s[70:71]\n\t"
    "v_cndmask_b32_e64 v9, v252, v9, s[72:73]\n\t"
    "v_cndmask_b32_e64 v57, v252, v57, s[72:73]\n\t"
    "v_cndmask_b32_e64 v10, v252, v10, s[74:75]\n\t"
    "v_cndmask_b32_e64 v58, v252, v58, s[74:75]\n\t"
    "v_cndmask_b32_e64 v11, v252, v11, s[76:77]\n\t"
    "v_cndmask_b32_e64 v59, v252, v59, s[76:77]\n\t"
    "v_cndmask_b32_e64 v12, v252, v12, s[78:79]\n\t"
    "v_cndmask_b32_e64 v60, v252, v60, s[78:79]\n\t"
    "v_cndmask_b32_e64 v13, v252, v13, s[80:81]\n\t"
    "v_cndmask_b32_e64 v61, v252, v61, s[80:81]\n\t"
    "v_cndmask_b32_e64 v14, v252, v14, s[82:83]\n\t"
    "v_cndmask_b32_e64 v62, v252, v62, s[82:83]\n\t"
    "v_cndmask_b32_e64 v15, v252, v15, s[84:85]\n\t"
    "v_cndmask_b32_e64 v63, v252, v63, s[84:85]\n\t"
    "s_cbranch_scc0 Lfull_108_%=\n\t"
    "v_pk_mov_b32 v[16:17], v[252:253], v[252:253] op_sel:[0,0]\n\t"
    "v_pk_mov_b32 v[18:19], v[252:253], v[252:253] op_sel:[0,0]\n\t"
    "v_pk_mov_b32 v[20:21], v[252:253], v[252:253] op_sel:[0,0]\n\t"
    "v_pk_mov_b32 v[22:23], v[252:253], v[252:253] op_sel:[0,0]\n\t"
    "v_pk_mov_b32 v[24:25], v[252:253], v[252:253] op_sel:[0,0]\n\t"
    "v_pk_mov_b32 v[26:27], v[252:253], v[252:253] op_sel:[0,0]\n\t"
    "v_pk_mov_b32 v[28:29], v[252:253], v[252:253] op_sel:[0,0]\n\t"
    "v_pk_mov_b32 v[30:31], v[252:253], v[252:253] op_sel:[0,0]\n\t"
    "Lfull_108_%=:\n\t"
    "Lin_109_%=:\n\t"
    "v_mfma_f32_32x32x16_bf16 a[0:15], v[160:163], v[64:67], a[0:15]\n\t"
    "s_and_b32 s69, s56, 0xc000\n\t"
    "v_add_u32 v226, s69, v227\n\t"
    "s_nop 3\n\t"
    "v_mfma_f32_32x32x16_bf16 a[16:31], v[160:163], v[80:83], a[16:31]\n\t"
    "v_max3_f32 v234, v0, v1, v2\n\t"
    "v_max3_f32 v235, v16, v17, v18\n\t"
    "v_max3_f32 v236, v32, v33, v34\n\t"
    "v_max3_f32 v237, v48, v49, v50\n\t"
    "v_mfma_f32_32x32x16_bf16 a[32:47], v[164:167], v[64:67], a[32:47]\n\t"
    "v_max3_f32 v234, v234, v3, v4\n\t"
    "v_max3_f32 v235, v235, v19, v20\n\t"
    "v_max3_f32 v236, v236, v35, v36\n\t"
    "v_max3_f32 v237, v237, v51, v52\n\t"
    "v_mfma_f32_32x32x16_bf16 a[48:63], v[164:167], v[80:83], a[48:63]\n\t"
    "v_max3_f32 v234, v234, v5, v6\n\t"
    "v_max3_f32 v235, v235, v21, v22\n\t"
    "v_max3_f32 v236, v236, v37, v38\n\t"
    "v_max3_f32 v237, v237, v53, v54\n\t"
    "v_mfma_f32_32x32x16_bf16 a[64:79], v[168:171], v[64:67], a[64:79]\n\t"
    "v_max3_f32 v234, v234, v7, v8\n\t"
    "v_max3_f32 v235, v235, v23, v24\n\t"
    "v_max3_f32 v236, v236, v39, v40\n\t"
    "v_max3_f32 v237, v237, v55, v56\n\t"
    "v_mfma_f32_32x32x16_bf16 a[80:95], v[168:171], v[80:83], a[80:95]\n\t"
    "v_max3_f32 v234, v234, v9, v10\n\t"
    "v_max3_f32 v235, v235, v25, v26\n\t"
    "v_max3_f32 v236, v236, v41, v42\n\t"
    "v_max3_f32 v237, v237, v57, v58\n\t"
    "v_mfma_f32_32x32x16_bf16 a[96:111], v[172:175], v[64:67], a[96:111]\n\t"
    "v_max3_f32 v234, v234, v11, v12\n\t"
    "v_max3_f32 v235, v235, v27, v28\n\t"
    "v_max3_f32 v236, v236, v43, v44\n\t"
    "v_max3_f32 v237, v237, v59, v60\n\t"
    "v_mfma_f32_32x32x16_bf16 a[112:127], v[172:175], v[80:83], a[112:127]\n\t"
    "v_max3_f32 v234, v234, v13, v14\n\t"
    "v_max3_f32 v235, v235, v29, v30\n\t"
    "v_max3_f32 v236, v236, v45, v46\n\t"
    "v_max3_f32 v237, v237, v61, v62\n\t"
    "v_mfma_f32_32x32x16_bf16 a[0:15], v[176:179], v[72:75], a[0:15]\n\t"
    "v_max_f32 v234, v234, v15\n\t"
    "v_max_f32 v235, v235, v31\n\t"
    "v_max_f32 v236, v236, v47\n\t"
    "v_max_f32 v237, v237, v63\n\t"
    "v_mfma_f32_32x32x16_bf16 a[16:31], v[176:179], v[88:91], a[16:31]\n\t"
    "v_max3_f32 v238, v234, v235, v236\n\t"
    "v_max_f32 v238, v238, v237\n\t"
    "v_cmp_lt_f32 vcc, s60, v238\n\t"
    "s_cbranch_vccnz Lst_110_%=\n\t"
    "Lbk_111_%=:\n\t"
    "v_exp_f32 v0, v0\n\t"
    "v_mfma_f32_32x32x16_bf16 a[32:47], v[180:183], v[72:75], a[32:47]\n\t"
    "s_lshl_b32 s69, %14, 18\n\t"
    "s_add_u32 s69, s69, 3145728\n\t"
    "s_and_b32 s69, s69, 3145728\n\t"
    "s_add_u32 m0, s68, 32768\n\t"
    "s_add_u32 s67, s69, 0\n\t"
    "buffer_load_dwordx4 v255, s[44:47], s67 offen offset:0 lds\n\t"
    "v_mfma_f32_32x32x16_bf16 a[48:63], v[180:183], v[88:91], a[48:63]\n\t"
    "s_add_u32 m0, s68, 33792\n\t"
    "s_add_u32 s67, s69, 4096\n\t"
    "buffer_load_dwordx4 v255, s[44:47], s67 offen offset:0 lds\n\t"
    "v_exp_f32 v1, v1\n\t"
    "v_mfma_f32_32x32x16_bf16 a[64:79], v[184:187], v[72:75], a[64:79]\n\t"
    "s_add_u32 m0, s68, 34816\n\t"
    "s_add_u32 s67, s69, 8192\n\t"
    "buffer_load_dwordx4 v255, s[44:47], s67 offen offset:0 lds\n\t"
    "v_exp_f32 v2, v2\n\t"
    "v_mfma_f32_32x32x16_bf16 a[80:95], v[184:187], v[88:91], a[80:95]\n\t"
    "s_add_u32 m0, s68, 35840\n\t"
    "s_add_u32 s67, s69, 12288\n\t"
    "buffer_load_dwordx4 v255, s[44:47], s67 offen offset:0 lds\n\t"
    "v_exp_f32 v3, v3\n\t"
    "v_mfma_f32_32x32x16_bf16 a[96:111], v[188:191], v[72:75], a[96:111]\n\t"
    "s_add_u32 m0, s68, 49152\n\t"
    "s_add_u32 s67, s69, 65536\n\t"
    "buffer_load_dwordx4 v255, s[44:47], s67 offen offset:0 lds\n\t"
    "v_exp_f32 v4, v4\n\t"
    "v_mfma_f32_32x32x16_bf16 a[112:127], v[188:191], v[88:91], a[112:127]\n\t"
    "s_add_u32 m0, s68, 50176\n\t"
    "s_add_u32 s67, s69, 69632\n\t"
    "buffer_load_dwordx4 v255, s[44:47], s67 offen offset:0 lds\n\t"
    "v_exp_f32 v5, v5\n\t"
    "v_mfma_f32_32x32x16_bf16 a[0:15], v[192:195], v[96:99], a[0:15]\n\t"
    "s_add_u32 m0, s68, 51200\n\t"
    "s_add_u32 s67, s69, 73728\n\t"
    "buffer_load_dwordx4 v255, s[44:47], s67 offen offset:0 lds\n\t"
    "v_exp_f32 v6, v6\n\t"
    "v_mfma_f32_32x32x16_bf16 a[16:31], v[192:195], v[112:115], a[16:31]\n\t"
    "s_add_u32 m0, s68, 52224\n\t"
    "s_add_u32 s67, s69, 77824\n\t"
    "buffer_load_dwordx4 v255, s[44:47], s67 offen offset:0 lds\n\t"
    "v_exp_f32 v7, v7\n\t"
    "v_mfma_f32_32x32x16_bf16 a[32:47], v[196:199], v[96:99], a[32:47]\n\t"
    "s_lshl_b32 s69, %14, 18\n\t"
    "s_add_u32 s69, s69, 4194304\n\t"
    "s_and_b32 s69, s69, 3145728\n\t"
    "s_add_u32 m0, s68, 147456\n\t"
    "s_add_u32 s67, s69, 0\n\t"
    "buffer_load_dwordx4 v255, s[44:47], s67 offen offset:0 lds\n\t"
    "v_mfma_f32_32x32x16_bf16 a[48:63], v[196:199], v[112:115], a[48:63]\n\t"
    "s_add_u32 m0, s68, 148480\n\t"
    "s_add_u32 s67, s69, 4096\n\t"
    "buffer_load_dwordx4 v255, s[44:47], s67 offen offset:0 lds\n\t"
    "v_exp_f32 v16, v16\n\t"
    "v_mfma_f32_32x32x16_bf16 a[64:79], v[200:203], v[96:99], a[64:79]\n\t"
    "s_add_u32 m0, s68, 149504\n\t"
    "s_add_u32 s67, s69, 8192\n\t"
    "buffer_load_dwordx4 v255, s[44:47], s67 offen offset:0 lds\n\t"
    "v_exp_f32 v17, v17\n\t"
    "v_mfma_f32_32x32x16_bf16 a[80:95], v[200:203], v[112:115], a[80:95]\n\t"
    "s_add_u32 m0, s68, 150528\n\t"
    "s_add_u32 s67, s69, 12288\n\t"
    "buffer_load_dwordx4 v255, s[44:47], s67 offen offset:0 lds\n\t"
    "v_exp_f32 v18, v18\n\t"
    "v_mfma_f32_32x32x16_bf16 a[96:111], v[204:207], v[96:99], a[96:111]\n\t"
    "v_exp_f32 v19, v19\n\t"
    "v_exp_f32 v20, v20\n\t"
    "v_add_f32 v228, v228, v0\n\t"
    "v_add_f32 v229, v229, v1\n\t"
    "v_mfma_f32_32x32x16_bf16 a[112:127], v[204:207], v[112:115], a[112:127]\n\t"
    "v_mov_b32 v243, %14\n\t"
    "v_and_b32 v243, -16, v243\n\t"
    "global_load_dwordx4 v[244:247], v243, %18 sc1\n\t"
    "v_exp_f32 v21, v21\n\t"
    "v_add_f32 v228, v228, v2\n\t"
    "v_mfma_f32_32x32x16_bf16 a[0:15], v[208:211], v[104:107], a[0:15]\n\t"
    "v_exp_f32 v22, v22\n\t"
    "v_exp_f32 v23, v23\n\t"
    "v_add_f32 v229, v229, v3\n\t"
    "v_add_f32 v228, v228, v4\n\t"
    "v_mfma_f32_32x32x16_bf16 a[16:31], v[208:211], v[120:123], a[16:31]\n\t"
    "v_exp_f32 v8, v8\n\t"
    "v_exp_f32 v9, v9\n\t"
    "v_add_f32 v229, v229, v5\n\t"
    "v_add_f32 v228, v228, v6\n\t"
    "v_mfma_f32_32x32x16_bf16 a[32:47], v[212:215], v[104:107], a[32:47]\n\t"
    "v_exp_f32 v10, v10\n\t"
    "v_exp_f32 v11, v11\n\t"
    "v_add_f32 v229, v229, v7\n\t"
    "v_add_f32 v230, v230, v16\n\t"
    "v_mfma_f32_32x32x16_bf16 a[48:63], v[212:215], v[120:123], a[48:63]\n\t"
    "v_exp_f32 v12, v12\n\t"
    "v_exp_f32 v13, v13\n\t"
    "v_add_f32 v231, v231, v17\n\t"
    "v_add_f32 v230, v230, v18\n\t"
    "v_mfma_f32_32x32x16_bf16 a[64:79], v[216:219], v[104:107], a[64:79]\n\t"
    "v_exp_f32 v14, v14\n\t"
    "v_exp_f32 v15, v15\n\t"
    "v_add_f32 v231, v231, v19\n\t"
    "v_add_f32 v230, v230, v20\n\t"
    "v_mfma_f32_32x32x16_bf16 a[80:95], v[216:219], v[120:123], a[80:95]\n\t"
    "v_exp_f32 v24, v24\n\t"
    "v_exp_f32 v25, v25\n\t"
    "v_add_f32 v231, v231, v21\n\t"
    "v_add_f32 v230, v230, v22\n\t"
    "v_mfma_f32_32x32x16_bf16 a[96:111], v[220:223], v[104:107], a[96:111]\n\t"
    "v_exp_f32 v26, v26\n\t"
    "v_exp_f32 v27, v27\n\t"
    "v_add_f32 v231, v231, v23\n\t"
    "v_add_f32 v228, v228, v8\n\t"
    "v_mfma_f32_32x32x16_bf16 a[112:127], v[220:223], v[120:123], a[112:127]\n\t"
    "v_add_f32 v229, v229, v9\n\t"
    "v_add_f32 v228, v228, v10\n\t"
    "v_add_f32 v229, v229, v11\n\t"
    "v_add_f32 v228, v228, v12\n\t"
    "v_add_f32 v229, v229, v13\n\t"
    "v_add_f32 v228, v228, v14\n\t"
    "v_add_f32 v229, v229, v15\n\t"
    "v_add_f32 v230, v230, v24\n\t"
    "v_add_f32 v231, v231, v25\n\t"
    "v_add_f32 v230, v230, v26\n\t"
    "v_add_f32 v231, v231, v27\n\t"
    "s_mov_b32 s56, s57\n\t"
    "s_mov_b32 s57, s58\n\t"
    "s_add_u32 s58, s58, 0x8000\n\t"
    "s_cmp_lg_u32 s62, 0\n\t"
    "s_cbranch_scc1 Lst_112_%=\n\t"
    "Lbk_113_%=:\n\t"
    "s_waitcnt vmcnt(22)\n\t"
    "s_waitcnt lgkmcnt(0)\n\t"
    "s_barrier\n\t"
    "s_add_u32 s52, s52, 1\n\t"
    "ds_read_b128 v[160:163], v226 offset:0\n\t"
    "ds_read_b128 v[164:167], v226 offset:2048\n\t"
    "ds_read_b128 v[168:171], v226 offset:4096\n\t"
    "ds_read_b128 v[172:175], v226 offset:6144\n\t"
    "ds_read_b128 v[176:179], v226 offset:1024\n\t"
    "ds_read_b128 v[180:183], v226 offset:3072\n\t"
    "ds_read_b128 v[184:187], v226 offset:5120\n\t"
    "ds_read_b128 v[188:191], v226 offset:7168\n\t"
    "ds_read_b128 v[192:195], v226 offset:8192\n\t"
    "ds_read_b128 v[196:199], v226 offset:10240\n\t"
    "ds_read_b128 v[200:203], v226 offset:12288\n\t"
    "ds_read_b128 v[204:207], v226 offset:14336\n\t"
    "ds_read_b128 v[208:211], v226 offset:9216\n\t"
    "ds_read_b128 v[212:215], v226 offset:11264\n\t"
    "ds_read_b128 v[216:219], v226 offset:13312\n\t"
    "ds_read_b128 v[220:223], v226 offset:15360\n\t"
    "v_cvt_pk_bf16_f32 v0, v0, v1\n\t"
    "v_cvt_pk_bf16_f32 v1, v2, v3\n\t"
    "v_cvt_pk_bf16_f32 v2, v4, v5\n\t"
    "v_cvt_pk_bf16_f32 v3, v6, v7\n\t"
    "v_cvt_pk_bf16_f32 v16, v16, v17\n\t"
    "v_cvt_pk_bf16_f32 v17, v18, v19\n\t"
    "v_cvt_pk_bf16_f32 v18, v20, v21\n\t"
    "v_cvt_pk_bf16_f32 v19, v22, v23\n\t"
    "s_nop 0\n\t"
    "s_nop 0\n\t"
    "s_waitcnt lgkmcnt(12)\n\t"
    "v_mfma_f32_32x32x16_bf16 a[0:15], v[160:163], v[0:3], a[0:15]\n\t"
    "v_cvt_pk_bf16_f32 v8, v8, v9\n\t"
    "v_cvt_pk_bf16_f32 v9, v10, v11\n\t"
    "v_cvt_pk_bf16_f32 v10, v12, v13\n\t"
    "v_cvt_pk_bf16_f32 v11, v14, v15\n\t"
    "v_exp_f32 v28, v28\n\t"
    "v_mfma_f32_32x32x16_bf16 a[16:31], v[160:163], v[16:19], a[16:31]\n\t"
    "v_exp_f32 v29, v29\n\t"
    "v_exp_f32 v30, v30\n\t"
    "v_add_f32 v230, v230, v28\n\t"
    "v_add_f32 v231, v231, v29\n\t"
    "v_mfma_f32_32x32x16_bf16 a[32:47], v[164:167], v[0:3], a[32:47]\n\t"
    "v_exp_f32 v31, v31\n\t"
    "v_add_f32 v230, v230, v30\n\t"
    "v_add_f32 v231, v231, v31\n\t"
    "v_cvt_pk_bf16_f32 v24, v24, v25\n\t"
    "v_cvt_pk_bf16_f32 v25, v26, v27\n\t"
    "v_mfma_f32_32x32x16_bf16 a[48:63], v[164:167], v[16:19], a[48:63]\n\t"
    "v_cvt_pk_bf16_f32 v26, v28, v29\n\t"
    "v_cvt_pk_bf16_f32 v27, v30, v31\n\t"
    "v_exp_f32 v32, v32\n\t"
    "v_exp_f32 v33, v33\n\t"
    "v_mfma_f32_32x32x16_bf16 a[64:79], v[168:171], v[0:3], a[64:79]\n\t"
    "v_exp_f32 v34, v34\n\t"
    "v_exp_f32 v35, v35\n\t"
    "v_add_f32 v228, v228, v32\n\t"
    "v_add_f32 v229, v229, v33\n\t"
    "v_mfma_f32_32x32x16_bf16 a[80:95], v[168:171], v[16:19], a[80:95]\n\t"
    "v_exp_f32 v36, v36\n\t"
    "v_exp_f32 v37, v37\n\t"
    "v_add_f32 v228, v228, v34\n\t"
    "v_add_f32 v229, v229, v35\n\t"
    "v_mfma_f32_32x32x16_bf16 a[96:111], v[172:175], v[0:3], a[96:111]\n\t"
    "v_exp_f32 v38, v38\n\t"
    "v_exp_f32 v39, v39\n\t"
    "v_add_f32 v228, v228, v36\n\t"
    "v_add_f32 v229, v229, v37\n\t"
    "v_mfma_f32_32x32x16_bf16 a[112:127], v[172:175], v[16:19], a[112:127]\n\t"
    "s_waitcnt lgkmcnt(8)\n\t"
    "v_add_f32 v228, v228, v38\n\t"
    "v_add_f32 v229, v229, v39\n\t"
    "v_cvt_pk_bf16_f32 v32, v32, v33\n\t"
    "v_cvt_pk_bf16_f32 v33, v34, v35\n\t"
    "v_cvt_pk_bf16_f32 v34, v36, v37\n\t"
    "v_mfma_f32_32x32x16_bf16 a[0:15], v[176:179], v[8:11], a[0:15]\n\t"
    "v_cvt_pk_bf16_f32 v35, v38, v39\n\t"
    "v_exp_f32 v48, v48\n\t"
    "v_exp_f32 v49, v49\n\t"
    "v_add_f32 v230, v230, v48\n\t"
    "v_mfma_f32_32x32x16_bf16 a[16:31], v[176:179], v[24:27], a[16:31]\n\t"
    "v_exp_f32 v50, v50\n\t"
    "v_exp_f32 v51, v51\n\t"
    "v_add_f32 v231, v231, v49\n\t"
    "v_add_f32 v230, v230, v50\n\t"
    "v_mfma_f32_32x32x16_bf16 a[32:47], v[180:183], v[8:11], a[32:47]\n\t"
    "v_exp_f32 v52, v52\n\t"
    "v_exp_f32 v53, v53\n\t"
    "v_add_f32 v231, v231, v51\n\t"
    "v_add_f32 v230, v230, v52\n\t"
    "v_mfma_f32_32x32x16_bf16 a[48:63], v[180:183], v[24:27], a[48:63]\n\t"
    "v_exp_f32 v54, v54\n\t"
    "v_exp_f32 v55, v55\n\t"
    "v_add_f32 v231, v231, v53\n\t"
    "v_add_f32 v230, v230, v54\n\t"
    "v_mfma_f32_32x32x16_bf16 a[64:79], v[184:187], v[8:11], a[64:79]\n\t"
    "v_add_f32 v231, v231, v55\n\t"
    "v_cvt_pk_bf16_f32 v48, v48, v49\n\t"
    "v_cvt_pk_bf16_f32 v49, v50, v51\n\t"
    "v_cvt_pk_bf16_f32 v50, v52, v53\n\t"
    "v_cvt_pk_bf16_f32 v51, v54, v55\n\t"
    "v_mfma_f32_32x32x16_bf16 a[80:95], v[184:187], v[24:27], a[80:95]\n\t"
    "v_exp_f32 v40, v40\n\t"
    "v_exp_f32 v41, v41\n\t"
    "v_add_f32 v228, v228, v40\n\t"
    "v_add_f32 v229, v229, v41\n\t"
    "v_mfma_f32_32x32x16_bf16 a[96:111], v[188:191], v[8:11], a[96:111]\n\t"
    "v_exp_f32 v42, v42\n\t"
    "v_exp_f32 v43, v43\n\t"
    "v_add_f32 v228, v228, v42\n\t"
    "v_add_f32 v229, v229, v43\n\t"
    "v_mfma_f32_32x32x16_bf16 a[112:127], v[188:191], v[24:27], a[112:127]\n\t"
    "s_waitcnt lgkmcnt(4)\n\t"
    "v_exp_f32 v44, v44\n\t"
    "v_exp_f32 v45, v45\n\t"
    "v_add_f32 v228, v228, v44\n\t"
    "v_mfma_f32_32x32x16_bf16 a[0:15], v[192:195], v[32:35], a[0:15]\n\t"
    "v_exp_f32 v46, v46\n\t"
    "v_exp_f32 v47, v47\n\t"
    "v_add_f32 v229, v229, v45\n\t"
    "v_add_f32 v228, v228, v46\n\t"
    "v_mfma_f32_32x32x16_bf16 a[16:31], v[192:195], v[48:51], a[16:31]\n\t"
    "v_add_f32 v229, v229, v47\n\t"
    "v_cvt_pk_bf16_f32 v40, v40, v41\n\t"
    "v_cvt_pk_bf16_f32 v41, v42, v43\n\t"
    "v_cvt_pk_bf16_f32 v42, v44, v45\n\t"
    "v_cvt_pk_bf16_f32 v43, v46, v47\n\t"
    "v_mfma_f32_32x32x16_bf16 a[32:47], v[196:199], v[32:35], a[32:47]\n\t"
    "v_exp_f32 v56, v56\n\t"
    "v_exp_f32 v57, v57\n\t"
    "v_add_f32 v230, v230, v56\n\t"
    "v_add_f32 v231, v231, v57\n\t"
    "v_mfma_f32_32x32x16_bf16 a[48:63], v[196:199], v[48:51], a[48:63]\n\t"
    "v_exp_f32 v58, v58\n\t"
    "v_exp_f32 v59, v59\n\t"
    "v_add_f32 v230, v230, v58\n\t"
    "v_add_f32 v231, v231, v59\n\t"
    "v_mfma_f32_32x32x16_bf16 a[64:79], v[200:203], v[32:35], a[64:79]\n\t"
    "s_and_b32 s70, %14, 12\n\t"
    "s_lshl_b32 s70, s70, 14\n\t"
    "s_sub_u32 s44, s48, s70\n\t"
    "s_subb_u32 s45, s49, 0\n\t"
    "v_exp_f32 v60, v60\n\t"
    "v_mfma_f32_32x32x16_bf16 a[80:95], v[200:203], v[48:51], a[80:95]\n\t"
    "s_lshl_b32 s70, %10, 2\n\t"
    "s_add_u32 s70, s70, s89\n\t"
    "s_lshl_b32 s70, s70, 18\n\t"
    "v_exp_f32 v61, v61\n\t"
    "v_add_f32 v230, v230, v60\n\t"
    "v_mfma_f32_32x32x16_bf16 a[96:111], v[204:207], v[32:35], a[96:111]\n\t"
    "v_exp_f32 v62, v62\n\t"
    "v_exp_f32 v63, v63\n\t"
    "v_add_f32 v231, v231, v61\n\t"
    "v_add_f32 v230, v230, v62\n\t"
    "v_mfma_f32_32x32x16_bf16 a[112:127], v[204:207], v[48:51], a[112:127]\n\t"
    "s_waitcnt lgkmcnt(0)\n\t"
    "v_add_f32 v231, v231, v63\n\t"
    "v_cvt_pk_bf16_f32 v56, v56, v57\n\t"
    "v_cvt_pk_bf16_f32 v57, v58, v59\n\t"
    "v_cvt_pk_bf16_f32 v58, v60, v61\n\t"
    "v_cvt_pk_bf16_f32 v59, v62, v63\n\t"
    "s_nop 0\n\t"
    "s_nop 0\n\t"
    "v_mfma_f32_32x32x16_bf16 a[0:15], v[208:211], v[40:43], a[0:15]\n\t"
    "s_waitcnt vmcnt(0)\n\t"
    "s_barrier\n\t"
    "v_max_u32 v244, v244, v245\n\t"
    "v_max3_u32 v244, v244, v246, v247\n\t"
    "v_cmp_eq_u32 vcc, -1, v244\n\t"
    "s_nop 0\n\t"
    "s_cmp_eq_u64 vcc, 0\n\t"
    "s_cselect_b32 s46, 0xff2000, 0\n\t"
    "v_mfma_f32_32x32x16_bf16 a[16:31], v[208:211], v[56:59], a[16:31]\n\t"
    "s_lshl_b32 s71, %14, 14\n\t"
    "s_add_u32 s71, s71, 131072\n\t"
    "s_and_b32 s71, s71, 196608\n\t"
    "s_add_u32 s71, s71, s70\n\t"
    "buffer_load_dwordx4 a[128:131], v224, s[44:47], s71 offen offset:0 sc1\n\t"
    "v_mfma_f32_32x32x16_bf16 a[32:47], v[212:215], v[40:43], a[32:47]\n\t"
    "buffer_load_dwordx4 a[132:135], v224, s[44:47], s71 offen offset:1024 sc1\n\t"
    "v_mfma_f32_32x32x16_bf16 a[48:63], v[212:215], v[56:59], a[48:63]\n\t"
    "buffer_load_dwordx4 a[136:139], v224, s[44:47], s71 offen offset:2048 sc1\n\t"
    "v_mfma_f32_32x32x16_bf16 a[64:79], v[216:219], v[40:43], a[64:79]\n\t"
    "buffer_load_dwordx4 a[140:143], v224, s[44:47], s71 offen offset:3072 sc1\n\t"
    "v_mfma_f32_32x32x16_bf16 a[80:95], v[216:219], v[56:59], a[80:95]\n\t"
    "s_add_u32 s71, s71, 0x1000\n\t"
    "buffer_load_dwordx4 a[144:147], v224, s[44:47], s71 offen offset:0 sc1\n\t"
    "v_mfma_f32_32x32x16_bf16 a[96:111], v[220:223], v[40:43], a[96:111]\n\t"
    "buffer_load_dwordx4 a[148:151], v224, s[44:47], s71 offen offset:1024 sc1\n\t"
    "v_mfma_f32_32x32x16_bf16 a[112:127], v[220:223], v[56:59], a[112:127]\n\t"
    "buffer_load_dwordx4 a[152:155], v224, s[44:47], s71 offen offset:2048 sc1\n\t"
    "v_add_f32 v228, v228, v229\n\t"
    "v_add_f32 v230, v230, v231\n\t"
    "v_mov_b32 v240, v228\n\t"
    "s_nop 1\n\t"
    "v_permlane32_swap_b32 v228, v240\n\t"
    "s_nop 0\n\t"
    "v_add_f32 v228, v228, v240\n\t"
    "v_mov_b32 v241, v230\n\t"
    "s_nop 1\n\t"
    "v_permlane32_swap_b32 v230, v241\n\t"
    "s_nop 0\n\t"
    "v_add_f32 v230, v230, v241\n\t"
    "s_lshl_b32 s66, s89, 14\n\t"
    "s_xor_b32 s67, s66, 0x8000\n\t"
    "v_add_u32 v244, s66, v227\n\t"
    "v_add_u32 v245, s67, v227\n\t"
    "s_lshr_b32 s66, s66, 4\n\t"
    "s_lshr_b32 s67, s67, 4\n\t"
    "v_lshrrev_b32 v246, 2, v224\n\t"
    "v_add_u32 v246, 0x20000, v246\n\t"
    "v_add_u32 v247, s67, v246\n\t"
    "v_add_u32 v246, s66, v246\n\t"
    "ds_write_b32 v246, v232 offset:0\n\t"
    "ds_write_b32 v246, v233 offset:256\n\t"
    "ds_write_b32 v246, v228 offset:512\n\t"
    "ds_write_b32 v246, v230 offset:768\n\t"
    "s_lshr_b32 s73, s89, 1\n\t"
    "s_lshl_b32 s73, s73, 12\n\t"
    "s_add_u32 s73, s73, s88\n\t"
    "s_cmp_eq_u32 s46, 0\n\t"
    "s_cbranch_scc1 Llate_%=\n\t"
    "Lnorm_%=:\n\t"
    "s_cmp_ge_u32 s89, 2\n\t"
    "s_cbranch_scc1 La1_148_%=\n\t"
    "ds_write_b128 v244, a[64:67] offset:0\n\t"
    "buffer_load_dwordx4 a[156:159], v224, s[44:47], s71 offen offset:3072 sc1\n\t"
    "ds_write_b128 v244, a[68:71] offset:1024\n\t"
    "s_lshl_b32 s71, %14, 14\n\t"
    "s_add_u32 s71, s71, 196608\n\t"
    "s_and_b32 s71, s71, 196608\n\t"
    "s_add_u32 s71, s71, s70\n\t"
    "buffer_load_dwordx4 a[160:163], v224, s[44:47], s71 offen offset:0 sc1\n\t"
    "ds_write_b128 v244, a[72:75] offset:2048\n\t"
    "ds_write_b128 v244, a[76:79] offset:3072\n\t"
    "buffer_load_dwordx4 a[164:167], v224, s[44:47], s71 offen offset:1024 sc1\n\t"
    "ds_write_b128 v244, a[80:83] offset:4096\n\t"
    "ds_write_b128 v244, a[84:87] offset:5120\n\t"
    "buffer_load_dwordx4 a[168:171], v224, s[44:47], s71 offen offset:2048 sc1\n\t"
    "ds_write_b128 v244, a[88:91] offset:6144\n\t"
    "ds_write_b128 v244, a[92:95] offset:7168\n\t"
    "buffer_load_dwordx4 a[172:175], v224, s[44:47], s71 offen offset:3072 sc1\n\t"
    "ds_write_b128 v244, a[96:99] offset:8192\n\t"
    "ds_write_b128 v244, a[100:103] offset:9216\n\t"
    "s_add_u32 s71, s71, 0x1000\n\t"
    "buffer_load_dwordx4 a[176:179], v224, s[44:47], s71 offen offset:0 sc1\n\t"
    "ds_write_b128 v244, a[104:107] offset:10240\n\t"
    "ds_write_b128 v244, a[108:111] offset:11264\n\t"
    "buffer_load_dwordx4 a[180:183], v224, s[44:47], s71 offen offset:1024 sc1\n\t"
    "ds_write_b128 v244, a[112:115] offset:12288\n\t"
    "ds_write_b128 v244, a[116:119] offset:13312\n\t"
    "buffer_load_dwordx4 a[184:187], v224, s[44:47], s71 offen offset:2048 sc1\n\t"
    "ds_write_b128 v244, a[120:123] offset:14336\n\t"
    "ds_write_b128 v244, a[124:127] offset:15360\n\t"
    "buffer_load_dwordx4 a[188:191], v224, s[44:47], s71 offen offset:3072 sc1\n\t"
    "s_branch La2_149_%=\n\t"
    "La1_148_%=:\n\t"
    "ds_write_b128 v244, a[0:3] offset:0\n\t"
    "buffer_load_dwordx4 a[156:159], v224, s[44:47], s71 offen offset:3072 sc1\n\t"
    "ds_write_b128 v244, a[4:7] offset:1024\n\t"
    "s_lshl_b32 s71, %14, 14\n\t"
    "s_add_u32 s71, s71, 196608\n\t"
    "s_and_b32 s71, s71, 196608\n\t"
    "s_add_u32 s71, s71, s70\n\t"
    "buffer_load_dwordx4 a[160:163], v224, s[44:47], s71 offen offset:0 sc1\n\t"
    "ds_write_b128 v244, a[8:11] offset:2048\n\t"
    "ds_write_b128 v244, a[12:15] offset:3072\n\t"
    "buffer_load_dwordx4 a[164:167], v224, s[44:47], s71 offen offset:1024 sc1\n\t"
    "ds_write_b128 v244, a[16:19] offset:4096\n\t"
    "ds_write_b128 v244, a[20:23] offset:5120\n\t"
    "buffer_load_dwordx4 a[168:171], v224, s[44:47], s71 offen offset:2048 sc1\n\t"
    "ds_write_b128 v244, a[24:27] offset:6144\n\t"
    "ds_write_b128 v244, a[28:31] offset:7168\n\t"
    "buffer_load_dwordx4 a[172:175], v224, s[44:47], s71 offen offset:3072 sc1\n\t"
    "ds_write_b128 v244, a[32:35] offset:8192\n\t"
    "ds_write_b128 v244, a[36:39] offset:9216\n\t"
    "s_add_u32 s71, s71, 0x1000\n\t"
    "buffer_load_dwordx4 a[176:179], v224, s[44:47], s71 offen offset:0 sc1\n\t"
    "ds_write_b128 v244, a[40:43] offset:10240\n\t"
    "ds_write_b128 v244, a[44:47] offset:11264\n\t"
    "buffer_load_dwordx4 a[180:183], v224, s[44:47], s71 offen offset:1024 sc1\n\t"
    "ds_write_b128 v244, a[48:51] offset:12288\n\t"
    "ds_write_b128 v244, a[52:55] offset:13312\n\t"
    "buffer_load_dwordx4 a[184:187], v224, s[44:47], s71 offen offset:2048 sc1\n\t"
    "ds_write_b128 v244, a[56:59] offset:14336\n\t"
    "ds_write_b128 v244, a[60:63] offset:15360\n\t"
    "buffer_load_dwordx4 a[188:191], v224, s[44:47], s71 offen offset:3072 sc1\n\t"
    "La2_149_%=:\n\t"
    "s_waitcnt lgkmcnt(0)\n\t"
    "s_barrier\n\t"
    "ds_read_b32 v240, v247 offset:0\n\t"
    "ds_read_b32 v241, v247 offset:256\n\t"
    "ds_read_b32 v242, v247 offset:512\n\t"
    "ds_read_b32 v243, v247 offset:768\n\t"
    "ds_read_b128 v[0:3], v245 offset:0\n\t"
    "ds_read_b128 v[4:7], v245 offset:1024\n\t"
    "ds_read_b128 v[8:11], v245 offset:2048\n\t"
    "ds_read_b128 v[12:15], v245 offset:3072\n\t"
    "ds_read_b128 v[16:19], v245 offset:4096\n\t"
    "ds_read_b128 v[20:23], v245 offset:5120\n\t"
    "ds_read_b128 v[24:27], v245 offset:6144\n\t"
    "ds_read_b128 v[28:31], v245 offset:7168\n\t"
    "ds_read_b128 v[32:35], v245 offset:8192\n\t"
    "ds_read_b128 v[36:39], v245 offset:9216\n\t"
    "ds_read_b128 v[40:43], v245 offset:10240\n\t"
    "ds_read_b128 v[44:47], v245 offset:11264\n\t"
    "ds_read_b128 v[48:51], v245 offset:12288\n\t"
    "ds_read_b128 v[52:55], v245 offset:13312\n\t"
    "ds_read_b128 v[56:59], v245 offset:14336\n\t"
    "ds_read_b128 v[60:63], v245 offset:15360\n\t"
    "s_cmp_ge_u32 s89, 2\n\t"
    "s_cbranch_scc1 La1_150_%=\n\t"
    "v_accvgpr_read_b32 v64, a0\n\t"
    "s_lshl_b32 s71, %14, 14\n\t"
    "s_add_u32 s71, s71, 262144\n\t"
    "s_and_b32 s71, s71, 196608\n\t"
    "s_add_u32 s71, s71, s70\n\t"
    "buffer_load_dwordx4 a[192:195], v224, s[44:47], s71 offen offset:0 sc1\n\t"
    "v_accvgpr_read_b32 v65, a1\n\t"
    "v_accvgpr_read_b32 v66, a2\n\t"
    "v_accvgpr_read_b32 v67, a3\n\t"
    "v_accvgpr_read_b32 v68, a4\n\t"
    "buffer_load_dwordx4 a[196:199], v224, s[44:47], s71 offen offset:1024 sc1\n\t"
    "v_accvgpr_read_b32 v69, a5\n\t"
    "v_accvgpr_read_b32 v70, a6\n\t"
    "v_accvgpr_read_b32 v71, a7\n\t"
    "v_accvgpr_read_b32 v72, a8\n\t"
    "buffer_load_dwordx4 a[200:203], v224, s[44:47], s71 offen offset:2048 sc1\n\t"
    "v_accvgpr_read_b32 v73, a9\n\t"
    "v_accvgpr_read_b32 v74, a10\n\t"
    "v_accvgpr_read_b32 v75, a11\n\t"
    "v_accvgpr_read_b32 v76, a12\n\t"
    "buffer_load_dwordx4 a[204:207], v224, s[44:47], s71 offen offset:3072 sc1\n\t"
    "v_accvgpr_read_b32 v77, a13\n\t"
    "v_accvgpr_read_b32 v78, a14\n\t"
    "v_accvgpr_read_b32 v79, a15\n\t"
    "v_accvgpr_read_b32 v80, a16\n\t"
    "s_add_u32 s71, s71, 0x1000\n\t"
    "buffer_load_dwordx4 a[208:211], v224, s[44:47], s71 offen offset:0 sc1\n\t"
    "v_accvgpr_read_b32 v81, a17\n\t"
    "v_accvgpr_read_b32 v82, a18\n\t"
    "v_accvgpr_read_b32 v83, a19\n\t"
    "v_accvgpr_read_b32 v84, a20\n\t"
    "buffer_load_dwordx4 a[212:215], v224, s[44:47], s71 offen offset:1024 sc1\n\t"
    "v_accvgpr_read_b32 v85, a21\n\t"
    "v_accvgpr_read_b32 v86, a22\n\t"
    "v_accvgpr_read_b32 v87, a23\n\t"
    "v_accvgpr_read_b32 v88, a24\n\t"
    "buffer_load_dwordx4 a[216:219], v224, s[44:47], s71 offen offset:2048 sc1\n\t"
    "v_accvgpr_read_b32 v89, a25\n\t"
    "v_accvgpr_read_b32 v90, a26\n\t"
    "v_accvgpr_read_b32 v91, a27\n\t"
    "v_accvgpr_read_b32 v92, a28\n\t"
    "buffer_load_dwordx4 a[220:223], v224, s[44:47], s71 offen offset:3072 sc1\n\t"
    "v_accvgpr_read_b32 v93, a29\n\t"
    "v_accvgpr_read_b32 v94, a30\n\t"
    "v_accvgpr_read_b32 v95, a31\n\t"
    "v_accvgpr_read_b32 v96, a32\n\t"
    "s_lshl_b32 s71, %14, 14\n\t"
    "s_add_u32 s71, s71, 327680\n\t"
    "s_and_b32 s71, s71, 196608\n\t"
    "s_add_u32 s71, s71, s70\n\t"
    "buffer_load_dwordx4 a[224:227], v224, s[44:47], s71 offen offset:0 sc1\n\t"
    "v_accvgpr_read_b32 v97, a33\n\t"
    "v_accvgpr_read_b32 v98, a34\n\t"
    "v_accvgpr_read_b32 v99, a35\n\t"
    "v_accvgpr_read_b32 v100, a36\n\t"
    "buffer_load_dwordx4 a[228:231], v224, s[44:47], s71 offen offset:1024 sc1\n\t"
    "v_accvgpr_read_b32 v101, a37\n\t"
    "v_accvgpr_read_b32 v102, a38\n\t"
    "v_accvgpr_read_b32 v103, a39\n\t"
    "v_accvgpr_read_b32 v104, a40\n\t"
    "buffer_load_dwordx4 a[232:235], v224, s[44:47], s71 offen offset:2048 sc1\n\t"
    "v_accvgpr_read_b32 v105, a41\n\t"
    "v_accvgpr_read_b32 v106, a42\n\t"
    "v_accvgpr_read_b32 v107, a43\n\t"
    "v_accvgpr_read_b32 v108, a44\n\t"
    "buffer_load_dwordx4 a[236:239], v224, s[44:47], s71 offen offset:3072 sc1\n\t"
    "v_accvgpr_read_b32 v109, a45\n\t"
    "v_accvgpr_read_b32 v110, a46\n\t"
    "v_accvgpr_read_b32 v111, a47\n\t"
    "v_accvgpr_read_b32 v112, a48\n\t"
    "s_add_u32 s71, s71, 0x1000\n\t"
    "buffer_load_dwordx4 a[240:243], v224, s[44:47], s71 offen offset:0 sc1\n\t"
    "v_accvgpr_read_b32 v113, a49\n\t"
    "v_accvgpr_read_b32 v114, a50\n\t"
    "v_accvgpr_read_b32 v115, a51\n\t"
    "v_accvgpr_read_b32 v116, a52\n\t"
    "buffer_load_dwordx4 a[244:247], v224, s[44:47], s71 offen offset:1024 sc1\n\t"
    "v_accvgpr_read_b32 v117, a53\n\t"
    "v_accvgpr_read_b32 v118, a54\n\t"
    "v_accvgpr_read_b32 v119, a55\n\t"
    "v_accvgpr_read_b32 v120, a56\n\t"
    "buffer_load_dwordx4 a[248:251], v224, s[44:47], s71 offen offset:2048 sc1\n\t"
    "v_accvgpr_read_b32 v121, a57\n\t"
    "v_accvgpr_read_b32 v122, a58\n\t"
    "v_accvgpr_read_b32 v123, a59\n\t"
    "v_accvgpr_read_b32 v124, a60\n\t"
    "buffer_load_dwordx4 a[252:255], v224, s[44:47], s71 offen offset:3072 sc1\n\t"
    "v_accvgpr_read_b32 v125, a61\n\t"
    "v_accvgpr_read_b32 v126, a62\n\t"
    "v_accvgpr_read_b32 v127, a63\n\t"
    "s_branch La2_151_%=\n\t"
    "La1_150_%=:\n\t"
    "v_accvgpr_read_b32 v64, a64\n\t"
    "s_lshl_b32 s71, %14, 14\n\t"
    "s_add_u32 s71, s71, 262144\n\t"
    "s_and_b32 s71, s71, 196608\n\t"
    "s_add_u32 s71, s71, s70\n\t"
    "buffer_load_dwordx4 a[192:195], v224, s[44:47], s71 offen offset:0 sc1\n\t"
    "v_accvgpr_read_b32 v65, a65\n\t"
    "v_accvgpr_read_b32 v66, a66\n\t"
    "v_accvgpr_read_b32 v67, a67\n\t"
    "v_accvgpr_read_b32 v68, a68\n\t"
    "buffer_load_dwordx4 a[196:199], v224, s[44:47], s71 offen offset:1024 sc1\n\t"
    "v_accvgpr_read_b32 v69, a69\n\t"
    "v_accvgpr_read_b32 v70, a70\n\t"
    "v_accvgpr_read_b32 v71, a71\n\t"
    "v_accvgpr_read_b32 v72, a72\n\t"
    "buffer_load_dwordx4 a[200:203], v224, s[44:47], s71 offen offset:2048 sc1\n\t"
    "v_accvgpr_read_b32 v73, a73\n\t"
    "v_accvgpr_read_b32 v74, a74\n\t"
    "v_accvgpr_read_b32 v75, a75\n\t"
    "v_accvgpr_read_b32 v76, a76\n\t"
    "buffer_load_dwordx4 a[204:207], v224, s[44:47], s71 offen offset:3072 sc1\n\t"
    "v_accvgpr_read_b32 v77, a77\n\t"
    "v_accvgpr_read_b32 v78, a78\n\t"
    "v_accvgpr_read_b32 v79, a79\n\t"
    "v_accvgpr_read_b32 v80, a80\n\t"
    "s_add_u32 s71, s71, 0x1000\n\t"
    "buffer_load_dwordx4 a[208:211], v224, s[44:47], s71 offen offset:0 sc1\n\t"
    "v_accvgpr_read_b32 v81, a81\n\t"
    "v_accvgpr_read_b32 v82, a82\n\t"
    "v_accvgpr_read_b32 v83, a83\n\t"
    "v_accvgpr_read_b32 v84, a84\n\t"
    "buffer_load_dwordx4 a[212:215], v224, s[44:47], s71 offen offset:1024 sc1\n\t"
    "v_accvgpr_read_b32 v85, a85\n\t"
    "v_accvgpr_read_b32 v86, a86\n\t"
    "v_accvgpr_read_b32 v87, a87\n\t"
    "v_accvgpr_read_b32 v88, a88\n\t"
    "buffer_load_dwordx4 a[216:219], v224, s[44:47], s71 offen offset:2048 sc1\n\t"
    "v_accvgpr_read_b32 v89, a89\n\t"
    "v_accvgpr_read_b32 v90, a90\n\t"
    "v_accvgpr_read_b32 v91, a91\n\t"
    "v_accvgpr_read_b32 v92, a92\n\t"
    "buffer_load_dwordx4 a[220:223], v224, s[44:47], s71 offen offset:3072 sc1\n\t"
    "v_accvgpr_read_b32 v93, a93\n\t"
    "v_accvgpr_read_b32 v94, a94\n\t"
    "v_accvgpr_read_b32 v95, a95\n\t"
    "v_accvgpr_read_b32 v96, a96\n\t"
    "s_lshl_b32 s71, %14, 14\n\t"
    "s_add_u32 s71, s71, 327680\n\t"
    "s_and_b32 s71, s71, 196608\n\t"
    "s_add_u32 s71, s71, s70\n\t"
    "buffer_load_dwordx4 a[224:227], v224, s[44:47], s71 offen offset:0 sc1\n\t"
    "v_accvgpr_read_b32 v97, a97\n\t"
    "v_accvgpr_read_b32 v98, a98\n\t"
    "v_accvgpr_read_b32 v99, a99\n\t"
    "v_accvgpr_read_b32 v100, a100\n\t"
    "buffer_load_dwordx4 a[228:231], v224, s[44:47], s71 offen offset:1024 sc1\n\t"
    "v_accvgpr_read_b32 v101, a101\n\t"
    "v_accvgpr_read_b32 v102, a102\n\t"
    "v_accvgpr_read_b32 v103, a103\n\t"
    "v_accvgpr_read_b32 v104, a104\n\t"
    "buffer_load_dwordx4 a[232:235], v224, s[44:47], s71 offen offset:2048 sc1\n\t"
    "v_accvgpr_read_b32 v105, a105\n\t"
    "v_accvgpr_read_b32 v106, a106\n\t"
    "v_accvgpr_read_b32 v107, a107\n\t"
    "v_accvgpr_read_b32 v108, a108\n\t"
    "buffer_load_dwordx4 a[236:239], v224, s[44:47], s71 offen offset:3072 sc1\n\t"
    "v_accvgpr_read_b32 v109, a109\n\t"
    "v_accvgpr_read_b32 v110, a110\n\t"
    "v_accvgpr_read_b32 v111, a111\n\t"
    "v_accvgpr_read_b32 v112, a112\n\t"
    "s_add_u32 s71, s71, 0x1000\n\t"
    "buffer_load_dwordx4 a[240:243], v224, s[44:47], s71 offen offset:0 sc1\n\t"
    "v_accvgpr_read_b32 v113, a113\n\t"
    "v_accvgpr_read_b32 v114, a114\n\t"
    "v_accvgpr_read_b32 v115, a115\n\t"
    "v_accvgpr_read_b32 v116, a116\n\t"
    "buffer_load_dwordx4 a[244:247], v224, s[44:47], s71 offen offset:1024 sc1\n\t"
    "v_accvgpr_read_b32 v117, a117\n\t"
    "v_accvgpr_read_b32 v118, a118\n\t"
    "v_accvgpr_read_b32 v119, a119\n\t"
    "v_accvgpr_read_b32 v120, a120\n\t"
    "buffer_load_dwordx4 a[248:251], v224, s[44:47], s71 offen offset:2048 sc1\n\t"
    "v_accvgpr_read_b32 v121, a121\n\t"
    "v_accvgpr_read_b32 v122, a122\n\t"
    "v_accvgpr_read_b32 v123, a123\n\t"
    "v_accvgpr_read_b32 v124, a124\n\t"
    "buffer_load_dwordx4 a[252:255], v224, s[44:47], s71 offen offset:3072 sc1\n\t"
    "v_accvgpr_read_b32 v125, a125\n\t"
    "v_accvgpr_read_b32 v126, a126\n\t"
    "v_accvgpr_read_b32 v127, a127\n\t"
    "La2_151_%=:\n\t"
    "s_waitcnt lgkmcnt(0)\n\t"
    "v_max_f32 v238, v232, v240\n\t"
    "v_sub_f32 v128, v232, v238\n\t"
    "v_sub_f32 v130, v240, v238\n\t"
    "v_exp_f32 v128, v128\n\t"
    "v_exp_f32 v130, v130\n\t"
    "s_nop 0\n\t"
    "v_mul_f32 v238, v128, v228\n\t"
    "v_fma_f32 v238, v130, v242, v238\n\t"
    "v_rcp_f32 v238, v238\n\t"
    "s_nop 0\n\t"
    "v_mul_f32 v128, v128, v238\n\t"
    "v_mul_f32 v130, v130, v238\n\t"
    "v_max_f32 v238, v233, v241\n\t"
    "v_sub_f32 v132, v233, v238\n\t"
    "v_sub_f32 v134, v241, v238\n\t"
    "v_exp_f32 v132, v132\n\t"
    "v_exp_f32 v134, v134\n\t"
    "s_nop 0\n\t"
    "v_mul_f32 v238, v132, v230\n\t"
    "v_fma_f32 v238, v134, v243, v238\n\t"
    "v_rcp_f32 v238, v238\n\t"
    "s_nop 0\n\t"
    "v_mul_f32 v132, v132, v238\n\t"
    "v_mul_f32 v134, v134, v238\n\t"
    "s_add_u32 s66, s73, 0\n\t"
    "v_pk_mul_f32 v[0:1], v[0:1], v[130:131] op_sel_hi:[1,0]\n\t"
    "v_pk_mul_f32 v[2:3], v[2:3], v[130:131] op_sel_hi:[1,0]\n\t"
    "v_pk_mul_f32 v[4:5], v[4:5], v[130:131] op_sel_hi:[1,0]\n\t"
    "v_pk_mul_f32 v[6:7], v[6:7], v[130:131] op_sel_hi:[1,0]\n\t"
    "v_pk_mul_f32 v[8:9], v[8:9], v[130:131] op_sel_hi:[1,0]\n\t"
    "v_pk_mul_f32 v[10:11], v[10:11], v[130:131] op_sel_hi:[1,0]\n\t"
    "v_pk_mul_f32 v[12:13], v[12:13], v[130:131] op_sel_hi:[1,0]\n\t"
    "v_pk_mul_f32 v[14:15], v[14:15], v[130:131] op_sel_hi:[1,0]\n\t"
    "v_pk_fma_f32 v[0:1], v[64:65], v[128:129], v[0:1] op_sel_hi:[1,0,1]\n\t"
    "v_pk_fma_f32 v[2:3], v[66:67], v[128:129], v[2:3] op_sel_hi:[1,0,1]\n\t"
    "v_pk_fma_f32 v[4:5], v[68:69], v[128:129], v[4:5] op_sel_hi:[1,0,1]\n\t"
    "v_pk_fma_f32 v[6:7], v[70:71], v[128:129], v[6:7] op_sel_hi:[1,0,1]\n\t"
    "v_pk_fma_f32 v[8:9], v[72:73], v[128:129], v[8:9] op_sel_hi:[1,0,1]\n\t"
    "v_pk_fma_f32 v[10:11], v[74:75], v[128:129], v[10:11] op_sel_hi:[1,0,1]\n\t"
    "v_pk_fma_f32 v[12:13], v[76:77], v[128:129], v[12:13] op_sel_hi:[1,0,1]\n\t"
    "v_pk_fma_f32 v[14:15], v[78:79], v[128:129], v[14:15] op_sel_hi:[1,0,1]\n\t"
    "v_cvt_pk_bf16_f32 v0, v0, v1\n\t"
    "v_cvt_pk_bf16_f32 v1, v2, v3\n\t"
    "v_cvt_pk_bf16_f32 v2, v4, v5\n\t"
    "v_cvt_pk_bf16_f32 v3, v6, v7\n\t"
    "v_cvt_pk_bf16_f32 v8, v8, v9\n\t"
    "v_cvt_pk_bf16_f32 v9, v10, v11\n\t"
    "v_cvt_pk_bf16_f32 v10, v12, v13\n\t"
    "v_cvt_pk_bf16_f32 v11, v14, v15\n\t"
    "buffer_store_dwordx4 v[0:3], v224, s[48:51], s66 offen offset:0 sc1\n\t"
    "buffer_store_dwordx4 v[8:11], v224, s[48:51], s66 offen offset:1024 sc1\n\t"
    "v_pk_mul_f32 v[32:33], v[32:33], v[130:131] op_sel_hi:[1,0]\n\t"
    "v_pk_mul_f32 v[34:35], v[34:35], v[130:131] op_sel_hi:[1,0]\n\t"
    "v_pk_mul_f32 v[36:37], v[36:37], v[130:131] op_sel_hi:[1,0]\n\t"
    "v_pk_mul_f32 v[38:39], v[38:39], v[130:131] op_sel_hi:[1,0]\n\t"
    "v_pk_mul_f32 v[40:41], v[40:41], v[130:131] op_sel_hi:[1,0]\n\t"
    "v_pk_mul_f32 v[42:43], v[42:43], v[130:131] op_sel_hi:[1,0]\n\t"
    "v_pk_mul_f32 v[44:45], v[44:45], v[130:131] op_sel_hi:[1,0]\n\t"
    "v_pk_mul_f32 v[46:47], v[46:47], v[130:131] op_sel_hi:[1,0]\n\t"
    "v_pk_fma_f32 v[32:33], v[96:97], v[128:129], v[32:33] op_sel_hi:[1,0,1]\n\t"
    "v_pk_fma_f32 v[34:35], v[98:99], v[128:129], v[34:35] op_sel_hi:[1,0,1]\n\t"
    "v_pk_fma_f32 v[36:37], v[100:101], v[128:129], v[36:37] op_sel_hi:[1,0,1]\n\t"
    "v_pk_fma_f32 v[38:39], v[102:103], v[128:129], v[38:39] op_sel_hi:[1,0,1]\n\t"
    "v_pk_fma_f32 v[40:41], v[104:105], v[128:129], v[40:41] op_sel_hi:[1,0,1]\n\t"
    "v_pk_fma_f32 v[42:43], v[106:107], v[128:129], v[42:43] op_sel_hi:[1,0,1]\n\t"
    "v_pk_fma_f32 v[44:45], v[108:109], v[128:129], v[44:45] op_sel_hi:[1,0,1]\n\t"
    "v_pk_fma_f32 v[46:47], v[110:111], v[128:129], v[46:47] op_sel_hi:[1,0,1]\n\t"
    "v_cvt_pk_bf16_f32 v32, v32, v33\n\t"
    "v_cvt_pk_bf16_f32 v33, v34, v35\n\t"
    "v_cvt_pk_bf16_f32 v34, v36, v37\n\t"
    "v_cvt_pk_bf16_f32 v35, v38, v39\n\t"
    "v_cvt_pk_bf16_f32 v40, v40, v41\n\t"
    "v_cvt_pk_bf16_f32 v41, v42, v43\n\t"
    "v_cvt_pk_bf16_f32 v42, v44, v45\n\t"
    "v_cvt_pk_bf16_f32 v43, v46, v47\n\t"
    "buffer_store_dwordx4 v[32:35], v224, s[48:51], s66 offen offset:2048 sc1\n\t"
    "buffer_store_dwordx4 v[40:43], v224, s[48:51], s66 offen offset:3072 sc1\n\t"
    "s_add_u32 s66, s73, 262144\n\t"
    "v_pk_mul_f32 v[16:17], v[16:17], v[134:135] op_sel_hi:[1,0]\n\t"
    "v_pk_mul_f32 v[18:19], v[18:19], v[134:135] op_sel_hi:[1,0]\n\t"
    "v_pk_mul_f32 v[20:21], v[20:21], v[134:135] op_sel_hi:[1,0]\n\t"
    "v_pk_mul_f32 v[22:23], v[22:23], v[134:135] op_sel_hi:[1,0]\n\t"
    "v_pk_mul_f32 v[24:25], v[24:25], v[134:135] op_sel_hi:[1,0]\n\t"
    "v_pk_mul_f32 v[26:27], v[26:27], v[134:135] op_sel_hi:[1,0]\n\t"
    "v_pk_mul_f32 v[28:29], v[28:29], v[134:135] op_sel_hi:[1,0]\n\t"
    "v_pk_mul_f32 v[30:31], v[30:31], v[134:135] op_sel_hi:[1,0]\n\t"
    "v_pk_fma_f32 v[16:17], v[80:81], v[132:133], v[16:17] op_sel_hi:[1,0,1]\n\t"
    "v_pk_fma_f32 v[18:19], v[82:83], v[132:133], v[18:19] op_sel_hi:[1,0,1]\n\t"
    "v_pk_fma_f32 v[20:21], v[84:85], v[132:133], v[20:21] op_sel_hi:[1,0,1]\n\t"
    "v_pk_fma_f32 v[22:23], v[86:87], v[132:133], v[22:23] op_sel_hi:[1,0,1]\n\t"
    "v_pk_fma_f32 v[24:25], v[88:89], v[132:133], v[24:25] op_sel_hi:[1,0,1]\n\t"
    "v_pk_fma_f32 v[26:27], v[90:91], v[132:133], v[26:27] op_sel_hi:[1,0,1]\n\t"
    "v_pk_fma_f32 v[28:29], v[92:93], v[132:133], v[28:29] op_sel_hi:[1,0,1]\n\t"
    "v_pk_fma_f32 v[30:31], v[94:95], v[132:133], v[30:31] op_sel_hi:[1,0,1]\n\t"
    "v_cvt_pk_bf16_f32 v16, v16, v17\n\t"
    "v_cvt_pk_bf16_f32 v17, v18, v19\n\t"
    "v_cvt_pk_bf16_f32 v18, v20, v21\n\t"
    "v_cvt_pk_bf16_f32 v19, v22, v23\n\t"
    "v_cvt_pk_bf16_f32 v24, v24, v25\n\t"
    "v_cvt_pk_bf16_f32 v25, v26, v27\n\t"
    "v_cvt_pk_bf16_f32 v26, v28, v29\n\t"
    "v_cvt_pk_bf16_f32 v27, v30, v31\n\t"
    "buffer_store_dwordx4 v[16:19], v224, s[48:51], s66 offen offset:0 sc1\n\t"
    "buffer_store_dwordx4 v[24:27], v224, s[48:51], s66 offen offset:1024 sc1\n\t"
    "v_pk_mul_f32 v[48:49], v[48:49], v[134:135] op_sel_hi:[1,0]\n\t"
    "v_pk_mul_f32 v[50:51], v[50:51], v[134:135] op_sel_hi:[1,0]\n\t"
    "v_pk_mul_f32 v[52:53], v[52:53], v[134:135] op_sel_hi:[1,0]\n\t"
    "v_pk_mul_f32 v[54:55], v[54:55], v[134:135] op_sel_hi:[1,0]\n\t"
    "v_pk_mul_f32 v[56:57], v[56:57], v[134:135] op_sel_hi:[1,0]\n\t"
    "v_pk_mul_f32 v[58:59], v[58:59], v[134:135] op_sel_hi:[1,0]\n\t"
    "v_pk_mul_f32 v[60:61], v[60:61], v[134:135] op_sel_hi:[1,0]\n\t"
    "v_pk_mul_f32 v[62:63], v[62:63], v[134:135] op_sel_hi:[1,0]\n\t"
    "v_pk_fma_f32 v[48:49], v[112:113], v[132:133], v[48:49] op_sel_hi:[1,0,1]\n\t"
    "v_pk_fma_f32 v[50:51], v[114:115], v[132:133], v[50:51] op_sel_hi:[1,0,1]\n\t"
    "v_pk_fma_f32 v[52:53], v[116:117], v[132:133], v[52:53] op_sel_hi:[1,0,1]\n\t"
    "v_pk_fma_f32 v[54:55], v[118:119], v[132:133], v[54:55] op_sel_hi:[1,0,1]\n\t"
    "v_pk_fma_f32 v[56:57], v[120:121], v[132:133], v[56:57] op_sel_hi:[1,0,1]\n\t"
    "v_pk_fma_f32 v[58:59], v[122:123], v[132:133], v[58:59] op_sel_hi:[1,0,1]\n\t"
    "v_pk_fma_f32 v[60:61], v[124:125], v[132:133], v[60:61] op_sel_hi:[1,0,1]\n\t"
    "v_pk_fma_f32 v[62:63], v[126:127], v[132:133], v[62:63] op_sel_hi:[1,0,1]\n\t"
    "v_cvt_pk_bf16_f32 v48, v48, v49\n\t"
    "v_cvt_pk_bf16_f32 v49, v50, v51\n\t"
    "v_cvt_pk_bf16_f32 v50, v52, v53\n\t"
    "v_cvt_pk_bf16_f32 v51, v54, v55\n\t"
    "v_cvt_pk_bf16_f32 v56, v56, v57\n\t"
    "v_cvt_pk_bf16_f32 v57, v58, v59\n\t"
    "v_cvt_pk_bf16_f32 v58, v60, v61\n\t"
    "v_cvt_pk_bf16_f32 v59, v62, v63\n\t"
    "buffer_store_dwordx4 v[48:51], v224, s[48:51], s66 offen offset:2048 sc1\n\t"
    "buffer_store_dwordx4 v[56:59], v224, s[48:51], s66 offen offset:3072 sc1\n\t"
    "s_branch LATT_END\n\t"
    "Lst_60_%=:\n\t"
    "s_sub_u32 s66, s56, s63\n\t"
    "s_cmp_ge_u32 s66, 0x4000\n\t"
    "s_cselect_b32 s66, -1, 0\n\t"
    "v_or_b32 v242, s66, v239\n\t"
    "v_cmp_le_i32_e64 s[70:71], 0, v242\n\t"
    "v_cmp_le_i32_e64 s[72:73], 1, v242\n\t"
    "v_cmp_le_i32_e64 s[74:75], 2, v242\n\t"
    "v_cmp_le_i32_e64 s[76:77], 3, v242\n\t"
    "v_cmp_le_i32_e64 s[78:79], 8, v242\n\t"
    "v_cmp_le_i32_e64 s[80:81], 9, v242\n\t"
    "v_cmp_le_i32_e64 s[82:83], 10, v242\n\t"
    "v_cmp_le_i32_e64 s[84:85], 11, v242\n\t"
    "v_pk_mov_b32 v[32:33], v[252:253], v[252:253] op_sel:[0,0]\n\t"
    "v_pk_mov_b32 v[34:35], v[252:253], v[252:253] op_sel:[0,0]\n\t"
    "v_pk_mov_b32 v[36:37], v[252:253], v[252:253] op_sel:[0,0]\n\t"
    "v_pk_mov_b32 v[38:39], v[252:253], v[252:253] op_sel:[0,0]\n\t"
    "v_cndmask_b32_e64 v0, v252, v0, s[70:71]\n\t"
    "v_cndmask_b32_e64 v48, v252, v48, s[70:71]\n\t"
    "v_cndmask_b32_e64 v1, v252, v1, s[72:73]\n\t"
    "v_cndmask_b32_e64 v49, v252, v49, s[72:73]\n\t"
    "v_cndmask_b32_e64 v2, v252, v2, s[74:75]\n\t"
    "v_cndmask_b32_e64 v50, v252, v50, s[74:75]\n\t"
    "v_cndmask_b32_e64 v3, v252, v3, s[76:77]\n\t"
    "v_cndmask_b32_e64 v51, v252, v51, s[76:77]\n\t"
    "v_cndmask_b32_e64 v4, v252, v4, s[78:79]\n\t"
    "v_cndmask_b32_e64 v52, v252, v52, s[78:79]\n\t"
    "v_cndmask_b32_e64 v5, v252, v5, s[80:81]\n\t"
    "v_cndmask_b32_e64 v53, v252, v53, s[80:81]\n\t"
    "v_cndmask_b32_e64 v6, v252, v6, s[82:83]\n\t"
    "v_cndmask_b32_e64 v54, v252, v54, s[82:83]\n\t"
    "v_cndmask_b32_e64 v7, v252, v7, s[84:85]\n\t"
    "v_cndmask_b32_e64 v55, v252, v55, s[84:85]\n\t"
    "v_cmp_le_i32_e64 s[70:71], 16, v242\n\t"
    "v_cmp_le_i32_e64 s[72:73], 17, v242\n\t"
    "v_cmp_le_i32_e64 s[74:75], 18, v242\n\t"
    "v_cmp_le_i32_e64 s[76:77], 19, v242\n\t"
    "v_cmp_le_i32_e64 s[78:79], 24, v242\n\t"
    "v_cmp_le_i32_e64 s[80:81], 25, v242\n\t"
    "v_cmp_le_i32_e64 s[82:83], 26, v242\n\t"
    "v_cmp_le_i32_e64 s[84:85], 27, v242\n\t"
    "v_pk_mov_b32 v[40:41], v[252:253], v[252:253] op_sel:[0,0]\n\t"
    "v_pk_mov_b32 v[42:43], v[252:253], v[252:253] op_sel:[0,0]\n\t"
    "v_pk_mov_b32 v[44:45], v[252:253], v[252:253] op_sel:[0,0]\n\t"
    "v_pk_mov_b32 v[46:47], v[252:253], v[252:253] op_sel:[0,0]\n\t"
    "v_cndmask_b32_e64 v8, v252, v8, s[70:71]\n\t"
    "v_cndmask_b32_e64 v56, v252, v56, s[70:71]\n\t"
    "v_cndmask_b32_e64 v9, v252, v9, s[72:73]\n\t"
    "v_cndmask_b32_e64 v57, v252, v57, s[72:73]\n\t"
    "v_cndmask_b32_e64 v10, v252, v10, s[74:75]\n\t"
    "v_cndmask_b32_e64 v58, v252, v58, s[74:75]\n\t"
    "v_cndmask_b32_e64 v11, v252, v11, s[76:77]\n\t"
    "v_cndmask_b32_e64 v59, v252, v59, s[76:77]\n\t"
    "v_cndmask_b32_e64 v12, v252, v12, s[78:79]\n\t"
    "v_cndmask_b32_e64 v60, v252, v60, s[78:79]\n\t"
    "v_cndmask_b32_e64 v13, v252, v13, s[80:81]\n\t"
    "v_cndmask_b32_e64 v61, v252, v61, s[80:81]\n\t"
    "v_cndmask_b32_e64 v14, v252, v14, s[82:83]\n\t"
    "v_cndmask_b32_e64 v62, v252, v62, s[82:83]\n\t"
    "v_cndmask_b32_e64 v15, v252, v15, s[84:85]\n\t"
    "v_cndmask_b32_e64 v63, v252, v63, s[84:85]\n\t"
    "s_cbranch_scc0 Lfull_59_%=\n\t"
    "v_pk_mov_b32 v[16:17], v[252:253], v[252:253] op_sel:[0,0]\n\t"
    "v_pk_mov_b32 v[18:19], v[252:253], v[252:253] op_sel:[0,0]\n\t"
    "v_pk_mov_b32 v[20:21], v[252:253], v[252:253] op_sel:[0,0]\n\t"
    "v_pk_mov_b32 v[22:23], v[252:253], v[252:253] op_sel:[0,0]\n\t"
    "v_pk_mov_b32 v[24:25], v[252:253], v[252:253] op_sel:[0,0]\n\t"
    "v_pk_mov_b32 v[26:27], v[252:253], v[252:253] op_sel:[0,0]\n\t"
    "v_pk_mov_b32 v[28:29], v[252:253], v[252:253] op_sel:[0,0]\n\t"
    "v_pk_mov_b32 v[30:31], v[252:253], v[252:253] op_sel:[0,0]\n\t"
    "Lfull_59_%=:\n\t"
    "s_branch Lbk_61_%=\n\t"
    "Lst_67_%=:\n\t"
    "s_call_b64 s[90:91], LRARE1_%=\n\t"
    "s_branch Lbk_68_%=\n\t"
    "Lst_69_%=:\n\t"
    "s_call_b64 s[90:91], LPEND_%=\n\t"
    "s_branch Lbk_70_%=\n\t"
    "Lst_71_%=:\n\t"
    "s_call_b64 s[90:91], LRARE0_%=\n\t"
    "s_branch Lbk_72_%=\n\t"
    "Lst_73_%=:\n\t"
    "s_call_b64 s[90:91], LPEND_%=\n\t"
    "s_branch Lbk_74_%=\n\t"
    "Lst_77_%=:\n\t"
    "s_call_b64 s[90:91], LRARE0_%=\n\t"
    "s_branch Lbk_78_%=\n\t"
    "Lst_79_%=:\n\t"
    "s_call_b64 s[90:91], LPEND_%=\n\t"
    "s_branch Lbk_80_%=\n\t"
    "Lst_84_%=:\n\t"
    "s_call_b64 s[90:91], LRARE1_%=\n\t"
    "s_branch Lbk_85_%=\n\t"
    "Lst_86_%=:\n\t"
    "s_call_b64 s[90:91], LPEND_%=\n\t"
    "s_branch Lbk_87_%=\n\t"
    "Lst_89_%=:\n\t"
    "s_call_b64 s[90:91], LRARE0_%=\n\t"
    "s_branch Lbk_90_%=\n\t"
    "Lst_92_%=:\n\t"
    "s_call_b64 s[90:91], LPEND_%=\n\t"
    "s_branch Lbk_93_%=\n\t"
    "Lst_96_%=:\n\t"
    "s_call_b64 s[90:91], LRARE1_%=\n\t"
    "s_branch Lbk_97_%=\n\t"
    "Lst_98_%=:\n\t"
    "s_call_b64 s[90:91], LPEND_%=\n\t"
    "s_branch Lbk_99_%=\n\t"
    "Lst_100_%=:\n\t"
    "s_call_b64 s[90:91], LRARE0_%=\n\t"
    "s_branch Lbk_101_%=\n\t"
    "Lst_102_%=:\n\t"
    "s_call_b64 s[90:91], LPEND_%=\n\t"
    "s_branch Lbk_103_%=\n\t"
    "Lst_104_%=:\n\t"
    "s_call_b64 s[90:91], LRARE1_%=\n\t"
    "s_branch Lbk_105_%=\n\t"
    "Lst_106_%=:\n\t"
    "s_call_b64 s[90:91], LPEND_%=\n\t"
    "s_branch Lbk_107_%=\n\t"
    "Lst_110_%=:\n\t"
    "s_call_b64 s[90:91], LRARE0_%=\n\t"
    "s_branch Lbk_111_%=\n\t"
    "Lst_112_%=:\n\t"
    "s_call_b64 s[90:91], LPEND_%=\n\t"
    "s_branch Lbk_113_%=\n\t"
    "Lst_117_%=:\n\t"
    "s_call_b64 s[90:91], LRARE1_%=\n\t"
    "s_branch Lbk_118_%=\n\t"
    "Lst_119_%=:\n\t"
    "s_call_b64 s[90:91], LPEND_%=\n\t"
    "s_branch Lbk_120_%=\n\t"
    "Lst_126_%=:\n\t"
    "s_call_b64 s[90:91], LRARE0_%=\n\t"
    "s_branch Lbk_127_%=\n\t"
    "Lst_129_%=:\n\t"
    "s_call_b64 s[90:91], LPEND_%=\n\t"
    "s_branch Lbk_130_%=\n\t"
    "LRARE0_%=:\n\t"
    "v_max_f32 v240, v234, v236\n\t"
    "v_max_f32 v241, v235, v237\n\t"
    "v_mov_b32 v242, v240\n\t"
    "v_mov_b32 v243, v241\n\t"
    "s_nop 1\n\t"
    "v_permlane32_swap_b32 v240, v242\n\t"
    "v_permlane32_swap_b32 v241, v243\n\t"
    "s_nop 0\n\t"
    "v_max_f32 v240, v240, v242\n\t"
    "v_max_f32 v241, v241, v243\n\t"
    "v_max_f32 v240, s61, v240\n\t"
    "v_max_f32 v241, s61, v241\n\t"
    "v_add_f32 v232, v232, v240\n\t"
    "v_add_f32 v233, v233, v241\n\t"
    "v_pk_add_f32 v[0:1], v[0:1], v[240:241] op_sel:[0,0] op_sel_hi:[1,0] neg_lo:[0,1] neg_hi:[0,1]\n\t"
    "v_pk_add_f32 v[2:3], v[2:3], v[240:241] op_sel:[0,0] op_sel_hi:[1,0] neg_lo:[0,1] neg_hi:[0,1]\n\t"
    "v_pk_add_f32 v[4:5], v[4:5], v[240:241] op_sel:[0,0] op_sel_hi:[1,0] neg_lo:[0,1] neg_hi:[0,1]\n\t"
    "v_pk_add_f32 v[6:7], v[6:7], v[240:241] op_sel:[0,0] op_sel_hi:[1,0] neg_lo:[0,1] neg_hi:[0,1]\n\t"
    "v_pk_add_f32 v[8:9], v[8:9], v[240:241] op_sel:[0,0] op_sel_hi:[1,0] neg_lo:[0,1] neg_hi:[0,1]\n\t"
    "v_pk_add_f32 v[10:11], v[10:11], v[240:241] op_sel:[0,0] op_sel_hi:[1,0] neg_lo:[0,1] neg_hi:[0,1]\n\t"
    "v_pk_add_f32 v[12:13], v[12:13], v[240:241] op_sel:[0,0] op_sel_hi:[1,0] neg_lo:[0,1] neg_hi:[0,1]\n\t"
    "v_pk_add_f32 v[14:15], v[14:15], v[240:241] op_sel:[0,0] op_sel_hi:[1,0] neg_lo:[0,1] neg_hi:[0,1]\n\t"
    "v_pk_add_f32 v[16:17], v[16:17], v[240:241] op_sel:[0,1] op_sel_hi:[1,1] neg_lo:[0,1] neg_hi:[0,1]\n\t"
    "v_pk_add_f32 v[18:19], v[18:19], v[240:241] op_sel:[0,1] op_sel_hi:[1,1] neg_lo:[0,1] neg_hi:[0,1]\n\t"
    "v_pk_add_f32 v[20:21], v[20:21], v[240:241] op_sel:[0,1] op_sel_hi:[1,1] neg_lo:[0,1] neg_hi:[0,1]\n\t"
    "v_pk_add_f32 v[22:23], v[22:23], v[240:241] op_sel:[0,1] op_sel_hi:[1,1] neg_lo:[0,1] neg_hi:[0,1]\n\t"
    "v_pk_add_f32 v[24:25], v[24:25], v[240:241] op_sel:[0,1] op_sel_hi:[1,1] neg_lo:[0,1] neg_hi:[0,1]\n\t"
    "v_pk_add_f32 v[26:27], v[26:27], v[240:241] op_sel:[0,1] op_sel_hi:[1,1] neg_lo:[0,1] neg_hi:[0,1]\n\t"
    "v_pk_add_f32 v[28:29], v[28:29], v[240:241] op_sel:[0,1] op_sel_hi:[1,1] neg_lo:[0,1] neg_hi:[0,1]\n\t"
    "v_pk_add_f32 v[30:31], v[30:31], v[240:241] op_sel:[0,1] op_sel_hi:[1,1] neg_lo:[0,1] neg_hi:[0,1]\n\t"
    "v_pk_add_f32 v[32:33], v[32:33], v[240:241] op_sel:[0,0] op_sel_hi:[1,0] neg_lo:[0,1] neg_hi:[0,1]\n\t"
    "v_pk_add_f32 v[34:35], v[34:35], v[240:241] op_sel:[0,0] op_sel_hi:[1,0] neg_lo:[0,1] neg_hi:[0,1]\n\t"
    "v_pk_add_f32 v[36:37], v[36:37], v[240:241] op_sel:[0,0] op_sel_hi:[1,0] neg_lo:[0,1] neg_hi:[0,1]\n\t"
    "v_pk_add_f32 v[38:39], v[38:39], v[240:241] op_sel:[0,0] op_sel_hi:[1,0] neg_lo:[0,1] neg_hi:[0,1]\n\t"
    "v_pk_add_f32 v[40:41], v[40:41], v[240:241] op_sel:[0,0] op_sel_hi:[1,0] neg_lo:[0,1] neg_hi:[0,1]\n\t"
    "v_pk_add_f32 v[42:43], v[42:43], v[240:241] op_sel:[0,0] op_sel_hi:[1,0] neg_lo:[0,1] neg_hi:[0,1]\n\t"
    "v_pk_add_f32 v[44:45], v[44:45], v[240:241] op_sel:[0,0] op_sel_hi:[1,0] neg_lo:[0,1] neg_hi:[0,1]\n\t"
    "v_pk_add_f32 v[46:47], v[46:47], v[240:241] op_sel:[0,0] op_sel_hi:[1,0] neg_lo:[0,1] neg_hi:[0,1]\n\t"
    "v_pk_add_f32 v[48:49], v[48:49], v[240:241] op_sel:[0,1] op_sel_hi:[1,1] neg_lo:[0,1] neg_hi:[0,1]\n\t"
    "v_pk_add_f32 v[50:51], v[50:51], v[240:241] op_sel:[0,1] op_sel_hi:[1,1] neg_lo:[0,1] neg_hi:[0,1]\n\t"
    "v_pk_add_f32 v[52:53], v[52:53], v[240:241] op_sel:[0,1] op_sel_hi:[1,1] neg_lo:[0,1] neg_hi:[0,1]\n\t"
    "v_pk_add_f32 v[54:55], v[54:55], v[240:241] op_sel:[0,1] op_sel_hi:[1,1] neg_lo:[0,1] neg_hi:[0,1]\n\t"
    "v_pk_add_f32 v[56:57], v[56:57], v[240:241] op_sel:[0,1] op_sel_hi:[1,1] neg_lo:[0,1] neg_hi:[0,1]\n\t"
    "v_pk_add_f32 v[58:59], v[58:59], v[240:241] op_sel:[0,1] op_sel_hi:[1,1] neg_lo:[0,1] neg_hi:[0,1]\n\t"
    "v_pk_add_f32 v[60:61], v[60:61], v[240:241] op_sel:[0,1] op_sel_hi:[1,1] neg_lo:[0,1] neg_hi:[0,1]\n\t"
    "v_pk_add_f32 v[62:63], v[62:63], v[240:241] op_sel:[0,1] op_sel_hi:[1,1] neg_lo:[0,1] neg_hi:[0,1]\n\t"
    "v_sub_f32 v242, 0, v232\n\t"
    "v_sub_f32 v243, 0, v233\n\t"
    "v_pk_mov_b32 v[128:129], v[242:243], v[242:243] op_sel:[0,0]\n\t"
    "v_pk_mov_b32 v[130:131], v[242:243], v[242:243] op_sel:[0,0]\n\t"
    "v_pk_mov_b32 v[132:133], v[242:243], v[242:243] op_sel:[0,0]\n\t"
    "v_pk_mov_b32 v[134:135], v[242:243], v[242:243] op_sel:[0,0]\n\t"
    "v_pk_mov_b32 v[136:137], v[242:243], v[242:243] op_sel:[0,0]\n\t"
    "v_pk_mov_b32 v[138:139], v[242:243], v[242:243] op_sel:[0,0]\n\t"
    "v_pk_mov_b32 v[140:141], v[242:243], v[242:243] op_sel:[0,0]\n\t"
    "v_pk_mov_b32 v[142:143], v[242:243], v[242:243] op_sel:[0,0]\n\t"
    "v_pk_mov_b32 v[144:145], v[242:243], v[242:243] op_sel:[1,1]\n\t"
    "v_pk_mov_b32 v[146:147], v[242:243], v[242:243] op_sel:[1,1]\n\t"
    "v_pk_mov_b32 v[148:149], v[242:243], v[242:243] op_sel:[1,1]\n\t"
    "v_pk_mov_b32 v[150:151], v[242:243], v[242:243] op_sel:[1,1]\n\t"
    "v_pk_mov_b32 v[152:153], v[242:243], v[242:243] op_sel:[1,1]\n\t"
    "v_pk_mov_b32 v[154:155], v[242:243], v[242:243] op_sel:[1,1]\n\t"
    "v_pk_mov_b32 v[156:157], v[242:243], v[242:243] op_sel:[1,1]\n\t"
    "v_pk_mov_b32 v[158:159], v[242:243], v[242:243] op_sel:[1,1]\n\t"
    "s_cmp_eq_u32 s93, 0\n\t"
    "s_cbranch_scc1 Lnf_136_%=\n\t"
    "Lls_139_%=:\n\t"
    "v_add_f32 v250, v228, v229\n\t"
    "v_mov_b32 v228, 0\n\t"
    "v_mov_b32 v229, 0\n\t"
    "v_add_f32 v251, v230, v231\n\t"
    "v_mov_b32 v230, 0\n\t"
    "v_mov_b32 v231, 0\n\t"
    "s_mov_b32 s93, 0\n\t"
    "s_mov_b32 s60, 0x41000000\n\t"
    "s_mov_b32 s61, 0\n\t"
    "s_branch Ldn_137_%=\n\t"
    "Lnf_136_%=:\n\t"
    "v_exp_f32_e64 v248, -v240\n\t"
    "v_exp_f32_e64 v249, -v241\n\t"
    "s_nop 0\n\t"
    "v_mul_f32 v228, v228, v248\n\t"
    "v_mul_f32 v229, v229, v248\n\t"
    "v_mul_f32 v230, v230, v249\n\t"
    "v_mul_f32 v231, v231, v249\n\t"
    "s_mov_b32 s62, 1\n\t"
    "Ldn_137_%=:\n\t"
    "s_setpc_b64 s[90:91]\n\t"
    "LRARE1_%=:\n\t"
    "v_max_f32 v240, v234, v236\n\t"
    "v_max_f32 v241, v235, v237\n\t"
    "v_mov_b32 v242, v240\n\t"
    "v_mov_b32 v243, v241\n\t"
    "s_nop 1\n\t"
    "v_permlane32_swap_b32 v240, v242\n\t"
    "v_permlane32_swap_b32 v241, v243\n\t"
    "s_nop 0\n\t"
    "v_max_f32 v240, v240, v242\n\t"
    "v_max_f32 v241, v241, v243\n\t"
    "v_max_f32 v240, s61, v240\n\t"
    "v_max_f32 v241, s61, v241\n\t"
    "v_add_f32 v232, v232, v240\n\t"
    "v_add_f32 v233, v233, v241\n\t"
    "v_pk_add_f32 v[64:65], v[64:65], v[240:241] op_sel:[0,0] op_sel_hi:[1,0] neg_lo:[0,1] neg_hi:[0,1]\n\t"
    "v_pk_add_f32 v[66:67], v[66:67], v[240:241] op_sel:[0,0] op_sel_hi:[1,0] neg_lo:[0,1] neg_hi:[0,1]\n\t"
    "v_pk_add_f32 v[68:69], v[68:69], v[240:241] op_sel:[0,0] op_sel_hi:[1,0] neg_lo:[0,1] neg_hi:[0,1]\n\t"
    "v_pk_add_f32 v[70:71], v[70:71], v[240:241] op_sel:[0,0] op_sel_hi:[1,0] neg_lo:[0,1] neg_hi:[0,1]\n\t"
    "v_pk_add_f32 v[72:73], v[72:73], v[240:241] op_sel:[0,0] op_sel_hi:[1,0] neg_lo:[0,1] neg_hi:[0,1]\n\t"
    "v_pk_add_f32 v[74:75], v[74:75], v[240:241] op_sel:[0,0] op_sel_hi:[1,0] neg_lo:[0,1] neg_hi:[0,1]\n\t"
    "v_pk_add_f32 v[76:77], v[76:77], v[240:241] op_sel:[0,0] op_sel_hi:[1,0] neg_lo:[0,1] neg_hi:[0,1]\n\t"
    "v_pk_add_f32 v[78:79], v[78:79], v[240:241] op_sel:[0,0] op_sel_hi:[1,0] neg_lo:[0,1] neg_hi:[0,1]\n\t"
    "v_pk_add_f32 v[80:81], v[80:81], v[240:241] op_sel:[0,1] op_sel_hi:[1,1] neg_lo:[0,1] neg_hi:[0,1]\n\t"
    "v_pk_add_f32 v[82:83], v[82:83], v[240:241] op_sel:[0,1] op_sel_hi:[1,1] neg_lo:[0,1] neg_hi:[0,1]\n\t"
    "v_pk_add_f32 v[84:85], v[84:85], v[240:241] op_sel:[0,1] op_sel_hi:[1,1] neg_lo:[0,1] neg_hi:[0,1]\n\t"
    "v_pk_add_f32 v[86:87], v[86:87], v[240:241] op_sel:[0,1] op_sel_hi:[1,1] neg_lo:[0,1] neg_hi:[0,1]\n\t"
    "v_pk_add_f32 v[88:89], v[88:89], v[240:241] op_sel:[0,1] op_sel_hi:[1,1] neg_lo:[0,1] neg_hi:[0,1]\n\t"
    "v_pk_add_f32 v[90:91], v[90:91], v[240:241] op_sel:[0,1] op_sel_hi:[1,1] neg_lo:[0,1] neg_hi:[0,1]\n\t"
    "v_pk_add_f32 v[92:93], v[92:93], v[240:241] op_sel:[0,1] op_sel_hi:[1,1] neg_lo:[0,1] neg_hi:[0,1]\n\t"
    "v_pk_add_f32 v[94:95], v[94:95], v[240:241] op_sel:[0,1] op_sel_hi:[1,1] neg_lo:[0,1] neg_hi:[0,1]\n\t"
    "v_pk_add_f32 v[96:97], v[96:97], v[240:241] op_sel:[0,0] op_sel_hi:[1,0] neg_lo:[0,1] neg_hi:[0,1]\n\t"
    "v_pk_add_f32 v[98:99], v[98:99], v[240:241] op_sel:[0,0] op_sel_hi:[1,0] neg_lo:[0,1] neg_hi:[0,1]\n\t"
    "v_pk_add_f32 v[100:101], v[100:101], v[240:241] op_sel:[0,0] op_sel_hi:[1,0] neg_lo:[0,1] neg_hi:[0,1]\n\t"
    "v_pk_add_f32 v[102:103], v[102:103], v[240:241] op_sel:[0,0] op_sel_hi:[1,0] neg_lo:[0,1] neg_hi:[0,1]\n\t"
    "v_pk_add_f32 v[104:105], v[104:105], v[240:241] op_sel:[0,0] op_sel_hi:[1,0] neg_lo:[0,1] neg_hi:[0,1]\n\t"
    "v_pk_add_f32 v[106:107], v[106:107], v[240:241] op_sel:[0,0] op_sel_hi:[1,0] neg_lo:[0,1] neg_hi:[0,1]\n\t"
    "v_pk_add_f32 v[108:109], v[108:109], v[240:241] op_sel:[0,0] op_sel_hi:[1,0] neg_lo:[0,1] neg_hi:[0,1]\n\t"
    "v_pk_add_f32 v[110:111], v[110:111], v[240:241] op_sel:[0,0] op_sel_hi:[1,0] neg_lo:[0,1] neg_hi:[0,1]\n\t"
    "v_pk_add_f32 v[112:113], v[112:113], v[240:241] op_sel:[0,1] op_sel_hi:[1,1] neg_lo:[0,1] neg_hi:[0,1]\n\t"
    "v_pk_add_f32 v[114:115], v[114:115], v[240:241] op_sel:[0,1] op_sel_hi:[1,1] neg_lo:[0,1] neg_hi:[0,1]\n\t"
    "v_pk_add_f32 v[116:117], v[116:117], v[240:241] op_sel:[0,1] op_sel_hi:[1,1] neg_lo:[0,1] neg_hi:[0,1]\n\t"
    "v_pk_add_f32 v[118:119], v[118:119], v[240:241] op_sel:[0,1] op_sel_hi:[1,1] neg_lo:[0,1] neg_hi:[0,1]\n\t"
    "v_pk_add_f32 v[120:121], v[120:121], v[240:241] op_sel:[0,1] op_sel_hi:[1,1] neg_lo:[0,1] neg_hi:[0,1]\n\t"
    "v_pk_add_f32 v[122:123], v[122:123], v[240:241] op_sel:[0,1] op_sel_hi:[1,1] neg_lo:[0,1] neg_hi:[0,1]\n\t"
    "v_pk_add_f32 v[124:125], v[124:125], v[240:241] op_sel:[0,1] op_sel_hi:[1,1] neg_lo:[0,1] neg_hi:[0,1]\n\t"
    "v_pk_add_f32 v[126:127], v[126:127], v[240:241] op_sel:[0,1] op_sel_hi:[1,1] neg_lo:[0,1] neg_hi:[0,1]\n\t"
    "v_sub_f32 v242, 0, v232\n\t"
    "v_sub_f32 v243, 0, v233\n\t"
    "v_pk_mov_b32 v[128:129], v[242:243], v[242:243] op_sel:[0,0]\n\t"
    "v_pk_mov_b32 v[130:131], v[242:243], v[242:243] op_sel:[0,0]\n\t"
    "v_pk_mov_b32 v[132:133], v[242:243], v[242:243] op_sel:[0,0]\n\t"
    "v_pk_mov_b32 v[134:135], v[242:243], v[242:243] op_sel:[0,0]\n\t"
    "v_pk_mov_b32 v[136:137], v[242:243], v[242:243] op_sel:[0,0]\n\t"
    "v_pk_mov_b32 v[138:139], v[242:243], v[242:243] op_sel:[0,0]\n\t"
    "v_pk_mov_b32 v[140:141], v[242:243], v[242:243] op_sel:[0,0]\n\t"
    "v_pk_mov_b32 v[142:143], v[242:243], v[242:243] op_sel:[0,0]\n\t"
    "v_pk_mov_b32 v[144:145], v[242:243], v[242:243] op_sel:[1,1]\n\t"
    "v_pk_mov_b32 v[146:147], v[242:243], v[242:243] op_sel:[1,1]\n\t"
    "v_pk_mov_b32 v[148:149], v[242:243], v[242:243] op_sel:[1,1]\n\t"
    "v_pk_mov_b32 v[150:151], v[242:243], v[242:243] op_sel:[1,1]\n\t"
    "v_pk_mov_b32 v[152:153], v[242:243], v[242:243] op_sel:[1,1]\n\t"
    "v_pk_mov_b32 v[154:155], v[242:243], v[242:243] op_sel:[1,1]\n\t"
    "v_pk_mov_b32 v[156:157], v[242:243], v[242:243] op_sel:[1,1]\n\t"
    "v_pk_mov_b32 v[158:159], v[242:243], v[242:243] op_sel:[1,1]\n\t"
    "s_cmp_eq_u32 s93, 0\n\t"
    "s_cbranch_scc1 Lnf_140_%=\n\t"
    "Lls_143_%=:\n\t"
    "v_add_f32 v250, v228, v229\n\t"
    "v_mov_b32 v228, 0\n\t"
    "v_mov_b32 v229, 0\n\t"
    "v_add_f32 v251, v230, v231\n\t"
    "v_mov_b32 v230, 0\n\t"
    "v_mov_b32 v231, 0\n\t"
    "s_mov_b32 s93, 0\n\t"
    "s_mov_b32 s60, 0x41000000\n\t"
    "s_mov_b32 s61, 0\n\t"
    "s_branch Ldn_141_%=\n\t"
    "Lnf_140_%=:\n\t"
    "v_exp_f32_e64 v248, -v240\n\t"
    "v_exp_f32_e64 v249, -v241\n\t"
    "s_nop 0\n\t"
    "v_mul_f32 v228, v228, v248\n\t"
    "v_mul_f32 v229, v229, v248\n\t"
    "v_mul_f32 v230, v230, v249\n\t"
    "v_mul_f32 v231, v231, v249\n\t"
    "s_mov_b32 s62, 1\n\t"
    "Ldn_141_%=:\n\t"
    "s_setpc_b64 s[90:91]\n\t"
    "LPEND_%=:\n\t"
    "s_nop 7\n\t"
    "s_nop 3\n\t"
    "v_accvgpr_read_b32 v240, a0\n\t"
    "v_accvgpr_read_b32 v241, a1\n\t"
    "v_accvgpr_read_b32 v242, a2\n\t"
    "v_accvgpr_read_b32 v243, a3\n\t"
    "v_mul_f32 v240, v240, v248\n\t"
    "v_mul_f32 v241, v241, v248\n\t"
    "v_mul_f32 v242, v242, v248\n\t"
    "v_mul_f32 v243, v243, v248\n\t"
    "v_accvgpr_write_b32 a0, v240\n\t"
    "v_accvgpr_write_b32 a1, v241\n\t"
    "v_accvgpr_write_b32 a2, v242\n\t"
    "v_accvgpr_write_b32 a3, v243\n\t"
    "v_accvgpr_read_b32 v240, a4\n\t"
    "v_accvgpr_read_b32 v241, a5\n\t"
    "v_accvgpr_read_b32 v242, a6\n\t"
    "v_accvgpr_read_b32 v243, a7\n\t"
    "v_mul_f32 v240, v240, v248\n\t"
    "v_mul_f32 v241, v241, v248\n\t"
    "v_mul_f32 v242, v242, v248\n\t"
    "v_mul_f32 v243, v243, v248\n\t"
    "v_accvgpr_write_b32 a4, v240\n\t"
    "v_accvgpr_write_b32 a5, v241\n\t"
    "v_accvgpr_write_b32 a6, v242\n\t"
    "v_accvgpr_write_b32 a7, v243\n\t"
    "v_accvgpr_read_b32 v240, a8\n\t"
    "v_accvgpr_read_b32 v241, a9\n\t"
    "v_accvgpr_read_b32 v242, a10\n\t"
    "v_accvgpr_read_b32 v243, a11\n\t"
    "v_mul_f32 v240, v240, v248\n\t"
    "v_mul_f32 v241, v241, v248\n\t"
    "v_mul_f32 v242, v242, v248\n\t"
    "v_mul_f32 v243, v243, v248\n\t"
    "v_accvgpr_write_b32 a8, v240\n\t"
    "v_accvgpr_write_b32 a9, v241\n\t"
    "v_accvgpr_write_b32 a10, v242\n\t"
    "v_accvgpr_write_b32 a11, v243\n\t"
    "v_accvgpr_read_b32 v240, a12\n\t"
    "v_accvgpr_read_b32 v241, a13\n\t"
    "v_accvgpr_read_b32 v242, a14\n\t"
    "v_accvgpr_read_b32 v243, a15\n\t"
    "v_mul_f32 v240, v240, v248\n\t"
    "v_mul_f32 v241, v241, v248\n\t"
    "v_mul_f32 v242, v242, v248\n\t"
    "v_mul_f32 v243, v243, v248\n\t"
    "v_accvgpr_write_b32 a12, v240\n\t"
    "v_accvgpr_write_b32 a13, v241\n\t"
    "v_accvgpr_write_b32 a14, v242\n\t"
    "v_accvgpr_write_b32 a15, v243\n\t"
    "v_accvgpr_read_b32 v240, a16\n\t"
    "v_accvgpr_read_b32 v241, a17\n\t"
    "v_accvgpr_read_b32 v242, a18\n\t"
    "v_accvgpr_read_b32 v243, a19\n\t"
    "v_mul_f32 v240, v240, v249\n\t"
    "v_mul_f32 v241, v241, v249\n\t"
    "v_mul_f32 v242, v242, v249\n\t"
    "v_mul_f32 v243, v243, v249\n\t"
    "v_accvgpr_write_b32 a16, v240\n\t"
    "v_accvgpr_write_b32 a17, v241\n\t"
    "v_accvgpr_write_b32 a18, v242\n\t"
    "v_accvgpr_write_b32 a19, v243\n\t"
    "v_accvgpr_read_b32 v240, a20\n\t"
    "v_accvgpr_read_b32 v241, a21\n\t"
    "v_accvgpr_read_b32 v242, a22\n\t"
    "v_accvgpr_read_b32 v243, a23\n\t"
    "v_mul_f32 v240, v240, v249\n\t"
    "v_mul_f32 v241, v241, v249\n\t"
    "v_mul_f32 v242, v242, v249\n\t"
    "v_mul_f32 v243, v243, v249\n\t"
    "v_accvgpr_write_b32 a20, v240\n\t"
    "v_accvgpr_write_b32 a21, v241\n\t"
    "v_accvgpr_write_b32 a22, v242\n\t"
    "v_accvgpr_write_b32 a23, v243\n\t"
    "v_accvgpr_read_b32 v240, a24\n\t"
    "v_accvgpr_read_b32 v241, a25\n\t"
    "v_accvgpr_read_b32 v242, a26\n\t"
    "v_accvgpr_read_b32 v243, a27\n\t"
    "v_mul_f32 v240, v240, v249\n\t"
    "v_mul_f32 v241, v241, v249\n\t"
    "v_mul_f32 v242, v242, v249\n\t"
    "v_mul_f32 v243, v243, v249\n\t"
    "v_accvgpr_write_b32 a24, v240\n\t"
    "v_accvgpr_write_b32 a25, v241\n\t"
    "v_accvgpr_write_b32 a26, v242\n\t"
    "v_accvgpr_write_b32 a27, v243\n\t"
    "v_accvgpr_read_b32 v240, a28\n\t"
    "v_accvgpr_read_b32 v241, a29\n\t"
    "v_accvgpr_read_b32 v242, a30\n\t"
    "v_accvgpr_read_b32 v243, a31\n\t"
    "v_mul_f32 v240, v240, v249\n\t"
    "v_mul_f32 v241, v241, v249\n\t"
    "v_mul_f32 v242, v242, v249\n\t"
    "v_mul_f32 v243, v243, v249\n\t"
    "v_accvgpr_write_b32 a28, v240\n\t"
    "v_accvgpr_write_b32 a29, v241\n\t"
    "v_accvgpr_write_b32 a30, v242\n\t"
    "v_accvgpr_write_b32 a31, v243\n\t"
    "v_accvgpr_read_b32 v240, a32\n\t"
    "v_accvgpr_read_b32 v241, a33\n\t"
    "v_accvgpr_read_b32 v242, a34\n\t"
    "v_accvgpr_read_b32 v243, a35\n\t"
    "v_mul_f32 v240, v240, v248\n\t"
    "v_mul_f32 v241, v241, v248\n\t"
    "v_mul_f32 v242, v242, v248\n\t"
    "v_mul_f32 v243, v243, v248\n\t"
    "v_accvgpr_write_b32 a32, v240\n\t"
    "v_accvgpr_write_b32 a33, v241\n\t"
    "v_accvgpr_write_b32 a34, v242\n\t"
    "v_accvgpr_write_b32 a35, v243\n\t"
    "v_accvgpr_read_b32 v240, a36\n\t"
    "v_accvgpr_read_b32 v241, a37\n\t"
    "v_accvgpr_read_b32 v242, a38\n\t"
    "v_accvgpr_read_b32 v243, a39\n\t"
    "v_mul_f32 v240, v240, v248\n\t"
    "v_mul_f32 v241, v241, v248\n\t"
    "v_mul_f32 v242, v242, v248\n\t"
    "v_mul_f32 v243, v243, v248\n\t"
    "v_accvgpr_write_b32 a36, v240\n\t"
    "v_accvgpr_write_b32 a37, v241\n\t"
    "v_accvgpr_write_b32 a38, v242\n\t"
    "v_accvgpr_write_b32 a39, v243\n\t"
    "v_accvgpr_read_b32 v240, a40\n\t"
    "v_accvgpr_read_b32 v241, a41\n\t"
    "v_accvgpr_read_b32 v242, a42\n\t"
    "v_accvgpr_read_b32 v243, a43\n\t"
    "v_mul_f32 v240, v240, v248\n\t"
    "v_mul_f32 v241, v241, v248\n\t"
    "v_mul_f32 v242, v242, v248\n\t"
    "v_mul_f32 v243, v243, v248\n\t"
    "v_accvgpr_write_b32 a40, v240\n\t"
    "v_accvgpr_write_b32 a41, v241\n\t"
    "v_accvgpr_write_b32 a42, v242\n\t"
    "v_accvgpr_write_b32 a43, v243\n\t"
    "v_accvgpr_read_b32 v240, a44\n\t"
    "v_accvgpr_read_b32 v241, a45\n\t"
    "v_accvgpr_read_b32 v242, a46\n\t"
    "v_accvgpr_read_b32 v243, a47\n\t"
    "v_mul_f32 v240, v240, v248\n\t"
    "v_mul_f32 v241, v241, v248\n\t"
    "v_mul_f32 v242, v242, v248\n\t"
    "v_mul_f32 v243, v243, v248\n\t"
    "v_accvgpr_write_b32 a44, v240\n\t"
    "v_accvgpr_write_b32 a45, v241\n\t"
    "v_accvgpr_write_b32 a46, v242\n\t"
    "v_accvgpr_write_b32 a47, v243\n\t"
    "v_accvgpr_read_b32 v240, a48\n\t"
    "v_accvgpr_read_b32 v241, a49\n\t"
    "v_accvgpr_read_b32 v242, a50\n\t"
    "v_accvgpr_read_b32 v243, a51\n\t"
    "v_mul_f32 v240, v240, v249\n\t"
    "v_mul_f32 v241, v241, v249\n\t"
    "v_mul_f32 v242, v242, v249\n\t"
    "v_mul_f32 v243, v243, v249\n\t"
    "v_accvgpr_write_b32 a48, v240\n\t"
    "v_accvgpr_write_b32 a49, v241\n\t"
    "v_accvgpr_write_b32 a50, v242\n\t"
    "v_accvgpr_write_b32 a51, v243\n\t"
    "v_accvgpr_read_b32 v240, a52\n\t"
    "v_accvgpr_read_b32 v241, a53\n\t"
    "v_accvgpr_read_b32 v242, a54\n\t"
    "v_accvgpr_read_b32 v243, a55\n\t"
    "v_mul_f32 v240, v240, v249\n\t"
    "v_mul_f32 v241, v241, v249\n\t"
    "v_mul_f32 v242, v242, v249\n\t"
    "v_mul_f32 v243, v243, v249\n\t"
    "v_accvgpr_write_b32 a52, v240\n\t"
    "v_accvgpr_write_b32 a53, v241\n\t"
    "v_accvgpr_write_b32 a54, v242\n\t"
    "v_accvgpr_write_b32 a55, v243\n\t"
    "v_accvgpr_read_b32 v240, a56\n\t"
    "v_accvgpr_read_b32 v241, a57\n\t"
    "v_accvgpr_read_b32 v242, a58\n\t"
    "v_accvgpr_read_b32 v243, a59\n\t"
    "v_mul_f32 v240, v240, v249\n\t"
    "v_mul_f32 v241, v241, v249\n\t"
    "v_mul_f32 v242, v242, v249\n\t"
    "v_mul_f32 v243, v243, v249\n\t"
    "v_accvgpr_write_b32 a56, v240\n\t"
    "v_accvgpr_write_b32 a57, v241\n\t"
    "v_accvgpr_write_b32 a58, v242\n\t"
    "v_accvgpr_write_b32 a59, v243\n\t"
    "v_accvgpr_read_b32 v240, a60\n\t"
    "v_accvgpr_read_b32 v241, a61\n\t"
    "v_accvgpr_read_b32 v242, a62\n\t"
    "v_accvgpr_read_b32 v243, a63\n\t"
    "v_mul_f32 v240, v240, v249\n\t"
    "v_mul_f32 v241, v241, v249\n\t"
    "v_mul_f32 v242, v242, v249\n\t"
    "v_mul_f32 v243, v243, v249\n\t"
    "v_accvgpr_write_b32 a60, v240\n\t"
    "v_accvgpr_write_b32 a61, v241\n\t"
    "v_accvgpr_write_b32 a62, v242\n\t"
    "v_accvgpr_write_b32 a63, v243\n\t"
    "v_accvgpr_read_b32 v240, a64\n\t"
    "v_accvgpr_read_b32 v241, a65\n\t"
    "v_accvgpr_read_b32 v242, a66\n\t"
    "v_accvgpr_read_b32 v243, a67\n\t"
    "v_mul_f32 v240, v240, v248\n\t"
    "v_mul_f32 v241, v241, v248\n\t"
    "v_mul_f32 v242, v242, v248\n\t"
    "v_mul_f32 v243, v243, v248\n\t"
    "v_accvgpr_write_b32 a64, v240\n\t"
    "v_accvgpr_write_b32 a65, v241\n\t"
    "v_accvgpr_write_b32 a66, v242\n\t"
    "v_accvgpr_write_b32 a67, v243\n\t"
    "v_accvgpr_read_b32 v240, a68\n\t"
    "v_accvgpr_read_b32 v241, a69\n\t"
    "v_accvgpr_read_b32 v242, a70\n\t"
    "v_accvgpr_read_b32 v243, a71\n\t"
    "v_mul_f32 v240, v240, v248\n\t"
    "v_mul_f32 v241, v241, v248\n\t"
    "v_mul_f32 v242, v242, v248\n\t"
    "v_mul_f32 v243, v243, v248\n\t"
    "v_accvgpr_write_b32 a68, v240\n\t"
    "v_accvgpr_write_b32 a69, v241\n\t"
    "v_accvgpr_write_b32 a70, v242\n\t"
    "v_accvgpr_write_b32 a71, v243\n\t"
    "v_accvgpr_read_b32 v240, a72\n\t"
    "v_accvgpr_read_b32 v241, a73\n\t"
    "v_accvgpr_read_b32 v242, a74\n\t"
    "v_accvgpr_read_b32 v243, a75\n\t"
    "v_mul_f32 v240, v240, v248\n\t"
    "v_mul_f32 v241, v241, v248\n\t"
    "v_mul_f32 v242, v242, v248\n\t"
    "v_mul_f32 v243, v243, v248\n\t"
    "v_accvgpr_write_b32 a72, v240\n\t"
    "v_accvgpr_write_b32 a73, v241\n\t"
    "v_accvgpr_write_b32 a74, v242\n\t"
    "v_accvgpr_write_b32 a75, v243\n\t"
    "v_accvgpr_read_b32 v240, a76\n\t"
    "v_accvgpr_read_b32 v241, a77\n\t"
    "v_accvgpr_read_b32 v242, a78\n\t"
    "v_accvgpr_read_b32 v243, a79\n\t"
    "v_mul_f32 v240, v240, v248\n\t"
    "v_mul_f32 v241, v241, v248\n\t"
    "v_mul_f32 v242, v242, v248\n\t"
    "v_mul_f32 v243, v243, v248\n\t"
    "v_accvgpr_write_b32 a76, v240\n\t"
    "v_accvgpr_write_b32 a77, v241\n\t"
    "v_accvgpr_write_b32 a78, v242\n\t"
    "v_accvgpr_write_b32 a79, v243\n\t"
    "v_accvgpr_read_b32 v240, a80\n\t"
    "v_accvgpr_read_b32 v241, a81\n\t"
    "v_accvgpr_read_b32 v242, a82\n\t"
    "v_accvgpr_read_b32 v243, a83\n\t"
    "v_mul_f32 v240, v240, v249\n\t"
    "v_mul_f32 v241, v241, v249\n\t"
    "v_mul_f32 v242, v242, v249\n\t"
    "v_mul_f32 v243, v243, v249\n\t"
    "v_accvgpr_write_b32 a80, v240\n\t"
    "v_accvgpr_write_b32 a81, v241\n\t"
    "v_accvgpr_write_b32 a82, v242\n\t"
    "v_accvgpr_write_b32 a83, v243\n\t"
    "v_accvgpr_read_b32 v240, a84\n\t"
    "v_accvgpr_read_b32 v241, a85\n\t"
    "v_accvgpr_read_b32 v242, a86\n\t"
    "v_accvgpr_read_b32 v243, a87\n\t"
    "v_mul_f32 v240, v240, v249\n\t"
    "v_mul_f32 v241, v241, v249\n\t"
    "v_mul_f32 v242, v242, v249\n\t"
    "v_mul_f32 v243, v243, v249\n\t"
    "v_accvgpr_write_b32 a84, v240\n\t"
    "v_accvgpr_write_b32 a85, v241\n\t"
    "v_accvgpr_write_b32 a86, v242\n\t"
    "v_accvgpr_write_b32 a87, v243\n\t"
    "v_accvgpr_read_b32 v240, a88\n\t"
    "v_accvgpr_read_b32 v241, a89\n\t"
    "v_accvgpr_read_b32 v242, a90\n\t"
    "v_accvgpr_read_b32 v243, a91\n\t"
    "v_mul_f32 v240, v240, v249\n\t"
    "v_mul_f32 v241, v241, v249\n\t"
    "v_mul_f32 v242, v242, v249\n\t"
    "v_mul_f32 v243, v243, v249\n\t"
    "v_accvgpr_write_b32 a88, v240\n\t"
    "v_accvgpr_write_b32 a89, v241\n\t"
    "v_accvgpr_write_b32 a90, v242\n\t"
    "v_accvgpr_write_b32 a91, v243\n\t"
    "v_accvgpr_read_b32 v240, a92\n\t"
    "v_accvgpr_read_b32 v241, a93\n\t"
    "v_accvgpr_read_b32 v242, a94\n\t"
    "v_accvgpr_read_b32 v243, a95\n\t"
    "v_mul_f32 v240, v240, v249\n\t"
    "v_mul_f32 v241, v241, v249\n\t"
    "v_mul_f32 v242, v242, v249\n\t"
    "v_mul_f32 v243, v243, v249\n\t"
    "v_accvgpr_write_b32 a92, v240\n\t"
    "v_accvgpr_write_b32 a93, v241\n\t"
    "v_accvgpr_write_b32 a94, v242\n\t"
    "v_accvgpr_write_b32 a95, v243\n\t"
    "v_accvgpr_read_b32 v240, a96\n\t"
    "v_accvgpr_read_b32 v241, a97\n\t"
    "v_accvgpr_read_b32 v242, a98\n\t"
    "v_accvgpr_read_b32 v243, a99\n\t"
    "v_mul_f32 v240, v240, v248\n\t"
    "v_mul_f32 v241, v241, v248\n\t"
    "v_mul_f32 v242, v242, v248\n\t"
    "v_mul_f32 v243, v243, v248\n\t"
    "v_accvgpr_write_b32 a96, v240\n\t"
    "v_accvgpr_write_b32 a97, v241\n\t"
    "v_accvgpr_write_b32 a98, v242\n\t"
    "v_accvgpr_write_b32 a99, v243\n\t"
    "v_accvgpr_read_b32 v240, a100\n\t"
    "v_accvgpr_read_b32 v241, a101\n\t"
    "v_accvgpr_read_b32 v242, a102\n\t"
    "v_accvgpr_read_b32 v243, a103\n\t"
    "v_mul_f32 v240, v240, v248\n\t"
    "v_mul_f32 v241, v241, v248\n\t"
    "v_mul_f32 v242, v242, v248\n\t"
    "v_mul_f32 v243, v243, v248\n\t"
    "v_accvgpr_write_b32 a100, v240\n\t"
    "v_accvgpr_write_b32 a101, v241\n\t"
    "v_accvgpr_write_b32 a102, v242\n\t"
    "v_accvgpr_write_b32 a103, v243\n\t"
    "v_accvgpr_read_b32 v240, a104\n\t"
    "v_accvgpr_read_b32 v241, a105\n\t"
    "v_accvgpr_read_b32 v242, a106\n\t"
    "v_accvgpr_read_b32 v243, a107\n\t"
    "v_mul_f32 v240, v240, v248\n\t"
    "v_mul_f32 v241, v241, v248\n\t"
    "v_mul_f32 v242, v242, v248\n\t"
    "v_mul_f32 v243, v243, v248\n\t"
    "v_accvgpr_write_b32 a104, v240\n\t"
    "v_accvgpr_write_b32 a105, v241\n\t"
    "v_accvgpr_write_b32 a106, v242\n\t"
    "v_accvgpr_write_b32 a107, v243\n\t"
    "v_accvgpr_read_b32 v240, a108\n\t"
    "v_accvgpr_read_b32 v241, a109\n\t"
    "v_accvgpr_read_b32 v242, a110\n\t"
    "v_accvgpr_read_b32 v243, a111\n\t"
    "v_mul_f32 v240, v240, v248\n\t"
    "v_mul_f32 v241, v241, v248\n\t"
    "v_mul_f32 v242, v242, v248\n\t"
    "v_mul_f32 v243, v243, v248\n\t"
    "v_accvgpr_write_b32 a108, v240\n\t"
    "v_accvgpr_write_b32 a109, v241\n\t"
    "v_accvgpr_write_b32 a110, v242\n\t"
    "v_accvgpr_write_b32 a111, v243\n\t"
    "v_accvgpr_read_b32 v240, a112\n\t"
    "v_accvgpr_read_b32 v241, a113\n\t"
    "v_accvgpr_read_b32 v242, a114\n\t"
    "v_accvgpr_read_b32 v243, a115\n\t"
    "v_mul_f32 v240, v240, v249\n\t"
    "v_mul_f32 v241, v241, v249\n\t"
    "v_mul_f32 v242, v242, v249\n\t"
    "v_mul_f32 v243, v243, v249\n\t"
    "v_accvgpr_write_b32 a112, v240\n\t"
    "v_accvgpr_write_b32 a113, v241\n\t"
    "v_accvgpr_write_b32 a114, v242\n\t"
    "v_accvgpr_write_b32 a115, v243\n\t"
    "v_accvgpr_read_b32 v240, a116\n\t"
    "v_accvgpr_read_b32 v241, a117\n\t"
    "v_accvgpr_read_b32 v242, a118\n\t"
    "v_accvgpr_read_b32 v243, a119\n\t"
    "v_mul_f32 v240, v240, v249\n\t"
    "v_mul_f32 v241, v241, v249\n\t"
    "v_mul_f32 v242, v242, v249\n\t"
    "v_mul_f32 v243, v243, v249\n\t"
    "v_accvgpr_write_b32 a116, v240\n\t"
    "v_accvgpr_write_b32 a117, v241\n\t"
    "v_accvgpr_write_b32 a118, v242\n\t"
    "v_accvgpr_write_b32 a119, v243\n\t"
    "v_accvgpr_read_b32 v240, a120\n\t"
    "v_accvgpr_read_b32 v241, a121\n\t"
    "v_accvgpr_read_b32 v242, a122\n\t"
    "v_accvgpr_read_b32 v243, a123\n\t"
    "v_mul_f32 v240, v240, v249\n\t"
    "v_mul_f32 v241, v241, v249\n\t"
    "v_mul_f32 v242, v242, v249\n\t"
    "v_mul_f32 v243, v243, v249\n\t"
    "v_accvgpr_write_b32 a120, v240\n\t"
    "v_accvgpr_write_b32 a121, v241\n\t"
    "v_accvgpr_write_b32 a122, v242\n\t"
    "v_accvgpr_write_b32 a123, v243\n\t"
    "v_accvgpr_read_b32 v240, a124\n\t"
    "v_accvgpr_read_b32 v241, a125\n\t"
    "v_accvgpr_read_b32 v242, a126\n\t"
    "v_accvgpr_read_b32 v243, a127\n\t"
    "v_mul_f32 v240, v240, v249\n\t"
    "v_mul_f32 v241, v241, v249\n\t"
    "v_mul_f32 v242, v242, v249\n\t"
    "v_mul_f32 v243, v243, v249\n\t"
    "v_accvgpr_write_b32 a124, v240\n\t"
    "v_accvgpr_write_b32 a125, v241\n\t"
    "v_accvgpr_write_b32 a126, v242\n\t"
    "v_accvgpr_write_b32 a127, v243\n\t"
    "s_mov_b32 s62, 0\n\t"
    "s_nop 1\n\t"
    "s_setpc_b64 s[90:91]\n\t"
    "Llate2_134_%=:\n\t"
    "v_mov_b32 v136, %14\n\t"
    "v_and_b32 v136, -16, v136\n\t"
    "global_load_dwordx4 v[248:251], v136, %18 sc1\n\t"
    "ds_read_b32 v240, v247 offset:0\n\t"
    "ds_read_b32 v241, v247 offset:256\n\t"
    "ds_read_b32 v242, v247 offset:512\n\t"
    "ds_read_b32 v243, v247 offset:768\n\t"
    "ds_read_b128 v[0:3], v245 offset:0\n\t"
    "ds_read_b128 v[4:7], v245 offset:1024\n\t"
    "ds_read_b128 v[8:11], v245 offset:2048\n\t"
    "ds_read_b128 v[12:15], v245 offset:3072\n\t"
    "ds_read_b128 v[16:19], v245 offset:4096\n\t"
    "ds_read_b128 v[20:23], v245 offset:5120\n\t"
    "ds_read_b128 v[24:27], v245 offset:6144\n\t"
    "ds_read_b128 v[28:31], v245 offset:7168\n\t"
    "ds_read_b128 v[32:35], v245 offset:8192\n\t"
    "ds_read_b128 v[36:39], v245 offset:9216\n\t"
    "ds_read_b128 v[40:43], v245 offset:10240\n\t"
    "ds_read_b128 v[44:47], v245 offset:11264\n\t"
    "ds_read_b128 v[48:51], v245 offset:12288\n\t"
    "ds_read_b128 v[52:55], v245 offset:13312\n\t"
    "ds_read_b128 v[56:59], v245 offset:14336\n\t"
    "ds_read_b128 v[60:63], v245 offset:15360\n\t"
    "s_cmp_ge_u32 s89, 2\n\t"
    "s_cbranch_scc1 La1_152_%=\n\t"
    "v_accvgpr_read_b32 v64, a0\n\t"
    "v_accvgpr_read_b32 v65, a1\n\t"
    "v_accvgpr_read_b32 v66, a2\n\t"
    "v_accvgpr_read_b32 v67, a3\n\t"
    "v_accvgpr_read_b32 v68, a4\n\t"
    "v_accvgpr_read_b32 v69, a5\n\t"
    "v_accvgpr_read_b32 v70, a6\n\t"
    "v_accvgpr_read_b32 v71, a7\n\t"
    "v_accvgpr_read_b32 v72, a8\n\t"
    "v_accvgpr_read_b32 v73, a9\n\t"
    "v_accvgpr_read_b32 v74, a10\n\t"
    "v_accvgpr_read_b32 v75, a11\n\t"
    "v_accvgpr_read_b32 v76, a12\n\t"
    "v_accvgpr_read_b32 v77, a13\n\t"
    "v_accvgpr_read_b32 v78, a14\n\t"
    "v_accvgpr_read_b32 v79, a15\n\t"
    "v_accvgpr_read_b32 v80, a16\n\t"
    "v_accvgpr_read_b32 v81, a17\n\t"
    "v_accvgpr_read_b32 v82, a18\n\t"
    "v_accvgpr_read_b32 v83, a19\n\t"
    "v_accvgpr_read_b32 v84, a20\n\t"
    "v_accvgpr_read_b32 v85, a21\n\t"
    "v_accvgpr_read_b32 v86, a22\n\t"
    "v_accvgpr_read_b32 v87, a23\n\t"
    "v_accvgpr_read_b32 v88, a24\n\t"
    "v_accvgpr_read_b32 v89, a25\n\t"
    "v_accvgpr_read_b32 v90, a26\n\t"
    "v_accvgpr_read_b32 v91, a27\n\t"
    "v_accvgpr_read_b32 v92, a28\n\t"
    "v_accvgpr_read_b32 v93, a29\n\t"
    "v_accvgpr_read_b32 v94, a30\n\t"
    "v_accvgpr_read_b32 v95, a31\n\t"
    "v_accvgpr_read_b32 v96, a32\n\t"
    "v_accvgpr_read_b32 v97, a33\n\t"
    "v_accvgpr_read_b32 v98, a34\n\t"
    "v_accvgpr_read_b32 v99, a35\n\t"
    "v_accvgpr_read_b32 v100, a36\n\t"
    "v_accvgpr_read_b32 v101, a37\n\t"
    "v_accvgpr_read_b32 v102, a38\n\t"
    "v_accvgpr_read_b32 v103, a39\n\t"
    "v_accvgpr_read_b32 v104, a40\n\t"
    "v_accvgpr_read_b32 v105, a41\n\t"
    "v_accvgpr_read_b32 v106, a42\n\t"
    "v_accvgpr_read_b32 v107, a43\n\t"
    "v_accvgpr_read_b32 v108, a44\n\t"
    "v_accvgpr_read_b32 v109, a45\n\t"
    "v_accvgpr_read_b32 v110, a46\n\t"
    "v_accvgpr_read_b32 v111, a47\n\t"
    "v_accvgpr_read_b32 v112, a48\n\t"
    "v_accvgpr_read_b32 v113, a49\n\t"
    "v_accvgpr_read_b32 v114, a50\n\t"
    "v_accvgpr_read_b32 v115, a51\n\t"
    "v_accvgpr_read_b32 v116, a52\n\t"
    "v_accvgpr_read_b32 v117, a53\n\t"
    "v_accvgpr_read_b32 v118, a54\n\t"
    "v_accvgpr_read_b32 v119, a55\n\t"
    "v_accvgpr_read_b32 v120, a56\n\t"
    "v_accvgpr_read_b32 v121, a57\n\t"
    "v_accvgpr_read_b32 v122, a58\n\t"
    "v_accvgpr_read_b32 v123, a59\n\t"
    "v_accvgpr_read_b32 v124, a60\n\t"
    "v_accvgpr_read_b32 v125, a61\n\t"
    "v_accvgpr_read_b32 v126, a62\n\t"
    "v_accvgpr_read_b32 v127, a63\n\t"
    "s_branch La2_153_%=\n\t"
    "La1_152_%=:\n\t"
    "v_accvgpr_read_b32 v64, a64\n\t"
    "v_accvgpr_read_b32 v65, a65\n\t"
    "v_accvgpr_read_b32 v66, a66\n\t"
    "v_accvgpr_read_b32 v67, a67\n\t"
    "v_accvgpr_read_b32 v68, a68\n\t"
    "v_accvgpr_read_b32 v69, a69\n\t"
    "v_accvgpr_read_b32 v70, a70\n\t"
    "v_accvgpr_read_b32 v71, a71\n\t"
    "v_accvgpr_read_b32 v72, a72\n\t"
    "v_accvgpr_read_b32 v73, a73\n\t"
    "v_accvgpr_read_b32 v74, a74\n\t"
    "v_accvgpr_read_b32 v75, a75\n\t"
    "v_accvgpr_read_b32 v76, a76\n\t"
    "v_accvgpr_read_b32 v77, a77\n\t"
    "v_accvgpr_read_b32 v78, a78\n\t"
    "v_accvgpr_read_b32 v79, a79\n\t"
    "v_accvgpr_read_b32 v80, a80\n\t"
    "v_accvgpr_read_b32 v81, a81\n\t"
    "v_accvgpr_read_b32 v82, a82\n\t"
    "v_accvgpr_read_b32 v83, a83\n\t"
    "v_accvgpr_read_b32 v84, a84\n\t"
    "v_accvgpr_read_b32 v85, a85\n\t"
    "v_accvgpr_read_b32 v86, a86\n\t"
    "v_accvgpr_read_b32 v87, a87\n\t"
    "v_accvgpr_read_b32 v88, a88\n\t"
    "v_accvgpr_read_b32 v89, a89\n\t"
    "v_accvgpr_read_b32 v90, a90\n\t"
    "v_accvgpr_read_b32 v91, a91\n\t"
    "v_accvgpr_read_b32 v92, a92\n\t"
    "v_accvgpr_read_b32 v93, a93\n\t"
    "v_accvgpr_read_b32 v94, a94\n\t"
    "v_accvgpr_read_b32 v95, a95\n\t"
    "v_accvgpr_read_b32 v96, a96\n\t"
    "v_accvgpr_read_b32 v97, a97\n\t"
    "v_accvgpr_read_b32 v98, a98\n\t"
    "v_accvgpr_read_b32 v99, a99\n\t"
    "v_accvgpr_read_b32 v100, a100\n\t"
    "v_accvgpr_read_b32 v101, a101\n\t"
    "v_accvgpr_read_b32 v102, a102\n\t"
    "v_accvgpr_read_b32 v103, a103\n\t"
    "v_accvgpr_read_b32 v104, a104\n\t"
    "v_accvgpr_read_b32 v105, a105\n\t"
    "v_accvgpr_read_b32 v106, a106\n\t"
    "v_accvgpr_read_b32 v107, a107\n\t"
    "v_accvgpr_read_b32 v108, a108\n\t"
    "v_accvgpr_read_b32 v109, a109\n\t"
    "v_accvgpr_read_b32 v110, a110\n\t"
    "v_accvgpr_read_b32 v111, a111\n\t"
    "v_accvgpr_read_b32 v112, a112\n\t"
    "v_accvgpr_read_b32 v113, a113\n\t"
    "v_accvgpr_read_b32 v114, a114\n\t"
    "v_accvgpr_read_b32 v115, a115\n\t"
    "v_accvgpr_read_b32 v116, a116\n\t"
    "v_accvgpr_read_b32 v117, a117\n\t"
    "v_accvgpr_read_b32 v118, a118\n\t"
    "v_accvgpr_read_b32 v119, a119\n\t"
    "v_accvgpr_read_b32 v120, a120\n\t"
    "v_accvgpr_read_b32 v121, a121\n\t"
    "v_accvgpr_read_b32 v122, a122\n\t"
    "v_accvgpr_read_b32 v123, a123\n\t"
    "v_accvgpr_read_b32 v124, a124\n\t"
    "v_accvgpr_read_b32 v125, a125\n\t"
    "v_accvgpr_read_b32 v126, a126\n\t"
    "v_accvgpr_read_b32 v127, a127\n\t"
    "La2_153_%=:\n\t"
    "s_waitcnt lgkmcnt(0)\n\t"
    "s_waitcnt vmcnt(0)\n\t"
    "v_max_u32 v248, v248, v249\n\t"
    "v_max3_u32 v248, v248, v250, v251\n\t"
    "v_cmp_eq_u32 vcc, -1, v248\n\t"
    "s_nop 0\n\t"
    "s_cmp_eq_u64 vcc, 0\n\t"
    "s_cselect_b32 s46, 0xff2000, 0\n\t"
    "s_cbranch_scc1 Lp3_135_%=\n\t"
    "v_mov_b32 v136, %14\n\t"
    "v_and_b32 v136, -16, v136\n\t"
    "global_load_dwordx4 v[248:251], v136, %18 sc1\n\t"
    "v_max_f32 v238, v232, v240\n\t"
    "v_sub_f32 v128, v232, v238\n\t"
    "v_sub_f32 v130, v240, v238\n\t"
    "v_exp_f32 v128, v128\n\t"
    "v_exp_f32 v130, v130\n\t"
    "s_nop 0\n\t"
    "v_mul_f32 v238, v128, v228\n\t"
    "v_fma_f32 v238, v130, v242, v238\n\t"
    "v_rcp_f32 v238, v238\n\t"
    "s_nop 0\n\t"
    "v_mul_f32 v128, v128, v238\n\t"
    "v_mul_f32 v130, v130, v238\n\t"
    "v_max_f32 v238, v233, v241\n\t"
    "v_sub_f32 v132, v233, v238\n\t"
    "v_sub_f32 v134, v241, v238\n\t"
    "v_exp_f32 v132, v132\n\t"
    "v_exp_f32 v134, v134\n\t"
    "s_nop 0\n\t"
    "v_mul_f32 v238, v132, v230\n\t"
    "v_fma_f32 v238, v134, v243, v238\n\t"
    "v_rcp_f32 v238, v238\n\t"
    "s_nop 0\n\t"
    "v_mul_f32 v132, v132, v238\n\t"
    "v_mul_f32 v134, v134, v238\n\t"
    "s_add_u32 s66, s73, 0\n\t"
    "v_pk_mul_f32 v[0:1], v[0:1], v[130:131] op_sel_hi:[1,0]\n\t"
    "v_pk_mul_f32 v[2:3], v[2:3], v[130:131] op_sel_hi:[1,0]\n\t"
    "v_pk_mul_f32 v[4:5], v[4:5], v[130:131] op_sel_hi:[1,0]\n\t"
    "v_pk_mul_f32 v[6:7], v[6:7], v[130:131] op_sel_hi:[1,0]\n\t"
    "v_pk_mul_f32 v[8:9], v[8:9], v[130:131] op_sel_hi:[1,0]\n\t"
    "v_pk_mul_f32 v[10:11], v[10:11], v[130:131] op_sel_hi:[1,0]\n\t"
    "v_pk_mul_f32 v[12:13], v[12:13], v[130:131] op_sel_hi:[1,0]\n\t"
    "v_pk_mul_f32 v[14:15], v[14:15], v[130:131] op_sel_hi:[1,0]\n\t"
    "v_pk_fma_f32 v[0:1], v[64:65], v[128:129], v[0:1] op_sel_hi:[1,0,1]\n\t"
    "v_pk_fma_f32 v[2:3], v[66:67], v[128:129], v[2:3] op_sel_hi:[1,0,1]\n\t"
    "v_pk_fma_f32 v[4:5], v[68:69], v[128:129], v[4:5] op_sel_hi:[1,0,1]\n\t"
    "v_pk_fma_f32 v[6:7], v[70:71], v[128:129], v[6:7] op_sel_hi:[1,0,1]\n\t"
    "v_pk_fma_f32 v[8:9], v[72:73], v[128:129], v[8:9] op_sel_hi:[1,0,1]\n\t"
    "v_pk_fma_f32 v[10:11], v[74:75], v[128:129], v[10:11] op_sel_hi:[1,0,1]\n\t"
    "v_pk_fma_f32 v[12:13], v[76:77], v[128:129], v[12:13] op_sel_hi:[1,0,1]\n\t"
    "v_pk_fma_f32 v[14:15], v[78:79], v[128:129], v[14:15] op_sel_hi:[1,0,1]\n\t"
    "v_cvt_pk_bf16_f32 v0, v0, v1\n\t"
    "v_cvt_pk_bf16_f32 v1, v2, v3\n\t"
    "v_cvt_pk_bf16_f32 v2, v4, v5\n\t"
    "v_cvt_pk_bf16_f32 v3, v6, v7\n\t"
    "v_cvt_pk_bf16_f32 v8, v8, v9\n\t"
    "v_cvt_pk_bf16_f32 v9, v10, v11\n\t"
    "v_cvt_pk_bf16_f32 v10, v12, v13\n\t"
    "v_cvt_pk_bf16_f32 v11, v14, v15\n\t"
    "buffer_store_dwordx4 v[0:3], v224, s[48:51], s66 offen offset:0 sc1\n\t"
    "buffer_store_dwordx4 v[8:11], v224, s[48:51], s66 offen offset:1024 sc1\n\t"
    "v_pk_mul_f32 v[32:33], v[32:33], v[130:131] op_sel_hi:[1,0]\n\t"
    "v_pk_mul_f32 v[34:35], v[34:35], v[130:131] op_sel_hi:[1,0]\n\t"
    "v_pk_mul_f32 v[36:37], v[36:37], v[130:131] op_sel_hi:[1,0]\n\t"
    "v_pk_mul_f32 v[38:39], v[38:39], v[130:131] op_sel_hi:[1,0]\n\t"
    "v_pk_mul_f32 v[40:41], v[40:41], v[130:131] op_sel_hi:[1,0]\n\t"
    "v_pk_mul_f32 v[42:43], v[42:43], v[130:131] op_sel_hi:[1,0]\n\t"
    "v_pk_mul_f32 v[44:45], v[44:45], v[130:131] op_sel_hi:[1,0]\n\t"
    "v_pk_mul_f32 v[46:47], v[46:47], v[130:131] op_sel_hi:[1,0]\n\t"
    "v_pk_fma_f32 v[32:33], v[96:97], v[128:129], v[32:33] op_sel_hi:[1,0,1]\n\t"
    "v_pk_fma_f32 v[34:35], v[98:99], v[128:129], v[34:35] op_sel_hi:[1,0,1]\n\t"
    "v_pk_fma_f32 v[36:37], v[100:101], v[128:129], v[36:37] op_sel_hi:[1,0,1]\n\t"
    "v_pk_fma_f32 v[38:39], v[102:103], v[128:129], v[38:39] op_sel_hi:[1,0,1]\n\t"
    "v_pk_fma_f32 v[40:41], v[104:105], v[128:129], v[40:41] op_sel_hi:[1,0,1]\n\t"
    "v_pk_fma_f32 v[42:43], v[106:107], v[128:129], v[42:43] op_sel_hi:[1,0,1]\n\t"
    "v_pk_fma_f32 v[44:45], v[108:109], v[128:129], v[44:45] op_sel_hi:[1,0,1]\n\t"
    "v_pk_fma_f32 v[46:47], v[110:111], v[128:129], v[46:47] op_sel_hi:[1,0,1]\n\t"
    "v_cvt_pk_bf16_f32 v32, v32, v33\n\t"
    "v_cvt_pk_bf16_f32 v33, v34, v35\n\t"
    "v_cvt_pk_bf16_f32 v34, v36, v37\n\t"
    "v_cvt_pk_bf16_f32 v35, v38, v39\n\t"
    "v_cvt_pk_bf16_f32 v40, v40, v41\n\t"
    "v_cvt_pk_bf16_f32 v41, v42, v43\n\t"
    "v_cvt_pk_bf16_f32 v42, v44, v45\n\t"
    "v_cvt_pk_bf16_f32 v43, v46, v47\n\t"
    "buffer_store_dwordx4 v[32:35], v224, s[48:51], s66 offen offset:2048 sc1\n\t"
    "buffer_store_dwordx4 v[40:43], v224, s[48:51], s66 offen offset:3072 sc1\n\t"
    "s_add_u32 s66, s73, 262144\n\t"
    "v_pk_mul_f32 v[16:17], v[16:17], v[134:135] op_sel_hi:[1,0]\n\t"
    "v_pk_mul_f32 v[18:19], v[18:19], v[134:135] op_sel_hi:[1,0]\n\t"
    "v_pk_mul_f32 v[20:21], v[20:21], v[134:135] op_sel_hi:[1,0]\n\t"
    "v_pk_mul_f32 v[22:23], v[22:23], v[134:135] op_sel_hi:[1,0]\n\t"
    "v_pk_mul_f32 v[24:25], v[24:25], v[134:135] op_sel_hi:[1,0]\n\t"
    "v_pk_mul_f32 v[26:27], v[26:27], v[134:135] op_sel_hi:[1,0]\n\t"
    "v_pk_mul_f32 v[28:29], v[28:29], v[134:135] op_sel_hi:[1,0]\n\t"
    "v_pk_mul_f32 v[30:31], v[30:31], v[134:135] op_sel_hi:[1,0]\n\t"
    "v_pk_fma_f32 v[16:17], v[80:81], v[132:133], v[16:17] op_sel_hi:[1,0,1]\n\t"
    "v_pk_fma_f32 v[18:19], v[82:83], v[132:133], v[18:19] op_sel_hi:[1,0,1]\n\t"
    "v_pk_fma_f32 v[20:21], v[84:85], v[132:133], v[20:21] op_sel_hi:[1,0,1]\n\t"
    "v_pk_fma_f32 v[22:23], v[86:87], v[132:133], v[22:23] op_sel_hi:[1,0,1]\n\t"
    "v_pk_fma_f32 v[24:25], v[88:89], v[132:133], v[24:25] op_sel_hi:[1,0,1]\n\t"
    "v_pk_fma_f32 v[26:27], v[90:91], v[132:133], v[26:27] op_sel_hi:[1,0,1]\n\t"
    "v_pk_fma_f32 v[28:29], v[92:93], v[132:133], v[28:29] op_sel_hi:[1,0,1]\n\t"
    "v_pk_fma_f32 v[30:31], v[94:95], v[132:133], v[30:31] op_sel_hi:[1,0,1]\n\t"
    "v_cvt_pk_bf16_f32 v16, v16, v17\n\t"
    "v_cvt_pk_bf16_f32 v17, v18, v19\n\t"
    "v_cvt_pk_bf16_f32 v18, v20, v21\n\t"
    "v_cvt_pk_bf16_f32 v19, v22, v23\n\t"
    "v_cvt_pk_bf16_f32 v24, v24, v25\n\t"
    "v_cvt_pk_bf16_f32 v25, v26, v27\n\t"
    "v_cvt_pk_bf16_f32 v26, v28, v29\n\t"
    "v_cvt_pk_bf16_f32 v27, v30, v31\n\t"
    "buffer_store_dwordx4 v[16:19], v224, s[48:51], s66 offen offset:0 sc1\n\t"
    "buffer_store_dwordx4 v[24:27], v224, s[48:51], s66 offen offset:1024 sc1\n\t"
    "v_pk_mul_f32 v[48:49], v[48:49], v[134:135] op_sel_hi:[1,0]\n\t"
    "v_pk_mul_f32 v[50:51], v[50:51], v[134:135] op_sel_hi:[1,0]\n\t"
    "v_pk_mul_f32 v[52:53], v[52:53], v[134:135] op_sel_hi:[1,0]\n\t"
    "v_pk_mul_f32 v[54:55], v[54:55], v[134:135] op_sel_hi:[1,0]\n\t"
    "v_pk_mul_f32 v[56:57], v[56:57], v[134:135] op_sel_hi:[1,0]\n\t"
    "v_pk_mul_f32 v[58:59], v[58:59], v[134:135] op_sel_hi:[1,0]\n\t"
    "v_pk_mul_f32 v[60:61], v[60:61], v[134:135] op_sel_hi:[1,0]\n\t"
    "v_pk_mul_f32 v[62:63], v[62:63], v[134:135] op_sel_hi:[1,0]\n\t"
    "v_pk_fma_f32 v[48:49], v[112:113], v[132:133], v[48:49] op_sel_hi:[1,0,1]\n\t"
    "v_pk_fma_f32 v[50:51], v[114:115], v[132:133], v[50:51] op_sel_hi:[1,0,1]\n\t"
    "v_pk_fma_f32 v[52:53], v[116:117], v[132:133], v[52:53] op_sel_hi:[1,0,1]\n\t"
    "v_pk_fma_f32 v[54:55], v[118:119], v[132:133], v[54:55] op_sel_hi:[1,0,1]\n\t"
    "v_pk_fma_f32 v[56:57], v[120:121], v[132:133], v[56:57] op_sel_hi:[1,0,1]\n\t"
    "v_pk_fma_f32 v[58:59], v[122:123], v[132:133], v[58:59] op_sel_hi:[1,0,1]\n\t"
    "v_pk_fma_f32 v[60:61], v[124:125], v[132:133], v[60:61] op_sel_hi:[1,0,1]\n\t"
    "v_pk_fma_f32 v[62:63], v[126:127], v[132:133], v[62:63] op_sel_hi:[1,0,1]\n\t"
    "v_cvt_pk_bf16_f32 v48, v48, v49\n\t"
    "v_cvt_pk_bf16_f32 v49, v50, v51\n\t"
    "v_cvt_pk_bf16_f32 v50, v52, v53\n\t"
    "v_cvt_pk_bf16_f32 v51, v54, v55\n\t"
    "v_cvt_pk_bf16_f32 v56, v56, v57\n\t"
    "v_cvt_pk_bf16_f32 v57, v58, v59\n\t"
    "v_cvt_pk_bf16_f32 v58, v60, v61\n\t"
    "v_cvt_pk_bf16_f32 v59, v62, v63\n\t"
    "buffer_store_dwordx4 v[48:51], v224, s[48:51], s66 offen offset:2048 sc1\n\t"
    "buffer_store_dwordx4 v[56:59], v224, s[48:51], s66 offen offset:3072 sc1\n\t"
    "s_branch LEND_%=\n\t"
    "Lp3_135_%=:\n\t"
    "s_lshl_b32 s71, %14, 14\n\t"
    "s_add_u32 s71, s71, 131072\n\t"
    "s_and_b32 s71, s71, 196608\n\t"
    "s_add_u32 s71, s71, s70\n\t"
    "buffer_load_dwordx4 a[128:131], v224, s[44:47], s71 offen offset:0 sc1\n\t"
    "v_max_f32 v238, v232, v240\n\t"
    "v_sub_f32 v128, v232, v238\n\t"
    "v_sub_f32 v130, v240, v238\n\t"
    "buffer_load_dwordx4 a[132:135], v224, s[44:47], s71 offen offset:1024 sc1\n\t"
    "v_exp_f32 v128, v128\n\t"
    "v_exp_f32 v130, v130\n\t"
    "s_nop 0\n\t"
    "buffer_load_dwordx4 a[136:139], v224, s[44:47], s71 offen offset:2048 sc1\n\t"
    "v_mul_f32 v238, v128, v228\n\t"
    "v_fma_f32 v238, v130, v242, v238\n\t"
    "v_rcp_f32 v238, v238\n\t"
    "buffer_load_dwordx4 a[140:143], v224, s[44:47], s71 offen offset:3072 sc1\n\t"
    "s_nop 0\n\t"
    "v_mul_f32 v128, v128, v238\n\t"
    "v_mul_f32 v130, v130, v238\n\t"
    "s_add_u32 s71, s71, 0x1000\n\t"
    "buffer_load_dwordx4 a[144:147], v224, s[44:47], s71 offen offset:0 sc1\n\t"
    "v_max_f32 v238, v233, v241\n\t"
    "v_sub_f32 v132, v233, v238\n\t"
    "v_sub_f32 v134, v241, v238\n\t"
    "buffer_load_dwordx4 a[148:151], v224, s[44:47], s71 offen offset:1024 sc1\n\t"
    "v_exp_f32 v132, v132\n\t"
    "v_exp_f32 v134, v134\n\t"
    "s_nop 0\n\t"
    "buffer_load_dwordx4 a[152:155], v224, s[44:47], s71 offen offset:2048 sc1\n\t"
    "v_mul_f32 v238, v132, v230\n\t"
    "v_fma_f32 v238, v134, v243, v238\n\t"
    "v_rcp_f32 v238, v238\n\t"
    "buffer_load_dwordx4 a[156:159], v224, s[44:47], s71 offen offset:3072 sc1\n\t"
    "s_nop 0\n\t"
    "v_mul_f32 v132, v132, v238\n\t"
    "v_mul_f32 v134, v134, v238\n\t"
    "s_lshl_b32 s71, %14, 14\n\t"
    "s_add_u32 s71, s71, 196608\n\t"
    "s_and_b32 s71, s71, 196608\n\t"
    "s_add_u32 s71, s71, s70\n\t"
    "buffer_load_dwordx4 a[160:163], v224, s[44:47], s71 offen offset:0 sc1\n\t"
    "s_add_u32 s66, s73, 0\n\t"
    "v_pk_mul_f32 v[0:1], v[0:1], v[130:131] op_sel_hi:[1,0]\n\t"
    "v_pk_mul_f32 v[2:3], v[2:3], v[130:131] op_sel_hi:[1,0]\n\t"
    "buffer_load_dwordx4 a[164:167], v224, s[44:47], s71 offen offset:1024 sc1\n\t"
    "v_pk_mul_f32 v[4:5], v[4:5], v[130:131] op_sel_hi:[1,0]\n\t"
    "v_pk_mul_f32 v[6:7], v[6:7], v[130:131] op_sel_hi:[1,0]\n\t"
    "v_pk_mul_f32 v[8:9], v[8:9], v[130:131] op_sel_hi:[1,0]\n\t"
    "buffer_load_dwordx4 a[168:171], v224, s[44:47], s71 offen offset:2048 sc1\n\t"
    "v_pk_mul_f32 v[10:11], v[10:11], v[130:131] op_sel_hi:[1,0]\n\t"
    "v_pk_mul_f32 v[12:13], v[12:13], v[130:131] op_sel_hi:[1,0]\n\t"
    "v_pk_mul_f32 v[14:15], v[14:15], v[130:131] op_sel_hi:[1,0]\n\t"
    "buffer_load_dwordx4 a[172:175], v224, s[44:47], s71 offen offset:3072 sc1\n\t"
    "v_pk_fma_f32 v[0:1], v[64:65], v[128:129], v[0:1] op_sel_hi:[1,0,1]\n\t"
    "v_pk_fma_f32 v[2:3], v[66:67], v[128:129], v[2:3] op_sel_hi:[1,0,1]\n\t"
    "v_pk_fma_f32 v[4:5], v[68:69], v[128:129], v[4:5] op_sel_hi:[1,0,1]\n\t"
    "s_add_u32 s71, s71, 0x1000\n\t"
    "buffer_load_dwordx4 a[176:179], v224, s[44:47], s71 offen offset:0 sc1\n\t"
    "v_pk_fma_f32 v[6:7], v[70:71], v[128:129], v[6:7] op_sel_hi:[1,0,1]\n\t"
    "v_pk_fma_f32 v[8:9], v[72:73], v[128:129], v[8:9] op_sel_hi:[1,0,1]\n\t"
    "v_pk_fma_f32 v[10:11], v[74:75], v[128:129], v[10:11] op_sel_hi:[1,0,1]\n\t"
    "buffer_load_dwordx4 a[180:183], v224, s[44:47], s71 offen offset:1024 sc1\n\t"
    "v_pk_fma_f32 v[12:13], v[76:77], v[128:129], v[12:13] op_sel_hi:[1,0,1]\n\t"
    "v_pk_fma_f32 v[14:15], v[78:79], v[128:129], v[14:15] op_sel_hi:[1,0,1]\n\t"
    "v_cvt_pk_bf16_f32 v0, v0, v1\n\t"
    "buffer_load_dwordx4 a[184:187], v224, s[44:47], s71 offen offset:2048 sc1\n\t"
    "v_cvt_pk_bf16_f32 v1, v2, v3\n\t"
    "v_cvt_pk_bf16_f32 v2, v4, v5\n\t"
    "v_cvt_pk_bf16_f32 v3, v6, v7\n\t"
    "buffer_load_dwordx4 a[188:191], v224, s[44:47], s71 offen offset:3072 sc1\n\t"
    "v_cvt_pk_bf16_f32 v8, v8, v9\n\t"
    "v_cvt_pk_bf16_f32 v9, v10, v11\n\t"
    "v_cvt_pk_bf16_f32 v10, v12, v13\n\t"
    "s_lshl_b32 s71, %14, 14\n\t"
    "s_add_u32 s71, s71, 262144\n\t"
    "s_and_b32 s71, s71, 196608\n\t"
    "s_add_u32 s71, s71, s70\n\t"
    "buffer_load_dwordx4 a[192:195], v224, s[44:47], s71 offen offset:0 sc1\n\t"
    "v_cvt_pk_bf16_f32 v11, v14, v15\n\t"
    "buffer_store_dwordx4 v[0:3], v224, s[48:51], s66 offen offset:0 sc1\n\t"
    "buffer_store_dwordx4 v[8:11], v224, s[48:51], s66 offen offset:1024 sc1\n\t"
    "v_pk_mul_f32 v[32:33], v[32:33], v[130:131] op_sel_hi:[1,0]\n\t"
    "v_pk_mul_f32 v[34:35], v[34:35], v[130:131] op_sel_hi:[1,0]\n\t"
    "buffer_load_dwordx4 a[196:199], v224, s[44:47], s71 offen offset:1024 sc1\n\t"
    "v_pk_mul_f32 v[36:37], v[36:37], v[130:131] op_sel_hi:[1,0]\n\t"
    "v_pk_mul_f32 v[38:39], v[38:39], v[130:131] op_sel_hi:[1,0]\n\t"
    "v_pk_mul_f32 v[40:41], v[40:41], v[130:131] op_sel_hi:[1,0]\n\t"
    "buffer_load_dwordx4 a[200:203], v224, s[44:47], s71 offen offset:2048 sc1\n\t"
    "v_pk_mul_f32 v[42:43], v[42:43], v[130:131] op_sel_hi:[1,0]\n\t"
    "v_pk_mul_f32 v[44:45], v[44:45], v[130:131] op_sel_hi:[1,0]\n\t"
    "v_pk_mul_f32 v[46:47], v[46:47], v[130:131] op_sel_hi:[1,0]\n\t"
    "buffer_load_dwordx4 a[204:207], v224, s[44:47], s71 offen offset:3072 sc1\n\t"
    "v_pk_fma_f32 v[32:33], v[96:97], v[128:129], v[32:33] op_sel_hi:[1,0,1]\n\t"
    "v_pk_fma_f32 v[34:35], v[98:99], v[128:129], v[34:35] op_sel_hi:[1,0,1]\n\t"
    "v_pk_fma_f32 v[36:37], v[100:101], v[128:129], v[36:37] op_sel_hi:[1,0,1]\n\t"
    "s_add_u32 s71, s71, 0x1000\n\t"
    "buffer_load_dwordx4 a[208:211], v224, s[44:47], s71 offen offset:0 sc1\n\t"
    "v_pk_fma_f32 v[38:39], v[102:103], v[128:129], v[38:39] op_sel_hi:[1,0,1]\n\t"
    "v_pk_fma_f32 v[40:41], v[104:105], v[128:129], v[40:41] op_sel_hi:[1,0,1]\n\t"
    "v_pk_fma_f32 v[42:43], v[106:107], v[128:129], v[42:43] op_sel_hi:[1,0,1]\n\t"
    "buffer_load_dwordx4 a[212:215], v224, s[44:47], s71 offen offset:1024 sc1\n\t"
    "v_pk_fma_f32 v[44:45], v[108:109], v[128:129], v[44:45] op_sel_hi:[1,0,1]\n\t"
    "v_pk_fma_f32 v[46:47], v[110:111], v[128:129], v[46:47] op_sel_hi:[1,0,1]\n\t"
    "v_cvt_pk_bf16_f32 v32, v32, v33\n\t"
    "buffer_load_dwordx4 a[216:219], v224, s[44:47], s71 offen offset:2048 sc1\n\t"
    "v_cvt_pk_bf16_f32 v33, v34, v35\n\t"
    "v_cvt_pk_bf16_f32 v34, v36, v37\n\t"
    "v_cvt_pk_bf16_f32 v35, v38, v39\n\t"
    "buffer_load_dwordx4 a[220:223], v224, s[44:47], s71 offen offset:3072 sc1\n\t"
    "v_cvt_pk_bf16_f32 v40, v40, v41\n\t"
    "v_cvt_pk_bf16_f32 v41, v42, v43\n\t"
    "v_cvt_pk_bf16_f32 v42, v44, v45\n\t"
    "s_lshl_b32 s71, %14, 14\n\t"
    "s_add_u32 s71, s71, 327680\n\t"
    "s_and_b32 s71, s71, 196608\n\t"
    "s_add_u32 s71, s71, s70\n\t"
    "buffer_load_dwordx4 a[224:227], v224, s[44:47], s71 offen offset:0 sc1\n\t"
    "v_cvt_pk_bf16_f32 v43, v46, v47\n\t"
    "buffer_store_dwordx4 v[32:35], v224, s[48:51], s66 offen offset:2048 sc1\n\t"
    "buffer_store_dwordx4 v[40:43], v224, s[48:51], s66 offen offset:3072 sc1\n\t"
    "s_add_u32 s66, s73, 262144\n\t"
    "v_pk_mul_f32 v[16:17], v[16:17], v[134:135] op_sel_hi:[1,0]\n\t"
    "buffer_load_dwordx4 a[228:231], v224, s[44:47], s71 offen offset:1024 sc1\n\t"
    "v_pk_mul_f32 v[18:19], v[18:19], v[134:135] op_sel_hi:[1,0]\n\t"
    "v_pk_mul_f32 v[20:21], v[20:21], v[134:135] op_sel_hi:[1,0]\n\t"
    "v_pk_mul_f32 v[22:23], v[22:23], v[134:135] op_sel_hi:[1,0]\n\t"
    "buffer_load_dwordx4 a[232:235], v224, s[44:47], s71 offen offset:2048 sc1\n\t"
    "v_pk_mul_f32 v[24:25], v[24:25], v[134:135] op_sel_hi:[1,0]\n\t"
    "v_pk_mul_f32 v[26:27], v[26:27], v[134:135] op_sel_hi:[1,0]\n\t"
    "v_pk_mul_f32 v[28:29], v[28:29], v[134:135] op_sel_hi:[1,0]\n\t"
    "buffer_load_dwordx4 a[236:239], v224, s[44:47], s71 offen offset:3072 sc1\n\t"
    "v_pk_mul_f32 v[30:31], v[30:31], v[134:135] op_sel_hi:[1,0]\n\t"
    "v_pk_fma_f32 v[16:17], v[80:81], v[132:133], v[16:17] op_sel_hi:[1,0,1]\n\t"
    "v_pk_fma_f32 v[18:19], v[82:83], v[132:133], v[18:19] op_sel_hi:[1,0,1]\n\t"
    "s_add_u32 s71, s71, 0x1000\n\t"
    "buffer_load_dwordx4 a[240:243], v224, s[44:47], s71 offen offset:0 sc1\n\t"
    "v_pk_fma_f32 v[20:21], v[84:85], v[132:133], v[20:21] op_sel_hi:[1,0,1]\n\t"
    "v_pk_fma_f32 v[22:23], v[86:87], v[132:133], v[22:23] op_sel_hi:[1,0,1]\n\t"
    "v_pk_fma_f32 v[24:25], v[88:89], v[132:133], v[24:25] op_sel_hi:[1,0,1]\n\t"
    "buffer_load_dwordx4 a[244:247], v224, s[44:47], s71 offen offset:1024 sc1\n\t"
    "v_pk_fma_f32 v[26:27], v[90:91], v[132:133], v[26:27] op_sel_hi:[1,0,1]\n\t"
    "v_pk_fma_f32 v[28:29], v[92:93], v[132:133], v[28:29] op_sel_hi:[1,0,1]\n\t"
    "v_pk_fma_f32 v[30:31], v[94:95], v[132:133], v[30:31] op_sel_hi:[1,0,1]\n\t"
    "buffer_load_dwordx4 a[248:251], v224, s[44:47], s71 offen offset:2048 sc1\n\t"
    "v_cvt_pk_bf16_f32 v16, v16, v17\n\t"
    "v_cvt_pk_bf16_f32 v17, v18, v19\n\t"
    "v_cvt_pk_bf16_f32 v18, v20, v21\n\t"
    "buffer_load_dwordx4 a[252:255], v224, s[44:47], s71 offen offset:3072 sc1\n\t"
    "v_cvt_pk_bf16_f32 v19, v22, v23\n\t"
    "v_cvt_pk_bf16_f32 v24, v24, v25\n\t"
    "v_cvt_pk_bf16_f32 v25, v26, v27\n\t"
    "v_cvt_pk_bf16_f32 v26, v28, v29\n\t"
    "v_cvt_pk_bf16_f32 v27, v30, v31\n\t"
    "buffer_store_dwordx4 v[16:19], v224, s[48:51], s66 offen offset:0 sc1\n\t"
    "buffer_store_dwordx4 v[24:27], v224, s[48:51], s66 offen offset:1024 sc1\n\t"
    "v_pk_mul_f32 v[48:49], v[48:49], v[134:135] op_sel_hi:[1,0]\n\t"
    "v_pk_mul_f32 v[50:51], v[50:51], v[134:135] op_sel_hi:[1,0]\n\t"
    "v_pk_mul_f32 v[52:53], v[52:53], v[134:135] op_sel_hi:[1,0]\n\t"
    "v_pk_mul_f32 v[54:55], v[54:55], v[134:135] op_sel_hi:[1,0]\n\t"
    "v_pk_mul_f32 v[56:57], v[56:57], v[134:135] op_sel_hi:[1,0]\n\t"
    "v_pk_mul_f32 v[58:59], v[58:59], v[134:135] op_sel_hi:[1,0]\n\t"
    "v_pk_mul_f32 v[60:61], v[60:61], v[134:135] op_sel_hi:[1,0]\n\t"
    "v_pk_mul_f32 v[62:63], v[62:63], v[134:135] op_sel_hi:[1,0]\n\t"
    "v_pk_fma_f32 v[48:49], v[112:113], v[132:133], v[48:49] op_sel_hi:[1,0,1]\n\t"
    "v_pk_fma_f32 v[50:51], v[114:115], v[132:133], v[50:51] op_sel_hi:[1,0,1]\n\t"
    "v_pk_fma_f32 v[52:53], v[116:117], v[132:133], v[52:53] op_sel_hi:[1,0,1]\n\t"
    "v_pk_fma_f32 v[54:55], v[118:119], v[132:133], v[54:55] op_sel_hi:[1,0,1]\n\t"
    "v_pk_fma_f32 v[56:57], v[120:121], v[132:133], v[56:57] op_sel_hi:[1,0,1]\n\t"
    "v_pk_fma_f32 v[58:59], v[122:123], v[132:133], v[58:59] op_sel_hi:[1,0,1]\n\t"
    "v_pk_fma_f32 v[60:61], v[124:125], v[132:133], v[60:61] op_sel_hi:[1,0,1]\n\t"
    "v_pk_fma_f32 v[62:63], v[126:127], v[132:133], v[62:63] op_sel_hi:[1,0,1]\n\t"
    "v_cvt_pk_bf16_f32 v48, v48, v49\n\t"
    "v_cvt_pk_bf16_f32 v49, v50, v51\n\t"
    "v_cvt_pk_bf16_f32 v50, v52, v53\n\t"
    "v_cvt_pk_bf16_f32 v51, v54, v55\n\t"
    "v_cvt_pk_bf16_f32 v56, v56, v57\n\t"
    "v_cvt_pk_bf16_f32 v57, v58, v59\n\t"
    "v_cvt_pk_bf16_f32 v58, v60, v61\n\t"
    "v_cvt_pk_bf16_f32 v59, v62, v63\n\t"
    "buffer_store_dwordx4 v[48:51], v224, s[48:51], s66 offen offset:2048 sc1\n\t"
    "buffer_store_dwordx4 v[56:59], v224, s[48:51], s66 offen offset:3072 sc1\n\t"
    "s_branch LEND_%=\n\t"
    "L7_%=:\n\t"
    "v_mfma_f32_32x32x16_bf16 v[64:79], a[192:195], a[128:131], v[128:143]\n\t"
    "ds_read_b128 v[160:163], v226 offset:0\n\t"
    "v_exp_f32 v28, v28\n\t"
    "v_exp_f32 v29, v29\n\t"
    "v_add_f32 v230, v230, v28\n\t"
    "v_mfma_f32_32x32x16_bf16 v[80:95], a[192:195], a[160:163], v[144:159]\n\t"
    "ds_read_b128 v[164:167], v226 offset:2048\n\t"
    "v_exp_f32 v30, v30\n\t"
    "v_exp_f32 v31, v31\n\t"
    "v_add_f32 v231, v231, v29\n\t"
    "v_mfma_f32_32x32x16_bf16 v[96:111], a[224:227], a[128:131], v[128:143]\n\t"
    "ds_read_b128 v[168:171], v226 offset:4096\n\t"
    "v_exp_f32 v32, v32\n\t"
    "v_exp_f32 v33, v33\n\t"
    "v_add_f32 v230, v230, v30\n\t"
    "v_mfma_f32_32x32x16_bf16 v[112:127], a[224:227], a[160:163], v[144:159]\n\t"
    "ds_read_b128 v[172:175], v226 offset:6144\n\t"
    "v_exp_f32 v34, v34\n\t"
    "v_exp_f32 v35, v35\n\t"
    "v_add_f32 v231, v231, v31\n\t"
    "v_mfma_f32_32x32x16_bf16 v[64:79], a[196:199], a[132:135], v[64:79]\n\t"
    "ds_read_b128 v[176:179], v226 offset:1024\n\t"
    "v_exp_f32 v36, v36\n\t"
    "v_exp_f32 v37, v37\n\t"
    "v_add_f32 v228, v228, v32\n\t"
    "v_mfma_f32_32x32x16_bf16 v[80:95], a[196:199], a[164:167], v[80:95]\n\t"
    "ds_read_b128 v[180:183], v226 offset:3072\n\t"
    "v_exp_f32 v38, v38\n\t"
    "v_exp_f32 v39, v39\n\t"
    "v_add_f32 v229, v229, v33\n\t"
    "v_mfma_f32_32x32x16_bf16 v[96:111], a[228:231], a[132:135], v[96:111]\n\t"
    "ds_read_b128 v[184:187], v226 offset:5120\n\t"
    "v_exp_f32 v48, v48\n\t"
    "v_exp_f32 v49, v49\n\t"
    "v_add_f32 v228, v228, v34\n\t"
    "v_mfma_f32_32x32x16_bf16 v[112:127], a[228:231], a[164:167], v[112:127]\n\t"
    "ds_read_b128 v[188:191], v226 offset:7168\n\t"
    "v_exp_f32 v50, v50\n\t"
    "v_exp_f32 v51, v51\n\t"
    "v_add_f32 v229, v229, v35\n\t"
    "v_mfma_f32_32x32x16_bf16 v[64:79], a[200:203], a[136:139], v[64:79]\n\t"
    "ds_read_b128 v[192:195], v226 offset:8192\n\t"
    "v_exp_f32 v52, v52\n\t"
    "v_exp_f32 v53, v53\n\t"
    "v_add_f32 v228, v228, v36\n\t"
    "v_mfma_f32_32x32x16_bf16 v[80:95], a[200:203], a[168:171], v[80:95]\n\t"
    "ds_read_b128 v[196:199], v226 offset:10240\n\t"
    "v_exp_f32 v54, v54\n\t"
    "v_exp_f32 v55, v55\n\t"
    "v_add_f32 v229, v229, v37\n\t"
    "v_mfma_f32_32x32x16_bf16 v[96:111], a[232:235], a[136:139], v[96:111]\n\t"
    "ds_read_b128 v[200:203], v226 offset:12288\n\t"
    "v_exp_f32 v40, v40\n\t"
    "v_exp_f32 v41, v41\n\t"
    "v_add_f32 v228, v228, v38\n\t"
    "v_mfma_f32_32x32x16_bf16 v[112:127], a[232:235], a[168:171], v[112:127]\n\t"
    "ds_read_b128 v[204:207], v226 offset:14336\n\t"
    "v_exp_f32 v42, v42\n\t"
    "v_exp_f32 v43, v43\n\t"
    "v_add_f32 v229, v229, v39\n\t"
    "v_mfma_f32_32x32x16_bf16 v[64:79], a[204:207], a[140:143], v[64:79]\n\t"
    "ds_read_b128 v[208:211], v226 offset:9216\n\t"
    "v_exp_f32 v44, v44\n\t"
    "v_exp_f32 v45, v45\n\t"
    "v_add_f32 v230, v230, v48\n\t"
    "v_mfma_f32_32x32x16_bf16 v[80:95], a[204:207], a[172:175], v[80:95]\n\t"
    "ds_read_b128 v[212:215], v226 offset:11264\n\t"
    "v_exp_f32 v46, v46\n\t"
    "v_exp_f32 v47, v47\n\t"
    "v_add_f32 v231, v231, v49\n\t"
    "v_mfma_f32_32x32x16_bf16 v[96:111], a[236:239], a[140:143], v[96:111]\n\t"
    "ds_read_b128 v[216:219], v226 offset:13312\n\t"
    "v_exp_f32 v56, v56\n\t"
    "v_exp_f32 v57, v57\n\t"
    "v_add_f32 v230, v230, v50\n\t"
    "v_mfma_f32_32x32x16_bf16 v[112:127], a[236:239], a[172:175], v[112:127]\n\t"
    "ds_read_b128 v[220:223], v226 offset:15360\n\t"
    "v_exp_f32 v58, v58\n\t"
    "v_exp_f32 v59, v59\n\t"
    "v_add_f32 v231, v231, v51\n\t"
    "v_mfma_f32_32x32x16_bf16 v[64:79], a[208:211], a[144:147], v[64:79]\n\t"
    "v_exp_f32 v60, v60\n\t"
    "v_exp_f32 v61, v61\n\t"
    "v_add_f32 v230, v230, v52\n\t"
    "v_add_f32 v231, v231, v53\n\t"
    "v_mfma_f32_32x32x16_bf16 v[80:95], a[208:211], a[176:179], v[80:95]\n\t"
    "v_exp_f32 v62, v62\n\t"
    "v_exp_f32 v63, v63\n\t"
    "v_add_f32 v230, v230, v54\n\t"
    "v_add_f32 v231, v231, v55\n\t"
    "v_mfma_f32_32x32x16_bf16 v[96:111], a[240:243], a[144:147], v[96:111]\n\t"
    "v_add_f32 v228, v228, v40\n\t"
    "v_add_f32 v229, v229, v41\n\t"
    "v_add_f32 v228, v228, v42\n\t"
    "v_add_f32 v229, v229, v43\n\t"
    "v_add_f32 v228, v228, v44\n\t"
    "v_add_f32 v229, v229, v45\n\t"
    "v_mfma_f32_32x32x16_bf16 v[112:127], a[240:243], a[176:179], v[112:127]\n\t"
    "v_add_f32 v228, v228, v46\n\t"
    "v_add_f32 v229, v229, v47\n\t"
    "v_add_f32 v230, v230, v56\n\t"
    "v_add_f32 v231, v231, v57\n\t"
    "v_add_f32 v230, v230, v58\n\t"
    "v_add_f32 v231, v231, v59\n\t"
    "v_mfma_f32_32x32x16_bf16 v[64:79], a[212:215], a[148:151], v[64:79]\n\t"
    "s_and_b32 s66, s57, 0xc000\n\t"
    "v_add_f32 v230, v230, v60\n\t"
    "v_add_f32 v231, v231, v61\n\t"
    "v_add_f32 v230, v230, v62\n\t"
    "v_add_f32 v231, v231, v63\n\t"
    "v_cvt_pk_bf16_f32 v0, v0, v1\n\t"
    "v_mfma_f32_32x32x16_bf16 v[80:95], a[212:215], a[180:183], v[80:95]\n\t"
    "v_add_u32 v225, s66, v224\n\t"
    "v_cvt_pk_bf16_f32 v1, v2, v3\n\t"
    "v_cvt_pk_bf16_f32 v2, v4, v5\n\t"
    "v_cvt_pk_bf16_f32 v3, v6, v7\n\t"
    "v_cvt_pk_bf16_f32 v16, v16, v17\n\t"
    "v_cvt_pk_bf16_f32 v17, v18, v19\n\t"
    "v_mfma_f32_32x32x16_bf16 v[96:111], a[244:247], a[148:151], v[96:111]\n\t"
    "v_cvt_pk_bf16_f32 v18, v20, v21\n\t"
    "v_cvt_pk_bf16_f32 v19, v22, v23\n\t"
    "v_cvt_pk_bf16_f32 v8, v8, v9\n\t"
    "v_cvt_pk_bf16_f32 v9, v10, v11\n\t"
    "v_cvt_pk_bf16_f32 v10, v12, v13\n\t"
    "v_cvt_pk_bf16_f32 v11, v14, v15\n\t"
    "v_mfma_f32_32x32x16_bf16 v[112:127], a[244:247], a[180:183], v[112:127]\n\t"
    "v_cvt_pk_bf16_f32 v24, v24, v25\n\t"
    "v_cvt_pk_bf16_f32 v25, v26, v27\n\t"
    "v_cvt_pk_bf16_f32 v26, v28, v29\n\t"
    "v_cvt_pk_bf16_f32 v27, v30, v31\n\t"
    "v_cvt_pk_bf16_f32 v32, v32, v33\n\t"
    "v_cvt_pk_bf16_f32 v33, v34, v35\n\t"
    "v_mfma_f32_32x32x16_bf16 v[64:79], a[216:219], a[152:155], v[64:79]\n\t"
    "v_cvt_pk_bf16_f32 v34, v36, v37\n\t"
    "v_cvt_pk_bf16_f32 v35, v38, v39\n\t"
    "v_cvt_pk_bf16_f32 v48, v48, v49\n\t"
    "v_cvt_pk_bf16_f32 v49, v50, v51\n\t"
    "v_cvt_pk_bf16_f32 v50, v52, v53\n\t"
    "v_cvt_pk_bf16_f32 v51, v54, v55\n\t"
    "v_mfma_f32_32x32x16_bf16 v[80:95], a[216:219], a[184:187], v[80:95]\n\t"
    "v_cvt_pk_bf16_f32 v40, v40, v41\n\t"
    "v_cvt_pk_bf16_f32 v41, v42, v43\n\t"
    "v_cvt_pk_bf16_f32 v42, v44, v45\n\t"
    "v_cvt_pk_bf16_f32 v43, v46, v47\n\t"
    "v_cvt_pk_bf16_f32 v56, v56, v57\n\t"
    "v_cvt_pk_bf16_f32 v57, v58, v59\n\t"
    "v_mfma_f32_32x32x16_bf16 v[96:111], a[248:251], a[152:155], v[96:111]\n\t"
    "v_cvt_pk_bf16_f32 v58, v60, v61\n\t"
    "v_cvt_pk_bf16_f32 v59, v62, v63\n\t"
    "v_mfma_f32_32x32x16_bf16 v[112:127], a[248:251], a[184:187], v[112:127]\n\t"
    "v_mfma_f32_32x32x16_bf16 v[64:79], a[220:223], a[156:159], v[64:79]\n\t"
    "v_mfma_f32_32x32x16_bf16 v[80:95], a[220:223], a[188:191], v[80:95]\n\t"
    "v_mfma_f32_32x32x16_bf16 v[96:111], a[252:255], a[156:159], v[96:111]\n\t"
    "v_mfma_f32_32x32x16_bf16 v[112:127], a[252:255], a[188:191], v[112:127]\n\t"
    "s_waitcnt vmcnt(4) lgkmcnt(0)\n\t"
    "s_barrier\n\t"
    "s_cmp_eq_u32 s52, s64\n\t"
    "s_cbranch_scc0 Lrx_114_%=\n\t"
    "v_add_u32 v240, s86, v224\n\t"
    "s_and_b32 s66, s57, 0xffff\n\t"
    "v_add_u32 v241, s66, v227\n\t"
    "ds_read_b64 v[250:251], v241 offset:0\n\t"
    "ds_read_b128 a[128:131], v240 offset:0\n\t"
    "ds_read_b128 a[132:135], v240 offset:1024\n\t"
    "ds_read_b128 a[136:139], v240 offset:2048\n\t"
    "ds_read_b128 a[140:143], v240 offset:3072\n\t"
    "ds_read_b128 a[144:147], v240 offset:4096\n\t"
    "ds_read_b128 a[148:151], v240 offset:5120\n\t"
    "ds_read_b128 a[152:155], v240 offset:6144\n\t"
    "ds_read_b128 a[156:159], v240 offset:7168\n\t"
    "ds_read_b128 a[160:163], v240 offset:8192\n\t"
    "ds_read_b128 a[164:167], v240 offset:9216\n\t"
    "ds_read_b128 a[168:171], v240 offset:10240\n\t"
    "ds_read_b128 a[172:175], v240 offset:11264\n\t"
    "ds_read_b128 a[176:179], v240 offset:12288\n\t"
    "ds_read_b128 a[180:183], v240 offset:13312\n\t"
    "ds_read_b128 a[184:187], v240 offset:14336\n\t"
    "ds_read_b128 a[188:191], v240 offset:15360\n\t"
    "s_waitcnt lgkmcnt(15)\n\t"
    "s_sub_u32 s66, s56, s63\n\t"
    "s_cmp_ge_u32 s66, 0x4000\n\t"
    "s_cselect_b32 s66, -1, 0\n\t"
    "v_or_b32 v242, s66, v239\n\t"
    "v_cmp_le_i32_e64 s[70:71], 0, v242\n\t"
    "v_cmp_le_i32_e64 s[72:73], 1, v242\n\t"
    "v_cmp_le_i32_e64 s[74:75], 2, v242\n\t"
    "v_cmp_le_i32_e64 s[76:77], 3, v242\n\t"
    "v_cmp_le_i32_e64 s[78:79], 8, v242\n\t"
    "v_cmp_le_i32_e64 s[80:81], 9, v242\n\t"
    "v_cmp_le_i32_e64 s[82:83], 10, v242\n\t"
    "v_cmp_le_i32_e64 s[84:85], 11, v242\n\t"
    "v_pk_mov_b32 v[96:97], v[252:253], v[252:253] op_sel:[0,0]\n\t"
    "v_pk_mov_b32 v[98:99], v[252:253], v[252:253] op_sel:[0,0]\n\t"
    "v_pk_mov_b32 v[100:101], v[252:253], v[252:253] op_sel:[0,0]\n\t"
    "v_pk_mov_b32 v[102:103], v[252:253], v[252:253] op_sel:[0,0]\n\t"
    "v_cndmask_b32_e64 v64, v252, v64, s[70:71]\n\t"
    "v_cndmask_b32_e64 v112, v252, v112, s[70:71]\n\t"
    "v_cndmask_b32_e64 v65, v252, v65, s[72:73]\n\t"
    "v_cndmask_b32_e64 v113, v252, v113, s[72:73]\n\t"
    "v_cndmask_b32_e64 v66, v252, v66, s[74:75]\n\t"
    "v_cndmask_b32_e64 v114, v252, v114, s[74:75]\n\t"
    "v_cndmask_b32_e64 v67, v252, v67, s[76:77]\n\t"
    "v_cndmask_b32_e64 v115, v252, v115, s[76:77]\n\t"
    "v_cndmask_b32_e64 v68, v252, v68, s[78:79]\n\t"
    "v_cndmask_b32_e64 v116, v252, v116, s[78:79]\n\t"
    "v_cndmask_b32_e64 v69, v252, v69, s[80:81]\n\t"
    "v_cndmask_b32_e64 v117, v252, v117, s[80:81]\n\t"
    "v_cndmask_b32_e64 v70, v252, v70, s[82:83]\n\t"
    "v_cndmask_b32_e64 v118, v252, v118, s[82:83]\n\t"
    "v_cndmask_b32_e64 v71, v252, v71, s[84:85]\n\t"
    "v_cndmask_b32_e64 v119, v252, v119, s[84:85]\n\t"
    "v_cmp_le_i32_e64 s[70:71], 16, v242\n\t"
    "v_cmp_le_i32_e64 s[72:73], 17, v242\n\t"
    "v_cmp_le_i32_e64 s[74:75], 18, v242\n\t"
    "v_cmp_le_i32_e64 s[76:77], 19, v242\n\t"
    "v_cmp_le_i32_e64 s[78:79], 24, v242\n\t"
    "v_cmp_le_i32_e64 s[80:81], 25, v242\n\t"
    "v_cmp_le_i32_e64 s[82:83], 26, v242\n\t"
    "v_cmp_le_i32_e64 s[84:85], 27, v242\n\t"
    "v_pk_mov_b32 v[104:105], v[252:253], v[252:253] op_sel:[0,0]\n\t"
    "v_pk_mov_b32 v[106:107], v[252:253], v[252:253] op_sel:[0,0]\n\t"
    "v_pk_mov_b32 v[108:109], v[252:253], v[252:253] op_sel:[0,0]\n\t"
    "v_pk_mov_b32 v[110:111], v[252:253], v[252:253] op_sel:[0,0]\n\t"
    "v_cndmask_b32_e64 v72, v252, v72, s[70:71]\n\t"
    "v_cndmask_b32_e64 v120, v252, v120, s[70:71]\n\t"
    "v_cndmask_b32_e64 v73, v252, v73, s[72:73]\n\t"
    "v_cndmask_b32_e64 v121, v252, v121, s[72:73]\n\t"
    "v_cndmask_b32_e64 v74, v252, v74, s[74:75]\n\t"
    "v_cndmask_b32_e64 v122, v252, v122, s[74:75]\n\t"
    "v_cndmask_b32_e64 v75, v252, v75, s[76:77]\n\t"
    "v_cndmask_b32_e64 v123, v252, v123, s[76:77]\n\t"
    "v_cndmask_b32_e64 v76, v252, v76, s[78:79]\n\t"
    "v_cndmask_b32_e64 v124, v252, v124, s[78:79]\n\t"
    "v_cndmask_b32_e64 v77, v252, v77, s[80:81]\n\t"
    "v_cndmask_b32_e64 v125, v252, v125, s[80:81]\n\t"
    "v_cndmask_b32_e64 v78, v252, v78, s[82:83]\n\t"
    "v_cndmask_b32_e64 v126, v252, v126, s[82:83]\n\t"
    "v_cndmask_b32_e64 v79, v252, v79, s[84:85]\n\t"
    "v_cndmask_b32_e64 v127, v252, v127, s[84:85]\n\t"
    "s_cbranch_scc0 Lfull_116_%=\n\t"
    "v_pk_mov_b32 v[80:81], v[252:253], v[252:253] op_sel:[0,0]\n\t"
    "v_pk_mov_b32 v[82:83], v[252:253], v[252:253] op_sel:[0,0]\n\t"
    "v_pk_mov_b32 v[84:85], v[252:253], v[252:253] op_sel:[0,0]\n\t"
    "v_pk_mov_b32 v[86:87], v[252:253], v[252:253] op_sel:[0,0]\n\t"
    "v_pk_mov_b32 v[88:89], v[252:253], v[252:253] op_sel:[0,0]\n\t"
    "v_pk_mov_b32 v[90:91], v[252:253], v[252:253] op_sel:[0,0]\n\t"
    "v_pk_mov_b32 v[92:93], v[252:253], v[252:253] op_sel:[0,0]\n\t"
    "v_pk_mov_b32 v[94:95], v[252:253], v[252:253] op_sel:[0,0]\n\t"
    "Lfull_116_%=:\n\t"
    "s_branch Lin_115_%=\n\t"
    "Lrx_114_%=:\n\t"
    "s_add_u32 s70, s88, 0\n\t"
    "buffer_store_dwordx4 v[244:247], v224, s[48:51], s70 offen offset:0 sc1\n\t"
    "buffer_store_dwordx4 v[244:247], v224, s[48:51], s70 offen offset:1024 sc1\n\t"
    "buffer_store_dwordx4 v[244:247], v224, s[48:51], s70 offen offset:2048 sc1\n\t"
    "buffer_store_dwordx4 v[244:247], v224, s[48:51], s70 offen offset:3072 sc1\n\t"
    "s_add_u32 s70, s88, 4096\n\t"
    "buffer_store_dwordx4 v[244:247], v224, s[48:51], s70 offen offset:0 sc1\n\t"
    "buffer_store_dwordx4 v[244:247], v224, s[48:51], s70 offen offset:1024 sc1\n\t"
    "buffer_store_dwordx4 v[244:247], v224, s[48:51], s70 offen offset:2048 sc1\n\t"
    "buffer_store_dwordx4 v[244:247], v224, s[48:51], s70 offen offset:3072 sc1\n\t"
    "s_add_u32 s70, s88, 262144\n\t"
    "buffer_store_dwordx4 v[244:247], v224, s[48:51], s70 offen offset:0 sc1\n\t"
    "buffer_store_dwordx4 v[244:247], v224, s[48:51], s70 offen offset:1024 sc1\n\t"
    "buffer_store_dwordx4 v[244:247], v224, s[48:51], s70 offen offset:2048 sc1\n\t"
    "buffer_store_dwordx4 v[244:247], v224, s[48:51], s70 offen offset:3072 sc1\n\t"
    "s_add_u32 s70, s88, 266240\n\t"
    "buffer_store_dwordx4 v[244:247], v224, s[48:51], s70 offen offset:0 sc1\n\t"
    "buffer_store_dwordx4 v[244:247], v224, s[48:51], s70 offen offset:1024 sc1\n\t"
    "buffer_store_dwordx4 v[244:247], v224, s[48:51], s70 offen offset:2048 sc1\n\t"
    "buffer_store_dwordx4 v[244:247], v224, s[48:51], s70 offen offset:3072 sc1\n\t"
    "Lin_115_%=:\n\t"
    "v_mfma_f32_32x32x16_bf16 a[0:15], v[160:163], v[0:3], a[0:15]\n\t"
    "ds_read_b128 a[192:195], v225 offset:0\n\t"
    "ds_read_b128 a[196:199], v225 offset:1024\n\t"
    "ds_read_b128 a[200:203], v225 offset:2048\n\t"
    "ds_read_b128 a[204:207], v225 offset:3072\n\t"
    "s_and_b32 s69, s56, 0xc000\n\t"
    "v_add_u32 v226, s69, v227\n\t"
    "v_mfma_f32_32x32x16_bf16 a[16:31], v[160:163], v[16:19], a[16:31]\n\t"
    "v_max3_f32 v234, v64, v65, v66\n\t"
    "v_max3_f32 v235, v80, v81, v82\n\t"
    "v_max3_f32 v236, v96, v97, v98\n\t"
    "v_max3_f32 v237, v112, v113, v114\n\t"
    "ds_read_b128 a[208:211], v225 offset:4096\n\t"
    "v_mfma_f32_32x32x16_bf16 a[32:47], v[164:167], v[0:3], a[32:47]\n\t"
    "v_max3_f32 v234, v234, v67, v68\n\t"
    "v_max3_f32 v235, v235, v83, v84\n\t"
    "v_max3_f32 v236, v236, v99, v100\n\t"
    "v_max3_f32 v237, v237, v115, v116\n\t"
    "ds_read_b128 a[212:215], v225 offset:5120\n\t"
    "v_mfma_f32_32x32x16_bf16 a[48:63], v[164:167], v[16:19], a[48:63]\n\t"
    "v_max3_f32 v234, v234, v69, v70\n\t"
    "v_max3_f32 v235, v235, v85, v86\n\t"
    "v_max3_f32 v236, v236, v101, v102\n\t"
    "v_max3_f32 v237, v237, v117, v118\n\t"
    "ds_read_b128 a[216:219], v225 offset:6144\n\t"
    "v_mfma_f32_32x32x16_bf16 a[64:79], v[168:171], v[0:3], a[64:79]\n\t"
    "v_max3_f32 v234, v234, v71, v72\n\t"
    "v_max3_f32 v235, v235, v87, v88\n\t"
    "v_max3_f32 v236, v236, v103, v104\n\t"
    "v_max3_f32 v237, v237, v119, v120\n\t"
    "ds_read_b128 a[220:223], v225 offset:7168\n\t"
    "v_mfma_f32_32x32x16_bf16 a[80:95], v[168:171], v[16:19], a[80:95]\n\t"
    "v_max3_f32 v234, v234, v73, v74\n\t"
    "v_max3_f32 v235, v235, v89, v90\n\t"
    "v_max3_f32 v236, v236, v105, v106\n\t"
    "v_max3_f32 v237, v237, v121, v122\n\t"
    "ds_read_b128 a[224:227], v225 offset:8192\n\t"
    "v_mfma_f32_32x32x16_bf16 a[96:111], v[172:175], v[0:3], a[96:111]\n\t"
    "v_max3_f32 v234, v234, v75, v76\n\t"
    "v_max3_f32 v235, v235, v91, v92\n\t"
    "v_max3_f32 v236, v236, v107, v108\n\t"
    "v_max3_f32 v237, v237, v123, v124\n\t"
    "ds_read_b128 a[228:231], v225 offset:9216\n\t"
    "v_mfma_f32_32x32x16_bf16 a[112:127], v[172:175], v[16:19], a[112:127]\n\t"
    "v_max3_f32 v234, v234, v77, v78\n\t"
    "v_max3_f32 v235, v235, v93, v94\n\t"
    "v_max3_f32 v236, v236, v109, v110\n\t"
    "v_max3_f32 v237, v237, v125, v126\n\t"
    "ds_read_b128 a[232:235], v225 offset:10240\n\t"
    "v_mfma_f32_32x32x16_bf16 a[0:15], v[176:179], v[8:11], a[0:15]\n\t"
    "v_max_f32 v234, v234, v79\n\t"
    "v_max_f32 v235, v235, v95\n\t"
    "v_max_f32 v236, v236, v111\n\t"
    "v_max_f32 v237, v237, v127\n\t"
    "ds_read_b128 a[236:239], v225 offset:11264\n\t"
    "v_mfma_f32_32x32x16_bf16 a[16:31], v[176:179], v[24:27], a[16:31]\n\t"
    "v_max3_f32 v238, v234, v235, v236\n\t"
    "v_max_f32 v238, v238, v237\n\t"
    "v_cmp_lt_f32 vcc, s60, v238\n\t"
    "s_cbranch_vccnz Lst_117_%=\n\t"
    "Lbk_118_%=:\n\t"
    "s_and_b32 s67, s57, 0xffff\n\t"
    "s_or_b32 m0, s67, 0x10000\n\t"
    "v_mfma_f32_32x32x16_bf16 a[32:47], v[180:183], v[8:11], a[32:47]\n\t"
    "ds_read_b128 a[240:243], v225 offset:12288\n\t"
    "buffer_load_dwordx4 v224, s[40:43], s57 offen offset:0 lds\n\t"
    "v_exp_f32 v64, v64\n\t"
    "v_mfma_f32_32x32x16_bf16 a[48:63], v[180:183], v[24:27], a[48:63]\n\t"
    "ds_read_b128 a[244:247], v225 offset:13312\n\t"
    "v_exp_f32 v65, v65\n\t"
    "v_exp_f32 v66, v66\n\t"
    "v_add_f32 v228, v228, v64\n\t"
    "v_mfma_f32_32x32x16_bf16 a[64:79], v[184:187], v[8:11], a[64:79]\n\t"
    "ds_read_b128 a[248:251], v225 offset:14336\n\t"
    "v_exp_f32 v67, v67\n\t"
    "v_exp_f32 v68, v68\n\t"
    "v_add_f32 v229, v229, v65\n\t"
    "v_mfma_f32_32x32x16_bf16 a[80:95], v[184:187], v[24:27], a[80:95]\n\t"
    "ds_read_b128 a[252:255], v225 offset:15360\n\t"
    "buffer_load_dwordx4 v224, s[40:43], s57 offen offset:1024 lds\n\t"
    "v_exp_f32 v69, v69\n\t"
    "v_add_f32 v228, v228, v66\n\t"
    "v_mfma_f32_32x32x16_bf16 a[96:111], v[188:191], v[8:11], a[96:111]\n\t"
    "v_exp_f32 v70, v70\n\t"
    "v_exp_f32 v71, v71\n\t"
    "v_add_f32 v229, v229, v67\n\t"
    "v_add_f32 v228, v228, v68\n\t"
    "v_mfma_f32_32x32x16_bf16 a[112:127], v[188:191], v[24:27], a[112:127]\n\t"
    "v_exp_f32 v80, v80\n\t"
    "v_exp_f32 v81, v81\n\t"
    "v_add_f32 v229, v229, v69\n\t"
    "v_add_f32 v228, v228, v70\n\t"
    "v_mfma_f32_32x32x16_bf16 a[0:15], v[192:195], v[32:35], a[0:15]\n\t"
    "buffer_load_dwordx4 v224, s[40:43], s57 offen offset:2048 lds\n\t"
    "v_exp_f32 v82, v82\n\t"
    "v_exp_f32 v83, v83\n\t"
    "v_mfma_f32_32x32x16_bf16 a[16:31], v[192:195], v[48:51], a[16:31]\n\t"
    "v_exp_f32 v84, v84\n\t"
    "v_exp_f32 v85, v85\n\t"
    "v_add_f32 v229, v229, v71\n\t"
    "v_add_f32 v230, v230, v80\n\t"
    "v_mfma_f32_32x32x16_bf16 a[32:47], v[196:199], v[32:35], a[32:47]\n\t"
    "v_exp_f32 v86, v86\n\t"
    "v_exp_f32 v87, v87\n\t"
    "v_add_f32 v231, v231, v81\n\t"
    "v_add_f32 v230, v230, v82\n\t"
    "v_mfma_f32_32x32x16_bf16 a[48:63], v[196:199], v[48:51], a[48:63]\n\t"
    "buffer_load_dwordx4 v224, s[40:43], s57 offen offset:3072 lds\n\t"
    "v_exp_f32 v72, v72\n\t"
    "v_exp_f32 v73, v73\n\t"
    "v_mfma_f32_32x32x16_bf16 a[64:79], v[200:203], v[32:35], a[64:79]\n\t"
    "v_exp_f32 v74, v74\n\t"
    "v_exp_f32 v75, v75\n\t"
    "v_add_f32 v231, v231, v83\n\t"
    "v_add_f32 v230, v230, v84\n\t"
    "v_mfma_f32_32x32x16_bf16 a[80:95], v[200:203], v[48:51], a[80:95]\n\t"
    "s_add_u32 m0, m0, 0x1000\n\t"
    "s_add_u32 s68, s57, 0x1000\n\t"
    "v_exp_f32 v76, v76\n\t"
    "v_exp_f32 v77, v77\n\t"
    "v_mfma_f32_32x32x16_bf16 a[96:111], v[204:207], v[32:35], a[96:111]\n\t"
    "buffer_load_dwordx4 v224, s[40:43], s68 offen offset:0 lds\n\t"
    "v_exp_f32 v78, v78\n\t"
    "v_exp_f32 v79, v79\n\t"
    "v_mfma_f32_32x32x16_bf16 a[112:127], v[204:207], v[48:51], a[112:127]\n\t"
    "v_exp_f32 v88, v88\n\t"
    "v_exp_f32 v89, v89\n\t"
    "v_add_f32 v231, v231, v85\n\t"
    "v_add_f32 v230, v230, v86\n\t"
    "v_mfma_f32_32x32x16_bf16 a[0:15], v[208:211], v[40:43], a[0:15]\n\t"
    "v_exp_f32 v90, v90\n\t"
    "v_exp_f32 v91, v91\n\t"
    "v_add_f32 v231, v231, v87\n\t"
    "v_add_f32 v228, v228, v72\n\t"
    "v_mfma_f32_32x32x16_bf16 a[16:31], v[208:211], v[56:59], a[16:31]\n\t"
    "buffer_load_dwordx4 v224, s[40:43], s68 offen offset:1024 lds\n\t"
    "v_add_f32 v229, v229, v73\n\t"
    "v_add_f32 v228, v228, v74\n\t"
    "v_add_f32 v229, v229, v75\n\t"
    "v_add_f32 v228, v228, v76\n\t"
    "v_mfma_f32_32x32x16_bf16 a[32:47], v[212:215], v[40:43], a[32:47]\n\t"
    "v_add_f32 v229, v229, v77\n\t"
    "v_add_f32 v228, v228, v78\n\t"
    "v_add_f32 v229, v229, v79\n\t"
    "v_add_f32 v230, v230, v88\n\t"
    "v_add_f32 v231, v231, v89\n\t"
    "v_add_f32 v230, v230, v90\n\t"
    "v_mfma_f32_32x32x16_bf16 a[48:63], v[212:215], v[56:59], a[48:63]\n\t"
    "v_add_f32 v231, v231, v91\n\t"
    "v_mfma_f32_32x32x16_bf16 a[64:79], v[216:219], v[40:43], a[64:79]\n\t"
    "buffer_load_dwordx4 v224, s[40:43], s68 offen offset:2048 lds\n\t"
    "v_mfma_f32_32x32x16_bf16 a[80:95], v[216:219], v[56:59], a[80:95]\n\t"
    "v_mfma_f32_32x32x16_bf16 a[96:111], v[220:223], v[40:43], a[96:111]\n\t"
    "v_mfma_f32_32x32x16_bf16 a[112:127], v[220:223], v[56:59], a[112:127]\n\t"
    "buffer_load_dwordx4 v224, s[40:43], s68 offen offset:3072 lds\n\t"
    "s_mov_b32 s56, s57\n\t"
    "s_mov_b32 s57, s58\n\t"
    "s_add_u32 s58, s58, 0x8000\n\t"
    "s_cmp_lg_u32 s62, 0\n\t"
    "s_cbranch_scc1 Lst_119_%=\n\t"
    "Lbk_120_%=:\n\t"
    "s_cmp_eq_u32 s52, s64\n\t"
    "s_cbranch_scc0 Lin_121_%=\n\t"
    "v_sub_f32 v242, 0, v250\n\t"
    "v_sub_f32 v243, 0, v251\n\t"
    "v_mov_b32 v232, v250\n\t"
    "v_mov_b32 v233, v251\n\t"
    "v_pk_mov_b32 v[128:129], v[242:243], v[242:243] op_sel:[0,0]\n\t"
    "v_pk_mov_b32 v[130:131], v[242:243], v[242:243] op_sel:[0,0]\n\t"
    "v_pk_mov_b32 v[132:133], v[242:243], v[242:243] op_sel:[0,0]\n\t"
    "v_pk_mov_b32 v[134:135], v[242:243], v[242:243] op_sel:[0,0]\n\t"
    "v_pk_mov_b32 v[136:137], v[242:243], v[242:243] op_sel:[0,0]\n\t"
    "v_pk_mov_b32 v[138:139], v[242:243], v[242:243] op_sel:[0,0]\n\t"
    "v_pk_mov_b32 v[140:141], v[242:243], v[242:243] op_sel:[0,0]\n\t"
    "v_pk_mov_b32 v[142:143], v[242:243], v[242:243] op_sel:[0,0]\n\t"
    "v_pk_mov_b32 v[144:145], v[242:243], v[242:243] op_sel:[1,1]\n\t"
    "v_pk_mov_b32 v[146:147], v[242:243], v[242:243] op_sel:[1,1]\n\t"
    "v_pk_mov_b32 v[148:149], v[242:243], v[242:243] op_sel:[1,1]\n\t"
    "v_pk_mov_b32 v[150:151], v[242:243], v[242:243] op_sel:[1,1]\n\t"
    "v_pk_mov_b32 v[152:153], v[242:243], v[242:243] op_sel:[1,1]\n\t"
    "v_pk_mov_b32 v[154:155], v[242:243], v[242:243] op_sel:[1,1]\n\t"
    "v_pk_mov_b32 v[156:157], v[242:243], v[242:243] op_sel:[1,1]\n\t"
    "v_pk_mov_b32 v[158:159], v[242:243], v[242:243] op_sel:[1,1]\n\t"
    "s_mov_b32 s93, 2\n\t"
    "s_mov_b32 s63, s92\n\t"
    "Lin_121_%=:\n\t"
    "s_waitcnt vmcnt(8) lgkmcnt(0)\n\t"
    "s_barrier\n\t"
    "s_add_u32 s52, s52, 1\n\t"
    "v_mfma_f32_32x32x16_bf16 v[0:15], a[192:195], a[128:131], v[128:143]\n\t"
    "s_cmp_lt_u32 s94, 16\n\t"
    "s_cbranch_scc0 Lpb_123_%=\n\t"
    "v_mov_b32 v241, %14\n\t"
    "s_mov_b64 exec, 1\n\t"
    "global_store_dword v241, v241, %18 sc1\n\t"
    "s_mov_b64 exec, -1\n\t"
    "s_nop 1\n\t"
    "Lpb_123_%=:\n\t"
    "ds_read_b128 v[160:163], v226 offset:0\n\t"
    "v_mfma_f32_32x32x16_bf16 v[16:31], a[192:195], a[160:163], v[144:159]\n\t"
    "s_getpc_b64 s[66:67]\n\t"
    "LPT_122_%=:\n\t"
    "s_add_u32 s66, s66, LTAIL_BEG-LPT_122_%=\n\t"
    "s_addc_u32 s67, s67, 0\n\t"
    "s_and_b32 s69, %10, 7\n\t"
    "s_lshl_b32 s69, s69, 2\n\t"
    "s_add_u32 s69, s69, s89\n\t"
    "s_lshl_b32 s69, s69, 7\n\t"
    "v_lshl_add_u32 v241, v224, 8, s69\n\t"
    "v_min_u32 v241, LTAIL_END-LTAIL_BEG-4+14080, v241\n\t"
    "global_load_dword v253, v241, s[66:67]\n\t"
    "ds_read_b128 v[164:167], v226 offset:2048\n\t"
    "v_mfma_f32_32x32x16_bf16 v[32:47], a[224:227], a[128:131], v[128:143]\n\t"
    "v_and_b32 v255, 48, v224\n\t"
    "v_bfe_u32 v243, v224, 6, 2\n\t"
    "v_lshl_add_u32 v255, v243, 10, v255\n\t"
    "v_lshrrev_b32 v243, 8, v224\n\t"
    "v_lshl_add_u32 v255, v243, 6, v255\n\t"
    "ds_read_b128 v[168:171], v226 offset:4096\n\t"
    "v_mfma_f32_32x32x16_bf16 v[48:63], a[224:227], a[160:163], v[144:159]\n\t"
    "s_mov_b64 s[44:45], %13\n\t"
    "s_mov_b32 s46, 0x314000\n\t"
    "s_lshl_b32 s68, s89, 12\n\t"
    "ds_read_b128 v[172:175], v226 offset:6144\n\t"
    "v_exp_f32 v92, v92\n\t"
    "v_mfma_f32_32x32x16_bf16 v[0:15], a[196:199], a[132:135], v[0:15]\n\t"
    "s_lshl_b32 s69, %14, 18\n\t"
    "s_add_u32 s69, s69, 2097152\n\t"
    "s_and_b32 s69, s69, 3145728\n\t"
    "s_add_u32 m0, s68, 0\n\t"
    "s_add_u32 s67, s69, 0\n\t"
    "buffer_load_dwordx4 v255, s[44:47], s67 offen offset:0 lds\n\t"
    "ds_read_b128 v[176:179], v226 offset:1024\n\t"
    "v_mfma_f32_32x32x16_bf16 v[16:31], a[196:199], a[164:167], v[16:31]\n\t"
    "ds_read_b128 v[180:183], v226 offset:3072\n\t"
    "v_exp_f32 v93, v93\n\t"
    "v_exp_f32 v94, v94\n\t"
    "v_add_f32 v230, v230, v92\n\t"
    "v_mfma_f32_32x32x16_bf16 v[32:47], a[228:231], a[132:135], v[32:47]\n\t"
    "ds_read_b128 v[184:187], v226 offset:5120\n\t"
    "v_exp_f32 v95, v95\n\t"
    "v_exp_f32 v96, v96\n\t"
    "v_add_f32 v231, v231, v93\n\t"
    "v_mfma_f32_32x32x16_bf16 v[48:63], a[228:231], a[164:167], v[48:63]\n\t"
    "s_add_u32 m0, s68, 1024\n\t"
    "s_add_u32 s67, s69, 4096\n\t"
    "buffer_load_dwordx4 v255, s[44:47], s67 offen offset:0 lds\n\t"
    "ds_read_b128 v[188:191], v226 offset:7168\n\t"
    "v_add_f32 v230, v230, v94\n\t"
    "v_mfma_f32_32x32x16_bf16 v[0:15], a[200:203], a[136:139], v[0:15]\n\t"
    "ds_read_b128 v[192:195], v226 offset:8192\n\t"
    "v_exp_f32 v97, v97\n\t"
    "v_exp_f32 v98, v98\n\t"
    "v_add_f32 v231, v231, v95\n\t"
    "v_mfma_f32_32x32x16_bf16 v[16:31], a[200:203], a[168:171], v[16:31]\n\t"
    "ds_read_b128 v[196:199], v226 offset:10240\n\t"
    "v_exp_f32 v99, v99\n\t"
    "v_exp_f32 v100, v100\n\t"
    "v_add_f32 v228, v228, v96\n\t"
    "v_mfma_f32_32x32x16_bf16 v[32:47], a[232:235], a[136:139], v[32:47]\n\t"
    "s_add_u32 m0, s68, 2048\n\t"
    "s_add_u32 s67, s69, 8192\n\t"
    "buffer_load_dwordx4 v255, s[44:47], s67 offen offset:0 lds\n\t"
    "ds_read_b128 v[200:203], v226 offset:12288\n\t"
    "v_add_f32 v229, v229, v97\n\t"
    "v_mfma_f32_32x32x16_bf16 v[48:63], a[232:235], a[168:171], v[48:63]\n\t"
    "ds_read_b128 v[204:207], v226 offset:14336\n\t"
    "v_exp_f32 v101, v101\n\t"
    "v_exp_f32 v102, v102\n\t"
    "v_add_f32 v228, v228, v98\n\t"
    "v_mfma_f32_32x32x16_bf16 v[0:15], a[204:207], a[140:143], v[0:15]\n\t"
    "ds_read_b128 v[208:211], v226 offset:9216\n\t"
    "v_exp_f32 v103, v103\n\t"
    "v_exp_f32 v112, v112\n\t"
    "v_add_f32 v229, v229, v99\n\t"
    "v_mfma_f32_32x32x16_bf16 v[16:31], a[204:207], a[172:175], v[16:31]\n\t"
    "s_add_u32 m0, s68, 3072\n\t"
    "s_add_u32 s67, s69, 12288\n\t"
    "buffer_load_dwordx4 v255, s[44:47], s67 offen offset:0 lds\n\t"
    "ds_read_b128 v[212:215], v226 offset:11264\n\t"
    "v_add_f32 v228, v228, v100\n\t"
    "v_mfma_f32_32x32x16_bf16 v[32:47], a[236:239], a[140:143], v[32:47]\n\t"
    "ds_read_b128 v[216:219], v226 offset:13312\n\t"
    "v_exp_f32 v113, v113\n\t"
    "v_exp_f32 v114, v114\n\t"
    "v_add_f32 v229, v229, v101\n\t"
    "v_mfma_f32_32x32x16_bf16 v[48:63], a[236:239], a[172:175], v[48:63]\n\t"
    "ds_read_b128 v[220:223], v226 offset:15360\n\t"
    "v_exp_f32 v115, v115\n\t"
    "v_exp_f32 v116, v116\n\t"
    "v_add_f32 v228, v228, v102\n\t"
    "v_mfma_f32_32x32x16_bf16 v[0:15], a[208:211], a[144:147], v[0:15]\n\t"
    "s_add_u32 m0, s68, 16384\n\t"
    "s_add_u32 s67, s69, 65536\n\t"
    "buffer_load_dwordx4 v255, s[44:47], s67 offen offset:0 lds\n\t"
    "v_exp_f32 v117, v117\n\t"
    "v_mfma_f32_32x32x16_bf16 v[16:31], a[208:211], a[176:179], v[16:31]\n\t"
    "v_exp_f32 v118, v118\n\t"
    "v_exp_f32 v119, v119\n\t"
    "v_add_f32 v229, v229, v103\n\t"
    "v_add_f32 v230, v230, v112\n\t"
    "v_mfma_f32_32x32x16_bf16 v[32:47], a[240:243], a[144:147], v[32:47]\n\t"
    "v_exp_f32 v104, v104\n\t"
    "v_exp_f32 v105, v105\n\t"
    "v_add_f32 v231, v231, v113\n\t"
    "v_add_f32 v230, v230, v114\n\t"
    "v_mfma_f32_32x32x16_bf16 v[48:63], a[240:243], a[176:179], v[48:63]\n\t"
    "s_add_u32 m0, s68, 17408\n\t"
    "s_add_u32 s67, s69, 69632\n\t"
    "buffer_load_dwordx4 v255, s[44:47], s67 offen offset:0 lds\n\t"
    "v_exp_f32 v106, v106\n\t"
    "v_mfma_f32_32x32x16_bf16 v[0:15], a[212:215], a[148:151], v[0:15]\n\t"
    "s_and_b32 s66, s57, 0xc000\n\t"
    "v_exp_f32 v107, v107\n\t"
    "v_exp_f32 v108, v108\n\t"
    "v_add_f32 v231, v231, v115\n\t"
    "v_mfma_f32_32x32x16_bf16 v[16:31], a[212:215], a[180:183], v[16:31]\n\t"
    "v_add_u32 v225, s66, v224\n\t"
    "v_exp_f32 v109, v109\n\t"
    "v_exp_f32 v110, v110\n\t"
    "v_add_f32 v230, v230, v116\n\t"
    "v_mfma_f32_32x32x16_bf16 v[32:47], a[244:247], a[148:151], v[32:47]\n\t"
    "s_add_u32 m0, s68, 18432\n\t"
    "s_add_u32 s67, s69, 73728\n\t"
    "buffer_load_dwordx4 v255, s[44:47], s67 offen offset:0 lds\n\t"
    "v_exp_f32 v111, v111\n\t"
    "v_mfma_f32_32x32x16_bf16 v[48:63], a[244:247], a[180:183], v[48:63]\n\t"
    "v_exp_f32 v120, v120\n\t"
    "v_exp_f32 v121, v121\n\t"
    "v_add_f32 v231, v231, v117\n\t"
    "v_add_f32 v230, v230, v118\n\t"
    "v_mfma_f32_32x32x16_bf16 v[0:15], a[216:219], a[152:155], v[0:15]\n\t"
    "v_exp_f32 v122, v122\n\t"
    "v_exp_f32 v123, v123\n\t"
    "v_add_f32 v231, v231, v119\n\t"
    "v_add_f32 v228, v228, v104\n\t"
    "v_mfma_f32_32x32x16_bf16 v[16:31], a[216:219], a[184:187], v[16:31]\n\t"
    "s_add_u32 m0, s68, 19456\n\t"
    "s_add_u32 s67, s69, 77824\n\t"
    "buffer_load_dwordx4 v255, s[44:47], s67 offen offset:0 lds\n\t"
    "v_exp_f32 v124, v124\n\t"
    "v_mfma_f32_32x32x16_bf16 v[32:47], a[248:251], a[152:155], v[32:47]\n\t"
    "v_exp_f32 v125, v125\n\t"
    "v_exp_f32 v126, v126\n\t"
    "v_add_f32 v229, v229, v105\n\t"
    "v_add_f32 v228, v228, v106\n\t"
    "v_mfma_f32_32x32x16_bf16 v[48:63], a[248:251], a[184:187], v[48:63]\n\t"
    "v_exp_f32 v127, v127\n\t"
    "v_add_f32 v229, v229, v107\n\t"
    "v_add_f32 v228, v228, v108\n\t"
    "v_add_f32 v229, v229, v109\n\t"
    "v_add_f32 v228, v228, v110\n\t"
    "v_mfma_f32_32x32x16_bf16 v[0:15], a[220:223], a[156:159], v[0:15]\n\t"
    "v_add_f32 v229, v229, v111\n\t"
    "v_add_f32 v230, v230, v120\n\t"
    "v_add_f32 v231, v231, v121\n\t"
    "v_add_f32 v230, v230, v122\n\t"
    "v_add_f32 v231, v231, v123\n\t"
    "v_add_f32 v230, v230, v124\n\t"
    "v_mfma_f32_32x32x16_bf16 v[16:31], a[220:223], a[188:191], v[16:31]\n\t"
    "v_add_f32 v231, v231, v125\n\t"
    "v_add_f32 v230, v230, v126\n\t"
    "v_add_f32 v231, v231, v127\n\t"
    "v_cvt_pk_bf16_f32 v64, v64, v65\n\t"
    "v_cvt_pk_bf16_f32 v65, v66, v67\n\t"
    "v_cvt_pk_bf16_f32 v66, v68, v69\n\t"
    "v_mfma_f32_32x32x16_bf16 v[32:47], a[252:255], a[156:159], v[32:47]\n\t"
    "v_cvt_pk_bf16_f32 v67, v70, v71\n\t"
    "v_cvt_pk_bf16_f32 v80, v80, v81\n\t"
    "v_cvt_pk_bf16_f32 v81, v82, v83\n\t"
    "v_cvt_pk_bf16_f32 v82, v84, v85\n\t"
    "v_cvt_pk_bf16_f32 v83, v86, v87\n\t"
    "v_cvt_pk_bf16_f32 v72, v72, v73\n\t"
    "v_mfma_f32_32x32x16_bf16 v[48:63], a[252:255], a[188:191], v[48:63]\n\t"
    "v_cvt_pk_bf16_f32 v73, v74, v75\n\t"
    "v_cvt_pk_bf16_f32 v74, v76, v77\n\t"
    "v_cvt_pk_bf16_f32 v75, v78, v79\n\t"
    "v_cvt_pk_bf16_f32 v88, v88, v89\n\t"
    "v_cvt_pk_bf16_f32 v89, v90, v91\n\t"
    "v_cvt_pk_bf16_f32 v90, v92, v93\n\t"
    "v_cvt_pk_bf16_f32 v91, v94, v95\n\t"
    "v_cvt_pk_bf16_f32 v96, v96, v97\n\t"
    "v_cvt_pk_bf16_f32 v97, v98, v99\n\t"
    "v_cvt_pk_bf16_f32 v98, v100, v101\n\t"
    "v_cvt_pk_bf16_f32 v99, v102, v103\n\t"
    "v_cvt_pk_bf16_f32 v112, v112, v113\n\t"
    "v_cvt_pk_bf16_f32 v113, v114, v115\n\t"
    "v_cvt_pk_bf16_f32 v114, v116, v117\n\t"
    "v_cvt_pk_bf16_f32 v115, v118, v119\n\t"
    "v_cvt_pk_bf16_f32 v104, v104, v105\n\t"
    "v_cvt_pk_bf16_f32 v105, v106, v107\n\t"
    "v_cvt_pk_bf16_f32 v106, v108, v109\n\t"
    "v_cvt_pk_bf16_f32 v107, v110, v111\n\t"
    "v_cvt_pk_bf16_f32 v120, v120, v121\n\t"
    "v_cvt_pk_bf16_f32 v121, v122, v123\n\t"
    "v_cvt_pk_bf16_f32 v122, v124, v125\n\t"
    "v_cvt_pk_bf16_f32 v123, v126, v127\n\t"
    "s_waitcnt vmcnt(17) lgkmcnt(0)\n\t"
    "s_barrier\n\t"
    "s_cmp_ge_u32 s56, s63\n\t"
    "s_cbranch_scc0 Lin_125_%=\n\t"
    "s_sub_u32 s66, s56, s63\n\t"
    "s_cmp_ge_u32 s66, 0x4000\n\t"
    "s_cselect_b32 s66, -1, 0\n\t"
    "v_or_b32 v242, s66, v239\n\t"
    "v_cmp_le_i32_e64 s[70:71], 0, v242\n\t"
    "v_cmp_le_i32_e64 s[72:73], 1, v242\n\t"
    "v_cmp_le_i32_e64 s[74:75], 2, v242\n\t"
    "v_cmp_le_i32_e64 s[76:77], 3, v242\n\t"
    "v_cmp_le_i32_e64 s[78:79], 8, v242\n\t"
    "v_cmp_le_i32_e64 s[80:81], 9, v242\n\t"
    "v_cmp_le_i32_e64 s[82:83], 10, v242\n\t"
    "v_cmp_le_i32_e64 s[84:85], 11, v242\n\t"
    "v_pk_mov_b32 v[32:33], v[252:253], v[252:253] op_sel:[0,0]\n\t"
    "v_pk_mov_b32 v[34:35], v[252:253], v[252:253] op_sel:[0,0]\n\t"
    "v_pk_mov_b32 v[36:37], v[252:253], v[252:253] op_sel:[0,0]\n\t"
    "v_pk_mov_b32 v[38:39], v[252:253], v[252:253] op_sel:[0,0]\n\t"
    "v_cndmask_b32_e64 v0, v252, v0, s[70:71]\n\t"
    "v_cndmask_b32_e64 v48, v252, v48, s[70:71]\n\t"
    "v_cndmask_b32_e64 v1, v252, v1, s[72:73]\n\t"
    "v_cndmask_b32_e64 v49, v252, v49, s[72:73]\n\t"
    "v_cndmask_b32_e64 v2, v252, v2, s[74:75]\n\t"
    "v_cndmask_b32_e64 v50, v252, v50, s[74:75]\n\t"
    "v_cndmask_b32_e64 v3, v252, v3, s[76:77]\n\t"
    "v_cndmask_b32_e64 v51, v252, v51, s[76:77]\n\t"
    "v_cndmask_b32_e64 v4, v252, v4, s[78:79]\n\t"
    "v_cndmask_b32_e64 v52, v252, v52, s[78:79]\n\t"
    "v_cndmask_b32_e64 v5, v252, v5, s[80:81]\n\t"
    "v_cndmask_b32_e64 v53, v252, v53, s[80:81]\n\t"
    "v_cndmask_b32_e64 v6, v252, v6, s[82:83]\n\t"
    "v_cndmask_b32_e64 v54, v252, v54, s[82:83]\n\t"
    "v_cndmask_b32_e64 v7, v252, v7, s[84:85]\n\t"
    "v_cndmask_b32_e64 v55, v252, v55, s[84:85]\n\t"
    "v_cmp_le_i32_e64 s[70:71], 16, v242\n\t"
    "v_cmp_le_i32_e64 s[72:73], 17, v242\n\t"
    "v_cmp_le_i32_e64 s[74:75], 18, v242\n\t"
    "v_cmp_le_i32_e64 s[76:77], 19, v242\n\t"
    "v_cmp_le_i32_e64 s[78:79], 24, v242\n\t"
    "v_cmp_le_i32_e64 s[80:81], 25, v242\n\t"
    "v_cmp_le_i32_e64 s[82:83], 26, v242\n\t"
    "v_cmp_le_i32_e64 s[84:85], 27, v242\n\t"
    "v_pk_mov_b32 v[40:41], v[252:253], v[252:253] op_sel:[0,0]\n\t"
    "v_pk_mov_b32 v[42:43], v[252:253], v[252:253] op_sel:[0,0]\n\t"
    "v_pk_mov_b32 v[44:45], v[252:253], v[252:253] op_sel:[0,0]\n\t"
    "v_pk_mov_b32 v[46:47], v[252:253], v[252:253] op_sel:[0,0]\n\t"
    "v_cndmask_b32_e64 v8, v252, v8, s[70:71]\n\t"
    "v_cndmask_b32_e64 v56, v252, v56, s[70:71]\n\t"
    "v_cndmask_b32_e64 v9, v252, v9, s[72:73]\n\t"
    "v_cndmask_b32_e64 v57, v252, v57, s[72:73]\n\t"
    "v_cndmask_b32_e64 v10, v252, v10, s[74:75]\n\t"
    "v_cndmask_b32_e64 v58, v252, v58, s[74:75]\n\t"
    "v_cndmask_b32_e64 v11, v252, v11, s[76:77]\n\t"
    "v_cndmask_b32_e64 v59, v252, v59, s[76:77]\n\t"
    "v_cndmask_b32_e64 v12, v252, v12, s[78:79]\n\t"
    "v_cndmask_b32_e64 v60, v252, v60, s[78:79]\n\t"
    "v_cndmask_b32_e64 v13, v252, v13, s[80:81]\n\t"
    "v_cndmask_b32_e64 v61, v252, v61, s[80:81]\n\t"
    "v_cndmask_b32_e64 v14, v252, v14, s[82:83]\n\t"
    "v_cndmask_b32_e64 v62, v252, v62, s[82:83]\n\t"
    "v_cndmask_b32_e64 v15, v252, v15, s[84:85]\n\t"
    "v_cndmask_b32_e64 v63, v252, v63, s[84:85]\n\t"
    "s_cbranch_scc0 Lfull_124_%=\n\t"
    "v_pk_mov_b32 v[16:17], v[252:253], v[252:253] op_sel:[0,0]\n\t"
    "v_pk_mov_b32 v[18:19], v[252:253], v[252:253] op_sel:[0,0]\n\t"
    "v_pk_mov_b32 v[20:21], v[252:253], v[252:253] op_sel:[0,0]\n\t"
    "v_pk_mov_b32 v[22:23], v[252:253], v[252:253] op_sel:[0,0]\n\t"
    "v_pk_mov_b32 v[24:25], v[252:253], v[252:253] op_sel:[0,0]\n\t"
    "v_pk_mov_b32 v[26:27], v[252:253], v[252:253] op_sel:[0,0]\n\t"
    "v_pk_mov_b32 v[28:29], v[252:253], v[252:253] op_sel:[0,0]\n\t"
    "v_pk_mov_b32 v[30:31], v[252:253], v[252:253] op_sel:[0,0]\n\t"
    "Lfull_124_%=:\n\t"
    "Lin_125_%=:\n\t"
    "v_mfma_f32_32x32x16_bf16 a[0:15], v[160:163], v[64:67], a[0:15]\n\t"
    "s_and_b32 s69, s56, 0xc000\n\t"
    "v_add_u32 v226, s69, v227\n\t"
    "s_nop 3\n\t"
    "v_mfma_f32_32x32x16_bf16 a[16:31], v[160:163], v[80:83], a[16:31]\n\t"
    "v_max3_f32 v234, v0, v1, v2\n\t"
    "v_max3_f32 v235, v16, v17, v18\n\t"
    "v_max3_f32 v236, v32, v33, v34\n\t"
    "v_max3_f32 v237, v48, v49, v50\n\t"
    "v_mfma_f32_32x32x16_bf16 a[32:47], v[164:167], v[64:67], a[32:47]\n\t"
    "v_max3_f32 v234, v234, v3, v4\n\t"
    "v_max3_f32 v235, v235, v19, v20\n\t"
    "v_max3_f32 v236, v236, v35, v36\n\t"
    "v_max3_f32 v237, v237, v51, v52\n\t"
    "v_mfma_f32_32x32x16_bf16 a[48:63], v[164:167], v[80:83], a[48:63]\n\t"
    "v_max3_f32 v234, v234, v5, v6\n\t"
    "v_max3_f32 v235, v235, v21, v22\n\t"
    "v_max3_f32 v236, v236, v37, v38\n\t"
    "v_max3_f32 v237, v237, v53, v54\n\t"
    "v_mfma_f32_32x32x16_bf16 a[64:79], v[168:171], v[64:67], a[64:79]\n\t"
    "v_max3_f32 v234, v234, v7, v8\n\t"
    "v_max3_f32 v235, v235, v23, v24\n\t"
    "v_max3_f32 v236, v236, v39, v40\n\t"
    "v_max3_f32 v237, v237, v55, v56\n\t"
    "v_mfma_f32_32x32x16_bf16 a[80:95], v[168:171], v[80:83], a[80:95]\n\t"
    "v_max3_f32 v234, v234, v9, v10\n\t"
    "v_max3_f32 v235, v235, v25, v26\n\t"
    "v_max3_f32 v236, v236, v41, v42\n\t"
    "v_max3_f32 v237, v237, v57, v58\n\t"
    "v_mfma_f32_32x32x16_bf16 a[96:111], v[172:175], v[64:67], a[96:111]\n\t"
    "v_max3_f32 v234, v234, v11, v12\n\t"
    "v_max3_f32 v235, v235, v27, v28\n\t"
    "v_max3_f32 v236, v236, v43, v44\n\t"
    "v_max3_f32 v237, v237, v59, v60\n\t"
    "v_mfma_f32_32x32x16_bf16 a[112:127], v[172:175], v[80:83], a[112:127]\n\t"
    "v_max3_f32 v234, v234, v13, v14\n\t"
    "v_max3_f32 v235, v235, v29, v30\n\t"
    "v_max3_f32 v236, v236, v45, v46\n\t"
    "v_max3_f32 v237, v237, v61, v62\n\t"
    "v_mfma_f32_32x32x16_bf16 a[0:15], v[176:179], v[72:75], a[0:15]\n\t"
    "v_max_f32 v234, v234, v15\n\t"
    "v_max_f32 v235, v235, v31\n\t"
    "v_max_f32 v236, v236, v47\n\t"
    "v_max_f32 v237, v237, v63\n\t"
    "v_mfma_f32_32x32x16_bf16 a[16:31], v[176:179], v[88:91], a[16:31]\n\t"
    "v_max3_f32 v238, v234, v235, v236\n\t"
    "v_max_f32 v238, v238, v237\n\t"
    "v_cmp_lt_f32 vcc, s60, v238\n\t"
    "s_cbranch_vccnz Lst_126_%=\n\t"
    "Lbk_127_%=:\n\t"
    "s_cmp_eq_u32 s93, 0\n\t"
    "s_cbranch_scc1 Lin_128_%=\n\t"
    "v_add_f32 v250, v228, v229\n\t"
    "v_mov_b32 v228, 0\n\t"
    "v_mov_b32 v229, 0\n\t"
    "v_add_f32 v251, v230, v231\n\t"
    "v_mov_b32 v230, 0\n\t"
    "v_mov_b32 v231, 0\n\t"
    "s_mov_b32 s93, 0\n\t"
    "Lin_128_%=:\n\t"
    "v_mfma_f32_32x32x16_bf16 a[32:47], v[180:183], v[72:75], a[32:47]\n\t"
    "s_lshl_b32 s69, %14, 18\n\t"
    "s_add_u32 s69, s69, 3145728\n\t"
    "s_and_b32 s69, s69, 3145728\n\t"
    "s_add_u32 m0, s68, 32768\n\t"
    "s_add_u32 s67, s69, 0\n\t"
    "buffer_load_dwordx4 v255, s[44:47], s67 offen offset:0 lds\n\t"
    "v_mfma_f32_32x32x16_bf16 a[48:63], v[180:183], v[88:91], a[48:63]\n\t"
    "s_add_u32 m0, s68, 33792\n\t"
    "s_add_u32 s67, s69, 4096\n\t"
    "buffer_load_dwordx4 v255, s[44:47], s67 offen offset:0 lds\n\t"
    "v_exp_f32 v0, v0\n\t"
    "v_mfma_f32_32x32x16_bf16 a[64:79], v[184:187], v[72:75], a[64:79]\n\t"
    "s_add_u32 m0, s68, 34816\n\t"
    "s_add_u32 s67, s69, 8192\n\t"
    "buffer_load_dwordx4 v255, s[44:47], s67 offen offset:0 lds\n\t"
    "v_exp_f32 v1, v1\n\t"
    "v_mfma_f32_32x32x16_bf16 a[80:95], v[184:187], v[88:91], a[80:95]\n\t"
    "s_add_u32 m0, s68, 35840\n\t"
    "s_add_u32 s67, s69, 12288\n\t"
    "buffer_load_dwordx4 v255, s[44:47], s67 offen offset:0 lds\n\t"
    "v_exp_f32 v2, v2\n\t"
    "v_mfma_f32_32x32x16_bf16 a[96:111], v[188:191], v[72:75], a[96:111]\n\t"
    "s_add_u32 m0, s68, 49152\n\t"
    "s_add_u32 s67, s69, 65536\n\t"
    "buffer_load_dwordx4 v255, s[44:47], s67 offen offset:0 lds\n\t"
    "v_exp_f32 v3, v3\n\t"
    "v_mfma_f32_32x32x16_bf16 a[112:127], v[188:191], v[88:91], a[112:127]\n\t"
    "s_add_u32 m0, s68, 50176\n\t"
    "s_add_u32 s67, s69, 69632\n\t"
    "buffer_load_dwordx4 v255, s[44:47], s67 offen offset:0 lds\n\t"
    "v_exp_f32 v4, v4\n\t"
    "v_mfma_f32_32x32x16_bf16 a[0:15], v[192:195], v[96:99], a[0:15]\n\t"
    "s_add_u32 m0, s68, 51200\n\t"
    "s_add_u32 s67, s69, 73728\n\t"
    "buffer_load_dwordx4 v255, s[44:47], s67 offen offset:0 lds\n\t"
    "v_exp_f32 v5, v5\n\t"
    "v_mfma_f32_32x32x16_bf16 a[16:31], v[192:195], v[112:115], a[16:31]\n\t"
    "s_add_u32 m0, s68, 52224\n\t"
    "s_add_u32 s67, s69, 77824\n\t"
    "buffer_load_dwordx4 v255, s[44:47], s67 offen offset:0 lds\n\t"
    "v_exp_f32 v6, v6\n\t"
    "v_mfma_f32_32x32x16_bf16 a[32:47], v[196:199], v[96:99], a[32:47]\n\t"
    "s_lshl_b32 s69, %14, 18\n\t"
    "s_add_u32 s69, s69, 4194304\n\t"
    "s_and_b32 s69, s69, 3145728\n\t"
    "s_add_u32 m0, s68, 147456\n\t"
    "s_add_u32 s67, s69, 0\n\t"
    "buffer_load_dwordx4 v255, s[44:47], s67 offen offset:0 lds\n\t"
    "v_mfma_f32_32x32x16_bf16 a[48:63], v[196:199], v[112:115], a[48:63]\n\t"
    "s_add_u32 m0, s68, 148480\n\t"
    "s_add_u32 s67, s69, 4096\n\t"
    "buffer_load_dwordx4 v255, s[44:47], s67 offen offset:0 lds\n\t"
    "v_exp_f32 v7, v7\n\t"
    "v_mfma_f32_32x32x16_bf16 a[64:79], v[200:203], v[96:99], a[64:79]\n\t"
    "s_add_u32 m0, s68, 149504\n\t"
    "s_add_u32 s67, s69, 8192\n\t"
    "buffer_load_dwordx4 v255, s[44:47], s67 offen offset:0 lds\n\t"
    "v_exp_f32 v16, v16\n\t"
    "v_mfma_f32_32x32x16_bf16 a[80:95], v[200:203], v[112:115], a[80:95]\n\t"
    "s_add_u32 m0, s68, 150528\n\t"
    "s_add_u32 s67, s69, 12288\n\t"
    "buffer_load_dwordx4 v255, s[44:47], s67 offen offset:0 lds\n\t"
    "v_exp_f32 v17, v17\n\t"
    "v_mfma_f32_32x32x16_bf16 a[96:111], v[204:207], v[96:99], a[96:111]\n\t"
    "v_exp_f32 v18, v18\n\t"
    "v_exp_f32 v19, v19\n\t"
    "v_add_f32 v228, v228, v0\n\t"
    "v_add_f32 v229, v229, v1\n\t"
    "v_mfma_f32_32x32x16_bf16 a[112:127], v[204:207], v[112:115], a[112:127]\n\t"
    "v_exp_f32 v20, v20\n\t"
    "v_exp_f32 v21, v21\n\t"
    "v_add_f32 v228, v228, v2\n\t"
    "v_add_f32 v229, v229, v3\n\t"
    "v_mfma_f32_32x32x16_bf16 a[0:15], v[208:211], v[104:107], a[0:15]\n\t"
    "v_exp_f32 v22, v22\n\t"
    "v_exp_f32 v23, v23\n\t"
    "v_add_f32 v228, v228, v4\n\t"
    "v_add_f32 v229, v229, v5\n\t"
    "v_mfma_f32_32x32x16_bf16 a[16:31], v[208:211], v[120:123], a[16:31]\n\t"
    "v_exp_f32 v8, v8\n\t"
    "v_exp_f32 v9, v9\n\t"
    "v_add_f32 v228, v228, v6\n\t"
    "v_add_f32 v229, v229, v7\n\t"
    "v_mfma_f32_32x32x16_bf16 a[32:47], v[212:215], v[104:107], a[32:47]\n\t"
    "v_exp_f32 v10, v10\n\t"
    "v_exp_f32 v11, v11\n\t"
    "v_add_f32 v230, v230, v16\n\t"
    "v_add_f32 v231, v231, v17\n\t"
    "v_mfma_f32_32x32x16_bf16 a[48:63], v[212:215], v[120:123], a[48:63]\n\t"
    "v_exp_f32 v12, v12\n\t"
    "v_exp_f32 v13, v13\n\t"
    "v_add_f32 v230, v230, v18\n\t"
    "v_add_f32 v231, v231, v19\n\t"
    "v_mfma_f32_32x32x16_bf16 a[64:79], v[216:219], v[104:107], a[64:79]\n\t"
    "v_exp_f32 v14, v14\n\t"
    "v_exp_f32 v15, v15\n\t"
    "v_add_f32 v230, v230, v20\n\t"
    "v_add_f32 v231, v231, v21\n\t"
    "v_mfma_f32_32x32x16_bf16 a[80:95], v[216:219], v[120:123], a[80:95]\n\t"
    "v_exp_f32 v24, v24\n\t"
    "v_exp_f32 v25, v25\n\t"
    "v_add_f32 v230, v230, v22\n\t"
    "v_add_f32 v231, v231, v23\n\t"
    "v_mfma_f32_32x32x16_bf16 a[96:111], v[220:223], v[104:107], a[96:111]\n\t"
    "v_exp_f32 v26, v26\n\t"
    "v_exp_f32 v27, v27\n\t"
    "v_add_f32 v228, v228, v8\n\t"
    "v_add_f32 v229, v229, v9\n\t"
    "v_mfma_f32_32x32x16_bf16 a[112:127], v[220:223], v[120:123], a[112:127]\n\t"
    "v_add_f32 v228, v228, v10\n\t"
    "v_add_f32 v229, v229, v11\n\t"
    "v_add_f32 v228, v228, v12\n\t"
    "v_add_f32 v229, v229, v13\n\t"
    "v_add_f32 v228, v228, v14\n\t"
    "v_add_f32 v229, v229, v15\n\t"
    "v_add_f32 v230, v230, v24\n\t"
    "v_add_f32 v231, v231, v25\n\t"
    "v_add_f32 v230, v230, v26\n\t"
    "v_add_f32 v231, v231, v27\n\t"
    "s_mov_b32 s56, s57\n\t"
    "s_mov_b32 s57, s58\n\t"
    "s_add_u32 s58, s58, 0x8000\n\t"
    "s_cmp_lg_u32 s62, 0\n\t"
    "s_cbranch_scc1 Lst_129_%=\n\t"
    "Lbk_130_%=:\n\t"
    "s_waitcnt vmcnt(21)\n\t"
    "s_nop 3\n\t"
    "s_sub_u32 s67, s86, 0x8000\n\t"
    "v_add_u32 v164, s67, v224\n\t"
    "s_cmp_lt_u32 s89, 2\n\t"
    "s_cbranch_scc1 Lrx_131_%=\n\t"
    "ds_write_b128 v164, a[16:19] offset:0\n\t"
    "v_mov_b32 v241, v251\n\t"
    "s_nop 1\n\t"
    "v_permlane32_swap_b32 v251, v241\n\t"
    "s_nop 0\n\t"
    "ds_write_b128 v164, a[20:23] offset:1024\n\t"
    "v_add_f32 v251, v251, v241\n\t"
    "v_rcp_f32 v162, v251\n\t"
    "v_mov_b32 v240, v250\n\t"
    "s_nop 1\n\t"
    "ds_write_b128 v164, a[24:27] offset:2048\n\t"
    "v_permlane32_swap_b32 v250, v240\n\t"
    "s_nop 0\n\t"
    "v_add_f32 v250, v250, v240\n\t"
    "v_rcp_f32 v160, v250\n\t"
    "ds_write_b32 v164, v162 offset:15360\n\t"
    "v_mov_b32 v240, 0\n\t"
    "v_mov_b32 v241, 0\n\t"
    "v_mov_b32 v242, 0\n\t"
    "v_mov_b32 v243, 0\n\t"
    "ds_write_b128 v164, a[28:31] offset:3072\n\t"
    "s_add_u32 s66, s87, 0\n\t"
    "v_accvgpr_read_b32 v64, a0\n\t"
    "v_accvgpr_read_b32 v65, a1\n\t"
    "v_accvgpr_read_b32 v66, a2\n\t"
    "ds_write_b128 v164, a[48:51] offset:4096\n\t"
    "v_accvgpr_read_b32 v67, a3\n\t"
    "v_accvgpr_read_b32 v68, a4\n\t"
    "v_accvgpr_read_b32 v69, a5\n\t"
    "v_accvgpr_read_b32 v70, a6\n\t"
    "ds_write_b128 v164, a[52:55] offset:5120\n\t"
    "v_accvgpr_read_b32 v71, a7\n\t"
    "v_accvgpr_read_b32 v72, a8\n\t"
    "v_accvgpr_read_b32 v73, a9\n\t"
    "v_accvgpr_read_b32 v74, a10\n\t"
    "ds_write_b128 v164, a[56:59] offset:6144\n\t"
    "v_accvgpr_read_b32 v75, a11\n\t"
    "v_accvgpr_read_b32 v76, a12\n\t"
    "v_accvgpr_read_b32 v77, a13\n\t"
    "v_accvgpr_read_b32 v78, a14\n\t"
    "ds_write_b128 v164, a[60:63] offset:7168\n\t"
    "v_accvgpr_read_b32 v79, a15\n\t"
    "v_pk_mul_f32 v[64:65], v[64:65], v[160:161] op_sel_hi:[1,0]\n\t"
    "v_pk_mul_f32 v[66:67], v[66:67], v[160:161] op_sel_hi:[1,0]\n\t"
    "v_pk_mul_f32 v[68:69], v[68:69], v[160:161] op_sel_hi:[1,0]\n\t"
    "ds_write_b128 v164, a[80:83] offset:8192\n\t"
    "v_pk_mul_f32 v[70:71], v[70:71], v[160:161] op_sel_hi:[1,0]\n\t"
    "v_pk_mul_f32 v[72:73], v[72:73], v[160:161] op_sel_hi:[1,0]\n\t"
    "v_pk_mul_f32 v[74:75], v[74:75], v[160:161] op_sel_hi:[1,0]\n\t"
    "v_pk_mul_f32 v[76:77], v[76:77], v[160:161] op_sel_hi:[1,0]\n\t"
    "ds_write_b128 v164, a[84:87] offset:9216\n\t"
    "v_pk_mul_f32 v[78:79], v[78:79], v[160:161] op_sel_hi:[1,0]\n\t"
    "v_mfma_f32_32x32x16_bf16 a[0:15], v[240:243], v[240:243], 0\n\t"
    "v_cvt_pk_bf16_f32 v64, v64, v65\n\t"
    "v_cvt_pk_bf16_f32 v65, v66, v67\n\t"
    "ds_write_b128 v164, a[88:91] offset:10240\n\t"
    "v_cvt_pk_bf16_f32 v66, v68, v69\n\t"
    "v_cvt_pk_bf16_f32 v67, v70, v71\n\t"
    "v_cvt_pk_bf16_f32 v72, v72, v73\n\t"
    "v_cvt_pk_bf16_f32 v73, v74, v75\n\t"
    "ds_write_b128 v164, a[92:95] offset:11264\n\t"
    "v_cvt_pk_bf16_f32 v74, v76, v77\n\t"
    "v_cvt_pk_bf16_f32 v75, v78, v79\n\t"
    "buffer_store_dwordx4 v[64:67], v224, s[48:51], s66 offen offset:0 sc1\n\t"
    "buffer_store_dwordx4 v[72:75], v224, s[48:51], s66 offen offset:1024 sc1\n\t"
    "ds_write_b128 v164, a[112:115] offset:12288\n\t"
    "s_add_u32 s66, s87, 0\n\t"
    "v_accvgpr_read_b32 v80, a32\n\t"
    "v_accvgpr_read_b32 v81, a33\n\t"
    "v_accvgpr_read_b32 v82, a34\n\t"
    "ds_write_b128 v164, a[116:119] offset:13312\n\t"
    "v_accvgpr_read_b32 v83, a35\n\t"
    "v_accvgpr_read_b32 v84, a36\n\t"
    "v_accvgpr_read_b32 v85, a37\n\t"
    "v_accvgpr_read_b32 v86, a38\n\t"
    "v_accvgpr_read_b32 v87, a39\n\t"
    "v_accvgpr_read_b32 v88, a40\n\t"
    "v_accvgpr_read_b32 v89, a41\n\t"
    "v_accvgpr_read_b32 v90, a42\n\t"
    "v_accvgpr_read_b32 v91, a43\n\t"
    "s_waitcnt lgkmcnt(0)\n\t"
    "s_barrier\n\t"
    "v_accvgpr_read_b32 v92, a44\n\t"
    "v_accvgpr_read_b32 v93, a45\n\t"
    "v_accvgpr_read_b32 v94, a46\n\t"
    "v_accvgpr_read_b32 v95, a47\n\t"
    "v_pk_mul_f32 v[80:81], v[80:81], v[160:161] op_sel_hi:[1,0]\n\t"
    "v_pk_mul_f32 v[82:83], v[82:83], v[160:161] op_sel_hi:[1,0]\n\t"
    "v_pk_mul_f32 v[84:85], v[84:85], v[160:161] op_sel_hi:[1,0]\n\t"
    "v_pk_mul_f32 v[86:87], v[86:87], v[160:161] op_sel_hi:[1,0]\n\t"
    "v_pk_mul_f32 v[88:89], v[88:89], v[160:161] op_sel_hi:[1,0]\n\t"
    "v_pk_mul_f32 v[90:91], v[90:91], v[160:161] op_sel_hi:[1,0]\n\t"
    "v_pk_mul_f32 v[92:93], v[92:93], v[160:161] op_sel_hi:[1,0]\n\t"
    "v_pk_mul_f32 v[94:95], v[94:95], v[160:161] op_sel_hi:[1,0]\n\t"
    "v_mfma_f32_32x32x16_bf16 a[32:47], v[240:243], v[240:243], 0\n\t"
    "v_cvt_pk_bf16_f32 v80, v80, v81\n\t"
    "v_cvt_pk_bf16_f32 v81, v82, v83\n\t"
    "v_cvt_pk_bf16_f32 v82, v84, v85\n\t"
    "v_cvt_pk_bf16_f32 v83, v86, v87\n\t"
    "v_cvt_pk_bf16_f32 v88, v88, v89\n\t"
    "v_cvt_pk_bf16_f32 v89, v90, v91\n\t"
    "v_cvt_pk_bf16_f32 v90, v92, v93\n\t"
    "v_cvt_pk_bf16_f32 v91, v94, v95\n\t"
    "buffer_store_dwordx4 v[80:83], v224, s[48:51], s66 offen offset:2048 sc1\n\t"
    "buffer_store_dwordx4 v[88:91], v224, s[48:51], s66 offen offset:3072 sc1\n\t"
    "s_add_u32 s66, s87, 4096\n\t"
    "v_accvgpr_read_b32 v96, a64\n\t"
    "v_accvgpr_read_b32 v97, a65\n\t"
    "v_accvgpr_read_b32 v98, a66\n\t"
    "v_accvgpr_read_b32 v99, a67\n\t"
    "v_accvgpr_read_b32 v100, a68\n\t"
    "v_accvgpr_read_b32 v101, a69\n\t"
    "v_accvgpr_read_b32 v102, a70\n\t"
    "v_accvgpr_read_b32 v103, a71\n\t"
    "v_accvgpr_read_b32 v104, a72\n\t"
    "v_accvgpr_read_b32 v105, a73\n\t"
    "v_accvgpr_read_b32 v106, a74\n\t"
    "v_accvgpr_read_b32 v107, a75\n\t"
    "v_accvgpr_read_b32 v108, a76\n\t"
    "v_accvgpr_read_b32 v109, a77\n\t"
    "v_accvgpr_read_b32 v110, a78\n\t"
    "v_accvgpr_read_b32 v111, a79\n\t"
    "v_pk_mul_f32 v[96:97], v[96:97], v[160:161] op_sel_hi:[1,0]\n\t"
    "v_pk_mul_f32 v[98:99], v[98:99], v[160:161] op_sel_hi:[1,0]\n\t"
    "v_pk_mul_f32 v[100:101], v[100:101], v[160:161] op_sel_hi:[1,0]\n\t"
    "v_pk_mul_f32 v[102:103], v[102:103], v[160:161] op_sel_hi:[1,0]\n\t"
    "v_pk_mul_f32 v[104:105], v[104:105], v[160:161] op_sel_hi:[1,0]\n\t"
    "v_pk_mul_f32 v[106:107], v[106:107], v[160:161] op_sel_hi:[1,0]\n\t"
    "v_pk_mul_f32 v[108:109], v[108:109], v[160:161] op_sel_hi:[1,0]\n\t"
    "v_pk_mul_f32 v[110:111], v[110:111], v[160:161] op_sel_hi:[1,0]\n\t"
    "v_mfma_f32_32x32x16_bf16 a[64:79], v[240:243], v[240:243], 0\n\t"
    "v_cvt_pk_bf16_f32 v96, v96, v97\n\t"
    "v_cvt_pk_bf16_f32 v97, v98, v99\n\t"
    "v_cvt_pk_bf16_f32 v98, v100, v101\n\t"
    "v_cvt_pk_bf16_f32 v99, v102, v103\n\t"
    "v_cvt_pk_bf16_f32 v104, v104, v105\n\t"
    "v_cvt_pk_bf16_f32 v105, v106, v107\n\t"
    "v_cvt_pk_bf16_f32 v106, v108, v109\n\t"
    "v_cvt_pk_bf16_f32 v107, v110, v111\n\t"
    "v_mfma_f32_32x32x16_bf16 a[16:31], v[240:243], v[240:243], 0\n\t"
    "buffer_store_dwordx4 v[96:99], v224, s[48:51], s66 offen offset:0 sc1\n\t"
    "buffer_store_dwordx4 v[104:107], v224, s[48:51], s66 offen offset:1024 sc1\n\t"
    "s_add_u32 s66, s87, 4096\n\t"
    "v_accvgpr_read_b32 v112, a96\n\t"
    "v_accvgpr_read_b32 v113, a97\n\t"
    "v_accvgpr_read_b32 v114, a98\n\t"
    "v_accvgpr_read_b32 v115, a99\n\t"
    "v_accvgpr_read_b32 v116, a100\n\t"
    "v_accvgpr_read_b32 v117, a101\n\t"
    "v_accvgpr_read_b32 v118, a102\n\t"
    "v_accvgpr_read_b32 v119, a103\n\t"
    "v_accvgpr_read_b32 v120, a104\n\t"
    "v_accvgpr_read_b32 v121, a105\n\t"
    "v_accvgpr_read_b32 v122, a106\n\t"
    "v_accvgpr_read_b32 v123, a107\n\t"
    "v_accvgpr_read_b32 v124, a108\n\t"
    "v_accvgpr_read_b32 v125, a109\n\t"
    "v_accvgpr_read_b32 v126, a110\n\t"
    "v_accvgpr_read_b32 v127, a111\n\t"
    "v_pk_mul_f32 v[112:113], v[112:113], v[160:161] op_sel_hi:[1,0]\n\t"
    "v_pk_mul_f32 v[114:115], v[114:115], v[160:161] op_sel_hi:[1,0]\n\t"
    "v_pk_mul_f32 v[116:117], v[116:117], v[160:161] op_sel_hi:[1,0]\n\t"
    "v_pk_mul_f32 v[118:119], v[118:119], v[160:161] op_sel_hi:[1,0]\n\t"
    "v_pk_mul_f32 v[120:121], v[120:121], v[160:161] op_sel_hi:[1,0]\n\t"
    "v_pk_mul_f32 v[122:123], v[122:123], v[160:161] op_sel_hi:[1,0]\n\t"
    "v_pk_mul_f32 v[124:125], v[124:125], v[160:161] op_sel_hi:[1,0]\n\t"
    "v_pk_mul_f32 v[126:127], v[126:127], v[160:161] op_sel_hi:[1,0]\n\t"
    "v_mfma_f32_32x32x16_bf16 a[96:111], v[240:243], v[240:243], 0\n\t"
    "v_cvt_pk_bf16_f32 v112, v112, v113\n\t"
    "v_cvt_pk_bf16_f32 v113, v114, v115\n\t"
    "v_cvt_pk_bf16_f32 v114, v116, v117\n\t"
    "v_cvt_pk_bf16_f32 v115, v118, v119\n\t"
    "v_cvt_pk_bf16_f32 v120, v120, v121\n\t"
    "v_cvt_pk_bf16_f32 v121, v122, v123\n\t"
    "v_cvt_pk_bf16_f32 v122, v124, v125\n\t"
    "v_cvt_pk_bf16_f32 v123, v126, v127\n\t"
    "v_mfma_f32_32x32x16_bf16 a[48:63], v[240:243], v[240:243], 0\n\t"
    "buffer_store_dwordx4 v[112:115], v224, s[48:51], s66 offen offset:2048 sc1\n\t"
    "buffer_store_dwordx4 v[120:123], v224, s[48:51], s66 offen offset:3072 sc1\n\t"
    "s_add_u32 s66, s87, 266240\n\t"
    "v_accvgpr_read_b32 v72, a120\n\t"
    "v_accvgpr_read_b32 v73, a121\n\t"
    "v_accvgpr_read_b32 v74, a122\n\t"
    "v_accvgpr_read_b32 v75, a123\n\t"
    "v_accvgpr_read_b32 v76, a124\n\t"
    "v_accvgpr_read_b32 v77, a125\n\t"
    "v_accvgpr_read_b32 v78, a126\n\t"
    "v_accvgpr_read_b32 v79, a127\n\t"
    "v_pk_mul_f32 v[72:73], v[72:73], v[162:163] op_sel_hi:[1,0]\n\t"
    "v_pk_mul_f32 v[74:75], v[74:75], v[162:163] op_sel_hi:[1,0]\n\t"
    "v_pk_mul_f32 v[76:77], v[76:77], v[162:163] op_sel_hi:[1,0]\n\t"
    "v_pk_mul_f32 v[78:79], v[78:79], v[162:163] op_sel_hi:[1,0]\n\t"
    "v_mfma_f32_32x32x16_bf16 a[112:127], v[240:243], v[240:243], 0\n\t"
    "v_cvt_pk_bf16_f32 v72, v72, v73\n\t"
    "v_cvt_pk_bf16_f32 v73, v74, v75\n\t"
    "v_cvt_pk_bf16_f32 v74, v76, v77\n\t"
    "v_cvt_pk_bf16_f32 v75, v78, v79\n\t"
    "v_mfma_f32_32x32x16_bf16 a[80:95], v[240:243], v[240:243], 0\n\t"
    "buffer_store_dwordx4 v[72:75], v224, s[48:51], s66 offen offset:3072 sc1\n\t"
    "s_nop 1\n\t"
    "s_branch Lin_132_%=\n\t"
    "Lrx_131_%=:\n\t"
    "s_barrier\n\t"
    "ds_read_b32 v162, v164 offset:15360\n\t"
    "ds_read_b128 v[64:67], v164 offset:0\n\t"
    "ds_read_b128 v[68:71], v164 offset:1024\n\t"
    "ds_read_b128 v[72:75], v164 offset:2048\n\t"
    "ds_read_b128 v[76:79], v164 offset:3072\n\t"
    "ds_read_b128 v[80:83], v164 offset:4096\n\t"
    "ds_read_b128 v[84:87], v164 offset:5120\n\t"
    "ds_read_b128 v[88:91], v164 offset:6144\n\t"
    "ds_read_b128 v[92:95], v164 offset:7168\n\t"
    "ds_read_b128 v[96:99], v164 offset:8192\n\t"
    "ds_read_b128 v[100:103], v164 offset:9216\n\t"
    "ds_read_b128 v[104:107], v164 offset:10240\n\t"
    "ds_read_b128 v[108:111], v164 offset:11264\n\t"
    "ds_read_b128 v[112:115], v164 offset:12288\n\t"
    "ds_read_b128 v[116:119], v164 offset:13312\n\t"
    "s_add_u32 s66, s87, 262144\n\t"
    "s_waitcnt lgkmcnt(10)\n\t"
    "v_pk_mul_f32 v[64:65], v[64:65], v[162:163] op_sel_hi:[1,0]\n\t"
    "v_pk_mul_f32 v[66:67], v[66:67], v[162:163] op_sel_hi:[1,0]\n\t"
    "v_pk_mul_f32 v[68:69], v[68:69], v[162:163] op_sel_hi:[1,0]\n\t"
    "v_pk_mul_f32 v[70:71], v[70:71], v[162:163] op_sel_hi:[1,0]\n\t"
    "v_pk_mul_f32 v[72:73], v[72:73], v[162:163] op_sel_hi:[1,0]\n\t"
    "v_pk_mul_f32 v[74:75], v[74:75], v[162:163] op_sel_hi:[1,0]\n\t"
    "v_pk_mul_f32 v[76:77], v[76:77], v[162:163] op_sel_hi:[1,0]\n\t"
    "v_pk_mul_f32 v[78:79], v[78:79], v[162:163] op_sel_hi:[1,0]\n\t"
    "v_cvt_pk_bf16_f32 v64, v64, v65\n\t"
    "v_cvt_pk_bf16_f32 v65, v66, v67\n\t"
    "v_cvt_pk_bf16_f32 v66, v68, v69\n\t"
    "v_cvt_pk_bf16_f32 v67, v70, v71\n\t"
    "v_cvt_pk_bf16_f32 v72, v72, v73\n\t"
    "v_cvt_pk_bf16_f32 v73, v74, v75\n\t"
    "v_cvt_pk_bf16_f32 v74, v76, v77\n\t"
    "v_cvt_pk_bf16_f32 v75, v78, v79\n\t"
    "buffer_store_dwordx4 v[64:67], v224, s[48:51], s66 offen offset:0 sc1\n\t"
    "buffer_store_dwordx4 v[72:75], v224, s[48:51], s66 offen offset:1024 sc1\n\t"
    "s_add_u32 s66, s87, 262144\n\t"
    "s_waitcnt lgkmcnt(6)\n\t"
    "v_pk_mul_f32 v[80:81], v[80:81], v[162:163] op_sel_hi:[1,0]\n\t"
    "v_pk_mul_f32 v[82:83], v[82:83], v[162:163] op_sel_hi:[1,0]\n\t"
    "v_pk_mul_f32 v[84:85], v[84:85], v[162:163] op_sel_hi:[1,0]\n\t"
    "v_pk_mul_f32 v[86:87], v[86:87], v[162:163] op_sel_hi:[1,0]\n\t"
    "v_pk_mul_f32 v[88:89], v[88:89], v[162:163] op_sel_hi:[1,0]\n\t"
    "v_pk_mul_f32 v[90:91], v[90:91], v[162:163] op_sel_hi:[1,0]\n\t"
    "v_pk_mul_f32 v[92:93], v[92:93], v[162:163] op_sel_hi:[1,0]\n\t"
    "v_pk_mul_f32 v[94:95], v[94:95], v[162:163] op_sel_hi:[1,0]\n\t"
    "v_cvt_pk_bf16_f32 v80, v80, v81\n\t"
    "v_cvt_pk_bf16_f32 v81, v82, v83\n\t"
    "v_cvt_pk_bf16_f32 v82, v84, v85\n\t"
    "v_cvt_pk_bf16_f32 v83, v86, v87\n\t"
    "v_cvt_pk_bf16_f32 v88, v88, v89\n\t"
    "v_cvt_pk_bf16_f32 v89, v90, v91\n\t"
    "v_cvt_pk_bf16_f32 v90, v92, v93\n\t"
    "v_cvt_pk_bf16_f32 v91, v94, v95\n\t"
    "buffer_store_dwordx4 v[80:83], v224, s[48:51], s66 offen offset:2048 sc1\n\t"
    "buffer_store_dwordx4 v[88:91], v224, s[48:51], s66 offen offset:3072 sc1\n\t"
    "s_add_u32 s66, s87, 266240\n\t"
    "s_waitcnt lgkmcnt(2)\n\t"
    "v_pk_mul_f32 v[96:97], v[96:97], v[162:163] op_sel_hi:[1,0]\n\t"
    "v_pk_mul_f32 v[98:99], v[98:99], v[162:163] op_sel_hi:[1,0]\n\t"
    "v_pk_mul_f32 v[100:101], v[100:101], v[162:163] op_sel_hi:[1,0]\n\t"
    "v_pk_mul_f32 v[102:103], v[102:103], v[162:163] op_sel_hi:[1,0]\n\t"
    "v_pk_mul_f32 v[104:105], v[104:105], v[162:163] op_sel_hi:[1,0]\n\t"
    "v_pk_mul_f32 v[106:107], v[106:107], v[162:163] op_sel_hi:[1,0]\n\t"
    "v_pk_mul_f32 v[108:109], v[108:109], v[162:163] op_sel_hi:[1,0]\n\t"
    "v_pk_mul_f32 v[110:111], v[110:111], v[162:163] op_sel_hi:[1,0]\n\t"
    "v_cvt_pk_bf16_f32 v96, v96, v97\n\t"
    "v_cvt_pk_bf16_f32 v97, v98, v99\n\t"
    "v_cvt_pk_bf16_f32 v98, v100, v101\n\t"
    "v_cvt_pk_bf16_f32 v99, v102, v103\n\t"
    "v_cvt_pk_bf16_f32 v104, v104, v105\n\t"
    "v_cvt_pk_bf16_f32 v105, v106, v107\n\t"
    "v_cvt_pk_bf16_f32 v106, v108, v109\n\t"
    "v_cvt_pk_bf16_f32 v107, v110, v111\n\t"
    "buffer_store_dwordx4 v[96:99], v224, s[48:51], s66 offen offset:0 sc1\n\t"
    "buffer_store_dwordx4 v[104:107], v224, s[48:51], s66 offen offset:1024 sc1\n\t"
    "s_add_u32 s66, s87, 266240\n\t"
    "s_waitcnt lgkmcnt(0)\n\t"
    "v_pk_mul_f32 v[112:113], v[112:113], v[162:163] op_sel_hi:[1,0]\n\t"
    "v_pk_mul_f32 v[114:115], v[114:115], v[162:163] op_sel_hi:[1,0]\n\t"
    "v_pk_mul_f32 v[116:117], v[116:117], v[162:163] op_sel_hi:[1,0]\n\t"
    "v_pk_mul_f32 v[118:119], v[118:119], v[162:163] op_sel_hi:[1,0]\n\t"
    "v_cvt_pk_bf16_f32 v112, v112, v113\n\t"
    "v_cvt_pk_bf16_f32 v113, v114, v115\n\t"
    "v_cvt_pk_bf16_f32 v114, v116, v117\n\t"
    "v_cvt_pk_bf16_f32 v115, v118, v119\n\t"
    "buffer_store_dwordx4 v[112:115], v224, s[48:51], s66 offen offset:2048 sc1\n\t"
    "s_nop 1\n\t"
    "Lin_132_%=:\n\t"
    "s_waitcnt lgkmcnt(0)\n\t"
    "s_barrier\n\t"
    "s_add_u32 s52, s52, 1\n\t"
    "ds_read_b128 v[160:163], v226 offset:0\n\t"
    "ds_read_b128 v[164:167], v226 offset:2048\n\t"
    "ds_read_b128 v[168:171], v226 offset:4096\n\t"
    "ds_read_b128 v[172:175], v226 offset:6144\n\t"
    "ds_read_b128 v[176:179], v226 offset:1024\n\t"
    "ds_read_b128 v[180:183], v226 offset:3072\n\t"
    "ds_read_b128 v[184:187], v226 offset:5120\n\t"
    "ds_read_b128 v[188:191], v226 offset:7168\n\t"
    "ds_read_b128 v[192:195], v226 offset:8192\n\t"
    "ds_read_b128 v[196:199], v226 offset:10240\n\t"
    "ds_read_b128 v[200:203], v226 offset:12288\n\t"
    "ds_read_b128 v[204:207], v226 offset:14336\n\t"
    "ds_read_b128 v[208:211], v226 offset:9216\n\t"
    "ds_read_b128 v[212:215], v226 offset:11264\n\t"
    "ds_read_b128 v[216:219], v226 offset:13312\n\t"
    "ds_read_b128 v[220:223], v226 offset:15360\n\t"
    "v_cvt_pk_bf16_f32 v0, v0, v1\n\t"
    "v_cvt_pk_bf16_f32 v1, v2, v3\n\t"
    "v_cvt_pk_bf16_f32 v2, v4, v5\n\t"
    "v_cvt_pk_bf16_f32 v3, v6, v7\n\t"
    "v_cvt_pk_bf16_f32 v16, v16, v17\n\t"
    "v_cvt_pk_bf16_f32 v17, v18, v19\n\t"
    "v_cvt_pk_bf16_f32 v18, v20, v21\n\t"
    "v_cvt_pk_bf16_f32 v19, v22, v23\n\t"
    "s_nop 0\n\t"
    "s_nop 0\n\t"
    "s_waitcnt lgkmcnt(12)\n\t"
    "v_mfma_f32_32x32x16_bf16 a[0:15], v[160:163], v[0:3], a[0:15]\n\t"
    "v_cvt_pk_bf16_f32 v8, v8, v9\n\t"
    "v_cvt_pk_bf16_f32 v9, v10, v11\n\t"
    "v_cvt_pk_bf16_f32 v10, v12, v13\n\t"
    "v_cvt_pk_bf16_f32 v11, v14, v15\n\t"
    "v_exp_f32 v28, v28\n\t"
    "v_mfma_f32_32x32x16_bf16 a[16:31], v[160:163], v[16:19], a[16:31]\n\t"
    "v_exp_f32 v29, v29\n\t"
    "v_exp_f32 v30, v30\n\t"
    "v_add_f32 v230, v230, v28\n\t"
    "v_add_f32 v231, v231, v29\n\t"
    "v_mfma_f32_32x32x16_bf16 a[32:47], v[164:167], v[0:3], a[32:47]\n\t"
    "v_exp_f32 v31, v31\n\t"
    "v_add_f32 v230, v230, v30\n\t"
    "v_add_f32 v231, v231, v31\n\t"
    "v_cvt_pk_bf16_f32 v24, v24, v25\n\t"
    "v_cvt_pk_bf16_f32 v25, v26, v27\n\t"
    "v_mfma_f32_32x32x16_bf16 a[48:63], v[164:167], v[16:19], a[48:63]\n\t"
    "v_cvt_pk_bf16_f32 v26, v28, v29\n\t"
    "v_cvt_pk_bf16_f32 v27, v30, v31\n\t"
    "v_exp_f32 v32, v32\n\t"
    "v_exp_f32 v33, v33\n\t"
    "v_mfma_f32_32x32x16_bf16 a[64:79], v[168:171], v[0:3], a[64:79]\n\t"
    "v_exp_f32 v34, v34\n\t"
    "v_exp_f32 v35, v35\n\t"
    "v_add_f32 v228, v228, v32\n\t"
    "v_add_f32 v229, v229, v33\n\t"
    "v_mfma_f32_32x32x16_bf16 a[80:95], v[168:171], v[16:19], a[80:95]\n\t"
    "v_exp_f32 v36, v36\n\t"
    "v_exp_f32 v37, v37\n\t"
    "v_add_f32 v228, v228, v34\n\t"
    "v_add_f32 v229, v229, v35\n\t"
    "v_mfma_f32_32x32x16_bf16 a[96:111], v[172:175], v[0:3], a[96:111]\n\t"
    "s_waitcnt vmcnt(0)\n\t"
    "s_barrier\n\t"
    "s_cmp_eq_u32 s94, 16\n\t"
    "s_cbranch_scc0 Lpb_133_%=\n\t"
    "v_mov_b32 v241, %14\n\t"
    "s_mov_b64 exec, 1\n\t"
    "global_store_dword v241, v241, %18 sc1\n\t"
    "s_mov_b64 exec, -1\n\t"
    "s_nop 1\n\t"
    "Lpb_133_%=:\n\t"
    "v_mfma_f32_32x32x16_bf16 a[112:127], v[172:175], v[16:19], a[112:127]\n\t"
    "s_waitcnt lgkmcnt(8)\n\t"
    "v_exp_f32 v38, v38\n\t"
    "v_exp_f32 v39, v39\n\t"
    "v_add_f32 v228, v228, v36\n\t"
    "v_mfma_f32_32x32x16_bf16 a[0:15], v[176:179], v[8:11], a[0:15]\n\t"
    "v_add_f32 v229, v229, v37\n\t"
    "v_add_f32 v228, v228, v38\n\t"
    "v_add_f32 v229, v229, v39\n\t"
    "v_cvt_pk_bf16_f32 v32, v32, v33\n\t"
    "v_cvt_pk_bf16_f32 v33, v34, v35\n\t"
    "v_cvt_pk_bf16_f32 v34, v36, v37\n\t"
    "v_mfma_f32_32x32x16_bf16 a[16:31], v[176:179], v[24:27], a[16:31]\n\t"
    "v_cvt_pk_bf16_f32 v35, v38, v39\n\t"
    "v_exp_f32 v48, v48\n\t"
    "v_exp_f32 v49, v49\n\t"
    "v_add_f32 v230, v230, v48\n\t"
    "v_mfma_f32_32x32x16_bf16 a[32:47], v[180:183], v[8:11], a[32:47]\n\t"
    "v_exp_f32 v50, v50\n\t"
    "v_exp_f32 v51, v51\n\t"
    "v_add_f32 v231, v231, v49\n\t"
    "v_add_f32 v230, v230, v50\n\t"
    "v_mfma_f32_32x32x16_bf16 a[48:63], v[180:183], v[24:27], a[48:63]\n\t"
    "v_exp_f32 v52, v52\n\t"
    "v_exp_f32 v53, v53\n\t"
    "v_add_f32 v231, v231, v51\n\t"
    "v_add_f32 v230, v230, v52\n\t"
    "v_mfma_f32_32x32x16_bf16 a[64:79], v[184:187], v[8:11], a[64:79]\n\t"
    "v_exp_f32 v54, v54\n\t"
    "v_exp_f32 v55, v55\n\t"
    "v_add_f32 v231, v231, v53\n\t"
    "v_add_f32 v230, v230, v54\n\t"
    "v_mfma_f32_32x32x16_bf16 a[80:95], v[184:187], v[24:27], a[80:95]\n\t"
    "v_add_f32 v231, v231, v55\n\t"
    "v_cvt_pk_bf16_f32 v48, v48, v49\n\t"
    "v_cvt_pk_bf16_f32 v49, v50, v51\n\t"
    "v_cvt_pk_bf16_f32 v50, v52, v53\n\t"
    "v_cvt_pk_bf16_f32 v51, v54, v55\n\t"
    "v_mfma_f32_32x32x16_bf16 a[96:111], v[188:191], v[8:11], a[96:111]\n\t"
    "v_exp_f32 v40, v40\n\t"
    "v_exp_f32 v41, v41\n\t"
    "v_add_f32 v228, v228, v40\n\t"
    "v_add_f32 v229, v229, v41\n\t"
    "v_mfma_f32_32x32x16_bf16 a[112:127], v[188:191], v[24:27], a[112:127]\n\t"
    "s_waitcnt lgkmcnt(4)\n\t"
    "v_exp_f32 v42, v42\n\t"
    "v_exp_f32 v43, v43\n\t"
    "v_add_f32 v228, v228, v42\n\t"
    "v_mfma_f32_32x32x16_bf16 a[0:15], v[192:195], v[32:35], a[0:15]\n\t"
    "v_exp_f32 v44, v44\n\t"
    "v_exp_f32 v45, v45\n\t"
    "v_add_f32 v229, v229, v43\n\t"
    "v_add_f32 v228, v228, v44\n\t"
    "v_mfma_f32_32x32x16_bf16 a[16:31], v[192:195], v[48:51], a[16:31]\n\t"
    "v_exp_f32 v46, v46\n\t"
    "v_exp_f32 v47, v47\n\t"
    "v_add_f32 v229, v229, v45\n\t"
    "v_add_f32 v228, v228, v46\n\t"
    "v_mfma_f32_32x32x16_bf16 a[32:47], v[196:199], v[32:35], a[32:47]\n\t"
    "v_mov_b32 v243, %14\n\t"
    "v_and_b32 v243, -16, v243\n\t"
    "global_load_dwordx4 v[244:247], v243, %18 sc1\n\t"
    "v_add_f32 v229, v229, v47\n\t"
    "v_cvt_pk_bf16_f32 v40, v40, v41\n\t"
    "v_cvt_pk_bf16_f32 v41, v42, v43\n\t"
    "v_mfma_f32_32x32x16_bf16 a[48:63], v[196:199], v[48:51], a[48:63]\n\t"
    "v_cvt_pk_bf16_f32 v42, v44, v45\n\t"
    "v_cvt_pk_bf16_f32 v43, v46, v47\n\t"
    "v_exp_f32 v56, v56\n\t"
    "v_exp_f32 v57, v57\n\t"
    "v_mfma_f32_32x32x16_bf16 a[64:79], v[200:203], v[32:35], a[64:79]\n\t"
    "s_and_b32 s70, %14, 12\n\t"
    "s_lshl_b32 s70, s70, 14\n\t"
    "s_sub_u32 s44, s48, s70\n\t"
    "s_subb_u32 s45, s49, 0\n\t"
    "v_exp_f32 v58, v58\n\t"
    "v_mfma_f32_32x32x16_bf16 a[80:95], v[200:203], v[48:51], a[80:95]\n\t"
    "s_lshl_b32 s70, %10, 2\n\t"
    "s_add_u32 s70, s70, s89\n\t"
    "s_lshl_b32 s70, s70, 18\n\t"
    "v_exp_f32 v59, v59\n\t"
    "v_add_f32 v230, v230, v56\n\t"
    "v_mfma_f32_32x32x16_bf16 a[96:111], v[204:207], v[32:35], a[96:111]\n\t"
    "v_exp_f32 v60, v60\n\t"
    "v_exp_f32 v61, v61\n\t"
    "v_add_f32 v231, v231, v57\n\t"
    "v_add_f32 v230, v230, v58\n\t"
    "v_mfma_f32_32x32x16_bf16 a[112:127], v[204:207], v[48:51], a[112:127]\n\t"
    "s_waitcnt lgkmcnt(0)\n\t"
    "v_exp_f32 v62, v62\n\t"
    "v_exp_f32 v63, v63\n\t"
    "v_add_f32 v231, v231, v59\n\t"
    "v_add_f32 v230, v230, v60\n\t"
    "v_add_f32 v231, v231, v61\n\t"
    "v_add_f32 v230, v230, v62\n\t"
    "v_add_f32 v231, v231, v63\n\t"
    "v_cvt_pk_bf16_f32 v56, v56, v57\n\t"
    "v_cvt_pk_bf16_f32 v57, v58, v59\n\t"
    "v_cvt_pk_bf16_f32 v58, v60, v61\n\t"
    "v_cvt_pk_bf16_f32 v59, v62, v63\n\t"
    "s_nop 0\n\t"
    "s_nop 0\n\t"
    "v_mfma_f32_32x32x16_bf16 a[0:15], v[208:211], v[40:43], a[0:15]\n\t"
    "s_waitcnt vmcnt(0)\n\t"
    "s_barrier\n\t"
    "v_max_u32 v244, v244, v245\n\t"
    "v_max3_u32 v244, v244, v246, v247\n\t"
    "v_cmp_eq_u32 vcc, -1, v244\n\t"
    "s_nop 0\n\t"
    "s_cmp_eq_u64 vcc, 0\n\t"
    "s_cselect_b32 s46, 0xff2000, 0\n\t"
    "v_mfma_f32_32x32x16_bf16 a[16:31], v[208:211], v[56:59], a[16:31]\n\t"
    "s_lshl_b32 s71, %14, 14\n\t"
    "s_add_u32 s71, s71, 131072\n\t"
    "s_and_b32 s71, s71, 196608\n\t"
    "s_add_u32 s71, s71, s70\n\t"
    "buffer_load_dwordx4 a[128:131], v224, s[44:47], s71 offen offset:0 sc1\n\t"
    "v_mfma_f32_32x32x16_bf16 a[32:47], v[212:215], v[40:43], a[32:47]\n\t"
    "buffer_load_dwordx4 a[132:135], v224, s[44:47], s71 offen offset:1024 sc1\n\t"
    "v_mfma_f32_32x32x16_bf16 a[48:63], v[212:215], v[56:59], a[48:63]\n\t"
    "buffer_load_dwordx4 a[136:139], v224, s[44:47], s71 offen offset:2048 sc1\n\t"
    "v_mfma_f32_32x32x16_bf16 a[64:79], v[216:219], v[40:43], a[64:79]\n\t"
    "buffer_load_dwordx4 a[140:143], v224, s[44:47], s71 offen offset:3072 sc1\n\t"
    "v_mfma_f32_32x32x16_bf16 a[80:95], v[216:219], v[56:59], a[80:95]\n\t"
    "s_add_u32 s71, s71, 0x1000\n\t"
    "buffer_load_dwordx4 a[144:147], v224, s[44:47], s71 offen offset:0 sc1\n\t"
    "v_mfma_f32_32x32x16_bf16 a[96:111], v[220:223], v[40:43], a[96:111]\n\t"
    "buffer_load_dwordx4 a[148:151], v224, s[44:47], s71 offen offset:1024 sc1\n\t"
    "v_mfma_f32_32x32x16_bf16 a[112:127], v[220:223], v[56:59], a[112:127]\n\t"
    "buffer_load_dwordx4 a[152:155], v224, s[44:47], s71 offen offset:2048 sc1\n\t"
    "v_add_f32 v228, v228, v229\n\t"
    "v_add_f32 v230, v230, v231\n\t"
    "v_mov_b32 v240, v228\n\t"
    "s_nop 1\n\t"
    "v_permlane32_swap_b32 v228, v240\n\t"
    "s_nop 0\n\t"
    "v_add_f32 v228, v228, v240\n\t"
    "v_mov_b32 v241, v230\n\t"
    "s_nop 1\n\t"
    "v_permlane32_swap_b32 v230, v241\n\t"
    "s_nop 0\n\t"
    "v_add_f32 v230, v230, v241\n\t"
    "s_lshl_b32 s66, s89, 14\n\t"
    "s_xor_b32 s67, s66, 0x8000\n\t"
    "v_add_u32 v244, s66, v227\n\t"
    "v_add_u32 v245, s67, v227\n\t"
    "s_lshr_b32 s66, s66, 4\n\t"
    "s_lshr_b32 s67, s67, 4\n\t"
    "v_lshrrev_b32 v246, 2, v224\n\t"
    "v_add_u32 v246, 0x20000, v246\n\t"
    "v_add_u32 v247, s67, v246\n\t"
    "v_add_u32 v246, s66, v246\n\t"
    "ds_write_b32 v246, v232 offset:0\n\t"
    "ds_write_b32 v246, v233 offset:256\n\t"
    "ds_write_b32 v246, v228 offset:512\n\t"
    "ds_write_b32 v246, v230 offset:768\n\t"
    "s_lshr_b32 s73, s89, 1\n\t"
    "s_lshl_b32 s73, s73, 12\n\t"
    "s_add_u32 s73, s73, s88\n\t"
    "s_cmp_lg_u32 s46, 0\n\t"
    "s_cbranch_scc1 Lnorm_%=\n\t"
    "Llate_%=:\n\t"
    "s_cmp_ge_u32 s89, 2\n\t"
    "s_cbranch_scc1 La1_144_%=\n\t"
    "ds_write_b128 v244, a[64:67] offset:0\n\t"
    "ds_write_b128 v244, a[68:71] offset:1024\n\t"
    "v_mov_b32 v136, %14\n\t"
    "v_and_b32 v136, -16, v136\n\t"
    "global_load_dwordx4 v[248:251], v136, %18 sc1\n\t"
    "ds_write_b128 v244, a[72:75] offset:2048\n\t"
    "ds_write_b128 v244, a[76:79] offset:3072\n\t"
    "ds_write_b128 v244, a[80:83] offset:4096\n\t"
    "ds_write_b128 v244, a[84:87] offset:5120\n\t"
    "ds_write_b128 v244, a[88:91] offset:6144\n\t"
    "ds_write_b128 v244, a[92:95] offset:7168\n\t"
    "ds_write_b128 v244, a[96:99] offset:8192\n\t"
    "ds_write_b128 v244, a[100:103] offset:9216\n\t"
    "ds_write_b128 v244, a[104:107] offset:10240\n\t"
    "ds_write_b128 v244, a[108:111] offset:11264\n\t"
    "ds_write_b128 v244, a[112:115] offset:12288\n\t"
    "ds_write_b128 v244, a[116:119] offset:13312\n\t"
    "ds_write_b128 v244, a[120:123] offset:14336\n\t"
    "ds_write_b128 v244, a[124:127] offset:15360\n\t"
    "s_branch La2_145_%=\n\t"
    "La1_144_%=:\n\t"
    "ds_write_b128 v244, a[0:3] offset:0\n\t"
    "ds_write_b128 v244, a[4:7] offset:1024\n\t"
    "v_mov_b32 v136, %14\n\t"
    "v_and_b32 v136, -16, v136\n\t"
    "global_load_dwordx4 v[248:251], v136, %18 sc1\n\t"
    "ds_write_b128 v244, a[8:11] offset:2048\n\t"
    "ds_write_b128 v244, a[12:15] offset:3072\n\t"
    "ds_write_b128 v244, a[16:19] offset:4096\n\t"
    "ds_write_b128 v244, a[20:23] offset:5120\n\t"
    "ds_write_b128 v244, a[24:27] offset:6144\n\t"
    "ds_write_b128 v244, a[28:31] offset:7168\n\t"
    "ds_write_b128 v244, a[32:35] offset:8192\n\t"
    "ds_write_b128 v244, a[36:39] offset:9216\n\t"
    "ds_write_b128 v244, a[40:43] offset:10240\n\t"
    "ds_write_b128 v244, a[44:47] offset:11264\n\t"
    "ds_write_b128 v244, a[48:51] offset:12288\n\t"
    "ds_write_b128 v244, a[52:55] offset:13312\n\t"
    "ds_write_b128 v244, a[56:59] offset:14336\n\t"
    "ds_write_b128 v244, a[60:63] offset:15360\n\t"
    "La2_145_%=:\n\t"
    "s_waitcnt lgkmcnt(0)\n\t"
    "s_barrier\n\t"
    "s_waitcnt vmcnt(0)\n\t"
    "v_max_u32 v248, v248, v249\n\t"
    "v_max3_u32 v248, v248, v250, v251\n\t"
    "v_cmp_eq_u32 vcc, -1, v248\n\t"
    "s_nop 0\n\t"
    "s_cmp_eq_u64 vcc, 0\n\t"
    "s_cselect_b32 s46, 0xff2000, 0\n\t"
    "s_cbranch_scc0 Llate2_134_%=\n\t"
    "ds_read_b32 v240, v247 offset:0\n\t"
    "ds_read_b32 v241, v247 offset:256\n\t"
    "ds_read_b32 v242, v247 offset:512\n\t"
    "ds_read_b32 v243, v247 offset:768\n\t"
    "ds_read_b128 v[0:3], v245 offset:0\n\t"
    "ds_read_b128 v[4:7], v245 offset:1024\n\t"
    "ds_read_b128 v[8:11], v245 offset:2048\n\t"
    "ds_read_b128 v[12:15], v245 offset:3072\n\t"
    "ds_read_b128 v[16:19], v245 offset:4096\n\t"
    "ds_read_b128 v[20:23], v245 offset:5120\n\t"
    "ds_read_b128 v[24:27], v245 offset:6144\n\t"
    "ds_read_b128 v[28:31], v245 offset:7168\n\t"
    "ds_read_b128 v[32:35], v245 offset:8192\n\t"
    "ds_read_b128 v[36:39], v245 offset:9216\n\t"
    "ds_read_b128 v[40:43], v245 offset:10240\n\t"
    "ds_read_b128 v[44:47], v245 offset:11264\n\t"
    "ds_read_b128 v[48:51], v245 offset:12288\n\t"
    "ds_read_b128 v[52:55], v245 offset:13312\n\t"
    "ds_read_b128 v[56:59], v245 offset:14336\n\t"
    "ds_read_b128 v[60:63], v245 offset:15360\n\t"
    "s_cmp_ge_u32 s89, 2\n\t"
    "s_cbranch_scc1 La1_146_%=\n\t"
    "v_accvgpr_read_b32 v64, a0\n\t"
    "s_lshl_b32 s71, %14, 14\n\t"
    "s_add_u32 s71, s71, 131072\n\t"
    "s_and_b32 s71, s71, 196608\n\t"
    "s_add_u32 s71, s71, s70\n\t"
    "buffer_load_dwordx4 a[128:131], v224, s[44:47], s71 offen offset:0 sc1\n\t"
    "v_accvgpr_read_b32 v65, a1\n\t"
    "v_accvgpr_read_b32 v66, a2\n\t"
    "v_accvgpr_read_b32 v67, a3\n\t"
    "v_accvgpr_read_b32 v68, a4\n\t"
    "buffer_load_dwordx4 a[132:135], v224, s[44:47], s71 offen offset:1024 sc1\n\t"
    "v_accvgpr_read_b32 v69, a5\n\t"
    "v_accvgpr_read_b32 v70, a6\n\t"
    "v_accvgpr_read_b32 v71, a7\n\t"
    "v_accvgpr_read_b32 v72, a8\n\t"
    "buffer_load_dwordx4 a[136:139], v224, s[44:47], s71 offen offset:2048 sc1\n\t"
    "v_accvgpr_read_b32 v73, a9\n\t"
    "v_accvgpr_read_b32 v74, a10\n\t"
    "v_accvgpr_read_b32 v75, a11\n\t"
    "v_accvgpr_read_b32 v76, a12\n\t"
    "buffer_load_dwordx4 a[140:143], v224, s[44:47], s71 offen offset:3072 sc1\n\t"
    "v_accvgpr_read_b32 v77, a13\n\t"
    "v_accvgpr_read_b32 v78, a14\n\t"
    "v_accvgpr_read_b32 v79, a15\n\t"
    "v_accvgpr_read_b32 v80, a16\n\t"
    "s_add_u32 s71, s71, 0x1000\n\t"
    "buffer_load_dwordx4 a[144:147], v224, s[44:47], s71 offen offset:0 sc1\n\t"
    "v_accvgpr_read_b32 v81, a17\n\t"
    "v_accvgpr_read_b32 v82, a18\n\t"
    "v_accvgpr_read_b32 v83, a19\n\t"
    "v_accvgpr_read_b32 v84, a20\n\t"
    "buffer_load_dwordx4 a[148:151], v224, s[44:47], s71 offen offset:1024 sc1\n\t"
    "v_accvgpr_read_b32 v85, a21\n\t"
    "v_accvgpr_read_b32 v86, a22\n\t"
    "v_accvgpr_read_b32 v87, a23\n\t"
    "v_accvgpr_read_b32 v88, a24\n\t"
    "buffer_load_dwordx4 a[152:155], v224, s[44:47], s71 offen offset:2048 sc1\n\t"
    "v_accvgpr_read_b32 v89, a25\n\t"
    "v_accvgpr_read_b32 v90, a26\n\t"
    "v_accvgpr_read_b32 v91, a27\n\t"
    "v_accvgpr_read_b32 v92, a28\n\t"
    "buffer_load_dwordx4 a[156:159], v224, s[44:47], s71 offen offset:3072 sc1\n\t"
    "v_accvgpr_read_b32 v93, a29\n\t"
    "v_accvgpr_read_b32 v94, a30\n\t"
    "v_accvgpr_read_b32 v95, a31\n\t"
    "v_accvgpr_read_b32 v96, a32\n\t"
    "s_lshl_b32 s71, %14, 14\n\t"
    "s_add_u32 s71, s71, 196608\n\t"
    "s_and_b32 s71, s71, 196608\n\t"
    "s_add_u32 s71, s71, s70\n\t"
    "buffer_load_dwordx4 a[160:163], v224, s[44:47], s71 offen offset:0 sc1\n\t"
    "v_accvgpr_read_b32 v97, a33\n\t"
    "v_accvgpr_read_b32 v98, a34\n\t"
    "v_accvgpr_read_b32 v99, a35\n\t"
    "v_accvgpr_read_b32 v100, a36\n\t"
    "buffer_load_dwordx4 a[164:167], v224, s[44:47], s71 offen offset:1024 sc1\n\t"
    "v_accvgpr_read_b32 v101, a37\n\t"
    "v_accvgpr_read_b32 v102, a38\n\t"
    "v_accvgpr_read_b32 v103, a39\n\t"
    "v_accvgpr_read_b32 v104, a40\n\t"
    "buffer_load_dwordx4 a[168:171], v224, s[44:47], s71 offen offset:2048 sc1\n\t"
    "v_accvgpr_read_b32 v105, a41\n\t"
    "v_accvgpr_read_b32 v106, a42\n\t"
    "v_accvgpr_read_b32 v107, a43\n\t"
    "v_accvgpr_read_b32 v108, a44\n\t"
    "buffer_load_dwordx4 a[172:175], v224, s[44:47], s71 offen offset:3072 sc1\n\t"
    "v_accvgpr_read_b32 v109, a45\n\t"
    "v_accvgpr_read_b32 v110, a46\n\t"
    "v_accvgpr_read_b32 v111, a47\n\t"
    "v_accvgpr_read_b32 v112, a48\n\t"
    "s_add_u32 s71, s71, 0x1000\n\t"
    "buffer_load_dwordx4 a[176:179], v224, s[44:47], s71 offen offset:0 sc1\n\t"
    "v_accvgpr_read_b32 v113, a49\n\t"
    "v_accvgpr_read_b32 v114, a50\n\t"
    "v_accvgpr_read_b32 v115, a51\n\t"
    "v_accvgpr_read_b32 v116, a52\n\t"
    "buffer_load_dwordx4 a[180:183], v224, s[44:47], s71 offen offset:1024 sc1\n\t"
    "v_accvgpr_read_b32 v117, a53\n\t"
    "v_accvgpr_read_b32 v118, a54\n\t"
    "v_accvgpr_read_b32 v119, a55\n\t"
    "v_accvgpr_read_b32 v120, a56\n\t"
    "buffer_load_dwordx4 a[184:187], v224, s[44:47], s71 offen offset:2048 sc1\n\t"
    "v_accvgpr_read_b32 v121, a57\n\t"
    "v_accvgpr_read_b32 v122, a58\n\t"
    "v_accvgpr_read_b32 v123, a59\n\t"
    "v_accvgpr_read_b32 v124, a60\n\t"
    "buffer_load_dwordx4 a[188:191], v224, s[44:47], s71 offen offset:3072 sc1\n\t"
    "v_accvgpr_read_b32 v125, a61\n\t"
    "v_accvgpr_read_b32 v126, a62\n\t"
    "v_accvgpr_read_b32 v127, a63\n\t"
    "s_branch La2_147_%=\n\t"
    "La1_146_%=:\n\t"
    "v_accvgpr_read_b32 v64, a64\n\t"
    "s_lshl_b32 s71, %14, 14\n\t"
    "s_add_u32 s71, s71, 131072\n\t"
    "s_and_b32 s71, s71, 196608\n\t"
    "s_add_u32 s71, s71, s70\n\t"
    "buffer_load_dwordx4 a[128:131], v224, s[44:47], s71 offen offset:0 sc1\n\t"
    "v_accvgpr_read_b32 v65, a65\n\t"
    "v_accvgpr_read_b32 v66, a66\n\t"
    "v_accvgpr_read_b32 v67, a67\n\t"
    "v_accvgpr_read_b32 v68, a68\n\t"
    "buffer_load_dwordx4 a[132:135], v224, s[44:47], s71 offen offset:1024 sc1\n\t"
    "v_accvgpr_read_b32 v69, a69\n\t"
    "v_accvgpr_read_b32 v70, a70\n\t"
    "v_accvgpr_read_b32 v71, a71\n\t"
    "v_accvgpr_read_b32 v72, a72\n\t"
    "buffer_load_dwordx4 a[136:139], v224, s[44:47], s71 offen offset:2048 sc1\n\t"
    "v_accvgpr_read_b32 v73, a73\n\t"
    "v_accvgpr_read_b32 v74, a74\n\t"
    "v_accvgpr_read_b32 v75, a75\n\t"
    "v_accvgpr_read_b32 v76, a76\n\t"
    "buffer_load_dwordx4 a[140:143], v224, s[44:47], s71 offen offset:3072 sc1\n\t"
    "v_accvgpr_read_b32 v77, a77\n\t"
    "v_accvgpr_read_b32 v78, a78\n\t"
    "v_accvgpr_read_b32 v79, a79\n\t"
    "v_accvgpr_read_b32 v80, a80\n\t"
    "s_add_u32 s71, s71, 0x1000\n\t"
    "buffer_load_dwordx4 a[144:147], v224, s[44:47], s71 offen offset:0 sc1\n\t"
    "v_accvgpr_read_b32 v81, a81\n\t"
    "v_accvgpr_read_b32 v82, a82\n\t"
    "v_accvgpr_read_b32 v83, a83\n\t"
    "v_accvgpr_read_b32 v84, a84\n\t"
    "buffer_load_dwordx4 a[148:151], v224, s[44:47], s71 offen offset:1024 sc1\n\t"
    "v_accvgpr_read_b32 v85, a85\n\t"
    "v_accvgpr_read_b32 v86, a86\n\t"
    "v_accvgpr_read_b32 v87, a87\n\t"
    "v_accvgpr_read_b32 v88, a88\n\t"
    "buffer_load_dwordx4 a[152:155], v224, s[44:47], s71 offen offset:2048 sc1\n\t"
    "v_accvgpr_read_b32 v89, a89\n\t"
    "v_accvgpr_read_b32 v90, a90\n\t"
    "v_accvgpr_read_b32 v91, a91\n\t"
    "v_accvgpr_read_b32 v92, a92\n\t"
    "buffer_load_dwordx4 a[156:159], v224, s[44:47], s71 offen offset:3072 sc1\n\t"
    "v_accvgpr_read_b32 v93, a93\n\t"
    "v_accvgpr_read_b32 v94, a94\n\t"
    "v_accvgpr_read_b32 v95, a95\n\t"
    "v_accvgpr_read_b32 v96, a96\n\t"
    "s_lshl_b32 s71, %14, 14\n\t"
    "s_add_u32 s71, s71, 196608\n\t"
    "s_and_b32 s71, s71, 196608\n\t"
    "s_add_u32 s71, s71, s70\n\t"
    "buffer_load_dwordx4 a[160:163], v224, s[44:47], s71 offen offset:0 sc1\n\t"
    "v_accvgpr_read_b32 v97, a97\n\t"
    "v_accvgpr_read_b32 v98, a98\n\t"
    "v_accvgpr_read_b32 v99, a99\n\t"
    "v_accvgpr_read_b32 v100, a100\n\t"
    "buffer_load_dwordx4 a[164:167], v224, s[44:47], s71 offen offset:1024 sc1\n\t"
    "v_accvgpr_read_b32 v101, a101\n\t"
    "v_accvgpr_read_b32 v102, a102\n\t"
    "v_accvgpr_read_b32 v103, a103\n\t"
    "v_accvgpr_read_b32 v104, a104\n\t"
    "buffer_load_dwordx4 a[168:171], v224, s[44:47], s71 offen offset:2048 sc1\n\t"
    "v_accvgpr_read_b32 v105, a105\n\t"
    "v_accvgpr_read_b32 v106, a106\n\t"
    "v_accvgpr_read_b32 v107, a107\n\t"
    "v_accvgpr_read_b32 v108, a108\n\t"
    "buffer_load_dwordx4 a[172:175], v224, s[44:47], s71 offen offset:3072 sc1\n\t"
    "v_accvgpr_read_b32 v109, a109\n\t"
    "v_accvgpr_read_b32 v110, a110\n\t"
    "v_accvgpr_read_b32 v111, a111\n\t"
    "v_accvgpr_read_b32 v112, a112\n\t"
    "s_add_u32 s71, s71, 0x1000\n\t"
    "buffer_load_dwordx4 a[176:179], v224, s[44:47], s71 offen offset:0 sc1\n\t"
    "v_accvgpr_read_b32 v113, a113\n\t"
    "v_accvgpr_read_b32 v114, a114\n\t"
    "v_accvgpr_read_b32 v115, a115\n\t"
    "v_accvgpr_read_b32 v116, a116\n\t"
    "buffer_load_dwordx4 a[180:183], v224, s[44:47], s71 offen offset:1024 sc1\n\t"
    "v_accvgpr_read_b32 v117, a117\n\t"
    "v_accvgpr_read_b32 v118, a118\n\t"
    "v_accvgpr_read_b32 v119, a119\n\t"
    "v_accvgpr_read_b32 v120, a120\n\t"
    "buffer_load_dwordx4 a[184:187], v224, s[44:47], s71 offen offset:2048 sc1\n\t"
    "v_accvgpr_read_b32 v121, a121\n\t"
    "v_accvgpr_read_b32 v122, a122\n\t"
    "v_accvgpr_read_b32 v123, a123\n\t"
    "v_accvgpr_read_b32 v124, a124\n\t"
    "buffer_load_dwordx4 a[188:191], v224, s[44:47], s71 offen offset:3072 sc1\n\t"
    "v_accvgpr_read_b32 v125, a125\n\t"
    "v_accvgpr_read_b32 v126, a126\n\t"
    "v_accvgpr_read_b32 v127, a127\n\t"
    "La2_147_%=:\n\t"
    "s_waitcnt lgkmcnt(0)\n\t"
    "s_lshl_b32 s71, %14, 14\n\t"
    "s_add_u32 s71, s71, 262144\n\t"
    "s_and_b32 s71, s71, 196608\n\t"
    "s_add_u32 s71, s71, s70\n\t"
    "buffer_load_dwordx4 a[192:195], v224, s[44:47], s71 offen offset:0 sc1\n\t"
    "v_max_f32 v238, v232, v240\n\t"
    "v_sub_f32 v128, v232, v238\n\t"
    "v_sub_f32 v130, v240, v238\n\t"
    "v_exp_f32 v128, v128\n\t"
    "v_exp_f32 v130, v130\n\t"
    "s_nop 0\n\t"
    "v_mul_f32 v238, v128, v228\n\t"
    "buffer_load_dwordx4 a[196:199], v224, s[44:47], s71 offen offset:1024 sc1\n\t"
    "v_fma_f32 v238, v130, v242, v238\n\t"
    "v_rcp_f32 v238, v238\n\t"
    "s_nop 0\n\t"
    "v_mul_f32 v128, v128, v238\n\t"
    "v_mul_f32 v130, v130, v238\n\t"
    "v_max_f32 v238, v233, v241\n\t"
    "v_sub_f32 v132, v233, v238\n\t"
    "buffer_load_dwordx4 a[200:203], v224, s[44:47], s71 offen offset:2048 sc1\n\t"
    "v_sub_f32 v134, v241, v238\n\t"
    "v_exp_f32 v132, v132\n\t"
    "v_exp_f32 v134, v134\n\t"
    "s_nop 0\n\t"
    "v_mul_f32 v238, v132, v230\n\t"
    "v_fma_f32 v238, v134, v243, v238\n\t"
    "v_rcp_f32 v238, v238\n\t"
    "buffer_load_dwordx4 a[204:207], v224, s[44:47], s71 offen offset:3072 sc1\n\t"
    "s_nop 0\n\t"
    "v_mul_f32 v132, v132, v238\n\t"
    "v_mul_f32 v134, v134, v238\n\t"
    "s_add_u32 s66, s73, 0\n\t"
    "v_pk_mul_f32 v[0:1], v[0:1], v[130:131] op_sel_hi:[1,0]\n\t"
    "v_pk_mul_f32 v[2:3], v[2:3], v[130:131] op_sel_hi:[1,0]\n\t"
    "v_pk_mul_f32 v[4:5], v[4:5], v[130:131] op_sel_hi:[1,0]\n\t"
    "s_add_u32 s71, s71, 0x1000\n\t"
    "buffer_load_dwordx4 a[208:211], v224, s[44:47], s71 offen offset:0 sc1\n\t"
    "v_pk_mul_f32 v[6:7], v[6:7], v[130:131] op_sel_hi:[1,0]\n\t"
    "v_pk_mul_f32 v[8:9], v[8:9], v[130:131] op_sel_hi:[1,0]\n\t"
    "v_pk_mul_f32 v[10:11], v[10:11], v[130:131] op_sel_hi:[1,0]\n\t"
    "v_pk_mul_f32 v[12:13], v[12:13], v[130:131] op_sel_hi:[1,0]\n\t"
    "v_pk_mul_f32 v[14:15], v[14:15], v[130:131] op_sel_hi:[1,0]\n\t"
    "v_pk_fma_f32 v[0:1], v[64:65], v[128:129], v[0:1] op_sel_hi:[1,0,1]\n\t"
    "v_pk_fma_f32 v[2:3], v[66:67], v[128:129], v[2:3] op_sel_hi:[1,0,1]\n\t"
    "buffer_load_dwordx4 a[212:215], v224, s[44:47], s71 offen offset:1024 sc1\n\t"
    "v_pk_fma_f32 v[4:5], v[68:69], v[128:129], v[4:5] op_sel_hi:[1,0,1]\n\t"
    "v_pk_fma_f32 v[6:7], v[70:71], v[128:129], v[6:7] op_sel_hi:[1,0,1]\n\t"
    "v_pk_fma_f32 v[8:9], v[72:73], v[128:129], v[8:9] op_sel_hi:[1,0,1]\n\t"
    "v_pk_fma_f32 v[10:11], v[74:75], v[128:129], v[10:11] op_sel_hi:[1,0,1]\n\t"
    "v_pk_fma_f32 v[12:13], v[76:77], v[128:129], v[12:13] op_sel_hi:[1,0,1]\n\t"
    "v_pk_fma_f32 v[14:15], v[78:79], v[128:129], v[14:15] op_sel_hi:[1,0,1]\n\t"
    "v_cvt_pk_bf16_f32 v0, v0, v1\n\t"
    "buffer_load_dwordx4 a[216:219], v224, s[44:47], s71 offen offset:2048 sc1\n\t"
    "v_cvt_pk_bf16_f32 v1, v2, v3\n\t"
    "v_cvt_pk_bf16_f32 v2, v4, v5\n\t"
    "v_cvt_pk_bf16_f32 v3, v6, v7\n\t"
    "v_cvt_pk_bf16_f32 v8, v8, v9\n\t"
    "v_cvt_pk_bf16_f32 v9, v10, v11\n\t"
    "v_cvt_pk_bf16_f32 v10, v12, v13\n\t"
    "v_cvt_pk_bf16_f32 v11, v14, v15\n\t"
    "buffer_store_dwordx4 v[0:3], v224, s[48:51], s66 offen offset:0 sc1\n\t"
    "buffer_store_dwordx4 v[8:11], v224, s[48:51], s66 offen offset:1024 sc1\n\t"
    "buffer_load_dwordx4 a[220:223], v224, s[44:47], s71 offen offset:3072 sc1\n\t"
    "v_pk_mul_f32 v[32:33], v[32:33], v[130:131] op_sel_hi:[1,0]\n\t"
    "v_pk_mul_f32 v[34:35], v[34:35], v[130:131] op_sel_hi:[1,0]\n\t"
    "v_pk_mul_f32 v[36:37], v[36:37], v[130:131] op_sel_hi:[1,0]\n\t"
    "v_pk_mul_f32 v[38:39], v[38:39], v[130:131] op_sel_hi:[1,0]\n\t"
    "v_pk_mul_f32 v[40:41], v[40:41], v[130:131] op_sel_hi:[1,0]\n\t"
    "v_pk_mul_f32 v[42:43], v[42:43], v[130:131] op_sel_hi:[1,0]\n\t"
    "v_pk_mul_f32 v[44:45], v[44:45], v[130:131] op_sel_hi:[1,0]\n\t"
    "s_lshl_b32 s71, %14, 14\n\t"
    "s_add_u32 s71, s71, 327680\n\t"
    "s_and_b32 s71, s71, 196608\n\t"
    "s_add_u32 s71, s71, s70\n\t"
    "buffer_load_dwordx4 a[224:227], v224, s[44:47], s71 offen offset:0 sc1\n\t"
    "v_pk_mul_f32 v[46:47], v[46:47], v[130:131] op_sel_hi:[1,0]\n\t"
    "v_pk_fma_f32 v[32:33], v[96:97], v[128:129], v[32:33] op_sel_hi:[1,0,1]\n\t"
    "v_pk_fma_f32 v[34:35], v[98:99], v[128:129], v[34:35] op_sel_hi:[1,0,1]\n\t"
    "v_pk_fma_f32 v[36:37], v[100:101], v[128:129], v[36:37] op_sel_hi:[1,0,1]\n\t"
    "v_pk_fma_f32 v[38:39], v[102:103], v[128:129], v[38:39] op_sel_hi:[1,0,1]\n\t"
    "v_pk_fma_f32 v[40:41], v[104:105], v[128:129], v[40:41] op_sel_hi:[1,0,1]\n\t"
    "v_pk_fma_f32 v[42:43], v[106:107], v[128:129], v[42:43] op_sel_hi:[1,0,1]\n\t"
    "buffer_load_dwordx4 a[228:231], v224, s[44:47], s71 offen offset:1024 sc1\n\t"
    "v_pk_fma_f32 v[44:45], v[108:109], v[128:129], v[44:45] op_sel_hi:[1,0,1]\n\t"
    "v_pk_fma_f32 v[46:47], v[110:111], v[128:129], v[46:47] op_sel_hi:[1,0,1]\n\t"
    "v_cvt_pk_bf16_f32 v32, v32, v33\n\t"
    "v_cvt_pk_bf16_f32 v33, v34, v35\n\t"
    "v_cvt_pk_bf16_f32 v34, v36, v37\n\t"
    "v_cvt_pk_bf16_f32 v35, v38, v39\n\t"
    "v_cvt_pk_bf16_f32 v40, v40, v41\n\t"
    "buffer_load_dwordx4 a[232:235], v224, s[44:47], s71 offen offset:2048 sc1\n\t"
    "v_cvt_pk_bf16_f32 v41, v42, v43\n\t"
    "v_cvt_pk_bf16_f32 v42, v44, v45\n\t"
    "v_cvt_pk_bf16_f32 v43, v46, v47\n\t"
    "buffer_store_dwordx4 v[32:35], v224, s[48:51], s66 offen offset:2048 sc1\n\t"
    "buffer_store_dwordx4 v[40:43], v224, s[48:51], s66 offen offset:3072 sc1\n\t"
    "s_add_u32 s66, s73, 262144\n\t"
    "v_pk_mul_f32 v[16:17], v[16:17], v[134:135] op_sel_hi:[1,0]\n\t"
    "v_pk_mul_f32 v[18:19], v[18:19], v[134:135] op_sel_hi:[1,0]\n\t"
    "v_pk_mul_f32 v[20:21], v[20:21], v[134:135] op_sel_hi:[1,0]\n\t"
    "buffer_load_dwordx4 a[236:239], v224, s[44:47], s71 offen offset:3072 sc1\n\t"
    "v_pk_mul_f32 v[22:23], v[22:23], v[134:135] op_sel_hi:[1,0]\n\t"
    "v_pk_mul_f32 v[24:25], v[24:25], v[134:135] op_sel_hi:[1,0]\n\t"
    "v_pk_mul_f32 v[26:27], v[26:27], v[134:135] op_sel_hi:[1,0]\n\t"
    "v_pk_mul_f32 v[28:29], v[28:29], v[134:135] op_sel_hi:[1,0]\n\t"
    "v_pk_mul_f32 v[30:31], v[30:31], v[134:135] op_sel_hi:[1,0]\n\t"
    "v_pk_fma_f32 v[16:17], v[80:81], v[132:133], v[16:17] op_sel_hi:[1,0,1]\n\t"
    "v_pk_fma_f32 v[18:19], v[82:83], v[132:133], v[18:19] op_sel_hi:[1,0,1]\n\t"
    "s_add_u32 s71, s71, 0x1000\n\t"
    "buffer_load_dwordx4 a[240:243], v224, s[44:47], s71 offen offset:0 sc1\n\t"
    "v_pk_fma_f32 v[20:21], v[84:85], v[132:133], v[20:21] op_sel_hi:[1,0,1]\n\t"
    "v_pk_fma_f32 v[22:23], v[86:87], v[132:133], v[22:23] op_sel_hi:[1,0,1]\n\t"
    "v_pk_fma_f32 v[24:25], v[88:89], v[132:133], v[24:25] op_sel_hi:[1,0,1]\n\t"
    "v_pk_fma_f32 v[26:27], v[90:91], v[132:133], v[26:27] op_sel_hi:[1,0,1]\n\t"
    "v_pk_fma_f32 v[28:29], v[92:93], v[132:133], v[28:29] op_sel_hi:[1,0,1]\n\t"
    "v_pk_fma_f32 v[30:31], v[94:95], v[132:133], v[30:31] op_sel_hi:[1,0,1]\n\t"
    "v_cvt_pk_bf16_f32 v16, v16, v17\n\t"
    "buffer_load_dwordx4 a[244:247], v224, s[44:47], s71 offen offset:1024 sc1\n\t"
    "v_cvt_pk_bf16_f32 v17, v18, v19\n\t"
    "v_cvt_pk_bf16_f32 v18, v20, v21\n\t"
    "v_cvt_pk_bf16_f32 v19, v22, v23\n\t"
    "v_cvt_pk_bf16_f32 v24, v24, v25\n\t"
    "v_cvt_pk_bf16_f32 v25, v26, v27\n\t"
    "v_cvt_pk_bf16_f32 v26, v28, v29\n\t"
    "v_cvt_pk_bf16_f32 v27, v30, v31\n\t"
    "buffer_store_dwordx4 v[16:19], v224, s[48:51], s66 offen offset:0 sc1\n\t"
    "buffer_store_dwordx4 v[24:27], v224, s[48:51], s66 offen offset:1024 sc1\n\t"
    "buffer_load_dwordx4 a[248:251], v224, s[44:47], s71 offen offset:2048 sc1\n\t"
    "v_pk_mul_f32 v[48:49], v[48:49], v[134:135] op_sel_hi:[1,0]\n\t"
    "v_pk_mul_f32 v[50:51], v[50:51], v[134:135] op_sel_hi:[1,0]\n\t"
    "v_pk_mul_f32 v[52:53], v[52:53], v[134:135] op_sel_hi:[1,0]\n\t"
    "v_pk_mul_f32 v[54:55], v[54:55], v[134:135] op_sel_hi:[1,0]\n\t"
    "v_pk_mul_f32 v[56:57], v[56:57], v[134:135] op_sel_hi:[1,0]\n\t"
    "v_pk_mul_f32 v[58:59], v[58:59], v[134:135] op_sel_hi:[1,0]\n\t"
    "v_pk_mul_f32 v[60:61], v[60:61], v[134:135] op_sel_hi:[1,0]\n\t"
    "buffer_load_dwordx4 a[252:255], v224, s[44:47], s71 offen offset:3072 sc1\n\t"
    "v_pk_mul_f32 v[62:63], v[62:63], v[134:135] op_sel_hi:[1,0]\n\t"
    "v_pk_fma_f32 v[48:49], v[112:113], v[132:133], v[48:49] op_sel_hi:[1,0,1]\n\t"
    "v_pk_fma_f32 v[50:51], v[114:115], v[132:133], v[50:51] op_sel_hi:[1,0,1]\n\t"
    "v_pk_fma_f32 v[52:53], v[116:117], v[132:133], v[52:53] op_sel_hi:[1,0,1]\n\t"
    "v_pk_fma_f32 v[54:55], v[118:119], v[132:133], v[54:55] op_sel_hi:[1,0,1]\n\t"
    "v_pk_fma_f32 v[56:57], v[120:121], v[132:133], v[56:57] op_sel_hi:[1,0,1]\n\t"
    "v_pk_fma_f32 v[58:59], v[122:123], v[132:133], v[58:59] op_sel_hi:[1,0,1]\n\t"
    "v_pk_fma_f32 v[60:61], v[124:125], v[132:133], v[60:61] op_sel_hi:[1,0,1]\n\t"
    "v_pk_fma_f32 v[62:63], v[126:127], v[132:133], v[62:63] op_sel_hi:[1,0,1]\n\t"
    "v_cvt_pk_bf16_f32 v48, v48, v49\n\t"
    "v_cvt_pk_bf16_f32 v49, v50, v51\n\t"
    "v_cvt_pk_bf16_f32 v50, v52, v53\n\t"
    "v_cvt_pk_bf16_f32 v51, v54, v55\n\t"
    "v_cvt_pk_bf16_f32 v56, v56, v57\n\t"
    "v_cvt_pk_bf16_f32 v57, v58, v59\n\t"
    "v_cvt_pk_bf16_f32 v58, v60, v61\n\t"
    "v_cvt_pk_bf16_f32 v59, v62, v63\n\t"
    "buffer_store_dwordx4 v[48:51], v224, s[48:51], s66 offen offset:2048 sc1\n\t"
    "buffer_store_dwordx4 v[56:59], v224, s[48:51], s66 offen offset:3072 sc1\n\t"
    "LEND_%=:\n\t"
    "s_cmp_lg_u32 s46, 0\n\t"
    "s_cbranch_scc1 Ldn_154_%=\n\t"
    "s_mov_b32 s72, 0x4000\n\t"
    "s_waitcnt vmcnt(8)\n\t"
    "Llp_155_%=:\n\t"
    "v_max_u32 v248, v248, v249\n\t"
    "v_max3_u32 v248, v248, v250, v251\n\t"
    "v_cmp_eq_u32 vcc, -1, v248\n\t"
    "s_cbranch_vccz Lok_156_%=\n\t"
    "s_sleep 1\n\t"
    "v_mov_b32 v136, %14\n\t"
    "v_and_b32 v136, -16, v136\n\t"
    "global_load_dwordx4 v[248:251], v136, %18 sc1\n\t"
    "s_waitcnt vmcnt(0)\n\t"
    "s_sub_u32 s72, s72, 1\n\t"
    "s_cmp_lg_u32 s72, 0\n\t"
    "s_cbranch_scc1 Llp_155_%=\n\t"
    "Lok_156_%=:\n\t"
    "s_mov_b32 s46, 0xff2000\n\t"
    "s_lshl_b32 s71, %14, 14\n\t"
    "s_add_u32 s71, s71, 131072\n\t"
    "s_and_b32 s71, s71, 196608\n\t"
    "s_add_u32 s71, s71, s70\n\t"
    "buffer_load_dwordx4 a[128:131], v224, s[44:47], s71 offen offset:0 sc1\n\t"
    "buffer_load_dwordx4 a[132:135], v224, s[44:47], s71 offen offset:1024 sc1\n\t"
    "buffer_load_dwordx4 a[136:139], v224, s[44:47], s71 offen offset:2048 sc1\n\t"
    "buffer_load_dwordx4 a[140:143], v224, s[44:47], s71 offen offset:3072 sc1\n\t"
    "s_add_u32 s71, s71, 0x1000\n\t"
    "buffer_load_dwordx4 a[144:147], v224, s[44:47], s71 offen offset:0 sc1\n\t"
    "buffer_load_dwordx4 a[148:151], v224, s[44:47], s71 offen offset:1024 sc1\n\t"
    "buffer_load_dwordx4 a[152:155], v224, s[44:47], s71 offen offset:2048 sc1\n\t"
    "buffer_load_dwordx4 a[156:159], v224, s[44:47], s71 offen offset:3072 sc1\n\t"
    "s_lshl_b32 s71, %14, 14\n\t"
    "s_add_u32 s71, s71, 196608\n\t"
    "s_and_b32 s71, s71, 196608\n\t"
    "s_add_u32 s71, s71, s70\n\t"
    "buffer_load_dwordx4 a[160:163], v224, s[44:47], s71 offen offset:0 sc1\n\t"
    "buffer_load_dwordx4 a[164:167], v224, s[44:47], s71 offen offset:1024 sc1\n\t"
    "buffer_load_dwordx4 a[168:171], v224, s[44:47], s71 offen offset:2048 sc1\n\t"
    "buffer_load_dwordx4 a[172:175], v224, s[44:47], s71 offen offset:3072 sc1\n\t"
    "s_add_u32 s71, s71, 0x1000\n\t"
    "buffer_load_dwordx4 a[176:179], v224, s[44:47], s71 offen offset:0 sc1\n\t"
    "buffer_load_dwordx4 a[180:183], v224, s[44:47], s71 offen offset:1024 sc1\n\t"
    "buffer_load_dwordx4 a[184:187], v224, s[44:47], s71 offen offset:2048 sc1\n\t"
    "buffer_load_dwordx4 a[188:191], v224, s[44:47], s71 offen offset:3072 sc1\n\t"
    "s_lshl_b32 s71, %14, 14\n\t"
    "s_add_u32 s71, s71, 262144\n\t"
    "s_and_b32 s71, s71, 196608\n\t"
    "s_add_u32 s71, s71, s70\n\t"
    "buffer_load_dwordx4 a[192:195], v224, s[44:47], s71 offen offset:0 sc1\n\t"
    "buffer_load_dwordx4 a[196:199], v224, s[44:47], s71 offen offset:1024 sc1\n\t"
    "buffer_load_dwordx4 a[200:203], v224, s[44:47], s71 offen offset:2048 sc1\n\t"
    "buffer_load_dwordx4 a[204:207], v224, s[44:47], s71 offen offset:3072 sc1\n\t"
    "s_add_u32 s71, s71, 0x1000\n\t"
    "buffer_load_dwordx4 a[208:211], v224, s[44:47], s71 offen offset:0 sc1\n\t"
    "buffer_load_dwordx4 a[212:215], v224, s[44:47], s71 offen offset:1024 sc1\n\t"
    "buffer_load_dwordx4 a[216:219], v224, s[44:47], s71 offen offset:2048 sc1\n\t"
    "buffer_load_dwordx4 a[220:223], v224, s[44:47], s71 offen offset:3072 sc1\n\t"
    "s_lshl_b32 s71, %14, 14\n\t"
    "s_add_u32 s71, s71, 327680\n\t"
    "s_and_b32 s71, s71, 196608\n\t"
    "s_add_u32 s71, s71, s70\n\t"
    "buffer_load_dwordx4 a[224:227], v224, s[44:47], s71 offen offset:0 sc1\n\t"
    "buffer_load_dwordx4 a[228:231], v224, s[44:47], s71 offen offset:1024 sc1\n\t"
    "buffer_load_dwordx4 a[232:235], v224, s[44:47], s71 offen offset:2048 sc1\n\t"
    "buffer_load_dwordx4 a[236:239], v224, s[44:47], s71 offen offset:3072 sc1\n\t"
    "s_add_u32 s71, s71, 0x1000\n\t"
    "buffer_load_dwordx4 a[240:243], v224, s[44:47], s71 offen offset:0 sc1\n\t"
    "buffer_load_dwordx4 a[244:247], v224, s[44:47], s71 offen offset:1024 sc1\n\t"
    "buffer_load_dwordx4 a[248:251], v224, s[44:47], s71 offen offset:2048 sc1\n\t"
    "buffer_load_dwordx4 a[252:255], v224, s[44:47], s71 offen offset:3072 sc1\n\t"
    "Ldn_154_%=:\n\t"
    "LATT_END:\n\t"
    "LTAIL_BEG:\n\t"
    : "={v253}"(wl) : "s"((unsigned)pq), "s"((unsigned)(pq >> 32)), "s"((unsigned)pk), "s"((unsigned)(pk >> 32)), "s"((unsigned)pv), "s"((unsigned)(pv >> 32)),
       "s"((unsigned)pc), "s"((unsigned)(pc >> 32)), "s"(wave), "s"(pr), "i"(0xfc2000),
       "s"(w0), "s"(w1), "s"(wc), "s"((unsigned long long)dbg + (bid * 4 + wave) * 256ull), "s"((unsigned)pw), "s"((unsigned)(pw >> 32)), "s"((unsigned long long)mod), "s"((unsigned long long)pf + (bid * 4 + wave) * 26624ull)
    : "v0", "v1", "v2", "v3", "v4", "v5", "v6", "v7", "v8", "v9", "v10", "v11", "v12", "v13", "v14", "v15", "v16", "v17", "v18", "v19", "v20", "v21", "v22", "v23", "v24", "v25", "v26", "v27", "v28", "v29", "v30", "v31", "v32", "v33", "v34", "v35", "v36", "v37", "v38", "v39", "v40", "v41", "v42", "v43", "v44", "v45", "v46", "v47", "v48", "v49", "v50", "v51", "v52", "v53", "v54", "v55", "v56", "v57", "v58", "v59", "v60", "v61", "v62", "v63", "v64", "v65", "v66", "v67", "v68", "v69", "v70", "v71", "v72", "v73", "v74", "v75", "v76", "v77", "v78", "v79", "v80", "v81", "v82", "v83", "v84", "v85", "v86", "v87", "v88", "v89", "v90", "v91", "v92", "v93", "v94", "v95", "v96", "v97", "v98", "v99", "v100", "v101", "v102", "v103", "v104", "v105", "v106", "v107", "v108", "v109", "v110", "v111", "v112", "v113", "v114", "v115", "v116", "v117", "v118", "v119", "v120", "v121", "v122", "v123", "v124", "v125", "v126", "v127", "v128", "v129", "v130", "v131", "v132", "v133", "v134", "v135", "v136", "v137", "v138", "v139", "v140", "v141", "v142", "v143", "v144", "v145", "v146", "v147", "v148", "v149", "v150", "v151", "v152", "v153", "v154", "v155", "v156", "v157", "v158", "v159", "v160", "v161", "v162", "v163", "v164", "v165", "v166", "v167", "v168", "v169", "v170", "v171", "v172", "v173", "v174", "v175", "v176", "v177", "v178", "v179", "v180", "v181", "v182", "v183", "v184", "v185", "v186", "v187", "v188", "v189", "v190", "v191", "v192", "v193", "v194", "v195", "v196", "v197", "v198", "v199", "v200", "v201", "v202", "v203", "v204", "v205", "v206", "v207", "v208", "v209", "v210", "v211", "v212", "v213", "v214", "v215", "v216", "v217", "v218", "v219", "v220", "v221", "v222", "v223", "v224", "v225", "v226", "v227", "v228", "v229", "v230", "v231", "v232", "v233", "v234", "v235", "v236", "v237", "v238", "v239", "v240", "v241", "v242", "v243", "v244", "v245", "v246", "v247", "v248", "v249", "v250", "v251", "v252", "v254", "v255", "a0", "a1", "a2", "a3", "a4", "a5", "a6", "a7", "a8", "a9", "a10", "a11", "a12", "a13", "a14", "a15", "a16", "a17", "a18", "a19", "a20", "a21", "a22", "a23", "a24", "a25", "a26", "a27", "a28", "a29", "a30", "a31", "a32", "a33", "a34", "a35", "a36", "a37", "a38", "a39", "a40", "a41", "a42", "a43", "a44", "a45", "a46", "a47", "a48", "a49", "a50", "a51", "a52", "a53", "a54", "a55", "a56", "a57", "a58", "a59", "a60", "a61", "a62", "a63", "a64", "a65", "a66", "a67", "a68", "a69", "a70", "a71", "a72", "a73", "a74", "a75", "a76", "a77", "a78", "a79", "a80", "a81", "a82", "a83", "a84", "a85", "a86", "a87", "a88", "a89", "a90", "a91", "a92", "a93", "a94", "a95", "a96", "a97", "a98", "a99", "a100", "a101", "a102", "a103", "a104", "a105", "a106", "a107", "a108", "a109", "a110", "a111", "a112", "a113", "a114", "a115", "a116", "a117", "a118", "a119", "a120", "a121", "a122", "a123", "a124", "a125", "a126", "a127", "a128", "a129", "a130", "a131", "a132", "a133", "a134", "a135", "a136", "a137", "a138", "a139", "a140", "a141", "a142", "a143", "a144", "a145", "a146", "a147", "a148", "a149", "a150", "a151", "a152", "a153", "a154", "a155", "a156", "a157", "a158", "a159", "a160", "a161", "a162", "a163", "a164", "a165", "a166", "a167", "a168", "a169", "a170", "a171", "a172", "a173", "a174", "a175", "a176", "a177", "a178", "a179", "a180", "a181", "a182", "a183", "a184", "a185", "a186", "a187", "a188", "a189", "a190", "a191", "a192", "a193", "a194", "a195", "a196", "a197", "a198", "a199", "a200", "a201", "a202", "a203", "a204", "a205", "a206", "a207", "a208", "a209", "a210", "a211", "a212", "a213", "a214", "a215", "a216", "a217", "a218", "a219", "a220", "a221", "a222", "a223", "a224", "a225", "a226", "a227", "a228", "a229", "a230", "a231", "a232", "a233", "a234", "a235", "a236", "a237", "a238", "a239", "a240", "a241", "a242", "a243", "a244", "a245", "a246", "a247", "a248", "a249", "a250", "a251", "a252", "a253", "a254", "a255", "s36", "s37", "s38", "s39", "s40", "s41", "s42", "s43", "s44", "s45", "s46", "s47", "s48", "s49", "s50", "s51", "s52", "s53", "s54", "s55", "s56", "s57", "s58", "s59", "s60", "s61", "s62", "s63", "s64", "s65", "s66", "s67", "s68", "s69", "s70", "s71", "s72", "s73", "s74", "s75", "s76", "s77", "s78", "s79", "s80", "s81", "s82", "s83", "s84", "s85", "s86", "s87", "s88", "s89", "s90", "s91", "s92", "s93", "s94", "s95", "s96", "s97", "s98", "s99", "vcc", "m0", "scc", "memory");
  typedef const unsigned long long __attribute__((address_space(4))) * KA;
  KA ka = (KA)__builtin_amdgcn_kernarg_segment_ptr();
  int b2 = bid, w2 = wave;
  asm volatile("" : "+s"(ka), "+s"(b2), "+s"(w2));
  char* w_ = (char*)ka[15];
  if (w_ == nullptr) return;
  extern __shared__ __attribute__((aligned(16))) char smem[];
  tail4<true>(smem, b2, w2, __builtin_amdgcn_mbcnt_hi(~0u, __builtin_amdgcn_mbcnt_lo(~0u, 0u)), (const u16*)(w_ + WS_CTX), (const u16*)ka[5], (const u16*)ka[12], (const float*)w_,
              (const u16*)ka[6], (const u16*)ka[7], (const u16*)ka[13], (const u16*)ka[8], (u16*)(w_ + WS_PART), (u16*)(w_ + WS_X1), (u16*)(w_ + WS_H2), (u16*)(w_ + WS_Z2), (float*)(w_ + WS_ST2),
              (u16*)(w_ + WS_U), (u16*)ka[14], (unsigned*)(w_ + WS_FL), wl);
}

extern "C" void kernel_launch(void* const* d_in, const int* in_sizes, int n_in,
                              void* d_out, int out_size, void* d_ws, size_t ws_size,
                              hipStream_t stream) {
  const u16* x = (const u16*)d_in[0];
  const u16* ckv = (const u16*)d_in[1];
  const u16* wq = (const u16*)d_in[2];
  const u16* wuk = (const u16*)d_in[3];
  const u16* wuv = (const u16*)d_in[4];
  const u16* wo = (const u16*)d_in[5];
  const u16* win = (const u16*)d_in[7];
  const u16* wsp = (const u16*)d_in[8];
  const u16* bsp = (const u16*)d_in[9];
  const u16* wout = (const u16*)d_in[10];
  const u16* c = (const u16*)d_in[11];
  const u16* wmod = (const u16*)d_in[12];
  u16* out = (u16*)d_out;

  char* ws = (char*)d_ws;
  const size_t MB = 1024 * 1024;
  float* mod = (float*)(ws);
  u16* h = (u16*)(ws + 1 * MB);
  u16* kimg = (u16*)(ws + 20 * MB);
  u16* vimg = (u16*)(ws + 36 * MB);
  u16* ctx = (u16*)(ws + WS_CTX);
  u16* part = (u16*)(ws + WS_PART);
  u16* x1 = (u16*)(ws + WS_X1);
  u16* h2 = (u16*)(ws + WS_H2);
  u16* z1 = (u16*)(ws + 80 * MB);
  u16* z2n = (u16*)(ws + WS_Z2);
  u16* u = (u16*)(ws + WS_U);
  float* part32 = (float*)(ws + 96 * MB);
  u16* z2 = (u16*)(ws + 112 * MB);

  (void)hipFuncSetAttribute((const void*)k_attn, hipFuncAttributeMaxDynamicSharedMemorySize, 163840);
  (void)hipFuncSetAttribute((const void*)k_kv, hipFuncAttributeMaxDynamicSharedMemorySize, 163840);
  int dev = 0, cus = 0, occ = 0;
  const bool one = hipGetDevice(&dev) == hipSuccess && hipDeviceGetAttribute(&cus, hipDeviceAttributeMultiprocessorCount, dev) == hipSuccess &&
                   hipOccupancyMaxActiveBlocksPerMultiprocessor(&occ, (const void*)k_attn, 256, 163840) == hipSuccess && cus >= 256 && occ >= 1;

  k_kv<<<256, 256, 163840, stream>>>(ckv, wuk, wuv, kimg, vimg, x, h, c, wmod, mod, wq, (unsigned*)(ws + 121 * MB), part
  );
  k_attn<<<256, 256, 163840, stream>>>(h, wq, kimg, vimg, ctx, wo, win, wsp, wout, mod, (unsigned*)(ws + 120 * MB), part, x, bsp, out, one ? ws : nullptr);
  if (one) return;
  (void)hipFuncSetAttribute((const void*)k_oproj, hipFuncAttributeMaxDynamicSharedMemorySize, 7 * 16384);
  (void)hipFuncSetAttribute((const void*)k_in, hipFuncAttributeMaxDynamicSharedMemorySize, 147456);
  (void)hipFuncSetAttribute((const void*)k_sp, hipFuncAttributeMaxDynamicSharedMemorySize, 131072);
  (void)hipFuncSetAttribute((const void*)k_out, hipFuncAttributeMaxDynamicSharedMemorySize, 131072);

  k_oproj<<<256, 512, 7 * 16384, stream>>>(ctx, wo, part32);
  k_ln2<<<256, 256, 0, stream>>>(x, part32, mod, x1, h2);
  k_in<<<256, 512, 147456, stream>>>(h2, win, z1, z2);
  k_lnz<<<128, 512, 0, stream>>>(z2, z2n);
  k_sp<<<256, 512, 131072, stream>>>(wsp, z2n, bsp, z1, u);
  k_out<<<256, 512, 131072, stream>>>(u, wout, x1, mod, out);
}
```
